```python
import torch

def _softmax(x, axis=-1):
    return torch.softmax(x, dim=axis)
def _gelu(x):
    return torch.nn.functional.gelu(x, approximate="tanh")
def _silu(x):
    return torch.nn.functional.silu(x)
def _sigmoid(x):
    return torch.sigmoid(x)
def _layernorm(x, axis=-1, eps=1e-5):
    m = x.mean(dim=axis, keepdim=True)
    v = x.var(dim=axis, keepdim=True, unbiased=False)
    return (x - m) / torch.sqrt(v + eps)
def _scatter_add(tgt, idx, src, axis=0):
    return tgt.clone().index_add_(axis, idx, src)
def _segment_sum(src, seg, n):
    out = torch.zeros((n,) + src.shape[1:], dtype=src.dtype)
    return out.index_add_(0, seg, src)
def _pad(x, pw):
    flat = tuple(v for pair in reversed(pw) for v in pair)
    return torch.nn.functional.pad(x, flat)

kernel_name = 'recurrent_scan__compressed_kv_cla__geglu_ffn__prompt'

def setup_inputs():

    _w = (lambda a: (lambda t: t.to(torch.bfloat16) if t.is_floating_point() else t)(torch.as_tensor(a)))
    def _A_setup():
        B, H, Sq, Sk, D, M = 1, 32, 2048, 2048, 128, 512
        Dc = 4 * D
        return {
            "x": _w((lambda *s: torch.randn(*s, dtype=torch.bfloat16) * 0.05)(B, Sq, M)),
            "c_kv": _w((lambda *s: torch.randn(*s, dtype=torch.bfloat16) * 0.2)(B, Sk, Dc)),
            "w_q": _w((lambda *s: torch.randn(*s, dtype=torch.bfloat16) * 3.2)(M, H, D) * (M ** -0.5)),
            "w_uk": _w((lambda *s: torch.randn(*s, dtype=torch.bfloat16) * 3.2)(Dc, H, D) * (Dc ** -0.5)),
            "w_uv": _w((lambda *s: torch.randn(*s, dtype=torch.bfloat16) * 0.2)(Dc, H, D) * (Dc ** -0.5)),
            "w_o": _w((lambda *s: torch.randn(*s, dtype=torch.bfloat16) * 0.2)(H, D, M) * ((H * D) ** -0.5)),
        }
    _A_in = _A_setup()

    def _B_setup():
        B, S, D, F = 1, 2048, 512, 1792
        return {
            "x": _w((lambda *s: torch.randn(*s, dtype=torch.bfloat16) * 0.05)(B, S, D)),
            "w_gate": _w((lambda *s: torch.randn(*s, dtype=torch.bfloat16) * 0.1)(D, F)),
            "w_up": _w((lambda *s: torch.randn(*s, dtype=torch.bfloat16) * 0.1)(D, F)),
            "w_down": _w((lambda *s: torch.randn(*s, dtype=torch.bfloat16) * 0.1)(F, D)),
        }
    _B_in = _B_setup()

    return {
        "A_x": _A_in["x"],
        "A_c_kv": _A_in["c_kv"],
        "A_w_q": _A_in["w_q"],
        "A_w_uk": _A_in["w_uk"],
        "A_w_uv": _A_in["w_uv"],
        "A_w_o": _A_in["w_o"],
        "B_x": _B_in["x"],
        "B_w_gate": _B_in["w_gate"],
        "B_w_up": _B_in["w_up"],
        "B_w_down": _B_in["w_down"],
    }

def reference(A_x, A_c_kv, A_w_q, A_w_uk, A_w_uv, A_w_o, B_x, B_w_gate, B_w_up, B_w_down):
    def _A_ref(x, c_kv, w_q, w_uk, w_uv, w_o):
        B, Sq, M = x.shape[0], x.shape[1], x.shape[2]
        Sk = c_kv.shape[1]
        H, D = w_q.shape[1], w_q.shape[2]
        q = torch.einsum("bqm,mhd->bhqd", x, w_q)
        k = torch.einsum("bkc,chd->bhkd", c_kv, w_uk)
        v = torch.einsum("bkc,chd->bhkd", c_kv, w_uv)
        scores = torch.einsum("bhqd,bhkd->bhqk", q, k) * (D ** -0.5)
        off = Sk - Sq
        qi = torch.arange(Sq)[:, None]
        kj = torch.arange(Sk)[None, :]
        cmask = kj <= (qi + off)
        scores = torch.where(cmask[None, None], scores, -1e9)
        p = _softmax(scores, axis=-1)
        ctx = torch.einsum("bhqk,bhkd->bhqd", p, v)
        return torch.einsum("bhqd,hdm->bqm", ctx, w_o)

    def _B_ref(x, w_gate, w_up, w_down):
        return (_gelu(x @ w_gate) * (x @ w_up)) @ w_down

    x = A_x
    xn = _layernorm(x, axis=-1)
    s = _A_ref(xn, A_c_kv, A_w_q, A_w_uk, A_w_uv, A_w_o)
    g = _silu(_B_ref(xn, B_w_gate, B_w_up, B_w_down))
    return x + s * g
```

```cpp
#include <hip/hip_runtime.h>
#include <hip/hip_bf16.h>
#include <stdint.h>

typedef short bf16x8 __attribute__((ext_vector_type(8)));
typedef float f32x16 __attribute__((ext_vector_type(16)));
typedef float f32x2 __attribute__((ext_vector_type(2)));
typedef unsigned u32x4 __attribute__((ext_vector_type(4)));
typedef __bf16 bf2_t __attribute__((ext_vector_type(2)));
typedef unsigned short u16;
#define DI __device__ __forceinline__
#define MFMA(a, b, c) __builtin_amdgcn_mfma_f32_32x32x16_bf16((a), (b), (c), 0, 0, 0)

constexpr int S = 2048, M = 512, NH = 32, D = 128, F = 1792, HD = NH * D;
constexpr size_t MB = 1024 * 1024;
constexpr size_t OFF_XN = 0, OFF_C = 2 * MB, OFF_Q = 4 * MB, OFF_K = 20 * MB, OFF_V = 36 * MB, OFF_H = 52 * MB, OFF_CTX = 60 * MB;

DI float bf2f(u16 v) { return __uint_as_float(((unsigned)v) << 16); }
DI unsigned cvtpk(float lo, float hi) { f32x2 v = {lo, hi}; bf2_t b = __builtin_convertvector(v, bf2_t); return __builtin_bit_cast(unsigned, b); }
DI float rbf(float x) { return __uint_as_float(cvtpk(x, x) << 16); }
DI int crow(int reg, int h) { return (reg & 3) + 8 * (reg >> 2) + 4 * h; }
DI bf16x8 pack8(const f32x16& x, int s, float sc) {
  u32x4 p;
  p[0] = cvtpk(x[8 * s + 0] * sc, x[8 * s + 1] * sc); p[1] = cvtpk(x[8 * s + 2] * sc, x[8 * s + 3] * sc);
  p[2] = cvtpk(x[8 * s + 4] * sc, x[8 * s + 5] * sc); p[3] = cvtpk(x[8 * s + 6] * sc, x[8 * s + 7] * sc);
  return __builtin_bit_cast(bf16x8, p);
}

__global__ __launch_bounds__(128) void k0_ln(const u16* __restrict__ x, const u16* __restrict__ c, char* __restrict__ ws) {
  const int lane = threadIdx.x & 63, w = blockIdx.x * 2 + (threadIdx.x >> 6);
  const int which = w >> 8, r0 = (w & 255) * 8;
  const u16* src = which ? c : x;
  char* dst = ws + (which ? OFF_C : OFF_XN);
  const int tb = r0 >> 5, rr = r0 & 31, ks = lane >> 1, h = lane & 1;
  u32x4 v[8];
#pragma unroll
  for (int i = 0; i < 8; ++i) v[i] = *(const u32x4*)(src + (size_t)(r0 + i) * M + lane * 8);
  if (!which) {
#pragma unroll
    for (int i = 0; i < 8; ++i) {
      float f[8];
#pragma unroll
      for (int j = 0; j < 4; ++j) { f[2 * j] = __uint_as_float(v[i][j] << 16); f[2 * j + 1] = __uint_as_float(v[i][j] & 0xffff0000u); }
      float s = 0;
#pragma unroll
      for (int j = 0; j < 8; ++j) s += f[j];
#pragma unroll
      for (int o = 32; o; o >>= 1) s += __shfl_xor(s, o);
      const float mean = s * (1.f / M);
      float q = 0;
#pragma unroll
      for (int j = 0; j < 8; ++j) { float d = f[j] - mean; q += d * d; }
#pragma unroll
      for (int o = 32; o; o >>= 1) q += __shfl_xor(q, o);
      const float rstd = 1.0f / sqrtf(q * (1.f / M) + 1e-5f);
#pragma unroll
      for (int j = 0; j < 4; ++j) v[i][j] = cvtpk((f[2 * j] - mean) * rstd, (f[2 * j + 1] - mean) * rstd);
    }
  }
#pragma unroll
  for (int i = 0; i < 8; ++i) *(u32x4*)(dst + ((size_t)(tb * 32 + ks) * 64 + h * 32 + rr + i) * 16) = v[i];
}

template <bool PERM> DI bf16x8 wfrag(const u16* __restrict__ W, int ldw, int k0, int col, int h) {
  bf16x8 a;
#pragma unroll
  for (int e = 0; e < 8; ++e) { const int k = k0 + (PERM ? (8 * (e >> 2) + 4 * h + (e & 3)) : (8 * h + e)); a[e] = (short)W[(size_t)k * ldw + col]; }
  return a;
}
DI float gelu_tanh(float g) {
  const float u2 = g * (1.5957691216057308f + 0.07135481282975853f * g * g);
  return g / (1.f + __expf(-u2));
}

__global__ __launch_bounds__(256) void k1_simple(const u16* __restrict__ wq, const u16* __restrict__ wuk, const u16* __restrict__ wuv,
                                                 const u16* __restrict__ wg, const u16* __restrict__ wu, char* __restrict__ ws) {
  const int lane = threadIdx.x & 63, r = lane & 31, h = lane >> 5;
  const int gw = blockIdx.x * 4 + (threadIdx.x >> 6);
  const int tb = gw & 63, cb = gw >> 6;
  f32x16 acc = {}, acc2 = {};
  if (cb < 256) {
    const bool isq = cb < 128; const int c = cb & 127;
    const char* A = ws + (isq ? OFF_XN : OFF_C); const u16* W = isq ? wq : wuk;
    for (int ks = 0; ks < 32; ++ks) {
      bf16x8 xb = *(const bf16x8*)(A + ((size_t)(tb * 32 + ks) * 64 + lane) * 16);
      bf16x8 wa = wfrag<false>(W, HD, ks * 16, c * 32 + r, h);
      acc = MFMA(wa, xb, acc);
    }
    const int head = c >> 2, db = c & 3;
    const float sc = isq ? 0.08838834764831845f * 1.4426950408889634f : 1.f;
    char* dst = ws + (isq ? OFF_Q : OFF_K);
#pragma unroll
    for (int s = 0; s < 2; ++s) *(bf16x8*)(dst + ((((size_t)head * 64 + tb) * 8 + db * 2 + s) * 64 + lane) * 16) = pack8(acc, s, sc);
  } else if (cb < 384) {
    const int c = cb - 256;
    const char* A = ws + OFF_C;
    for (int ks = 0; ks < 32; ++ks) {
      bf16x8 xa = *(const bf16x8*)(A + ((size_t)(tb * 32 + ks) * 64 + lane) * 16);
      bf16x8 wb = wfrag<false>(wuv, HD, ks * 16, c * 32 + r, h);
      acc = MFMA(xa, wb, acc);
    }
    const int head = c >> 2, db = c & 3;
#pragma unroll
    for (int s = 0; s < 2; ++s) *(bf16x8*)(ws + OFF_V + (((((size_t)head * 64 + tb) * 2 + s) * 4 + db) * 64 + lane) * 16) = pack8(acc, s, 1.f);
  } else {
    const int c = cb - 384;
    const char* A = ws + OFF_XN;
    for (int ks = 0; ks < 32; ++ks) {
      bf16x8 xb = *(const bf16x8*)(A + ((size_t)(tb * 32 + ks) * 64 + lane) * 16);
      bf16x8 wa = wfrag<false>(wg, F, ks * 16, c * 32 + r, h);
      bf16x8 wa2 = wfrag<false>(wu, F, ks * 16, c * 32 + r, h);
      acc = MFMA(wa, xb, acc);
      acc2 = MFMA(wa2, xb, acc2);
    }
#pragma unroll
    for (int i = 0; i < 16; ++i) acc[i] = gelu_tanh(acc[i]) * acc2[i];
#pragma unroll
    for (int s = 0; s < 2; ++s) *(bf16x8*)(ws + OFF_H + (((size_t)tb * 112 + c * 2 + s) * 64 + lane) * 16) = pack8(acc, s, 1.f);
  }
}

__global__ __launch_bounds__(256) void k2_simple(char* __restrict__ ws) {
  const int lane = threadIdx.x & 63, r = lane & 31, h = lane >> 5;
  const int gw = blockIdx.x * 4 + (threadIdx.x >> 6);
  const int head = gw & 31, tb = 63 - (gw >> 5);
  bf16x8 q[8];
#pragma unroll
  for (int ks = 0; ks < 8; ++ks) q[ks] = *(const bf16x8*)(ws + OFF_Q + ((((size_t)head * 64 + tb) * 8 + ks) * 64 + lane) * 16);
  f32x16 o[4] = {};
  float m = -1e30f, l = 0.f;
  for (int kb = 0; kb <= tb; ++kb) {
    f32x16 s = {};
#pragma unroll
    for (int ks = 0; ks < 8; ++ks) {
      bf16x8 kf = *(const bf16x8*)(ws + OFF_K + ((((size_t)head * 64 + kb) * 8 + ks) * 64 + lane) * 16);
      s = MFMA(kf, q[ks], s);
    }
    if (kb == tb) {
#pragma unroll
      for (int i = 0; i < 16; ++i) if (crow(i, h) > r) s[i] = -__builtin_inff();
    }
    float mx = s[0];
#pragma unroll
    for (int i = 1; i < 16; ++i) mx = fmaxf(mx, s[i]);
    mx = fmaxf(mx, __shfl_xor(mx, 32));
    const float mn = fmaxf(m, mx);
    const float al = __builtin_amdgcn_exp2f(m - mn);
    m = mn;
    float ps = 0;
#pragma unroll
    for (int i = 0; i < 16; ++i) { s[i] = __builtin_amdgcn_exp2f(s[i] - mn); ps += s[i]; }
    ps += __shfl_xor(ps, 32);
    l = l * al + ps;
#pragma unroll
    for (int db = 0; db < 4; ++db)
#pragma unroll
      for (int i = 0; i < 16; ++i) o[db][i] *= al;
#pragma unroll
    for (int sx = 0; sx < 2; ++sx) {
      bf16x8 p = pack8(s, sx, 1.f);
#pragma unroll
      for (int db = 0; db < 4; ++db) {
        bf16x8 vf = *(const bf16x8*)(ws + OFF_V + (((((size_t)head * 64 + kb) * 2 + sx) * 4 + db) * 64 + lane) * 16);
        o[db] = MFMA(vf, p, o[db]);
      }
    }
  }
  const float il = 1.f / l;
#pragma unroll
  for (int db = 0; db < 4; ++db)
#pragma unroll
    for (int s = 0; s < 2; ++s) *(bf16x8*)(ws + OFF_CTX + (((size_t)tb * 256 + head * 8 + db * 2 + s) * 64 + lane) * 16) = pack8(o[db], s, il);
}

__global__ __launch_bounds__(64) void k3_simple(const u16* __restrict__ x, const u16* __restrict__ wo, const u16* __restrict__ wd, char* __restrict__ ws, u16* __restrict__ out) {
  const int lane = threadIdx.x & 63, r = lane & 31, h = lane >> 5;
  const int gw = blockIdx.x;
  const int tb = gw & 63, nb = gw >> 6;
  f32x16 sa = {}, da = {};
  for (int ks = 0; ks < 256; ++ks) {
    bf16x8 b = *(const bf16x8*)(ws + OFF_CTX + (((size_t)tb * 256 + ks) * 64 + lane) * 16);
    bf16x8 a = wfrag<true>(wo, M, ks * 16, nb * 32 + r, h);
    sa = MFMA(a, b, sa);
  }
  for (int ks = 0; ks < 112; ++ks) {
    bf16x8 b = *(const bf16x8*)(ws + OFF_H + (((size_t)tb * 112 + ks) * 64 + lane) * 16);
    bf16x8 a = wfrag<true>(wd, M, ks * 16, nb * 32 + r, h);
    da = MFMA(a, b, da);
  }
  const int tok = tb * 32 + r;
#pragma unroll
  for (int i = 0; i < 16; ++i) {
    const int n = nb * 32 + crow(i, h);
    const float d = da[i], g = d / (1.f + __expf(-d));
    const float o = bf2f(x[(size_t)tok * M + n]) + sa[i] * g;
    out[(size_t)tok * M + n] = (u16)(cvtpk(o, o) & 0xffff);
  }
}

extern "C" void kernel_launch(void* const* d_in, const int* in_sizes, int n_in, void* d_out, int out_size, void* d_ws, size_t ws_size, hipStream_t stream) {
  const u16* x = (const u16*)d_in[0]; const u16* c = (const u16*)d_in[1];
  const u16* wq = (const u16*)d_in[2]; const u16* wuk = (const u16*)d_in[3]; const u16* wuv = (const u16*)d_in[4]; const u16* wo = (const u16*)d_in[5];
  const u16* wg = (const u16*)d_in[7]; const u16* wu = (const u16*)d_in[8]; const u16* wd = (const u16*)d_in[9];
  char* ws = (char*)d_ws;
  k0_ln<<<256, 128, 0, stream>>>(x, c, ws);
  k1_simple<<<64 * 440 / 4, 256, 0, stream>>>(wq, wuk, wuv, wg, wu, ws);
  k2_simple<<<32 * 64 / 4, 256, 0, stream>>>(ws);
  k3_simple<<<64 * 16, 64, 0, stream>>>(x, wo, wd, ws, (u16*)d_out);
}
```

```cpp
#include <hip/hip_runtime.h>
#include <stdint.h>
typedef unsigned short u16;

__global__ __launch_bounds__(256, 1) __attribute__((aligned(4096))) void k1_mega(const u16* x, const u16* c, const u16* wuk, char* ws, const u16* wuv, const u16* wq, const u16* wg, const u16* wu,
                                                                                 const u16* wo, const u16* wd, u16* out) {
  __shared__ __attribute__((aligned(16))) char lds[163840];
  const unsigned wid = __builtin_amdgcn_readfirstlane(threadIdx.x >> 6);
  unsigned hw = wid;
  asm volatile("" : "+s"(hw) : "s"((unsigned)(size_t)lds));
  asm volatile(
R"(START_%=:
s_load_dwordx16 s[4:19], %0, 0x0
s_load_dwordx4 s[20:23], %0, 0x40
s_load_dwordx2 s[24:25], %0, 0x50
s_getpc_b64 s[58:59]
PC0_%=:
v_mbcnt_lo_u32_b32 v120, -1, 0
v_mbcnt_hi_u32_b32 v120, -1, v120
s_lshr_b32 s77, %1, 3
s_lshl2_add_u32 s77, s77, %2
s_lshl_b32 s77, s77, 10
s_min_u32 s77, s77, DONE_%=-PC0_%=-5120
v_min_u32 v31, 39, v120
v_lshlrev_b32 v31, 7, v31
s_mov_b32 exec_lo, 0
v_add_u32 v31, s77, v31
s_mov_b32 exec_lo, -1
global_load_dword v31, v31, s[58:59] offset:64
s_lshl_b32 s77, %1, 2
s_lshr_b32 s67, 0x56420713, s77
s_and_b32 s77, s67, 7
s_lshr_b32 s28, %1, 6
s_lshl2_add_u32 s28, s77, s28
s_and_b32 s77, s77, 6
s_lshr_b32 s77, 0xc9, s77
s_and_b32 s31, s77, 3
s_and_b32 s77, s67, 1
s_lshl_b32 s77, s77, 5
s_lshr_b32 s26, %1, 3
s_and_b32 s91, s26, 7
s_or_b32 s26, s26, s77
s_sub_u32 s77, 15, s91
s_bitcmp1_b32 %1, 6
s_cselect_b32 s29, s77, s91
s_xor_b32 s30, s91, s28
s_and_b32 s30, s30, 7
s_lshl_b32 s30, s30, 6
s_lshl_b32 s75, %2, 13
s_mov_b32 s83, 0x20000
s_mov_b32 s87, 0x20000
s_bitcmp1_b32 s28, 4
s_cselect_b32 s77, s77, s91
s_lshl_b32 s77, s77, 4
s_and_b32 s99, s28, 15
s_add_u32 s99, s99, s77
s_lshl_b32 s77, s99, 5
s_add_u32 s77, s77, %2
s_lshl_b32 s77, s77, 8
v_and_b32 v121, 7, v120
v_lshrrev_b32 v122, 3, v120
v_lshl_add_u32 v58, v122, 10, s77
v_lshl_add_u32 v58, v121, 4, v58
v_lshrrev_b32 v124, 4, v120
v_xor_b32 v124, v124, v121
v_lshlrev_b32 v124, 4, v124
v_lshl_add_u32 v112, v122, 10, v124
v_xor_b32 v124, 64, v112
v_add_u32 v113, 7168, v124
v_add_u32 v114, 14336, v112
v_add_u32 v115, 21504, v124
s_mov_b32 s86, 0x200000
s_lshl_b32 s67, s30, 1
s_lshl_b32 s44, s29, 2
s_add_u32 s44, s44, %2
s_lshl_b32 s44, s44, 15
s_or_b32 s44, s44, s67
s_lshl_b32 s46, s29, 2
s_sub_u32 s46, 60, s46
s_add_u32 s46, s46, %2
s_lshl_b32 s46, s46, 15
s_or_b32 s46, s46, s67
v_bfe_u32 v121, v120, 2, 3
v_lshrrev_b32 v122, 5, v120
v_lshlrev_b32 v122, 6, v122
v_and_b32 v123, 3, v120
v_lshl_add_u32 v108, v123, 4, v122
v_lshl_add_u32 v104, v121, 13, v108
v_xor_b32 v122, 16, v104
v_add_u32 v105, 64512, v122
v_add_u32 v106, 129024, v104
v_add_u32 v107, 193536, v122
s_lshl_b32 s66, %2, 18
s_and_b32 s77, s30, 0x1c0
s_lshl_b32 s77, s77, 13
s_xor_b32 s66, s66, s77
v_lshlrev_b32 v22, 4, v120
s_and_b32 s91, s26, 7
s_lshl_b32 s77, s28, 3
s_add_u32 s77, s77, s91
s_lshl_b32 s77, s77, 7
s_add_u32 s77, s77, 12288
v_and_b32 v121, 31, v120
v_lshl_add_u32 v100, v121, 2, s77
s_lshl_b32 s90, s28, 7
v_mov_b32 v101, s90
s_lshl_b32 s77, s91, 7
s_add_u32 s77, s77, 6144
s_mov_b32 exec_hi, 0
v_mov_b32 v101, s77
s_mov_b32 exec_hi, -1
s_and_b32 s91, s26, 7
s_and_b32 s57, s28, 7
s_cmp_eq_u32 s91, s57
s_cselect_b32 s57, 0xff, 0
s_lshl_b32 s91, s91, 2
s_cmp_eq_u32 s91, s28
s_cselect_b32 s56, -1, 0
s_waitcnt lgkmcnt(0)
global_load_dwordx4 v[2:5], v58, s[4:5] offset:0
global_load_dwordx4 v[6:9], v58, s[4:5] offset:128
s_mov_b64 s[92:93], s[10:11]
s_add_u32 s92, s92, 0x400000
s_addc_u32 s93, s93, 0
s_cmp_lg_u32 %3, 0
s_cbranch_scc1 W0N2_%=
s_mov_b32 exec_hi, 0
global_load_dword v24, v100, s[92:93] sc1
s_mov_b64 exec, s[56:57]
global_load_dword v103, v101, s[92:93] sc1
s_mov_b64 exec, -1
W0N2_%=:
s_mov_b64 s[84:85], s[6:7]
s_and_b32 s85, s85, 0xffff
s_add_u32 m0, s75, 0
s_xor_b32 s48, s44, 0
buffer_load_dwordx4 v112, s[84:87], s48 offen offset:0 lds
buffer_load_dwordx4 v113, s[84:87], s48 offen offset:1024 lds
buffer_load_dwordx4 v114, s[84:87], s48 offen offset:2048 lds
buffer_load_dwordx4 v115, s[84:87], s48 offen offset:3072 lds
s_lshl_b32 s48, s28, 8
s_mov_b64 s[80:81], s[8:9]
s_add_u32 s80, s80, s48
s_addc_u32 s81, s81, 0
s_and_b32 s81, s81, 0xffff
s_sub_u32 s82, 0x400000, s48
s_add_u32 m0, s75, 32768
s_xor_b32 s48, s66, 0
buffer_load_dwordx4 v104, s[80:83], s48 offen offset:0 lds
buffer_load_dwordx4 v105, s[80:83], s48 offen offset:1024 lds
buffer_load_dwordx4 v106, s[80:83], s48 offen offset:2048 lds
buffer_load_dwordx4 v107, s[80:83], s48 offen offset:3072 lds
s_add_u32 m0, s75, 4096
s_xor_b32 s48, s46, 0
buffer_load_dwordx4 v112, s[84:87], s48 offen offset:0 lds
buffer_load_dwordx4 v113, s[84:87], s48 offen offset:1024 lds
buffer_load_dwordx4 v114, s[84:87], s48 offen offset:2048 lds
buffer_load_dwordx4 v115, s[84:87], s48 offen offset:3072 lds
s_bfe_u32 s77, %1, 0x10004
s_or_b32 s30, s30, s77
s_xor_b32 s77, s26, s28
s_and_b32 s77, s77, 1
s_lshl2_add_u32 s30, s77, s30
s_mov_b32 s77, 0x30810000
s_cmp_eq_u32 s31, 1
s_cselect_b32 s77, 0x21c40000, s77
s_cmp_eq_u32 s31, 2
s_cselect_b32 s77, 0x11072000, s77
s_cmp_eq_u32 s31, 3
s_cselect_b32 s77, 0x24a2000, s77
s_or_b32 s30, s30, s77
s_lshr_b32 s77, s99, 1
s_lshl_b32 s91, s77, 2
s_lshr_b32 s91, 0x75316420, s91
s_and_b32 s91, s91, 7
s_andn2_b32 s77, s77, 7
s_or_b32 s77, s77, s91
s_lshl_b32 s77, s77, 2
s_add_u32 s77, s77, %2
s_lshl_b32 s77, s77, 5
s_and_b32 s99, s99, 1
s_add_u32 s77, s77, s99
s_lshl_b32 s77, s77, 7
v_and_b32 v121, 7, v120
v_lshrrev_b32 v122, 3, v120
v_lshl_add_u32 v63, v121, 4, s77
v_lshl_add_u32 v63, v122, 8, v63
s_lshl_b32 s77, %2, 3
v_lshl_add_u32 v61, v122, 5, s77
v_lshl_add_u32 v62, v122, 5, s75
v_lshlrev_b32 v18, 2, v122
v_lshl_add_u32 v18, v121, 5, v18
v_and_b32 v122, 3, v120
v_lshl_add_u32 v61, v122, 13, v61
v_add_u32 v61, 0x10000, v61
v_add_u32 v62, 0x10000, v62
v_bfe_u32 v122, v120, 1, 3
v_lshrrev_b32 v123, 4, v120
v_xor_b32 v122, v122, v123
v_lshl_add_u32 v122, v122, 4, s75
v_and_b32 v123, 15, v120
v_lshl_add_u32 v36, v123, 7, v122
v_xor_b32 v37, 64, v36
v_add_u32 v38, 0x10000, v36
v_add_u32 v39, 0x10000, v37
v_add_u32 v40, 0x20000, v36
v_add_u32 v41, 0x20000, v37
s_and_b32 s91, s26, 7
s_lshl_b32 s77, s28, 10
s_add_u32 s77, s77, 12296
v_lshl_add_u32 v25, v121, 7, s77
s_lshl_b32 s77, s91, 7
s_add_u32 s77, s77, 12288
s_mov_b32 exec_hi, 0
v_lshl_add_u32 v25, v120, 10, s77
s_mov_b32 exec_hi, -1
v_lshrrev_b32 v121, 4, v120
v_bfe_u32 v122, v120, 2, 2
v_lshlrev_b32 v122, 6, v122
v_bfe_u32 v123, v120, 1, 1
v_lshl_add_u32 v122, v123, 5, v122
v_and_b32 v123, 1, v120
v_lshl_add_u32 v122, v123, 3, v122
v_and_b32 v123, 1, v121
v_lshl_add_u32 v122, v123, 4, v122
v_lshl_add_u32 v96, v121, 10, v122
v_xor_b32 v97, 16, v96
v_add_u32 v98, 0x10000, v96
v_add_u32 v99, 0x10000, v97
v_add_u32 v26, 0x20000, v96
v_add_u32 v27, 0x20000, v97
s_add_u32 s77, s75, 0x20000
v_add_u32 v118, s77, v22
s_and_b32 s91, s30, 1
s_lshl_b32 s65, %2, 19
s_lshr_b32 s70, s65, 1
s_xor_b32 s70, s70, s65
s_lshl_b32 s77, s91, 7
s_or_b32 s70, s70, s77
s_bfe_u32 s67, s30, 0x10002
s_lshl_b32 s77, s67, 21
s_or_b32 s65, s65, s77
s_mul_i32 s67, s67, 0xe0000
s_lshl_b32 s77, s91, 12
v_or_b32 v117, s77, v22
s_and_b32 s77, s28, 25
s_cmp_eq_u32 s77, 25
s_cselect_b32 s77, 64, 0
v_xor_b32 v108, s77, v108
s_mul_i32 s77, %2, 114688
v_bfe_u32 v121, v120, 2, 3
v_mul_u32_u24 v121, 3584, v121
v_add3_u32 v108, v108, s77, v121
v_xor_b32 v121, 16, v108
v_add_u32 v109, 27648, v121
v_add_u32 v110, 55296, v108
v_add_u32 v111, 82944, v121
s_mov_b32 s43, 0x20000
s_mov_b32 s49, 0xbdd2d3e7
v_mov_b32 v119, 0xc0135761
s_mov_b32 s98, 0x3e0293ee
s_add_u32 s77, s28, 24
s_lshr_b32 s77, s77, 1
s_min_u32 s99, s28, s77
s_lshl_b32 s99, s99, 7
s_and_b32 s77, s26, 31
s_lshl2_add_u32 s77, s77, %2
s_lshl_b32 s77, s77, 7
s_and_b32 s91, s28, 28
s_add_u32 s77, s77, s91
s_lshl_b32 s77, s77, 8
v_lshrrev_b32 v121, 4, v120
v_lshl_add_u32 v121, v121, 13, s77
v_and_b32 v122, 15, v120
v_lshl_add_u32 v1, v122, 6, v121
s_and_b32 s91, s26, 7
s_lshl_b32 s91, s91, 1
s_and_b32 s77, %2, 1
s_add_u32 s77, s77, s91
s_mul_i32 s77, s77, 114688
s_add_u32 s77, s77, s99
v_lshrrev_b32 v121, 1, v120
v_mul_u32_u24 v121, 3584, v121
v_and_b32 v122, 1, v120
v_lshl_add_u32 v122, v122, 6, s77
v_add_u32 v23, v121, v122
s_mov_b32 exec_lo, 0xc000c0
s_mov_b32 exec_hi, 0xc000c0
global_load_dword v30, v1, s[8:9]
s_mov_b64 exec, -1
s_mov_b32 exec_lo, 0xc000c0
s_mov_b32 exec_hi, 0xc000c0
global_load_dword v30, v1, s[12:13]
s_mov_b64 exec, -1
s_cmp_lt_u32 %2, 2
s_cselect_b64 s[50:51], s[16:17], s[18:19]
s_lshl_b32 s77, s29, 2
s_add_u32 s77, s77, %2
s_bitcmp1_b32 6, %2
s_cselect_b32 s91, 3, 0
s_xor_b32 s77, s77, s91
s_lshl_b32 s91, s28, 6
s_add_u32 s77, s77, s91
s_lshl_b32 s77, s77, 13
s_mov_b64 s[94:95], s[10:11]
s_add_u32 s94, s94, 0x1401000
s_addc_u32 s95, s95, 0
s_add_u32 s94, s94, s77
s_addc_u32 s95, s95, 0
s_lshl_b32 s77, s29, 2
s_sub_u32 s77, 60, s77
s_add_u32 s77, s77, %2
s_bitcmp1_b32 6, %2
s_cselect_b32 s91, 3, 0
s_xor_b32 s77, s77, s91
s_lshl_b32 s91, s28, 6
s_add_u32 s77, s77, s91
s_lshl_b32 s77, s77, 13
s_mov_b64 s[96:97], s[10:11]
s_add_u32 s96, s96, 0x1401000
s_addc_u32 s97, s97, 0
s_add_u32 s96, s96, s77
s_addc_u32 s97, s97, 0
s_lshr_b32 s77, s28, 1
s_mul_i32 s77, s77, 12
s_add_u32 s77, s77, s29
s_lshl2_add_u32 s77, s77, %2
s_cmpk_lt_u32 s77, 736
s_cselect_b32 s77, s77, 512
s_cmpk_lt_u32 s77, 512
s_cselect_b64 s[34:35], s[20:21], s[22:23]
s_and_b32 s77, s77, 511
s_lshl_b32 s77, s77, 13
s_add_u32 s34, s34, s77
s_addc_u32 s35, s35, 0
s_getreg_b32 s77, hwreg(HW_REG_HW_ID, 8, 6)
s_and_b32 s72, s77, 1
s_lshl2_add_u32 s72, s72, %2
s_bitcmp1_b64 0x81, s77
s_cselect_b32 s73, 1, 0
s_cmp_ge_u32 s28, 24
s_cselect_b32 s77, 8, 0
s_or_b32 s72, s72, s77
s_mov_b64 exec, 0
WGO_%=:
s_lshl2_add_u32 s77, s72, WTAB_%=-PC0_%=
s_add_u32 vcc_lo, s58, s77
s_addc_u32 vcc_hi, s59, 0
s_setpc_b64 vcc
WTAB_%=:
s_branch PAD0_%=
s_branch PAD1_%=
s_branch PAD2_%=
s_branch PAD3_%=
s_branch PAD4_%=
s_branch PAD5_%=
s_branch PAD6_%=
s_branch PAD7_%=
s_branch PAD9_%=
s_branch PAD27_%=
s_branch PAD28_%=
s_branch PAD29_%=
s_branch PAD30_%=
s_branch PAD31_%=
s_branch PAD32_%=
s_branch PAD33_%=
WRET_%=:
s_xor_b32 s72, s72, 4
s_sub_u32 s73, s73, 1
s_cbranch_scc0 WGO_%=
s_mov_b64 exec, -1
s_waitcnt vmcnt(15)
v_lshlrev_b32 v42, 16, v2
v_and_b32 v43, 0xffff0000, v2
v_lshlrev_b32 v44, 16, v3
v_and_b32 v45, 0xffff0000, v3
v_lshlrev_b32 v46, 16, v4
v_and_b32 v47, 0xffff0000, v4
v_lshlrev_b32 v48, 16, v5
v_and_b32 v49, 0xffff0000, v5
s_waitcnt vmcnt(14)
v_lshlrev_b32 v50, 16, v6
v_and_b32 v51, 0xffff0000, v6
v_lshlrev_b32 v52, 16, v7
v_and_b32 v53, 0xffff0000, v7
v_lshlrev_b32 v54, 16, v8
v_and_b32 v55, 0xffff0000, v8
v_lshlrev_b32 v56, 16, v9
v_and_b32 v57, 0xffff0000, v9
v_add_f32 v58, v42, v44
v_add_f32 v59, v43, v45
v_add_f32 v58, v58, v46
v_add_f32 v59, v59, v47
v_add_f32 v58, v58, v48
v_add_f32 v59, v59, v49
v_add_f32 v58, v58, v50
v_add_f32 v59, v59, v51
v_add_f32 v58, v58, v52
v_add_f32 v59, v59, v53
v_add_f32 v58, v58, v54
v_add_f32 v59, v59, v55
v_add_f32 v58, v58, v56
v_add_f32 v59, v59, v57
v_add_f32 v58, v58, v59
s_nop 1
v_add_f32_dpp v58, v58, v58 quad_perm:[1,0,3,2] row_mask:0xf bank_mask:0xf
s_nop 1
v_add_f32_dpp v58, v58, v58 quad_perm:[2,3,0,1] row_mask:0xf bank_mask:0xf
s_nop 1
v_add_f32_dpp v58, v58, v58 row_half_mirror row_mask:0xf bank_mask:0xf
v_mul_f32 v58, 0x3c000000, v58
)"
R"(v_sub_f32 v10, v42, v58
v_sub_f32 v11, v43, v58
v_sub_f32 v12, v44, v58
v_sub_f32 v13, v45, v58
v_sub_f32 v14, v46, v58
v_sub_f32 v15, v47, v58
v_sub_f32 v16, v48, v58
v_sub_f32 v17, v49, v58
v_mul_f32 v59, v10, v10
v_mul_f32 v60, v11, v11
v_fmac_f32 v59, v12, v12
v_fmac_f32 v60, v13, v13
v_fmac_f32 v59, v14, v14
v_fmac_f32 v60, v15, v15
v_fmac_f32 v59, v16, v16
v_fmac_f32 v60, v17, v17
v_sub_f32 v10, v50, v58
v_sub_f32 v11, v51, v58
v_sub_f32 v12, v52, v58
v_sub_f32 v13, v53, v58
v_sub_f32 v14, v54, v58
v_sub_f32 v15, v55, v58
v_sub_f32 v16, v56, v58
v_sub_f32 v17, v57, v58
v_fmac_f32 v59, v10, v10
v_fmac_f32 v60, v11, v11
v_fmac_f32 v59, v12, v12
v_fmac_f32 v60, v13, v13
v_fmac_f32 v59, v14, v14
v_fmac_f32 v60, v15, v15
v_fmac_f32 v59, v16, v16
v_fmac_f32 v60, v17, v17
v_add_f32 v59, v59, v60
s_nop 1
v_add_f32_dpp v59, v59, v59 quad_perm:[1,0,3,2] row_mask:0xf bank_mask:0xf
s_nop 1
v_add_f32_dpp v59, v59, v59 quad_perm:[2,3,0,1] row_mask:0xf bank_mask:0xf
s_nop 1
v_add_f32_dpp v59, v59, v59 row_half_mirror row_mask:0xf bank_mask:0xf
ds_write_b64 v61, v[58:59] offset:36864
s_cmp_lg_u32 %3, 0
s_cbranch_scc1 W0N3_%=
v_lshrrev_b32 v121, v120, v24
v_and_b32 v121, 1, v121
v_cmp_eq_u32 vcc, 0, v121
s_lshl_b32 s77, s29, 5
s_add_u32 s77, s77, s28
s_lshl_b32 s77, s77, 2
s_add_u32 s77, s77, 49152
s_min_u32 s91, s28, 20
s_mul_i32 s91, s91, 205
s_and_b32 s91, s91, 0x1800
s_add_u32 s77, s77, s91
s_mov_b32 s55, vcc_lo
v_writelane_b32 v100, s77, 12
s_xor_b32 s77, s77, 1920
v_writelane_b32 v100, s77, 13
s_and_b32 s91, s26, 7
s_lshl_b32 s91, s91, 7
s_add_u32 s91, s91, 4096
s_cmp_lg_u32 s56, 0
s_cselect_b32 s77, s91, s77
v_writelane_b32 v100, s77, 14
v_add_u32 v103, 8, v103
v_mov_b32 v121, 0
v_writelane_b32 v101, s90, 1
s_mov_b32 exec_hi, 0
v_add_u32 v103, 24, v103
s_mov_b32 exec_lo, 0xff
v_mov_b32 v121, s55
s_mov_b32 exec_lo, 0x7f50
global_store_dword v100, v121, s[92:93] sc1
s_mov_b64 exec, -1
W0N3_%=:
s_waitcnt lgkmcnt(0)
s_barrier
ds_read_b128 v[10:13], v62 offset:36864
ds_read_b128 v[14:17], v62 offset:36880
s_add_u32 m0, s75, 36864
s_xor_b32 s48, s66, 128
buffer_load_dwordx4 v104, s[80:83], s48 offen offset:0 lds
s_waitcnt lgkmcnt(1)
v_add_f32 v59, v10, v12
s_waitcnt lgkmcnt(0)
v_add_f32 v60, v14, v16
v_add_f32 v59, v59, v60
v_mul_f32 v59, 0.25, v59
v_cvt_pk_bf16_f32 v58, 0, v59
v_sub_f32 v10, v10, v59
v_sub_f32 v12, v12, v59
v_sub_f32 v14, v14, v59
v_sub_f32 v16, v16, v59
v_add_f32 v11, v11, v13
v_add_f32 v15, v15, v17
v_add_f32 v11, v11, v15
v_mul_f32 v10, v10, v10
v_fmac_f32 v10, v12, v12
v_fmac_f32 v10, v14, v14
v_fmac_f32 v10, v16, v16
buffer_load_dwordx4 v105, s[80:83], s48 offen offset:1024 lds
v_mul_f32 v60, 0x3b000000, v11
v_fmac_f32 v60, 0.25, v10
v_cvt_pk_bf16_f32 v60, 0, v60
v_add_f32 v60, 0x3727c5ac, v60
v_cvt_pk_bf16_f32 v60, 0, v60
v_sqrt_f32 v60, v60
s_nop 0
v_cvt_pk_bf16_f32 v60, 0, v60
v_rcp_f32 v60, v60
v_sub_f32 v42, v42, v58
v_sub_f32 v43, v43, v58
v_sub_f32 v44, v44, v58
v_sub_f32 v45, v45, v58
v_sub_f32 v46, v46, v58
v_sub_f32 v47, v47, v58
v_sub_f32 v48, v48, v58
v_sub_f32 v49, v49, v58
buffer_load_dwordx4 v106, s[80:83], s48 offen offset:2048 lds
v_sub_f32 v50, v50, v58
v_sub_f32 v51, v51, v58
v_sub_f32 v52, v52, v58
v_sub_f32 v53, v53, v58
v_sub_f32 v54, v54, v58
v_sub_f32 v55, v55, v58
v_sub_f32 v56, v56, v58
v_sub_f32 v57, v57, v58
v_cvt_pk_bf16_f32 v42, 0, v42
v_cvt_pk_bf16_f32 v43, 0, v43
v_cvt_pk_bf16_f32 v44, 0, v44
v_cvt_pk_bf16_f32 v45, 0, v45
v_cvt_pk_bf16_f32 v46, 0, v46
v_cvt_pk_bf16_f32 v47, 0, v47
v_cvt_pk_bf16_f32 v48, 0, v48
v_cvt_pk_bf16_f32 v49, 0, v49
buffer_load_dwordx4 v107, s[80:83], s48 offen offset:3072 lds
v_cvt_pk_bf16_f32 v50, 0, v50
v_cvt_pk_bf16_f32 v51, 0, v51
v_cvt_pk_bf16_f32 v52, 0, v52
v_cvt_pk_bf16_f32 v53, 0, v53
v_cvt_pk_bf16_f32 v54, 0, v54
v_cvt_pk_bf16_f32 v55, 0, v55
v_cvt_pk_bf16_f32 v56, 0, v56
v_cvt_pk_bf16_f32 v57, 0, v57
v_mul_f32 v42, v42, v60
v_mul_f32 v43, v43, v60
v_mul_f32 v44, v44, v60
v_mul_f32 v45, v45, v60
v_mul_f32 v46, v46, v60
v_mul_f32 v47, v47, v60
v_mul_f32 v48, v48, v60
v_mul_f32 v49, v49, v60
s_add_u32 m0, s75, 131072
s_xor_b32 s48, s44, 128
buffer_load_dwordx4 v112, s[84:87], s48 offen offset:0 lds
v_mul_f32 v50, v50, v60
v_mul_f32 v51, v51, v60
v_mul_f32 v52, v52, v60
v_mul_f32 v53, v53, v60
v_mul_f32 v54, v54, v60
v_mul_f32 v55, v55, v60
v_mul_f32 v56, v56, v60
v_mul_f32 v57, v57, v60
v_cvt_pk_bf16_f32 v2, v42, v43
v_cvt_pk_bf16_f32 v3, v44, v45
v_cvt_pk_bf16_f32 v4, v46, v47
v_cvt_pk_bf16_f32 v5, v48, v49
v_cvt_pk_bf16_f32 v6, v50, v51
v_cvt_pk_bf16_f32 v7, v52, v53
v_cvt_pk_bf16_f32 v8, v54, v55
v_cvt_pk_bf16_f32 v9, v56, v57
buffer_load_dwordx4 v113, s[84:87], s48 offen offset:1024 lds
ds_bpermute_b32 v2, v18, v2
ds_bpermute_b32 v3, v18, v3
ds_bpermute_b32 v4, v18, v4
ds_bpermute_b32 v5, v18, v5
ds_bpermute_b32 v6, v18, v6
ds_bpermute_b32 v7, v18, v7
ds_bpermute_b32 v8, v18, v8
ds_bpermute_b32 v9, v18, v9
s_waitcnt vmcnt(8)
ds_read_b128 a[0:3], v36 offset:0
ds_read_b128 a[4:7], v36 offset:2048
ds_read_b128 a[8:11], v36 offset:4096
ds_read_b128 a[12:15], v36 offset:6144
ds_read_b128 a[16:19], v37 offset:0
ds_read_b128 a[20:23], v37 offset:2048
ds_read_b128 a[24:27], v37 offset:4096
ds_read_b128 a[28:31], v37 offset:6144
buffer_load_dwordx4 v114, s[84:87], s48 offen offset:2048 lds
buffer_load_dwordx4 v115, s[84:87], s48 offen offset:3072 lds
s_add_u32 m0, s75, 135168
s_xor_b32 s48, s46, 128
buffer_load_dwordx4 v112, s[84:87], s48 offen offset:0 lds
buffer_load_dwordx4 v113, s[84:87], s48 offen offset:1024 lds
buffer_load_dwordx4 v114, s[84:87], s48 offen offset:2048 lds
buffer_load_dwordx4 v115, s[84:87], s48 offen offset:3072 lds
s_waitcnt vmcnt(8) lgkmcnt(0)
s_barrier
s_cmp_ge_u32 s28, 24
s_cbranch_scc1 P1S_%=
ds_read_b64_tr_b16 v[64:65], v96 offset:32768
ds_read_b64_tr_b16 v[66:67], v96 offset:33024
ds_read_b64_tr_b16 v[68:69], v96 offset:40960
ds_read_b64_tr_b16 v[70:71], v96 offset:41216
ds_read_b64_tr_b16 v[72:73], v97 offset:32768
ds_read_b64_tr_b16 v[74:75], v97 offset:33024
ds_read_b64_tr_b16 v[76:77], v97 offset:40960
ds_read_b64_tr_b16 v[78:79], v97 offset:41216
ds_read_b64_tr_b16 v[80:81], v96 offset:33280
ds_read_b64_tr_b16 v[82:83], v96 offset:33536
ds_read_b64_tr_b16 v[84:85], v96 offset:41472
ds_read_b64_tr_b16 v[86:87], v96 offset:41728
s_waitcnt lgkmcnt(10)
v_mfma_f32_16x16x32_bf16 v[128:131], v[64:67], a[0:3], 0
ds_read_b64_tr_b16 v[88:89], v97 offset:33280
s_add_u32 m0, s75, 65536
s_waitcnt lgkmcnt(9)
v_mfma_f32_16x16x32_bf16 v[128:131], v[68:71], a[16:19], v[128:131]
ds_read_b64_tr_b16 v[90:91], v97 offset:33536
s_xor_b32 s48, s44, 256
v_mfma_f32_16x16x32_bf16 v[144:147], v[64:67], a[4:7], 0
buffer_load_dwordx4 v112, s[84:87], s48 offen offset:0 lds
v_mfma_f32_16x16x32_bf16 v[144:147], v[68:71], a[20:23], v[144:147]
v_mfma_f32_16x16x32_bf16 v[160:163], v[64:67], a[8:11], 0
ds_read_b64_tr_b16 v[92:93], v97 offset:41472
v_mfma_f32_16x16x32_bf16 v[160:163], v[68:71], a[24:27], v[160:163]
ds_read_b64_tr_b16 v[94:95], v97 offset:41728
v_mfma_f32_16x16x32_bf16 v[176:179], v[64:67], a[12:15], 0
buffer_load_dwordx4 v113, s[84:87], s48 offen offset:1024 lds
v_mfma_f32_16x16x32_bf16 v[176:179], v[68:71], a[28:31], v[176:179]
s_waitcnt lgkmcnt(8)
v_mfma_f32_16x16x32_bf16 v[132:135], v[72:75], a[0:3], 0
ds_read_b64_tr_b16 v[64:65], v96 offset:36864
v_mfma_f32_16x16x32_bf16 v[132:135], v[76:79], a[16:19], v[132:135]
ds_read_b64_tr_b16 v[66:67], v96 offset:37120
v_mfma_f32_16x16x32_bf16 v[148:151], v[72:75], a[4:7], 0
buffer_load_dwordx4 v114, s[84:87], s48 offen offset:2048 lds
v_mfma_f32_16x16x32_bf16 v[148:151], v[76:79], a[20:23], v[148:151]
v_mfma_f32_16x16x32_bf16 v[164:167], v[72:75], a[8:11], 0
ds_read_b64_tr_b16 v[68:69], v96 offset:45056
v_mfma_f32_16x16x32_bf16 v[164:167], v[76:79], a[24:27], v[164:167]
ds_read_b64_tr_b16 v[70:71], v96 offset:45312
v_mfma_f32_16x16x32_bf16 v[180:183], v[72:75], a[12:15], 0
buffer_load_dwordx4 v115, s[84:87], s48 offen offset:3072 lds
s_add_u32 m0, s75, 69632
v_mfma_f32_16x16x32_bf16 v[180:183], v[76:79], a[28:31], v[180:183]
s_xor_b32 s48, s46, 256
s_waitcnt lgkmcnt(8)
v_mfma_f32_16x16x32_bf16 v[136:139], v[80:83], a[0:3], 0
ds_read_b64_tr_b16 v[72:73], v97 offset:36864
v_mfma_f32_16x16x32_bf16 v[136:139], v[84:87], a[16:19], v[136:139]
ds_read_b64_tr_b16 v[74:75], v97 offset:37120
v_mfma_f32_16x16x32_bf16 v[152:155], v[80:83], a[4:7], 0
buffer_load_dwordx4 v112, s[84:87], s48 offen offset:0 lds
v_mfma_f32_16x16x32_bf16 v[152:155], v[84:87], a[20:23], v[152:155]
v_mfma_f32_16x16x32_bf16 v[168:171], v[80:83], a[8:11], 0
ds_read_b64_tr_b16 v[76:77], v97 offset:45056
v_mfma_f32_16x16x32_bf16 v[168:171], v[84:87], a[24:27], v[168:171]
ds_read_b64_tr_b16 v[78:79], v97 offset:45312
v_mfma_f32_16x16x32_bf16 v[184:187], v[80:83], a[12:15], 0
buffer_load_dwordx4 v113, s[84:87], s48 offen offset:1024 lds
v_mfma_f32_16x16x32_bf16 v[184:187], v[84:87], a[28:31], v[184:187]
buffer_load_dwordx4 v114, s[84:87], s48 offen offset:2048 lds
s_waitcnt lgkmcnt(8)
v_mfma_f32_16x16x32_bf16 v[140:143], v[88:91], a[0:3], 0
ds_read_b64_tr_b16 v[80:81], v96 offset:37376
v_mfma_f32_16x16x32_bf16 v[140:143], v[92:95], a[16:19], v[140:143]
ds_read_b64_tr_b16 v[82:83], v96 offset:37632
v_mfma_f32_16x16x32_bf16 v[156:159], v[88:91], a[4:7], 0
v_mfma_f32_16x16x32_bf16 v[156:159], v[92:95], a[20:23], v[156:159]
buffer_load_dwordx4 v115, s[84:87], s48 offen offset:3072 lds
s_add_u32 m0, s75, 0
v_mfma_f32_16x16x32_bf16 v[172:175], v[88:91], a[8:11], 0
ds_read_b64_tr_b16 v[84:85], v96 offset:45568
s_xor_b32 s48, s66, 1048576
v_mfma_f32_16x16x32_bf16 v[172:175], v[92:95], a[24:27], v[172:175]
ds_read_b64_tr_b16 v[86:87], v96 offset:45824
v_mfma_f32_16x16x32_bf16 v[188:191], v[88:91], a[12:15], 0
buffer_load_dwordx4 v104, s[80:83], s48 offen offset:0 lds
s_waitcnt vmcnt(9)
v_mfma_f32_16x16x32_bf16 v[188:191], v[92:95], a[28:31], v[188:191]
s_waitcnt lgkmcnt(8)
v_mfma_f32_16x16x32_bf16 v[192:195], v[64:67], a[0:3], 0
ds_read_b64_tr_b16 v[88:89], v97 offset:37376
ds_read_b128 a[32:35], v40 offset:0
v_mfma_f32_16x16x32_bf16 v[192:195], v[68:71], a[16:19], v[192:195]
ds_read_b64_tr_b16 v[90:91], v97 offset:37632
ds_read_b128 a[36:39], v40 offset:2048
v_mfma_f32_16x16x32_bf16 v[208:211], v[64:67], a[4:7], 0
buffer_load_dwordx4 v105, s[80:83], s48 offen offset:1024 lds
v_mfma_f32_16x16x32_bf16 v[208:211], v[68:71], a[20:23], v[208:211]
v_mfma_f32_16x16x32_bf16 v[224:227], v[64:67], a[8:11], 0
ds_read_b64_tr_b16 v[92:93], v97 offset:45568
ds_read_b128 a[40:43], v40 offset:4096
v_mfma_f32_16x16x32_bf16 v[224:227], v[68:71], a[24:27], v[224:227]
ds_read_b64_tr_b16 v[94:95], v97 offset:45824
ds_read_b128 a[44:47], v40 offset:6144
v_mfma_f32_16x16x32_bf16 v[240:243], v[64:67], a[12:15], 0
buffer_load_dwordx4 v106, s[80:83], s48 offen offset:2048 lds
v_mfma_f32_16x16x32_bf16 v[240:243], v[68:71], a[28:31], v[240:243]
s_waitcnt lgkmcnt(12)
v_mfma_f32_16x16x32_bf16 v[196:199], v[72:75], a[0:3], 0
ds_read_b64_tr_b16 v[64:65], v96 offset:49152
ds_read_b128 a[48:51], v41 offset:0
v_mfma_f32_16x16x32_bf16 v[196:199], v[76:79], a[16:19], v[196:199]
ds_read_b64_tr_b16 v[66:67], v96 offset:49408
ds_read_b128 a[52:55], v41 offset:2048
v_mfma_f32_16x16x32_bf16 v[212:215], v[72:75], a[4:7], 0
buffer_load_dwordx4 v107, s[80:83], s48 offen offset:3072 lds
s_add_u32 m0, s75, 4096
v_mfma_f32_16x16x32_bf16 v[212:215], v[76:79], a[20:23], v[212:215]
s_xor_b32 s48, s66, 1048704
PAD0_%=:
s_cbranch_execz PAD8_%=
v_mfma_f32_16x16x32_bf16 v[228:231], v[72:75], a[8:11], 0
ds_read_b64_tr_b16 v[68:69], v96 offset:57344
ds_read_b128 a[56:59], v41 offset:4096
v_mfma_f32_16x16x32_bf16 v[228:231], v[76:79], a[24:27], v[228:231]
ds_read_b64_tr_b16 v[70:71], v96 offset:57600
ds_read_b128 a[60:63], v41 offset:6144
v_mfma_f32_16x16x32_bf16 v[244:247], v[72:75], a[12:15], 0
buffer_load_dwordx4 v104, s[80:83], s48 offen offset:0 lds
v_mfma_f32_16x16x32_bf16 v[244:247], v[76:79], a[28:31], v[244:247]
s_waitcnt lgkmcnt(14)
v_mfma_f32_16x16x32_bf16 v[200:203], v[80:83], a[0:3], 0
ds_read_b64_tr_b16 v[72:73], v97 offset:49152
v_mfma_f32_16x16x32_bf16 v[200:203], v[84:87], a[16:19], v[200:203]
ds_read_b64_tr_b16 v[74:75], v97 offset:49408
v_mfma_f32_16x16x32_bf16 v[216:219], v[80:83], a[4:7], 0
buffer_load_dwordx4 v105, s[80:83], s48 offen offset:1024 lds
v_mfma_f32_16x16x32_bf16 v[216:219], v[84:87], a[20:23], v[216:219]
buffer_load_dwordx4 v106, s[80:83], s48 offen offset:2048 lds
v_mfma_f32_16x16x32_bf16 v[232:235], v[80:83], a[8:11], 0
ds_read_b64_tr_b16 v[76:77], v97 offset:57344
v_mfma_f32_16x16x32_bf16 v[232:235], v[84:87], a[24:27], v[232:235]
ds_read_b64_tr_b16 v[78:79], v97 offset:57600
v_mfma_f32_16x16x32_bf16 v[248:251], v[80:83], a[12:15], 0
v_mfma_f32_16x16x32_bf16 v[248:251], v[84:87], a[28:31], v[248:251]
buffer_load_dwordx4 v107, s[80:83], s48 offen offset:3072 lds
s_waitcnt lgkmcnt(13)
v_mfma_f32_16x16x32_bf16 v[204:207], v[88:91], a[0:3], 0
ds_read_b64_tr_b16 v[80:81], v96 offset:49664
s_add_u32 m0, s75, 98304
v_mfma_f32_16x16x32_bf16 v[204:207], v[92:95], a[16:19], v[204:207]
ds_read_b64_tr_b16 v[82:83], v96 offset:49920
s_xor_b32 s48, s44, 384
v_mfma_f32_16x16x32_bf16 v[220:223], v[88:91], a[4:7], 0
buffer_load_dwordx4 v112, s[84:87], s48 offen offset:0 lds
v_mfma_f32_16x16x32_bf16 v[220:223], v[92:95], a[20:23], v[220:223]
v_mfma_f32_16x16x32_bf16 v[236:239], v[88:91], a[8:11], 0
ds_read_b64_tr_b16 v[84:85], v96 offset:57856
v_mfma_f32_16x16x32_bf16 v[236:239], v[92:95], a[24:27], v[236:239]
ds_read_b64_tr_b16 v[86:87], v96 offset:58112
v_mfma_f32_16x16x32_bf16 v[252:255], v[88:91], a[12:15], 0
buffer_load_dwordx4 v113, s[84:87], s48 offen offset:1024 lds
v_mfma_f32_16x16x32_bf16 v[252:255], v[92:95], a[28:31], v[252:255]
s_waitcnt lgkmcnt(8)
v_mfma_f32_16x16x32_bf16 v[128:131], v[64:67], a[32:35], v[128:131]
ds_read_b64_tr_b16 v[88:89], v97 offset:49664
v_mfma_f32_16x16x32_bf16 v[128:131], v[68:71], a[48:51], v[128:131]
ds_read_b64_tr_b16 v[90:91], v97 offset:49920
v_mfma_f32_16x16x32_bf16 v[144:147], v[64:67], a[36:39], v[144:147]
buffer_load_dwordx4 v114, s[84:87], s48 offen offset:2048 lds
v_mfma_f32_16x16x32_bf16 v[144:147], v[68:71], a[52:55], v[144:147]
v_mfma_f32_16x16x32_bf16 v[160:163], v[64:67], a[40:43], v[160:163]
ds_read_b64_tr_b16 v[92:93], v97 offset:57856
v_mfma_f32_16x16x32_bf16 v[160:163], v[68:71], a[56:59], v[160:163]
ds_read_b64_tr_b16 v[94:95], v97 offset:58112
v_mfma_f32_16x16x32_bf16 v[176:179], v[64:67], a[44:47], v[176:179]
buffer_load_dwordx4 v115, s[84:87], s48 offen offset:3072 lds
s_add_u32 m0, s75, 102400
v_mfma_f32_16x16x32_bf16 v[176:179], v[68:71], a[60:63], v[176:179]
s_xor_b32 s48, s46, 384
s_waitcnt lgkmcnt(8)
v_mfma_f32_16x16x32_bf16 v[132:135], v[72:75], a[32:35], v[132:135]
ds_read_b64_tr_b16 v[64:65], v96 offset:53248
v_mfma_f32_16x16x32_bf16 v[132:135], v[76:79], a[48:51], v[132:135]
ds_read_b64_tr_b16 v[66:67], v96 offset:53504
v_mfma_f32_16x16x32_bf16 v[148:151], v[72:75], a[36:39], v[148:151]
buffer_load_dwordx4 v112, s[84:87], s48 offen offset:0 lds
v_mfma_f32_16x16x32_bf16 v[148:151], v[76:79], a[52:55], v[148:151]
v_mfma_f32_16x16x32_bf16 v[164:167], v[72:75], a[40:43], v[164:167]
ds_read_b64_tr_b16 v[68:69], v96 offset:61440
v_mfma_f32_16x16x32_bf16 v[164:167], v[76:79], a[56:59], v[164:167]
ds_read_b64_tr_b16 v[70:71], v96 offset:61696
v_mfma_f32_16x16x32_bf16 v[180:183], v[72:75], a[44:47], v[180:183]
buffer_load_dwordx4 v113, s[84:87], s48 offen offset:1024 lds
v_mfma_f32_16x16x32_bf16 v[180:183], v[76:79], a[60:63], v[180:183]
buffer_load_dwordx4 v114, s[84:87], s48 offen offset:2048 lds
s_waitcnt lgkmcnt(8)
v_mfma_f32_16x16x32_bf16 v[136:139], v[80:83], a[32:35], v[136:139]
ds_read_b64_tr_b16 v[72:73], v97 offset:53248
v_mfma_f32_16x16x32_bf16 v[136:139], v[84:87], a[48:51], v[136:139]
ds_read_b64_tr_b16 v[74:75], v97 offset:53504
v_mfma_f32_16x16x32_bf16 v[152:155], v[80:83], a[36:39], v[152:155]
v_mfma_f32_16x16x32_bf16 v[152:155], v[84:87], a[52:55], v[152:155]
buffer_load_dwordx4 v115, s[84:87], s48 offen offset:3072 lds
s_add_u32 m0, s75, 131072
v_mfma_f32_16x16x32_bf16 v[168:171], v[80:83], a[40:43], v[168:171]
ds_read_b64_tr_b16 v[76:77], v97 offset:61440
)"
R"(s_xor_b32 s48, s44, 512
v_mfma_f32_16x16x32_bf16 v[168:171], v[84:87], a[56:59], v[168:171]
ds_read_b64_tr_b16 v[78:79], v97 offset:61696
v_mfma_f32_16x16x32_bf16 v[184:187], v[80:83], a[44:47], v[184:187]
buffer_load_dwordx4 v112, s[84:87], s48 offen offset:0 lds
v_mfma_f32_16x16x32_bf16 v[184:187], v[84:87], a[60:63], v[184:187]
s_waitcnt lgkmcnt(8)
v_mfma_f32_16x16x32_bf16 v[140:143], v[88:91], a[32:35], v[140:143]
ds_read_b64_tr_b16 v[80:81], v96 offset:53760
v_mfma_f32_16x16x32_bf16 v[140:143], v[92:95], a[48:51], v[140:143]
ds_read_b64_tr_b16 v[82:83], v96 offset:54016
v_mfma_f32_16x16x32_bf16 v[156:159], v[88:91], a[36:39], v[156:159]
buffer_load_dwordx4 v113, s[84:87], s48 offen offset:1024 lds
s_waitcnt vmcnt(18)
v_mfma_f32_16x16x32_bf16 v[156:159], v[92:95], a[52:55], v[156:159]
v_mfma_f32_16x16x32_bf16 v[172:175], v[88:91], a[40:43], v[172:175]
ds_read_b64_tr_b16 v[84:85], v96 offset:61952
ds_read_b128 a[64:67], v38 offset:0
v_mfma_f32_16x16x32_bf16 v[172:175], v[92:95], a[56:59], v[172:175]
ds_read_b64_tr_b16 v[86:87], v96 offset:62208
ds_read_b128 a[68:71], v38 offset:2048
v_mfma_f32_16x16x32_bf16 v[188:191], v[88:91], a[44:47], v[188:191]
buffer_load_dwordx4 v114, s[84:87], s48 offen offset:2048 lds
v_mfma_f32_16x16x32_bf16 v[188:191], v[92:95], a[60:63], v[188:191]
s_waitcnt lgkmcnt(10)
v_mfma_f32_16x16x32_bf16 v[192:195], v[64:67], a[32:35], v[192:195]
ds_read_b64_tr_b16 v[88:89], v97 offset:53760
ds_read_b128 a[72:75], v38 offset:4096
v_mfma_f32_16x16x32_bf16 v[192:195], v[68:71], a[48:51], v[192:195]
ds_read_b64_tr_b16 v[90:91], v97 offset:54016
ds_read_b128 a[76:79], v38 offset:6144
v_mfma_f32_16x16x32_bf16 v[208:211], v[64:67], a[36:39], v[208:211]
buffer_load_dwordx4 v115, s[84:87], s48 offen offset:3072 lds
s_add_u32 m0, s75, 135168
v_mfma_f32_16x16x32_bf16 v[208:211], v[68:71], a[52:55], v[208:211]
s_xor_b32 s48, s46, 512
v_mfma_f32_16x16x32_bf16 v[224:227], v[64:67], a[40:43], v[224:227]
ds_read_b64_tr_b16 v[92:93], v97 offset:61952
ds_read_b128 a[80:83], v39 offset:0
v_mfma_f32_16x16x32_bf16 v[224:227], v[68:71], a[56:59], v[224:227]
ds_read_b64_tr_b16 v[94:95], v97 offset:62208
ds_read_b128 a[84:87], v39 offset:2048
v_mfma_f32_16x16x32_bf16 v[240:243], v[64:67], a[44:47], v[240:243]
buffer_load_dwordx4 v112, s[84:87], s48 offen offset:0 lds
v_mfma_f32_16x16x32_bf16 v[240:243], v[68:71], a[60:63], v[240:243]
s_waitcnt lgkmcnt(14)
v_mfma_f32_16x16x32_bf16 v[196:199], v[72:75], a[32:35], v[196:199]
buffer_load_dwordx4 v113, s[84:87], s48 offen offset:1024 lds
v_mfma_f32_16x16x32_bf16 v[196:199], v[76:79], a[48:51], v[196:199]
v_mfma_f32_16x16x32_bf16 v[212:215], v[72:75], a[36:39], v[212:215]
v_mfma_f32_16x16x32_bf16 v[212:215], v[76:79], a[52:55], v[212:215]
buffer_load_dwordx4 v114, s[84:87], s48 offen offset:2048 lds
v_mfma_f32_16x16x32_bf16 v[228:231], v[72:75], a[40:43], v[228:231]
v_mfma_f32_16x16x32_bf16 v[228:231], v[76:79], a[56:59], v[228:231]
s_waitcnt vmcnt(15) lgkmcnt(0)
s_barrier
v_mfma_f32_16x16x32_bf16 v[244:247], v[72:75], a[44:47], v[244:247]
ds_read_b64_tr_b16 v[64:65], v96 offset:0
ds_read_b128 a[88:91], v39 offset:4096
v_mfma_f32_16x16x32_bf16 v[244:247], v[76:79], a[60:63], v[244:247]
ds_read_b64_tr_b16 v[66:67], v96 offset:256
v_mfma_f32_16x16x32_bf16 v[200:203], v[80:83], a[32:35], v[200:203]
ds_read_b64_tr_b16 v[68:69], v96 offset:8192
ds_read_b128 a[92:95], v39 offset:6144
v_mfma_f32_16x16x32_bf16 v[200:203], v[84:87], a[48:51], v[200:203]
ds_read_b64_tr_b16 v[70:71], v96 offset:8448
v_mfma_f32_16x16x32_bf16 v[216:219], v[80:83], a[36:39], v[216:219]
ds_read_b64_tr_b16 v[72:73], v97 offset:0
v_mfma_f32_16x16x32_bf16 v[216:219], v[84:87], a[52:55], v[216:219]
ds_read_b64_tr_b16 v[74:75], v97 offset:256
v_mfma_f32_16x16x32_bf16 v[232:235], v[80:83], a[40:43], v[232:235]
ds_read_b64_tr_b16 v[76:77], v97 offset:8192
v_mfma_f32_16x16x32_bf16 v[232:235], v[84:87], a[56:59], v[232:235]
ds_read_b64_tr_b16 v[78:79], v97 offset:8448
v_mfma_f32_16x16x32_bf16 v[248:251], v[80:83], a[44:47], v[248:251]
buffer_load_dwordx4 v115, s[84:87], s48 offen offset:3072 lds
s_add_u32 m0, s75, 32768
v_mfma_f32_16x16x32_bf16 v[248:251], v[84:87], a[60:63], v[248:251]
s_xor_b32 s48, s66, 2097152
v_mfma_f32_16x16x32_bf16 v[204:207], v[88:91], a[32:35], v[204:207]
ds_read_b64_tr_b16 v[80:81], v96 offset:512
v_mfma_f32_16x16x32_bf16 v[204:207], v[92:95], a[48:51], v[204:207]
ds_read_b64_tr_b16 v[82:83], v96 offset:768
v_mfma_f32_16x16x32_bf16 v[220:223], v[88:91], a[36:39], v[220:223]
buffer_load_dwordx4 v104, s[80:83], s48 offen offset:0 lds
v_mfma_f32_16x16x32_bf16 v[220:223], v[92:95], a[52:55], v[220:223]
buffer_load_dwordx4 v105, s[80:83], s48 offen offset:1024 lds
v_mfma_f32_16x16x32_bf16 v[236:239], v[88:91], a[40:43], v[236:239]
ds_read_b64_tr_b16 v[84:85], v96 offset:8704
v_mfma_f32_16x16x32_bf16 v[236:239], v[92:95], a[56:59], v[236:239]
ds_read_b64_tr_b16 v[86:87], v96 offset:8960
v_mfma_f32_16x16x32_bf16 v[252:255], v[88:91], a[44:47], v[252:255]
v_mfma_f32_16x16x32_bf16 v[252:255], v[92:95], a[60:63], v[252:255]
buffer_load_dwordx4 v106, s[80:83], s48 offen offset:2048 lds
s_waitcnt lgkmcnt(8)
v_mfma_f32_16x16x32_bf16 v[128:131], v[64:67], a[64:67], v[128:131]
ds_read_b64_tr_b16 v[88:89], v97 offset:512
v_mfma_f32_16x16x32_bf16 v[128:131], v[68:71], a[80:83], v[128:131]
ds_read_b64_tr_b16 v[90:91], v97 offset:768
v_mfma_f32_16x16x32_bf16 v[144:147], v[64:67], a[68:71], v[144:147]
s_cmp_lg_u32 %3, 0
s_cbranch_scc1 W0N5_%=
s_mov_b64 exec, s[56:57]
global_load_dword v102, v25, s[92:93] offset:16 sc1
s_mov_b64 exec, -1
W0N5_%=:
v_mfma_f32_16x16x32_bf16 v[144:147], v[68:71], a[84:87], v[144:147]
buffer_load_dwordx4 v107, s[80:83], s48 offen offset:3072 lds
s_add_u32 m0, s75, 36864
v_mfma_f32_16x16x32_bf16 v[160:163], v[64:67], a[72:75], v[160:163]
ds_read_b64_tr_b16 v[92:93], v97 offset:8704
s_xor_b32 s48, s66, 2097280
v_mfma_f32_16x16x32_bf16 v[160:163], v[68:71], a[88:91], v[160:163]
ds_read_b64_tr_b16 v[94:95], v97 offset:8960
v_mfma_f32_16x16x32_bf16 v[176:179], v[64:67], a[76:79], v[176:179]
buffer_load_dwordx4 v104, s[80:83], s48 offen offset:0 lds
v_mfma_f32_16x16x32_bf16 v[176:179], v[68:71], a[92:95], v[176:179]
s_waitcnt lgkmcnt(8)
v_mfma_f32_16x16x32_bf16 v[132:135], v[72:75], a[64:67], v[132:135]
ds_read_b64_tr_b16 v[64:65], v96 offset:4096
v_mfma_f32_16x16x32_bf16 v[132:135], v[76:79], a[80:83], v[132:135]
ds_read_b64_tr_b16 v[66:67], v96 offset:4352
v_mfma_f32_16x16x32_bf16 v[148:151], v[72:75], a[68:71], v[148:151]
buffer_load_dwordx4 v105, s[80:83], s48 offen offset:1024 lds
v_mfma_f32_16x16x32_bf16 v[148:151], v[76:79], a[84:87], v[148:151]
v_mfma_f32_16x16x32_bf16 v[164:167], v[72:75], a[72:75], v[164:167]
ds_read_b64_tr_b16 v[68:69], v96 offset:12288
s_waitcnt vmcnt(14)
v_mfma_f32_16x16x32_bf16 v[164:167], v[76:79], a[88:91], v[164:167]
ds_read_b64_tr_b16 v[70:71], v96 offset:12544
ds_read_b128 a[96:99], v38 offset:32768
v_mfma_f32_16x16x32_bf16 v[180:183], v[72:75], a[76:79], v[180:183]
buffer_load_dwordx4 v106, s[80:83], s48 offen offset:2048 lds
v_mfma_f32_16x16x32_bf16 v[180:183], v[76:79], a[92:95], v[180:183]
s_waitcnt lgkmcnt(9)
v_mfma_f32_16x16x32_bf16 v[136:139], v[80:83], a[64:67], v[136:139]
ds_read_b64_tr_b16 v[72:73], v97 offset:4096
ds_read_b128 a[100:103], v38 offset:34816
v_mfma_f32_16x16x32_bf16 v[136:139], v[84:87], a[80:83], v[136:139]
ds_read_b64_tr_b16 v[74:75], v97 offset:4352
ds_read_b128 a[104:107], v38 offset:36864
v_mfma_f32_16x16x32_bf16 v[152:155], v[80:83], a[68:71], v[152:155]
buffer_load_dwordx4 v107, s[80:83], s48 offen offset:3072 lds
s_add_u32 m0, s75, 65536
v_mfma_f32_16x16x32_bf16 v[152:155], v[84:87], a[84:87], v[152:155]
s_xor_b32 s48, s44, 640
v_mfma_f32_16x16x32_bf16 v[168:171], v[80:83], a[72:75], v[168:171]
ds_read_b64_tr_b16 v[76:77], v97 offset:12288
ds_read_b128 a[108:111], v38 offset:38912
v_mfma_f32_16x16x32_bf16 v[168:171], v[84:87], a[88:91], v[168:171]
ds_read_b64_tr_b16 v[78:79], v97 offset:12544
ds_read_b128 a[112:115], v39 offset:32768
v_mfma_f32_16x16x32_bf16 v[184:187], v[80:83], a[76:79], v[184:187]
buffer_load_dwordx4 v112, s[84:87], s48 offen offset:0 lds
v_mfma_f32_16x16x32_bf16 v[184:187], v[84:87], a[92:95], v[184:187]
buffer_load_dwordx4 v113, s[84:87], s48 offen offset:1024 lds
s_waitcnt lgkmcnt(13)
v_mfma_f32_16x16x32_bf16 v[140:143], v[88:91], a[64:67], v[140:143]
ds_read_b64_tr_b16 v[80:81], v96 offset:4608
ds_read_b128 a[116:119], v39 offset:34816
v_mfma_f32_16x16x32_bf16 v[140:143], v[92:95], a[80:83], v[140:143]
ds_read_b64_tr_b16 v[82:83], v96 offset:4864
ds_read_b128 a[120:123], v39 offset:36864
v_mfma_f32_16x16x32_bf16 v[156:159], v[88:91], a[68:71], v[156:159]
v_mfma_f32_16x16x32_bf16 v[156:159], v[92:95], a[84:87], v[156:159]
buffer_load_dwordx4 v114, s[84:87], s48 offen offset:2048 lds
v_mfma_f32_16x16x32_bf16 v[172:175], v[88:91], a[72:75], v[172:175]
ds_read_b64_tr_b16 v[84:85], v96 offset:12800
ds_read_b128 a[124:127], v39 offset:38912
v_mfma_f32_16x16x32_bf16 v[172:175], v[92:95], a[88:91], v[172:175]
ds_read_b64_tr_b16 v[86:87], v96 offset:13056
v_mfma_f32_16x16x32_bf16 v[188:191], v[88:91], a[76:79], v[188:191]
buffer_load_dwordx4 v115, s[84:87], s48 offen offset:3072 lds
v_mfma_f32_16x16x32_bf16 v[188:191], v[92:95], a[92:95], v[188:191]
s_add_u32 m0, s75, 69632
s_waitcnt lgkmcnt(14)
v_mfma_f32_16x16x32_bf16 v[192:195], v[64:67], a[64:67], v[192:195]
ds_read_b64_tr_b16 v[88:89], v97 offset:4608
s_xor_b32 s48, s46, 640
v_mfma_f32_16x16x32_bf16 v[192:195], v[68:71], a[80:83], v[192:195]
ds_read_b64_tr_b16 v[90:91], v97 offset:4864
v_mfma_f32_16x16x32_bf16 v[208:211], v[64:67], a[68:71], v[208:211]
buffer_load_dwordx4 v112, s[84:87], s48 offen offset:0 lds
v_mfma_f32_16x16x32_bf16 v[208:211], v[68:71], a[84:87], v[208:211]
v_mfma_f32_16x16x32_bf16 v[224:227], v[64:67], a[72:75], v[224:227]
ds_read_b64_tr_b16 v[92:93], v97 offset:12800
v_mfma_f32_16x16x32_bf16 v[224:227], v[68:71], a[88:91], v[224:227]
ds_read_b64_tr_b16 v[94:95], v97 offset:13056
v_mfma_f32_16x16x32_bf16 v[240:243], v[64:67], a[76:79], v[240:243]
buffer_load_dwordx4 v113, s[84:87], s48 offen offset:1024 lds
v_mfma_f32_16x16x32_bf16 v[240:243], v[68:71], a[92:95], v[240:243]
s_waitcnt lgkmcnt(12)
v_mfma_f32_16x16x32_bf16 v[196:199], v[72:75], a[64:67], v[196:199]
ds_read_b64_tr_b16 v[64:65], v96 offset:16384
v_mfma_f32_16x16x32_bf16 v[196:199], v[76:79], a[80:83], v[196:199]
ds_read_b64_tr_b16 v[66:67], v96 offset:16640
v_mfma_f32_16x16x32_bf16 v[212:215], v[72:75], a[68:71], v[212:215]
buffer_load_dwordx4 v114, s[84:87], s48 offen offset:2048 lds
v_mfma_f32_16x16x32_bf16 v[212:215], v[76:79], a[84:87], v[212:215]
v_mfma_f32_16x16x32_bf16 v[228:231], v[72:75], a[72:75], v[228:231]
ds_read_b64_tr_b16 v[68:69], v96 offset:24576
v_mfma_f32_16x16x32_bf16 v[228:231], v[76:79], a[88:91], v[228:231]
ds_read_b64_tr_b16 v[70:71], v96 offset:24832
v_mfma_f32_16x16x32_bf16 v[244:247], v[72:75], a[76:79], v[244:247]
buffer_load_dwordx4 v115, s[84:87], s48 offen offset:3072 lds
v_mfma_f32_16x16x32_bf16 v[244:247], v[76:79], a[92:95], v[244:247]
s_waitcnt lgkmcnt(8)
v_mfma_f32_16x16x32_bf16 v[200:203], v[80:83], a[64:67], v[200:203]
ds_read_b64_tr_b16 v[72:73], v97 offset:16384
v_mfma_f32_16x16x32_bf16 v[200:203], v[84:87], a[80:83], v[200:203]
ds_read_b64_tr_b16 v[74:75], v97 offset:16640
v_mfma_f32_16x16x32_bf16 v[216:219], v[80:83], a[68:71], v[216:219]
global_store_dwordx4 v63, v[2:5], s[10:11] offset:0 sc1
v_mfma_f32_16x16x32_bf16 v[216:219], v[84:87], a[84:87], v[216:219]
s_waitcnt vmcnt(17)
v_mfma_f32_16x16x32_bf16 v[232:235], v[80:83], a[72:75], v[232:235]
ds_read_b64_tr_b16 v[76:77], v97 offset:24576
ds_read_b128 a[128:131], v40 offset:0
v_mfma_f32_16x16x32_bf16 v[232:235], v[84:87], a[88:91], v[232:235]
ds_read_b64_tr_b16 v[78:79], v97 offset:24832
ds_read_b128 a[132:135], v40 offset:2048
v_mfma_f32_16x16x32_bf16 v[248:251], v[80:83], a[76:79], v[248:251]
global_store_dwordx4 v63, v[6:9], s[10:11] offset:2048 sc1
v_mfma_f32_16x16x32_bf16 v[248:251], v[84:87], a[92:95], v[248:251]
s_add_u32 m0, s75, 98304
s_waitcnt lgkmcnt(10)
v_mfma_f32_16x16x32_bf16 v[204:207], v[88:91], a[64:67], v[204:207]
ds_read_b64_tr_b16 v[80:81], v96 offset:16896
ds_read_b128 a[136:139], v40 offset:4096
v_mfma_f32_16x16x32_bf16 v[204:207], v[92:95], a[80:83], v[204:207]
ds_read_b64_tr_b16 v[82:83], v96 offset:17152
ds_read_b128 a[140:143], v40 offset:6144
v_mfma_f32_16x16x32_bf16 v[220:223], v[88:91], a[68:71], v[220:223]
s_xor_b32 s48, s44, 768
v_mfma_f32_16x16x32_bf16 v[220:223], v[92:95], a[84:87], v[220:223]
buffer_load_dwordx4 v112, s[84:87], s48 offen offset:0 lds
v_mfma_f32_16x16x32_bf16 v[236:239], v[88:91], a[72:75], v[236:239]
ds_read_b64_tr_b16 v[84:85], v96 offset:25088
ds_read_b128 a[144:147], v41 offset:0
v_mfma_f32_16x16x32_bf16 v[236:239], v[92:95], a[88:91], v[236:239]
ds_read_b64_tr_b16 v[86:87], v96 offset:25344
ds_read_b128 a[148:151], v41 offset:2048
v_mfma_f32_16x16x32_bf16 v[252:255], v[88:91], a[76:79], v[252:255]
s_cmp_lg_u32 %3, 0
s_cbranch_scc1 W0N6_%=
s_mov_b64 exec, s[56:57]
s_waitcnt vmcnt(16)
global_store_dwordx2 v25, v[102:103], s[92:93] sc1
s_mov_b64 exec, -1
W0N6_%=:
v_mfma_f32_16x16x32_bf16 v[252:255], v[92:95], a[92:95], v[252:255]
buffer_load_dwordx4 v113, s[84:87], s48 offen offset:1024 lds
s_waitcnt lgkmcnt(14)
v_mfma_f32_16x16x32_bf16 v[128:131], v[64:67], a[96:99], v[128:131]
ds_read_b64_tr_b16 v[88:89], v97 offset:16896
ds_read_b128 a[152:155], v41 offset:4096
v_mfma_f32_16x16x32_bf16 v[128:131], v[68:71], a[112:115], v[128:131]
ds_read_b64_tr_b16 v[90:91], v97 offset:17152
ds_read_b128 a[156:159], v41 offset:6144
v_mfma_f32_16x16x32_bf16 v[144:147], v[64:67], a[100:103], v[144:147]
buffer_load_dwordx4 v114, s[84:87], s48 offen offset:2048 lds
v_mfma_f32_16x16x32_bf16 v[144:147], v[68:71], a[116:119], v[144:147]
v_mfma_f32_16x16x32_bf16 v[160:163], v[64:67], a[104:107], v[160:163]
ds_read_b64_tr_b16 v[92:93], v97 offset:25088
v_mfma_f32_16x16x32_bf16 v[160:163], v[68:71], a[120:123], v[160:163]
ds_read_b64_tr_b16 v[94:95], v97 offset:25344
v_mfma_f32_16x16x32_bf16 v[176:179], v[64:67], a[108:111], v[176:179]
buffer_load_dwordx4 v115, s[84:87], s48 offen offset:3072 lds
s_add_u32 m0, s75, 102400
v_mfma_f32_16x16x32_bf16 v[176:179], v[68:71], a[124:127], v[176:179]
s_xor_b32 s48, s46, 768
s_waitcnt lgkmcnt(14)
v_mfma_f32_16x16x32_bf16 v[132:135], v[72:75], a[96:99], v[132:135]
ds_read_b64_tr_b16 v[64:65], v96 offset:20480
v_mfma_f32_16x16x32_bf16 v[132:135], v[76:79], a[112:115], v[132:135]
ds_read_b64_tr_b16 v[66:67], v96 offset:20736
v_mfma_f32_16x16x32_bf16 v[148:151], v[72:75], a[100:103], v[148:151]
buffer_load_dwordx4 v112, s[84:87], s48 offen offset:0 lds
v_mfma_f32_16x16x32_bf16 v[148:151], v[76:79], a[116:119], v[148:151]
v_mfma_f32_16x16x32_bf16 v[164:167], v[72:75], a[104:107], v[164:167]
ds_read_b64_tr_b16 v[68:69], v96 offset:28672
v_mfma_f32_16x16x32_bf16 v[164:167], v[76:79], a[120:123], v[164:167]
ds_read_b64_tr_b16 v[70:71], v96 offset:28928
v_mfma_f32_16x16x32_bf16 v[180:183], v[72:75], a[108:111], v[180:183]
buffer_load_dwordx4 v113, s[84:87], s48 offen offset:1024 lds
v_mfma_f32_16x16x32_bf16 v[180:183], v[76:79], a[124:127], v[180:183]
buffer_load_dwordx4 v114, s[84:87], s48 offen offset:2048 lds
s_waitcnt lgkmcnt(11)
v_mfma_f32_16x16x32_bf16 v[136:139], v[80:83], a[96:99], v[136:139]
ds_read_b64_tr_b16 v[72:73], v97 offset:20480
v_mfma_f32_16x16x32_bf16 v[136:139], v[84:87], a[112:115], v[136:139]
ds_read_b64_tr_b16 v[74:75], v97 offset:20736
v_mfma_f32_16x16x32_bf16 v[152:155], v[80:83], a[100:103], v[152:155]
v_mfma_f32_16x16x32_bf16 v[152:155], v[84:87], a[116:119], v[152:155]
buffer_load_dwordx4 v115, s[84:87], s48 offen offset:3072 lds
s_waitcnt lgkmcnt(8)
s_add_u32 m0, s75, 131072
v_mfma_f32_16x16x32_bf16 v[168:171], v[80:83], a[104:107], v[168:171]
ds_read_b64_tr_b16 v[76:77], v97 offset:28672
s_xor_b32 s48, s44, 896
v_mfma_f32_16x16x32_bf16 v[168:171], v[84:87], a[120:123], v[168:171]
ds_read_b64_tr_b16 v[78:79], v97 offset:28928
v_mfma_f32_16x16x32_bf16 v[184:187], v[80:83], a[108:111], v[184:187]
buffer_load_dwordx4 v112, s[84:87], s48 offen offset:0 lds
v_mfma_f32_16x16x32_bf16 v[184:187], v[84:87], a[124:127], v[184:187]
s_waitcnt lgkmcnt(8)
v_mfma_f32_16x16x32_bf16 v[140:143], v[88:91], a[96:99], v[140:143]
ds_read_b64_tr_b16 v[80:81], v96 offset:20992
v_mfma_f32_16x16x32_bf16 v[140:143], v[92:95], a[112:115], v[140:143]
ds_read_b64_tr_b16 v[82:83], v96 offset:21248
v_mfma_f32_16x16x32_bf16 v[156:159], v[88:91], a[100:103], v[156:159]
buffer_load_dwordx4 v113, s[84:87], s48 offen offset:1024 lds
v_mfma_f32_16x16x32_bf16 v[156:159], v[92:95], a[116:119], v[156:159]
v_mfma_f32_16x16x32_bf16 v[172:175], v[88:91], a[104:107], v[172:175]
ds_read_b64_tr_b16 v[84:85], v96 offset:29184
v_mfma_f32_16x16x32_bf16 v[172:175], v[92:95], a[120:123], v[172:175]
ds_read_b64_tr_b16 v[86:87], v96 offset:29440
v_mfma_f32_16x16x32_bf16 v[188:191], v[88:91], a[108:111], v[188:191]
buffer_load_dwordx4 v114, s[84:87], s48 offen offset:2048 lds
v_mfma_f32_16x16x32_bf16 v[188:191], v[92:95], a[124:127], v[188:191]
s_waitcnt lgkmcnt(8)
v_mfma_f32_16x16x32_bf16 v[192:195], v[64:67], a[96:99], v[192:195]
ds_read_b64_tr_b16 v[88:89], v97 offset:20992
v_mfma_f32_16x16x32_bf16 v[192:195], v[68:71], a[112:115], v[192:195]
ds_read_b64_tr_b16 v[90:91], v97 offset:21248
v_mfma_f32_16x16x32_bf16 v[208:211], v[64:67], a[100:103], v[208:211]
buffer_load_dwordx4 v115, s[84:87], s48 offen offset:3072 lds
s_add_u32 m0, s75, 135168
v_mfma_f32_16x16x32_bf16 v[208:211], v[68:71], a[116:119], v[208:211]
s_xor_b32 s48, s46, 896
v_mfma_f32_16x16x32_bf16 v[224:227], v[64:67], a[104:107], v[224:227]
ds_read_b64_tr_b16 v[92:93], v97 offset:29184
v_mfma_f32_16x16x32_bf16 v[224:227], v[68:71], a[120:123], v[224:227]
ds_read_b64_tr_b16 v[94:95], v97 offset:29440
v_mfma_f32_16x16x32_bf16 v[240:243], v[64:67], a[108:111], v[240:243]
buffer_load_dwordx4 v112, s[84:87], s48 offen offset:0 lds
v_mfma_f32_16x16x32_bf16 v[240:243], v[68:71], a[124:127], v[240:243]
s_waitcnt lgkmcnt(8)
v_mfma_f32_16x16x32_bf16 v[196:199], v[72:75], a[96:99], v[196:199]
buffer_load_dwordx4 v113, s[84:87], s48 offen offset:1024 lds
v_mfma_f32_16x16x32_bf16 v[196:199], v[76:79], a[112:115], v[196:199]
v_mfma_f32_16x16x32_bf16 v[212:215], v[72:75], a[100:103], v[212:215]
v_mfma_f32_16x16x32_bf16 v[212:215], v[76:79], a[116:119], v[212:215]
buffer_load_dwordx4 v114, s[84:87], s48 offen offset:2048 lds
s_waitcnt vmcnt(17)
v_mfma_f32_16x16x32_bf16 v[228:231], v[72:75], a[104:107], v[228:231]
v_mfma_f32_16x16x32_bf16 v[228:231], v[76:79], a[120:123], v[228:231]
s_waitcnt lgkmcnt(0)
s_barrier
v_mfma_f32_16x16x32_bf16 v[244:247], v[72:75], a[108:111], v[244:247]
ds_read_b64_tr_b16 v[64:65], v96 offset:32768
ds_read_b128 a[160:163], v38 offset:0
v_mfma_f32_16x16x32_bf16 v[244:247], v[76:79], a[124:127], v[244:247]
ds_read_b64_tr_b16 v[66:67], v96 offset:33024
v_mfma_f32_16x16x32_bf16 v[200:203], v[80:83], a[96:99], v[200:203]
ds_read_b64_tr_b16 v[68:69], v96 offset:40960
ds_read_b128 a[164:167], v38 offset:2048
v_mfma_f32_16x16x32_bf16 v[200:203], v[84:87], a[112:115], v[200:203]
ds_read_b64_tr_b16 v[70:71], v96 offset:41216
ds_read_b128 a[168:171], v38 offset:4096
v_mfma_f32_16x16x32_bf16 v[216:219], v[80:83], a[100:103], v[216:219]
ds_read_b64_tr_b16 v[72:73], v97 offset:32768
ds_read_b128 a[172:175], v38 offset:6144
v_mfma_f32_16x16x32_bf16 v[216:219], v[84:87], a[116:119], v[216:219]
ds_read_b64_tr_b16 v[74:75], v97 offset:33024
ds_read_b128 a[176:179], v39 offset:0
v_mfma_f32_16x16x32_bf16 v[232:235], v[80:83], a[104:107], v[232:235]
ds_read_b64_tr_b16 v[76:77], v97 offset:40960
ds_read_b128 a[180:183], v39 offset:2048
v_mfma_f32_16x16x32_bf16 v[232:235], v[84:87], a[120:123], v[232:235]
ds_read_b64_tr_b16 v[78:79], v97 offset:41216
ds_read_b128 a[184:187], v39 offset:4096
v_mfma_f32_16x16x32_bf16 v[248:251], v[80:83], a[108:111], v[248:251]
buffer_load_dwordx4 v115, s[84:87], s48 offen offset:3072 lds
v_mfma_f32_16x16x32_bf16 v[248:251], v[84:87], a[124:127], v[248:251]
v_mfma_f32_16x16x32_bf16 v[204:207], v[88:91], a[96:99], v[204:207]
ds_read_b64_tr_b16 v[80:81], v96 offset:33280
ds_read_b128 a[188:191], v39 offset:6144
v_mfma_f32_16x16x32_bf16 v[204:207], v[92:95], a[112:115], v[204:207]
ds_read_b64_tr_b16 v[82:83], v96 offset:33536
v_mfma_f32_16x16x32_bf16 v[220:223], v[88:91], a[100:103], v[220:223]
v_mfma_f32_16x16x32_bf16 v[220:223], v[92:95], a[116:119], v[220:223]
v_mfma_f32_16x16x32_bf16 v[236:239], v[88:91], a[104:107], v[236:239]
ds_read_b64_tr_b16 v[84:85], v96 offset:41472
v_mfma_f32_16x16x32_bf16 v[236:239], v[92:95], a[120:123], v[236:239]
ds_read_b64_tr_b16 v[86:87], v96 offset:41728
v_mfma_f32_16x16x32_bf16 v[252:255], v[88:91], a[108:111], v[252:255]
v_mfma_f32_16x16x32_bf16 v[252:255], v[92:95], a[124:127], v[252:255]
s_waitcnt lgkmcnt(14)
v_mfma_f32_16x16x32_bf16 v[128:131], v[64:67], a[128:131], v[128:131]
ds_read_b64_tr_b16 v[88:89], v97 offset:33280
v_mfma_f32_16x16x32_bf16 v[128:131], v[68:71], a[144:147], v[128:131]
ds_read_b64_tr_b16 v[90:91], v97 offset:33536
v_mfma_f32_16x16x32_bf16 v[144:147], v[64:67], a[132:135], v[144:147]
v_mfma_f32_16x16x32_bf16 v[144:147], v[68:71], a[148:151], v[144:147]
v_mfma_f32_16x16x32_bf16 v[160:163], v[64:67], a[136:139], v[160:163]
ds_read_b64_tr_b16 v[92:93], v97 offset:41472
s_waitcnt lgkmcnt(6)
s_add_u32 m0, s75, 65536
v_mfma_f32_16x16x32_bf16 v[160:163], v[68:71], a[152:155], v[160:163]
)"
R"(ds_read_b64_tr_b16 v[94:95], v97 offset:41728
s_xor_b32 s48, s66, 3145728
v_mfma_f32_16x16x32_bf16 v[176:179], v[64:67], a[140:143], v[176:179]
buffer_load_dwordx4 v104, s[80:83], s48 offen offset:0 lds
v_mfma_f32_16x16x32_bf16 v[176:179], v[68:71], a[156:159], v[176:179]
v_mfma_f32_16x16x32_bf16 v[132:135], v[72:75], a[128:131], v[132:135]
ds_read_b64_tr_b16 v[64:65], v96 offset:36864
s_cmp_lg_u32 %3, 0
s_cbranch_scc1 W0N7_%=
s_mov_b64 exec, 15
global_load_dword v24, v100, s[92:93] offset:0 sc1
s_mov_b64 exec, -1
W0N7_%=:
v_mfma_f32_16x16x32_bf16 v[132:135], v[76:79], a[144:147], v[132:135]
ds_read_b64_tr_b16 v[66:67], v96 offset:37120
v_mfma_f32_16x16x32_bf16 v[148:151], v[72:75], a[132:135], v[148:151]
buffer_load_dwordx4 v105, s[80:83], s48 offen offset:1024 lds
v_mfma_f32_16x16x32_bf16 v[148:151], v[76:79], a[148:151], v[148:151]
buffer_load_dwordx4 v106, s[80:83], s48 offen offset:2048 lds
v_mfma_f32_16x16x32_bf16 v[164:167], v[72:75], a[136:139], v[164:167]
ds_read_b64_tr_b16 v[68:69], v96 offset:45056
v_mfma_f32_16x16x32_bf16 v[164:167], v[76:79], a[152:155], v[164:167]
ds_read_b64_tr_b16 v[70:71], v96 offset:45312
v_mfma_f32_16x16x32_bf16 v[180:183], v[72:75], a[140:143], v[180:183]
v_mfma_f32_16x16x32_bf16 v[180:183], v[76:79], a[156:159], v[180:183]
buffer_load_dwordx4 v107, s[80:83], s48 offen offset:3072 lds
s_waitcnt lgkmcnt(8)
v_mfma_f32_16x16x32_bf16 v[136:139], v[80:83], a[128:131], v[136:139]
ds_read_b64_tr_b16 v[72:73], v97 offset:36864
s_waitcnt vmcnt(12)
v_mfma_f32_16x16x32_bf16 v[136:139], v[84:87], a[144:147], v[136:139]
ds_read_b64_tr_b16 v[74:75], v97 offset:37120
ds_read_b128 a[192:195], v38 offset:32768
v_mfma_f32_16x16x32_bf16 v[152:155], v[80:83], a[132:135], v[152:155]
s_add_u32 m0, s75, 69632
s_xor_b32 s48, s66, 3145856
v_mfma_f32_16x16x32_bf16 v[152:155], v[84:87], a[148:151], v[152:155]
buffer_load_dwordx4 v104, s[80:83], s48 offen offset:0 lds
v_mfma_f32_16x16x32_bf16 v[168:171], v[80:83], a[136:139], v[168:171]
ds_read_b64_tr_b16 v[76:77], v97 offset:45056
ds_read_b128 a[196:199], v38 offset:34816
v_mfma_f32_16x16x32_bf16 v[168:171], v[84:87], a[152:155], v[168:171]
ds_read_b64_tr_b16 v[78:79], v97 offset:45312
ds_read_b128 a[200:203], v38 offset:36864
v_mfma_f32_16x16x32_bf16 v[184:187], v[80:83], a[140:143], v[184:187]
buffer_load_dwordx4 v105, s[80:83], s48 offen offset:1024 lds
v_mfma_f32_16x16x32_bf16 v[184:187], v[84:87], a[156:159], v[184:187]
s_waitcnt lgkmcnt(11)
v_mfma_f32_16x16x32_bf16 v[140:143], v[88:91], a[128:131], v[140:143]
ds_read_b64_tr_b16 v[80:81], v96 offset:37376
ds_read_b128 a[204:207], v38 offset:38912
v_mfma_f32_16x16x32_bf16 v[140:143], v[92:95], a[144:147], v[140:143]
ds_read_b64_tr_b16 v[82:83], v96 offset:37632
ds_read_b128 a[208:211], v39 offset:32768
PAD1_%=:
s_cbranch_execz PAD17_%=
v_mfma_f32_16x16x32_bf16 v[156:159], v[88:91], a[132:135], v[156:159]
buffer_load_dwordx4 v106, s[80:83], s48 offen offset:2048 lds
v_mfma_f32_16x16x32_bf16 v[156:159], v[92:95], a[148:151], v[156:159]
v_mfma_f32_16x16x32_bf16 v[172:175], v[88:91], a[136:139], v[172:175]
ds_read_b64_tr_b16 v[84:85], v96 offset:45568
ds_read_b128 a[212:215], v39 offset:34816
v_mfma_f32_16x16x32_bf16 v[172:175], v[92:95], a[152:155], v[172:175]
ds_read_b64_tr_b16 v[86:87], v96 offset:45824
ds_read_b128 a[216:219], v39 offset:36864
v_mfma_f32_16x16x32_bf16 v[188:191], v[88:91], a[140:143], v[188:191]
buffer_load_dwordx4 v107, s[80:83], s48 offen offset:3072 lds
v_mfma_f32_16x16x32_bf16 v[188:191], v[92:95], a[156:159], v[188:191]
s_waitcnt lgkmcnt(14)
v_mfma_f32_16x16x32_bf16 v[192:195], v[64:67], a[128:131], v[192:195]
ds_read_b64_tr_b16 v[88:89], v97 offset:37376
ds_read_b128 a[220:223], v39 offset:38912
v_mfma_f32_16x16x32_bf16 v[192:195], v[68:71], a[144:147], v[192:195]
ds_read_b64_tr_b16 v[90:91], v97 offset:37632
v_mfma_f32_16x16x32_bf16 v[208:211], v[64:67], a[132:135], v[208:211]
v_mfma_f32_16x16x32_bf16 v[208:211], v[68:71], a[148:151], v[208:211]
v_mfma_f32_16x16x32_bf16 v[224:227], v[64:67], a[136:139], v[224:227]
ds_read_b64_tr_b16 v[92:93], v97 offset:45568
v_mfma_f32_16x16x32_bf16 v[224:227], v[68:71], a[152:155], v[224:227]
ds_read_b64_tr_b16 v[94:95], v97 offset:45824
v_mfma_f32_16x16x32_bf16 v[240:243], v[64:67], a[140:143], v[240:243]
v_mfma_f32_16x16x32_bf16 v[240:243], v[68:71], a[156:159], v[240:243]
s_waitcnt lgkmcnt(14)
v_mfma_f32_16x16x32_bf16 v[196:199], v[72:75], a[128:131], v[196:199]
ds_read_b64_tr_b16 v[64:65], v96 offset:49152
v_mfma_f32_16x16x32_bf16 v[196:199], v[76:79], a[144:147], v[196:199]
ds_read_b64_tr_b16 v[66:67], v96 offset:49408
v_mfma_f32_16x16x32_bf16 v[212:215], v[72:75], a[132:135], v[212:215]
v_mfma_f32_16x16x32_bf16 v[212:215], v[76:79], a[148:151], v[212:215]
v_mfma_f32_16x16x32_bf16 v[228:231], v[72:75], a[136:139], v[228:231]
ds_read_b64_tr_b16 v[68:69], v96 offset:57344
v_mfma_f32_16x16x32_bf16 v[228:231], v[76:79], a[152:155], v[228:231]
ds_read_b64_tr_b16 v[70:71], v96 offset:57600
v_mfma_f32_16x16x32_bf16 v[244:247], v[72:75], a[140:143], v[244:247]
v_mfma_f32_16x16x32_bf16 v[244:247], v[76:79], a[156:159], v[244:247]
s_waitcnt lgkmcnt(10)
v_mfma_f32_16x16x32_bf16 v[200:203], v[80:83], a[128:131], v[200:203]
ds_read_b64_tr_b16 v[72:73], v97 offset:49152
v_mfma_f32_16x16x32_bf16 v[200:203], v[84:87], a[144:147], v[200:203]
ds_read_b64_tr_b16 v[74:75], v97 offset:49408
v_mfma_f32_16x16x32_bf16 v[216:219], v[80:83], a[132:135], v[216:219]
v_mfma_f32_16x16x32_bf16 v[216:219], v[84:87], a[148:151], v[216:219]
s_waitcnt vmcnt(8)
v_mfma_f32_16x16x32_bf16 v[232:235], v[80:83], a[136:139], v[232:235]
ds_read_b64_tr_b16 v[76:77], v97 offset:57344
ds_read_b128 a[224:227], v40 offset:0
v_mfma_f32_16x16x32_bf16 v[232:235], v[84:87], a[152:155], v[232:235]
ds_read_b64_tr_b16 v[78:79], v97 offset:57600
ds_read_b128 a[228:231], v40 offset:2048
v_mfma_f32_16x16x32_bf16 v[248:251], v[80:83], a[140:143], v[248:251]
v_mfma_f32_16x16x32_bf16 v[248:251], v[84:87], a[156:159], v[248:251]
s_waitcnt lgkmcnt(10)
v_mfma_f32_16x16x32_bf16 v[204:207], v[88:91], a[128:131], v[204:207]
ds_read_b64_tr_b16 v[80:81], v96 offset:49664
ds_read_b128 a[232:235], v40 offset:4096
v_mfma_f32_16x16x32_bf16 v[204:207], v[92:95], a[144:147], v[204:207]
ds_read_b64_tr_b16 v[82:83], v96 offset:49920
ds_read_b128 a[236:239], v40 offset:6144
v_mfma_f32_16x16x32_bf16 v[220:223], v[88:91], a[132:135], v[220:223]
s_cmp_lg_u32 %3, 0
s_cbranch_scc1 W0N8_%=
s_waitcnt vmcnt(7)
v_cmp_ne_u32 vcc, s55, v24
s_and_b32 s91, vcc_lo, 5
s_cbranch_scc1 HSP2N_%=
HOK2N_%=:
v_readlane_b32 s54, v24, 1
v_readlane_b32 s90, v24, 3
W0N8_%=:
v_mfma_f32_16x16x32_bf16 v[220:223], v[92:95], a[148:151], v[220:223]
s_lshl_b32 s48, s28, 8
s_mov_b64 s[80:81], s[12:13]
v_mfma_f32_16x16x32_bf16 v[236:239], v[88:91], a[136:139], v[236:239]
ds_read_b64_tr_b16 v[84:85], v96 offset:57856
ds_read_b128 a[240:243], v41 offset:0
v_mfma_f32_16x16x32_bf16 v[236:239], v[92:95], a[152:155], v[236:239]
ds_read_b64_tr_b16 v[86:87], v96 offset:58112
ds_read_b128 a[244:247], v41 offset:2048
v_mfma_f32_16x16x32_bf16 v[252:255], v[88:91], a[140:143], v[252:255]
s_waitcnt vmcnt(0)
s_barrier
v_mfma_f32_16x16x32_bf16 v[252:255], v[92:95], a[156:159], v[252:255]
s_cmp_lg_u32 %3, 0
s_cbranch_scc1 W0N9_%=
v_mov_b32 v24, 1
s_mov_b64 exec, 1
global_atomic_add v101, v24, s[92:93] sc1
s_mov_b64 exec, -1
W0N9_%=:
s_waitcnt lgkmcnt(14)
v_mfma_f32_16x16x32_bf16 v[128:131], v[64:67], a[160:163], v[128:131]
ds_read_b64_tr_b16 v[88:89], v97 offset:49664
ds_read_b128 a[248:251], v41 offset:4096
v_mfma_f32_16x16x32_bf16 v[128:131], v[68:71], a[176:179], v[128:131]
ds_read_b64_tr_b16 v[90:91], v97 offset:49920
ds_read_b128 a[252:255], v41 offset:6144
v_mfma_f32_16x16x32_bf16 v[144:147], v[64:67], a[164:167], v[144:147]
s_add_u32 s80, s80, s48
s_addc_u32 s81, s81, 0
v_mfma_f32_16x16x32_bf16 v[144:147], v[68:71], a[180:183], v[144:147]
s_and_b32 s81, s81, 0xffff
s_sub_u32 s82, 0x400000, s48
v_mfma_f32_16x16x32_bf16 v[160:163], v[64:67], a[168:171], v[160:163]
ds_read_b64_tr_b16 v[92:93], v97 offset:57856
s_xor_b32 s66, s66, s70
v_mfma_f32_16x16x32_bf16 v[160:163], v[68:71], a[184:187], v[160:163]
ds_read_b64_tr_b16 v[94:95], v97 offset:58112
s_add_u32 m0, s75, 98304
v_mfma_f32_16x16x32_bf16 v[176:179], v[64:67], a[172:175], v[176:179]
s_xor_b32 s48, s66, 0
v_mfma_f32_16x16x32_bf16 v[176:179], v[68:71], a[188:191], v[176:179]
buffer_load_dwordx4 v104, s[80:83], s48 offen offset:0 lds
s_waitcnt lgkmcnt(14)
v_mfma_f32_16x16x32_bf16 v[132:135], v[72:75], a[160:163], v[132:135]
ds_read_b64_tr_b16 v[64:65], v96 offset:53248
v_mfma_f32_16x16x32_bf16 v[132:135], v[76:79], a[176:179], v[132:135]
ds_read_b64_tr_b16 v[66:67], v96 offset:53504
v_mfma_f32_16x16x32_bf16 v[148:151], v[72:75], a[164:167], v[148:151]
buffer_load_dwordx4 v105, s[80:83], s48 offen offset:1024 lds
v_mfma_f32_16x16x32_bf16 v[148:151], v[76:79], a[180:183], v[148:151]
v_mfma_f32_16x16x32_bf16 v[164:167], v[72:75], a[168:171], v[164:167]
ds_read_b64_tr_b16 v[68:69], v96 offset:61440
v_mfma_f32_16x16x32_bf16 v[164:167], v[76:79], a[184:187], v[164:167]
ds_read_b64_tr_b16 v[70:71], v96 offset:61696
v_mfma_f32_16x16x32_bf16 v[180:183], v[72:75], a[172:175], v[180:183]
buffer_load_dwordx4 v106, s[80:83], s48 offen offset:2048 lds
v_mfma_f32_16x16x32_bf16 v[180:183], v[76:79], a[188:191], v[180:183]
s_waitcnt lgkmcnt(11)
v_mfma_f32_16x16x32_bf16 v[136:139], v[80:83], a[160:163], v[136:139]
ds_read_b64_tr_b16 v[72:73], v97 offset:53248
v_mfma_f32_16x16x32_bf16 v[136:139], v[84:87], a[176:179], v[136:139]
ds_read_b64_tr_b16 v[74:75], v97 offset:53504
v_mfma_f32_16x16x32_bf16 v[152:155], v[80:83], a[164:167], v[152:155]
buffer_load_dwordx4 v107, s[80:83], s48 offen offset:3072 lds
v_mfma_f32_16x16x32_bf16 v[152:155], v[84:87], a[180:183], v[152:155]
v_mfma_f32_16x16x32_bf16 v[168:171], v[80:83], a[168:171], v[168:171]
ds_read_b64_tr_b16 v[76:77], v97 offset:61440
s_add_u32 m0, s75, 102400
v_mfma_f32_16x16x32_bf16 v[168:171], v[84:87], a[184:187], v[168:171]
ds_read_b64_tr_b16 v[78:79], v97 offset:61696
s_xor_b32 s48, s66, 262144
v_mfma_f32_16x16x32_bf16 v[184:187], v[80:83], a[172:175], v[184:187]
v_mfma_f32_16x16x32_bf16 v[184:187], v[84:87], a[188:191], v[184:187]
buffer_load_dwordx4 v104, s[80:83], s48 offen offset:0 lds
s_waitcnt lgkmcnt(8)
v_mfma_f32_16x16x32_bf16 v[140:143], v[88:91], a[160:163], v[140:143]
ds_read_b64_tr_b16 v[80:81], v96 offset:53760
v_mfma_f32_16x16x32_bf16 v[140:143], v[92:95], a[176:179], v[140:143]
ds_read_b64_tr_b16 v[82:83], v96 offset:54016
v_mfma_f32_16x16x32_bf16 v[156:159], v[88:91], a[164:167], v[156:159]
v_mfma_f32_16x16x32_bf16 v[156:159], v[92:95], a[180:183], v[156:159]
buffer_load_dwordx4 v105, s[80:83], s48 offen offset:1024 lds
v_mfma_f32_16x16x32_bf16 v[172:175], v[88:91], a[168:171], v[172:175]
ds_read_b64_tr_b16 v[84:85], v96 offset:61952
v_mfma_f32_16x16x32_bf16 v[172:175], v[92:95], a[184:187], v[172:175]
ds_read_b64_tr_b16 v[86:87], v96 offset:62208
v_mfma_f32_16x16x32_bf16 v[188:191], v[88:91], a[172:175], v[188:191]
v_mfma_f32_16x16x32_bf16 v[188:191], v[92:95], a[188:191], v[188:191]
buffer_load_dwordx4 v106, s[80:83], s48 offen offset:2048 lds
s_waitcnt lgkmcnt(8)
v_mfma_f32_16x16x32_bf16 v[192:195], v[64:67], a[160:163], v[192:195]
ds_read_b64_tr_b16 v[88:89], v97 offset:53760
v_mfma_f32_16x16x32_bf16 v[192:195], v[68:71], a[176:179], v[192:195]
ds_read_b64_tr_b16 v[90:91], v97 offset:54016
v_mfma_f32_16x16x32_bf16 v[208:211], v[64:67], a[164:167], v[208:211]
v_mfma_f32_16x16x32_bf16 v[208:211], v[68:71], a[180:183], v[208:211]
v_mfma_f32_16x16x32_bf16 v[224:227], v[64:67], a[168:171], v[224:227]
ds_read_b64_tr_b16 v[92:93], v97 offset:61952
v_mfma_f32_16x16x32_bf16 v[224:227], v[68:71], a[184:187], v[224:227]
ds_read_b64_tr_b16 v[94:95], v97 offset:62208
v_mfma_f32_16x16x32_bf16 v[240:243], v[64:67], a[172:175], v[240:243]
global_load_dword v23, v23, s[50:51]
v_mfma_f32_16x16x32_bf16 v[240:243], v[68:71], a[188:191], v[240:243]
s_waitcnt lgkmcnt(8)
v_mfma_f32_16x16x32_bf16 v[196:199], v[72:75], a[160:163], v[196:199]
v_mfma_f32_16x16x32_bf16 v[196:199], v[76:79], a[176:179], v[196:199]
v_mfma_f32_16x16x32_bf16 v[212:215], v[72:75], a[164:167], v[212:215]
v_mfma_f32_16x16x32_bf16 v[212:215], v[76:79], a[180:183], v[212:215]
v_mfma_f32_16x16x32_bf16 v[228:231], v[72:75], a[168:171], v[228:231]
v_mfma_f32_16x16x32_bf16 v[228:231], v[76:79], a[184:187], v[228:231]
s_waitcnt lgkmcnt(0)
s_barrier
v_mfma_f32_16x16x32_bf16 v[244:247], v[72:75], a[172:175], v[244:247]
ds_read_b64_tr_b16 v[64:65], v98 offset:0
v_mfma_f32_16x16x32_bf16 v[244:247], v[76:79], a[188:191], v[244:247]
ds_read_b64_tr_b16 v[66:67], v98 offset:256
v_mfma_f32_16x16x32_bf16 v[200:203], v[80:83], a[160:163], v[200:203]
ds_read_b64_tr_b16 v[68:69], v98 offset:8192
v_mfma_f32_16x16x32_bf16 v[200:203], v[84:87], a[176:179], v[200:203]
ds_read_b64_tr_b16 v[70:71], v98 offset:8448
v_mfma_f32_16x16x32_bf16 v[216:219], v[80:83], a[164:167], v[216:219]
ds_read_b64_tr_b16 v[72:73], v99 offset:0
v_mfma_f32_16x16x32_bf16 v[216:219], v[84:87], a[180:183], v[216:219]
ds_read_b64_tr_b16 v[74:75], v99 offset:256
v_mfma_f32_16x16x32_bf16 v[232:235], v[80:83], a[168:171], v[232:235]
ds_read_b64_tr_b16 v[76:77], v99 offset:8192
v_mfma_f32_16x16x32_bf16 v[232:235], v[84:87], a[184:187], v[232:235]
ds_read_b64_tr_b16 v[78:79], v99 offset:8448
v_mfma_f32_16x16x32_bf16 v[248:251], v[80:83], a[172:175], v[248:251]
buffer_load_dwordx4 v107, s[80:83], s48 offen offset:3072 lds
v_mfma_f32_16x16x32_bf16 v[248:251], v[84:87], a[188:191], v[248:251]
v_mfma_f32_16x16x32_bf16 v[204:207], v[88:91], a[160:163], v[204:207]
ds_read_b64_tr_b16 v[80:81], v98 offset:512
v_mfma_f32_16x16x32_bf16 v[204:207], v[92:95], a[176:179], v[204:207]
ds_read_b64_tr_b16 v[82:83], v98 offset:768
v_mfma_f32_16x16x32_bf16 v[220:223], v[88:91], a[164:167], v[220:223]
v_mfma_f32_16x16x32_bf16 v[220:223], v[92:95], a[180:183], v[220:223]
v_mfma_f32_16x16x32_bf16 v[236:239], v[88:91], a[168:171], v[236:239]
ds_read_b64_tr_b16 v[84:85], v98 offset:8704
v_mfma_f32_16x16x32_bf16 v[236:239], v[92:95], a[184:187], v[236:239]
ds_read_b64_tr_b16 v[86:87], v98 offset:8960
v_mfma_f32_16x16x32_bf16 v[252:255], v[88:91], a[172:175], v[252:255]
v_mfma_f32_16x16x32_bf16 v[252:255], v[92:95], a[188:191], v[252:255]
s_waitcnt lgkmcnt(8)
v_mfma_f32_16x16x32_bf16 v[128:131], v[64:67], a[192:195], v[128:131]
ds_read_b64_tr_b16 v[88:89], v99 offset:512
v_mfma_f32_16x16x32_bf16 v[128:131], v[68:71], a[208:211], v[128:131]
ds_read_b64_tr_b16 v[90:91], v99 offset:768
v_mfma_f32_16x16x32_bf16 v[144:147], v[64:67], a[196:199], v[144:147]
v_mfma_f32_16x16x32_bf16 v[144:147], v[68:71], a[212:215], v[144:147]
v_mfma_f32_16x16x32_bf16 v[160:163], v[64:67], a[200:203], v[160:163]
ds_read_b64_tr_b16 v[92:93], v99 offset:8704
v_mfma_f32_16x16x32_bf16 v[160:163], v[68:71], a[216:219], v[160:163]
ds_read_b64_tr_b16 v[94:95], v99 offset:8960
v_mfma_f32_16x16x32_bf16 v[176:179], v[64:67], a[204:207], v[176:179]
v_mfma_f32_16x16x32_bf16 v[176:179], v[68:71], a[220:223], v[176:179]
s_waitcnt lgkmcnt(8)
v_mfma_f32_16x16x32_bf16 v[132:135], v[72:75], a[192:195], v[132:135]
ds_read_b64_tr_b16 v[64:65], v98 offset:16384
v_mfma_f32_16x16x32_bf16 v[132:135], v[76:79], a[208:211], v[132:135]
ds_read_b64_tr_b16 v[66:67], v98 offset:16640
v_mfma_f32_16x16x32_bf16 v[148:151], v[72:75], a[196:199], v[148:151]
v_mfma_f32_16x16x32_bf16 v[148:151], v[76:79], a[212:215], v[148:151]
v_mfma_f32_16x16x32_bf16 v[164:167], v[72:75], a[200:203], v[164:167]
ds_read_b64_tr_b16 v[68:69], v98 offset:24576
v_mfma_f32_16x16x32_bf16 v[164:167], v[76:79], a[216:219], v[164:167]
ds_read_b64_tr_b16 v[70:71], v98 offset:24832
v_mfma_f32_16x16x32_bf16 v[180:183], v[72:75], a[204:207], v[180:183]
v_mfma_f32_16x16x32_bf16 v[180:183], v[76:79], a[220:223], v[180:183]
s_waitcnt lgkmcnt(8)
v_mfma_f32_16x16x32_bf16 v[136:139], v[80:83], a[192:195], v[136:139]
ds_read_b64_tr_b16 v[72:73], v99 offset:16384
v_mfma_f32_16x16x32_bf16 v[136:139], v[84:87], a[208:211], v[136:139]
ds_read_b64_tr_b16 v[74:75], v99 offset:16640
v_mfma_f32_16x16x32_bf16 v[152:155], v[80:83], a[196:199], v[152:155]
global_load_dword v1, v1, s[14:15]
v_mfma_f32_16x16x32_bf16 v[152:155], v[84:87], a[212:215], v[152:155]
v_mfma_f32_16x16x32_bf16 v[168:171], v[80:83], a[200:203], v[168:171]
ds_read_b64_tr_b16 v[76:77], v99 offset:24576
v_mfma_f32_16x16x32_bf16 v[168:171], v[84:87], a[216:219], v[168:171]
ds_read_b64_tr_b16 v[78:79], v99 offset:24832
v_mfma_f32_16x16x32_bf16 v[184:187], v[80:83], a[204:207], v[184:187]
v_mfma_f32_16x16x32_bf16 v[184:187], v[84:87], a[220:223], v[184:187]
s_waitcnt lgkmcnt(8)
v_mfma_f32_16x16x32_bf16 v[140:143], v[88:91], a[192:195], v[140:143]
ds_read_b64_tr_b16 v[80:81], v98 offset:16896
v_mfma_f32_16x16x32_bf16 v[140:143], v[92:95], a[208:211], v[140:143]
ds_read_b64_tr_b16 v[82:83], v98 offset:17152
v_mfma_f32_16x16x32_bf16 v[156:159], v[88:91], a[196:199], v[156:159]
v_mfma_f32_16x16x32_bf16 v[156:159], v[92:95], a[212:215], v[156:159]
v_mfma_f32_16x16x32_bf16 v[172:175], v[88:91], a[200:203], v[172:175]
ds_read_b64_tr_b16 v[84:85], v98 offset:25088
v_mfma_f32_16x16x32_bf16 v[172:175], v[92:95], a[216:219], v[172:175]
ds_read_b64_tr_b16 v[86:87], v98 offset:25344
v_mfma_f32_16x16x32_bf16 v[188:191], v[88:91], a[204:207], v[188:191]
v_mfma_f32_16x16x32_bf16 v[188:191], v[92:95], a[220:223], v[188:191]
s_waitcnt lgkmcnt(8)
v_mfma_f32_16x16x32_bf16 v[128:131], v[64:67], a[224:227], v[128:131]
ds_read_b64_tr_b16 v[88:89], v99 offset:16896
v_mfma_f32_16x16x32_bf16 v[128:131], v[68:71], a[240:243], v[128:131]
ds_read_b64_tr_b16 v[90:91], v99 offset:17152
v_mfma_f32_16x16x32_bf16 v[144:147], v[64:67], a[228:231], v[144:147]
v_mfma_f32_16x16x32_bf16 v[144:147], v[68:71], a[244:247], v[144:147]
v_mfma_f32_16x16x32_bf16 v[160:163], v[64:67], a[232:235], v[160:163]
ds_read_b64_tr_b16 v[92:93], v99 offset:25088
v_mfma_f32_16x16x32_bf16 v[160:163], v[68:71], a[248:251], v[160:163]
ds_read_b64_tr_b16 v[94:95], v99 offset:25344
v_mfma_f32_16x16x32_bf16 v[176:179], v[64:67], a[236:239], v[176:179]
v_mfma_f32_16x16x32_bf16 v[176:179], v[68:71], a[252:255], v[176:179]
s_waitcnt lgkmcnt(8)
v_mfma_f32_16x16x32_bf16 v[132:135], v[72:75], a[224:227], v[132:135]
ds_read_b64_tr_b16 v[64:65], v98 offset:4096
v_mfma_f32_16x16x32_bf16 v[132:135], v[76:79], a[240:243], v[132:135]
ds_read_b64_tr_b16 v[66:67], v98 offset:4352
v_mfma_f32_16x16x32_bf16 v[148:151], v[72:75], a[228:231], v[148:151]
v_mfma_f32_16x16x32_bf16 v[148:151], v[76:79], a[244:247], v[148:151]
v_mfma_f32_16x16x32_bf16 v[164:167], v[72:75], a[232:235], v[164:167]
ds_read_b64_tr_b16 v[68:69], v98 offset:12288
v_mfma_f32_16x16x32_bf16 v[164:167], v[76:79], a[248:251], v[164:167]
ds_read_b64_tr_b16 v[70:71], v98 offset:12544
v_mfma_f32_16x16x32_bf16 v[180:183], v[72:75], a[236:239], v[180:183]
v_mfma_f32_16x16x32_bf16 v[180:183], v[76:79], a[252:255], v[180:183]
s_waitcnt lgkmcnt(8)
v_mfma_f32_16x16x32_bf16 v[136:139], v[80:83], a[224:227], v[136:139]
ds_read_b64_tr_b16 v[72:73], v99 offset:4096
v_mfma_f32_16x16x32_bf16 v[136:139], v[84:87], a[240:243], v[136:139]
ds_read_b64_tr_b16 v[74:75], v99 offset:4352
v_mfma_f32_16x16x32_bf16 v[152:155], v[80:83], a[228:231], v[152:155]
v_mfma_f32_16x16x32_bf16 v[152:155], v[84:87], a[244:247], v[152:155]
v_mfma_f32_16x16x32_bf16 v[168:171], v[80:83], a[232:235], v[168:171]
ds_read_b64_tr_b16 v[76:77], v99 offset:12288
v_mfma_f32_16x16x32_bf16 v[168:171], v[84:87], a[248:251], v[168:171]
ds_read_b64_tr_b16 v[78:79], v99 offset:12544
v_mfma_f32_16x16x32_bf16 v[184:187], v[80:83], a[236:239], v[184:187]
v_mfma_f32_16x16x32_bf16 v[184:187], v[84:87], a[252:255], v[184:187]
s_waitcnt lgkmcnt(8)
v_mfma_f32_16x16x32_bf16 v[140:143], v[88:91], a[224:227], v[140:143]
ds_read_b64_tr_b16 v[80:81], v98 offset:4608
v_mfma_f32_16x16x32_bf16 v[140:143], v[92:95], a[240:243], v[140:143]
ds_read_b64_tr_b16 v[82:83], v98 offset:4864
v_mfma_f32_16x16x32_bf16 v[156:159], v[88:91], a[228:231], v[156:159]
v_mfma_f32_16x16x32_bf16 v[156:159], v[92:95], a[244:247], v[156:159]
v_mfma_f32_16x16x32_bf16 v[172:175], v[88:91], a[232:235], v[172:175]
ds_read_b64_tr_b16 v[84:85], v98 offset:12800
v_mfma_f32_16x16x32_bf16 v[172:175], v[92:95], a[248:251], v[172:175]
ds_read_b64_tr_b16 v[86:87], v98 offset:13056
v_mfma_f32_16x16x32_bf16 v[188:191], v[88:91], a[236:239], v[188:191]
v_mfma_f32_16x16x32_bf16 v[188:191], v[92:95], a[252:255], v[188:191]
s_waitcnt lgkmcnt(8)
v_mfma_f32_16x16x32_bf16 v[192:195], v[64:67], a[192:195], v[192:195]
ds_read_b64_tr_b16 v[88:89], v99 offset:4608
v_mfma_f32_16x16x32_bf16 v[192:195], v[68:71], a[208:211], v[192:195]
ds_read_b64_tr_b16 v[90:91], v99 offset:4864
v_mfma_f32_16x16x32_bf16 v[208:211], v[64:67], a[196:199], v[208:211]
v_mfma_f32_16x16x32_bf16 v[208:211], v[68:71], a[212:215], v[208:211]
v_mfma_f32_16x16x32_bf16 v[224:227], v[64:67], a[200:203], v[224:227]
ds_read_b64_tr_b16 v[92:93], v99 offset:12800
v_cvt_pk_bf16_f32 v32, v128, v129
v_mfma_f32_16x16x32_bf16 v[224:227], v[68:71], a[216:219], v[224:227]
ds_read_b64_tr_b16 v[94:95], v99 offset:13056
v_cvt_pk_bf16_f32 v33, v130, v131
v_mfma_f32_16x16x32_bf16 v[240:243], v[64:67], a[204:207], v[240:243]
v_cvt_pk_bf16_f32 v34, v132, v133
v_cvt_pk_bf16_f32 v35, v134, v135
v_mfma_f32_16x16x32_bf16 v[240:243], v[68:71], a[220:223], v[240:243]
v_cvt_pk_bf16_f32 v36, v136, v137
s_waitcnt lgkmcnt(8)
v_mfma_f32_16x16x32_bf16 v[196:199], v[72:75], a[192:195], v[196:199]
ds_read_b64_tr_b16 v[64:65], v98 offset:20480
v_mfma_f32_16x16x32_bf16 v[196:199], v[76:79], a[208:211], v[196:199]
)"
R"(ds_read_b64_tr_b16 v[66:67], v98 offset:20736
v_cvt_pk_bf16_f32 v37, v138, v139
v_mfma_f32_16x16x32_bf16 v[212:215], v[72:75], a[196:199], v[212:215]
v_mfma_f32_16x16x32_bf16 v[212:215], v[76:79], a[212:215], v[212:215]
v_cvt_pk_bf16_f32 v38, v140, v141
v_mfma_f32_16x16x32_bf16 v[228:231], v[72:75], a[200:203], v[228:231]
ds_read_b64_tr_b16 v[68:69], v98 offset:28672
v_mfma_f32_16x16x32_bf16 v[228:231], v[76:79], a[216:219], v[228:231]
ds_read_b64_tr_b16 v[70:71], v98 offset:28928
v_cvt_pk_bf16_f32 v39, v142, v143
v_mfma_f32_16x16x32_bf16 v[244:247], v[72:75], a[204:207], v[244:247]
v_mfma_f32_16x16x32_bf16 v[244:247], v[76:79], a[220:223], v[244:247]
v_cvt_pk_bf16_f32 v40, v144, v145
s_waitcnt lgkmcnt(8)
v_mfma_f32_16x16x32_bf16 v[200:203], v[80:83], a[192:195], v[200:203]
ds_read_b64_tr_b16 v[72:73], v99 offset:20480
v_cvt_pk_bf16_f32 v41, v146, v147
v_mfma_f32_16x16x32_bf16 v[200:203], v[84:87], a[208:211], v[200:203]
ds_read_b64_tr_b16 v[74:75], v99 offset:20736
v_mfma_f32_16x16x32_bf16 v[216:219], v[80:83], a[196:199], v[216:219]
v_cvt_pk_bf16_f32 v42, v148, v149
v_mfma_f32_16x16x32_bf16 v[216:219], v[84:87], a[212:215], v[216:219]
v_mfma_f32_16x16x32_bf16 v[232:235], v[80:83], a[200:203], v[232:235]
ds_read_b64_tr_b16 v[76:77], v99 offset:28672
v_cvt_pk_bf16_f32 v43, v150, v151
v_mfma_f32_16x16x32_bf16 v[232:235], v[84:87], a[216:219], v[232:235]
ds_read_b64_tr_b16 v[78:79], v99 offset:28928
v_mfma_f32_16x16x32_bf16 v[248:251], v[80:83], a[204:207], v[248:251]
s_add_u32 m0, s75, 0
s_xor_b32 s48, s66, 128
v_mfma_f32_16x16x32_bf16 v[248:251], v[84:87], a[220:223], v[248:251]
buffer_load_dwordx4 v104, s[80:83], s48 offen offset:0 lds
s_waitcnt lgkmcnt(8)
v_mfma_f32_16x16x32_bf16 v[204:207], v[88:91], a[192:195], v[204:207]
ds_read_b64_tr_b16 v[80:81], v98 offset:20992
v_cvt_pk_bf16_f32 v44, v152, v153
v_mfma_f32_16x16x32_bf16 v[204:207], v[92:95], a[208:211], v[204:207]
ds_read_b64_tr_b16 v[82:83], v98 offset:21248
v_cvt_pk_bf16_f32 v45, v154, v155
v_mfma_f32_16x16x32_bf16 v[220:223], v[88:91], a[196:199], v[220:223]
v_cvt_pk_bf16_f32 v46, v156, v157
v_mfma_f32_16x16x32_bf16 v[220:223], v[92:95], a[212:215], v[220:223]
global_store_dwordx4 v22, v[32:35], s[94:95] offset:-4096 sc1
v_cvt_pk_bf16_f32 v47, v158, v159
v_mfma_f32_16x16x32_bf16 v[236:239], v[88:91], a[200:203], v[236:239]
ds_read_b64_tr_b16 v[84:85], v98 offset:29184
v_mfma_f32_16x16x32_bf16 v[236:239], v[92:95], a[216:219], v[236:239]
ds_read_b64_tr_b16 v[86:87], v98 offset:29440
v_cvt_pk_bf16_f32 v48, v160, v161
v_mfma_f32_16x16x32_bf16 v[252:255], v[88:91], a[204:207], v[252:255]
v_mfma_f32_16x16x32_bf16 v[252:255], v[92:95], a[220:223], v[252:255]
v_cvt_pk_bf16_f32 v49, v162, v163
s_waitcnt lgkmcnt(8)
v_mfma_f32_16x16x32_bf16 v[192:195], v[64:67], a[224:227], v[192:195]
ds_read_b64_tr_b16 v[88:89], v99 offset:20992
v_mfma_f32_16x16x32_bf16 v[192:195], v[68:71], a[240:243], v[192:195]
ds_read_b64_tr_b16 v[90:91], v99 offset:21248
v_cvt_pk_bf16_f32 v50, v164, v165
v_mfma_f32_16x16x32_bf16 v[208:211], v[64:67], a[228:231], v[208:211]
global_store_dwordx4 v22, v[36:39], s[94:95] offset:-3072 sc1
v_mfma_f32_16x16x32_bf16 v[208:211], v[68:71], a[244:247], v[208:211]
v_cvt_pk_bf16_f32 v51, v166, v167
v_mfma_f32_16x16x32_bf16 v[224:227], v[64:67], a[232:235], v[224:227]
ds_read_b64_tr_b16 v[92:93], v99 offset:29184
v_mfma_f32_16x16x32_bf16 v[224:227], v[68:71], a[248:251], v[224:227]
ds_read_b64_tr_b16 v[94:95], v99 offset:29440
v_cvt_pk_bf16_f32 v52, v168, v169
v_mfma_f32_16x16x32_bf16 v[240:243], v[64:67], a[236:239], v[240:243]
v_mfma_f32_16x16x32_bf16 v[240:243], v[68:71], a[252:255], v[240:243]
v_cvt_pk_bf16_f32 v53, v170, v171
s_waitcnt lgkmcnt(8)
v_mfma_f32_16x16x32_bf16 v[196:199], v[72:75], a[224:227], v[196:199]
global_store_dwordx4 v22, v[40:43], s[94:95] offset:0 sc1
v_cvt_pk_bf16_f32 v54, v172, v173
v_mfma_f32_16x16x32_bf16 v[196:199], v[76:79], a[240:243], v[196:199]
v_mfma_f32_16x16x32_bf16 v[212:215], v[72:75], a[228:231], v[212:215]
v_cvt_pk_bf16_f32 v55, v174, v175
v_mfma_f32_16x16x32_bf16 v[212:215], v[76:79], a[244:247], v[212:215]
v_mfma_f32_16x16x32_bf16 v[228:231], v[72:75], a[232:235], v[228:231]
v_cvt_pk_bf16_f32 v56, v176, v177
v_mfma_f32_16x16x32_bf16 v[228:231], v[76:79], a[248:251], v[228:231]
s_waitcnt vmcnt(5) lgkmcnt(0)
s_barrier
v_mfma_f32_16x16x32_bf16 v[244:247], v[72:75], a[236:239], v[244:247]
ds_read_b64_tr_b16 v[64:65], v98 offset:32768
v_cvt_pk_bf16_f32 v57, v178, v179
v_mfma_f32_16x16x32_bf16 v[244:247], v[76:79], a[252:255], v[244:247]
ds_read_b64_tr_b16 v[66:67], v98 offset:33024
v_mfma_f32_16x16x32_bf16 v[200:203], v[80:83], a[224:227], v[200:203]
ds_read_b64_tr_b16 v[68:69], v98 offset:36864
v_cvt_pk_bf16_f32 v58, v180, v181
v_mfma_f32_16x16x32_bf16 v[200:203], v[84:87], a[240:243], v[200:203]
ds_read_b64_tr_b16 v[70:71], v98 offset:37120
v_mfma_f32_16x16x32_bf16 v[216:219], v[80:83], a[228:231], v[216:219]
ds_read_b64_tr_b16 v[72:73], v99 offset:32768
v_cvt_pk_bf16_f32 v59, v182, v183
v_mfma_f32_16x16x32_bf16 v[216:219], v[84:87], a[244:247], v[216:219]
ds_read_b64_tr_b16 v[74:75], v99 offset:33024
v_cvt_pk_bf16_f32 v60, v184, v185
v_mfma_f32_16x16x32_bf16 v[232:235], v[80:83], a[232:235], v[232:235]
ds_read_b64_tr_b16 v[76:77], v99 offset:36864
v_mfma_f32_16x16x32_bf16 v[232:235], v[84:87], a[248:251], v[232:235]
ds_read_b64_tr_b16 v[78:79], v99 offset:37120
v_cvt_pk_bf16_f32 v61, v186, v187
v_mfma_f32_16x16x32_bf16 v[248:251], v[80:83], a[236:239], v[248:251]
buffer_load_dwordx4 v105, s[80:83], s48 offen offset:1024 lds
v_mfma_f32_16x16x32_bf16 v[248:251], v[84:87], a[252:255], v[248:251]
v_cvt_pk_bf16_f32 v62, v188, v189
v_mfma_f32_16x16x32_bf16 v[204:207], v[88:91], a[224:227], v[204:207]
ds_read_b64_tr_b16 v[80:81], v98 offset:33280
v_mfma_f32_16x16x32_bf16 v[204:207], v[92:95], a[240:243], v[204:207]
ds_read_b64_tr_b16 v[82:83], v98 offset:33536
v_cvt_pk_bf16_f32 v63, v190, v191
v_mfma_f32_16x16x32_bf16 v[220:223], v[88:91], a[228:231], v[220:223]
buffer_load_dwordx4 v106, s[80:83], s48 offen offset:2048 lds
v_mfma_f32_16x16x32_bf16 v[220:223], v[92:95], a[244:247], v[220:223]
v_mfma_f32_16x16x32_bf16 v[236:239], v[88:91], a[232:235], v[236:239]
ds_read_b64_tr_b16 v[84:85], v98 offset:37376
v_mfma_f32_16x16x32_bf16 v[236:239], v[92:95], a[248:251], v[236:239]
ds_read_b64_tr_b16 v[86:87], v98 offset:37632
v_mfma_f32_16x16x32_bf16 v[252:255], v[88:91], a[236:239], v[252:255]
buffer_load_dwordx4 v107, s[80:83], s48 offen offset:3072 lds
s_add_u32 m0, s75, 4096
v_mfma_f32_16x16x32_bf16 v[252:255], v[92:95], a[252:255], v[252:255]
s_xor_b32 s48, s66, 262272
s_waitcnt lgkmcnt(8)
v_mfma_f32_16x16x32_bf16 v[128:131], a[0:3], v[64:67], 0
ds_read_b64_tr_b16 v[88:89], v99 offset:33280
v_mfma_f32_16x16x32_bf16 v[128:131], a[16:19], v[68:71], v[128:131]
ds_read_b64_tr_b16 v[90:91], v99 offset:33536
v_mfma_f32_16x16x32_bf16 v[144:147], a[4:7], v[64:67], 0
buffer_load_dwordx4 v104, s[80:83], s48 offen offset:0 lds
v_mfma_f32_16x16x32_bf16 v[144:147], a[20:23], v[68:71], v[144:147]
v_mfma_f32_16x16x32_bf16 v[160:163], a[8:11], v[64:67], 0
ds_read_b64_tr_b16 v[92:93], v99 offset:37376
v_mul_f32 v192, s98, v192
v_mfma_f32_16x16x32_bf16 v[160:163], a[24:27], v[68:71], v[160:163]
ds_read_b64_tr_b16 v[94:95], v99 offset:37632
v_mul_f32 v193, s98, v193
v_mfma_f32_16x16x32_bf16 v[176:179], a[12:15], v[64:67], 0
buffer_load_dwordx4 v105, s[80:83], s48 offen offset:1024 lds
v_cvt_pk_bf16_f32 v2, v192, v193
v_mfma_f32_16x16x32_bf16 v[176:179], a[28:31], v[68:71], v[176:179]
v_mul_f32 v194, s98, v194
s_waitcnt lgkmcnt(8)
v_mfma_f32_16x16x32_bf16 v[132:135], a[0:3], v[72:75], 0
ds_read_b64_tr_b16 v[64:65], v98 offset:40960
v_mul_f32 v195, s98, v195
v_mfma_f32_16x16x32_bf16 v[132:135], a[16:19], v[76:79], v[132:135]
ds_read_b64_tr_b16 v[66:67], v98 offset:41216
v_cvt_pk_bf16_f32 v3, v194, v195
v_mfma_f32_16x16x32_bf16 v[148:151], a[4:7], v[72:75], 0
buffer_load_dwordx4 v106, s[80:83], s48 offen offset:2048 lds
v_mul_f32 v196, s98, v196
v_mfma_f32_16x16x32_bf16 v[148:151], a[20:23], v[76:79], v[148:151]
v_mul_f32 v197, s98, v197
v_cvt_pk_bf16_f32 v4, v196, v197
v_mfma_f32_16x16x32_bf16 v[164:167], a[8:11], v[72:75], 0
ds_read_b64_tr_b16 v[68:69], v98 offset:45056
v_mul_f32 v198, s98, v198
v_mfma_f32_16x16x32_bf16 v[164:167], a[24:27], v[76:79], v[164:167]
ds_read_b64_tr_b16 v[70:71], v98 offset:45312
v_mul_f32 v199, s98, v199
v_mfma_f32_16x16x32_bf16 v[180:183], a[12:15], v[72:75], 0
buffer_load_dwordx4 v107, s[80:83], s48 offen offset:3072 lds
v_cvt_pk_bf16_f32 v5, v198, v199
v_mfma_f32_16x16x32_bf16 v[180:183], a[28:31], v[76:79], v[180:183]
v_mul_f32 v200, s98, v200
s_waitcnt lgkmcnt(8)
v_mfma_f32_16x16x32_bf16 v[136:139], a[0:3], v[80:83], 0
ds_read_b64_tr_b16 v[72:73], v99 offset:40960
v_mul_f32 v201, s98, v201
v_mfma_f32_16x16x32_bf16 v[136:139], a[16:19], v[84:87], v[136:139]
ds_read_b64_tr_b16 v[74:75], v99 offset:41216
v_cvt_pk_bf16_f32 v6, v200, v201
v_mfma_f32_16x16x32_bf16 v[152:155], a[4:7], v[80:83], 0
v_mul_f32 v202, s98, v202
v_mul_f32 v203, s98, v203
v_mfma_f32_16x16x32_bf16 v[152:155], a[20:23], v[84:87], v[152:155]
global_store_dwordx4 v22, v[44:47], s[94:95] offset:1024 sc1
v_cvt_pk_bf16_f32 v7, v202, v203
v_mfma_f32_16x16x32_bf16 v[168:171], a[8:11], v[80:83], 0
ds_read_b64_tr_b16 v[76:77], v99 offset:45056
v_mul_f32 v204, s98, v204
v_mfma_f32_16x16x32_bf16 v[168:171], a[24:27], v[84:87], v[168:171]
ds_read_b64_tr_b16 v[78:79], v99 offset:45312
v_mul_f32 v205, s98, v205
v_mfma_f32_16x16x32_bf16 v[184:187], a[12:15], v[80:83], 0
v_cvt_pk_bf16_f32 v8, v204, v205
v_mul_f32 v206, s98, v206
v_mfma_f32_16x16x32_bf16 v[184:187], a[28:31], v[84:87], v[184:187]
v_mul_f32 v207, s98, v207
s_waitcnt lgkmcnt(8)
v_mfma_f32_16x16x32_bf16 v[140:143], a[0:3], v[88:91], 0
ds_read_b64_tr_b16 v[80:81], v98 offset:41472
v_cvt_pk_bf16_f32 v9, v206, v207
v_mfma_f32_16x16x32_bf16 v[140:143], a[16:19], v[92:95], v[140:143]
ds_read_b64_tr_b16 v[82:83], v98 offset:41728
v_mul_f32 v208, s98, v208
v_mfma_f32_16x16x32_bf16 v[156:159], a[4:7], v[88:91], 0
global_store_dwordx4 v22, v[48:51], s[96:97] offset:-4096 sc1
v_mul_f32 v209, s98, v209
v_mfma_f32_16x16x32_bf16 v[156:159], a[20:23], v[92:95], v[156:159]
v_cvt_pk_bf16_f32 v10, v208, v209
v_mul_f32 v210, s98, v210
PAD2_%=:
s_cbranch_execz PAD10_%=
v_mfma_f32_16x16x32_bf16 v[172:175], a[8:11], v[88:91], 0
ds_read_b64_tr_b16 v[84:85], v98 offset:45568
s_lshl_b32 s48, s28, 8
v_mfma_f32_16x16x32_bf16 v[172:175], a[24:27], v[92:95], v[172:175]
ds_read_b64_tr_b16 v[86:87], v98 offset:45824
s_mov_b64 s[84:85], s[14:15]
v_mfma_f32_16x16x32_bf16 v[188:191], a[12:15], v[88:91], 0
s_add_u32 s84, s84, s48
s_addc_u32 s85, s85, 0
v_mfma_f32_16x16x32_bf16 v[188:191], a[28:31], v[92:95], v[188:191]
s_and_b32 s85, s85, 0xffff
s_waitcnt lgkmcnt(8)
v_mfma_f32_16x16x32_bf16 v[128:131], a[32:35], v[64:67], v[128:131]
ds_read_b64_tr_b16 v[88:89], v99 offset:41472
s_sub_u32 s86, 0x400000, s48
v_mfma_f32_16x16x32_bf16 v[128:131], a[48:51], v[68:71], v[128:131]
ds_read_b64_tr_b16 v[90:91], v99 offset:41728
s_add_u32 m0, s75, 131072
v_mfma_f32_16x16x32_bf16 v[144:147], a[36:39], v[64:67], v[144:147]
s_xor_b32 s48, s65, 0
v_mul_f32 v211, s98, v211
v_mfma_f32_16x16x32_bf16 v[144:147], a[52:55], v[68:71], v[144:147]
buffer_load_dwordx4 v104, s[84:87], s48 offen offset:0 lds
v_cvt_pk_bf16_f32 v11, v210, v211
v_mfma_f32_16x16x32_bf16 v[160:163], a[40:43], v[64:67], v[160:163]
ds_read_b64_tr_b16 v[92:93], v99 offset:45568
v_mul_f32 v212, s98, v212
v_mfma_f32_16x16x32_bf16 v[160:163], a[56:59], v[68:71], v[160:163]
ds_read_b64_tr_b16 v[94:95], v99 offset:45824
v_mul_f32 v213, s98, v213
v_mfma_f32_16x16x32_bf16 v[176:179], a[44:47], v[64:67], v[176:179]
buffer_load_dwordx4 v105, s[84:87], s48 offen offset:1024 lds
v_cvt_pk_bf16_f32 v12, v212, v213
v_mfma_f32_16x16x32_bf16 v[176:179], a[60:63], v[68:71], v[176:179]
v_mul_f32 v214, s98, v214
s_waitcnt lgkmcnt(8)
v_mfma_f32_16x16x32_bf16 v[132:135], a[32:35], v[72:75], v[132:135]
ds_read_b64_tr_b16 v[64:65], v98 offset:49152
s_and_b32 s77, s30, 4
v_mfma_f32_16x16x32_bf16 v[132:135], a[48:51], v[76:79], v[132:135]
ds_read_b64_tr_b16 v[66:67], v98 offset:49408
s_lshl_b32 s77, s77, 11
v_mfma_f32_16x16x32_bf16 v[148:151], a[36:39], v[72:75], v[148:151]
v_or_b32 v115, s77, v22
v_xor_b32 v112, 4096, v115
v_mfma_f32_16x16x32_bf16 v[148:151], a[52:55], v[76:79], v[148:151]
v_xor_b32 v113, 12288, v115
v_xor_b32 v114, 20480, v115
v_mfma_f32_16x16x32_bf16 v[164:167], a[40:43], v[72:75], v[164:167]
ds_read_b64_tr_b16 v[68:69], v98 offset:53248
v_xor_b32 v115, 28672, v115
v_mfma_f32_16x16x32_bf16 v[164:167], a[56:59], v[76:79], v[164:167]
ds_read_b64_tr_b16 v[70:71], v98 offset:53504
s_lshl_b32 s77, s29, 2
v_mfma_f32_16x16x32_bf16 v[180:183], a[44:47], v[72:75], v[180:183]
s_add_u32 s77, s77, %2
s_lshl_b32 s77, s77, 15
v_mfma_f32_16x16x32_bf16 v[180:183], a[60:63], v[76:79], v[180:183]
s_mov_b64 s[44:45], s[10:11]
s_waitcnt lgkmcnt(8)
v_mfma_f32_16x16x32_bf16 v[136:139], a[32:35], v[80:83], v[136:139]
ds_read_b64_tr_b16 v[72:73], v99 offset:49152
v_mul_f32 v215, s98, v215
v_mfma_f32_16x16x32_bf16 v[136:139], a[48:51], v[84:87], v[136:139]
ds_read_b64_tr_b16 v[74:75], v99 offset:49408
v_cvt_pk_bf16_f32 v13, v214, v215
v_mfma_f32_16x16x32_bf16 v[152:155], a[36:39], v[80:83], v[152:155]
s_add_u32 s44, s44, s77
s_addc_u32 s45, s45, 0
v_mfma_f32_16x16x32_bf16 v[152:155], a[52:55], v[84:87], v[152:155]
s_lshl_b32 s77, s29, 2
s_sub_u32 s77, 60, s77
v_mfma_f32_16x16x32_bf16 v[168:171], a[40:43], v[80:83], v[168:171]
ds_read_b64_tr_b16 v[76:77], v99 offset:53248
s_add_u32 s77, s77, %2
v_mfma_f32_16x16x32_bf16 v[168:171], a[56:59], v[84:87], v[168:171]
ds_read_b64_tr_b16 v[78:79], v99 offset:53504
s_lshl_b32 s77, s77, 15
s_cmp_lg_u32 %3, 0
s_cbranch_scc1 W0N10_%=
s_mov_b64 exec, 1
global_load_dword v24, v101, s[92:93] offset:0 sc1
s_mov_b64 exec, -1
W0N10_%=:
v_mfma_f32_16x16x32_bf16 v[184:187], a[44:47], v[80:83], v[184:187]
buffer_load_dwordx4 v106, s[84:87], s48 offen offset:2048 lds
s_mov_b64 s[46:47], s[10:11]
v_mfma_f32_16x16x32_bf16 v[184:187], a[60:63], v[84:87], v[184:187]
v_mul_f32 v216, s98, v216
s_waitcnt lgkmcnt(8)
v_mfma_f32_16x16x32_bf16 v[140:143], a[32:35], v[88:91], v[140:143]
ds_read_b64_tr_b16 v[80:81], v98 offset:49664
v_mul_f32 v217, s98, v217
v_mfma_f32_16x16x32_bf16 v[140:143], a[48:51], v[92:95], v[140:143]
ds_read_b64_tr_b16 v[82:83], v98 offset:49920
v_cvt_pk_bf16_f32 v14, v216, v217
v_mfma_f32_16x16x32_bf16 v[156:159], a[36:39], v[88:91], v[156:159]
s_add_u32 s46, s46, s77
s_addc_u32 s47, s47, 0
v_mfma_f32_16x16x32_bf16 v[156:159], a[52:55], v[92:95], v[156:159]
buffer_load_dwordx4 v107, s[84:87], s48 offen offset:3072 lds
s_add_u32 m0, s75, 135168
v_mfma_f32_16x16x32_bf16 v[172:175], a[40:43], v[88:91], v[172:175]
ds_read_b64_tr_b16 v[84:85], v98 offset:53760
s_xor_b32 s48, s65, 262144
v_mfma_f32_16x16x32_bf16 v[172:175], a[56:59], v[92:95], v[172:175]
ds_read_b64_tr_b16 v[86:87], v98 offset:54016
v_mul_f32 v218, s98, v218
v_mfma_f32_16x16x32_bf16 v[188:191], a[44:47], v[88:91], v[188:191]
buffer_load_dwordx4 v104, s[84:87], s48 offen offset:0 lds
v_mul_f32 v219, s98, v219
v_mfma_f32_16x16x32_bf16 v[188:191], a[60:63], v[92:95], v[188:191]
v_cvt_pk_bf16_f32 v15, v218, v219
s_waitcnt lgkmcnt(8)
v_mfma_f32_16x16x32_bf16 v[128:131], a[64:67], v[64:67], v[128:131]
ds_read_b64_tr_b16 v[88:89], v99 offset:49664
v_mul_f32 v220, s98, v220
v_mfma_f32_16x16x32_bf16 v[128:131], a[80:83], v[68:71], v[128:131]
ds_read_b64_tr_b16 v[90:91], v99 offset:49920
v_mul_f32 v221, s98, v221
v_mfma_f32_16x16x32_bf16 v[144:147], a[68:71], v[64:67], v[144:147]
buffer_load_dwordx4 v105, s[84:87], s48 offen offset:1024 lds
v_cvt_pk_bf16_f32 v16, v220, v221
v_mfma_f32_16x16x32_bf16 v[144:147], a[84:87], v[68:71], v[144:147]
v_mul_f32 v222, s98, v222
v_mul_f32 v223, s98, v223
v_mfma_f32_16x16x32_bf16 v[160:163], a[72:75], v[64:67], v[160:163]
ds_read_b64_tr_b16 v[92:93], v99 offset:53760
v_cvt_pk_bf16_f32 v17, v222, v223
v_mfma_f32_16x16x32_bf16 v[160:163], a[88:91], v[68:71], v[160:163]
ds_read_b64_tr_b16 v[94:95], v99 offset:54016
v_mul_f32 v224, s98, v224
v_mfma_f32_16x16x32_bf16 v[176:179], a[76:79], v[64:67], v[176:179]
buffer_load_dwordx4 v106, s[84:87], s48 offen offset:2048 lds
v_mul_f32 v225, s98, v225
v_mfma_f32_16x16x32_bf16 v[176:179], a[92:95], v[68:71], v[176:179]
v_cvt_pk_bf16_f32 v18, v224, v225
s_waitcnt lgkmcnt(8)
v_mfma_f32_16x16x32_bf16 v[132:135], a[64:67], v[72:75], v[132:135]
ds_read_b64_tr_b16 v[64:65], v98 offset:57344
v_mul_f32 v226, s98, v226
v_mfma_f32_16x16x32_bf16 v[132:135], a[80:83], v[76:79], v[132:135]
ds_read_b64_tr_b16 v[66:67], v98 offset:57600
v_mul_f32 v227, s98, v227
v_mfma_f32_16x16x32_bf16 v[148:151], a[68:71], v[72:75], v[148:151]
v_cvt_pk_bf16_f32 v19, v226, v227
v_mul_f32 v228, s98, v228
v_mfma_f32_16x16x32_bf16 v[148:151], a[84:87], v[76:79], v[148:151]
buffer_load_dwordx4 v107, s[84:87], s48 offen offset:3072 lds
v_mul_f32 v229, s98, v229
v_mfma_f32_16x16x32_bf16 v[164:167], a[72:75], v[72:75], v[164:167]
ds_read_b64_tr_b16 v[68:69], v98 offset:61440
v_cvt_pk_bf16_f32 v20, v228, v229
v_mfma_f32_16x16x32_bf16 v[164:167], a[88:91], v[76:79], v[164:167]
ds_read_b64_tr_b16 v[70:71], v98 offset:61696
v_mul_f32 v230, s98, v230
v_mfma_f32_16x16x32_bf16 v[180:183], a[76:79], v[72:75], v[180:183]
v_mul_f32 v231, s98, v231
v_cvt_pk_bf16_f32 v21, v230, v231
v_mfma_f32_16x16x32_bf16 v[180:183], a[92:95], v[76:79], v[180:183]
global_store_dwordx4 v22, v[52:55], s[96:97] offset:-3072 sc1
s_waitcnt lgkmcnt(8)
v_mfma_f32_16x16x32_bf16 v[136:139], a[64:67], v[80:83], v[136:139]
ds_read_b64_tr_b16 v[72:73], v99 offset:57344
v_mul_f32 v232, s98, v232
v_mfma_f32_16x16x32_bf16 v[136:139], a[80:83], v[84:87], v[136:139]
ds_read_b64_tr_b16 v[74:75], v99 offset:57600
v_mul_f32 v233, s98, v233
v_mfma_f32_16x16x32_bf16 v[152:155], a[68:71], v[80:83], v[152:155]
v_cvt_pk_bf16_f32 v28, v232, v233
v_mul_f32 v234, s98, v234
v_mfma_f32_16x16x32_bf16 v[152:155], a[84:87], v[84:87], v[152:155]
v_mul_f32 v235, s98, v235
v_mfma_f32_16x16x32_bf16 v[168:171], a[72:75], v[80:83], v[168:171]
ds_read_b64_tr_b16 v[76:77], v99 offset:61440
v_cvt_pk_bf16_f32 v29, v234, v235
v_mfma_f32_16x16x32_bf16 v[168:171], a[88:91], v[84:87], v[168:171]
ds_read_b64_tr_b16 v[78:79], v99 offset:61696
v_mul_f32 v236, s98, v236
v_mfma_f32_16x16x32_bf16 v[184:187], a[76:79], v[80:83], v[184:187]
global_store_dwordx4 v22, v[56:59], s[96:97] offset:0 sc1
v_mfma_f32_16x16x32_bf16 v[184:187], a[92:95], v[84:87], v[184:187]
v_mul_f32 v237, s98, v237
s_waitcnt lgkmcnt(8)
v_mfma_f32_16x16x32_bf16 v[140:143], a[64:67], v[88:91], v[140:143]
ds_read_b64_tr_b16 v[80:81], v98 offset:57856
v_cvt_pk_bf16_f32 v30, v236, v237
v_mfma_f32_16x16x32_bf16 v[140:143], a[80:83], v[92:95], v[140:143]
ds_read_b64_tr_b16 v[82:83], v98 offset:58112
s_add_u32 m0, s75, 32768
v_mfma_f32_16x16x32_bf16 v[156:159], a[68:71], v[88:91], v[156:159]
s_xor_b32 s48, s66, 2097152
v_mul_f32 v238, s98, v238
)"
R"(v_mfma_f32_16x16x32_bf16 v[156:159], a[84:87], v[92:95], v[156:159]
v_mul_f32 v239, s98, v239
v_mfma_f32_16x16x32_bf16 v[172:175], a[72:75], v[88:91], v[172:175]
ds_read_b64_tr_b16 v[84:85], v98 offset:61952
v_cvt_pk_bf16_f32 v31, v238, v239
v_mfma_f32_16x16x32_bf16 v[172:175], a[88:91], v[92:95], v[172:175]
ds_read_b64_tr_b16 v[86:87], v98 offset:62208
v_mul_f32 v240, s98, v240
v_mfma_f32_16x16x32_bf16 v[188:191], a[76:79], v[88:91], v[188:191]
buffer_load_dwordx4 v104, s[80:83], s48 offen offset:0 lds
v_mul_f32 v241, s98, v241
v_mfma_f32_16x16x32_bf16 v[188:191], a[92:95], v[92:95], v[188:191]
s_waitcnt lgkmcnt(8)
v_mfma_f32_16x16x32_bf16 v[128:131], a[96:99], v[64:67], v[128:131]
ds_read_b64_tr_b16 v[88:89], v99 offset:57856
v_cvt_pk_bf16_f32 v120, v240, v241
v_mfma_f32_16x16x32_bf16 v[128:131], a[112:115], v[68:71], v[128:131]
ds_read_b64_tr_b16 v[90:91], v99 offset:58112
v_mul_f32 v242, s98, v242
v_mfma_f32_16x16x32_bf16 v[144:147], a[100:103], v[64:67], v[144:147]
buffer_load_dwordx4 v105, s[80:83], s48 offen offset:1024 lds
v_mul_f32 v243, s98, v243
v_mfma_f32_16x16x32_bf16 v[144:147], a[116:119], v[68:71], v[144:147]
v_mfma_f32_16x16x32_bf16 v[160:163], a[104:107], v[64:67], v[160:163]
ds_read_b64_tr_b16 v[92:93], v99 offset:61952
v_cvt_pk_bf16_f32 v121, v242, v243
v_mfma_f32_16x16x32_bf16 v[160:163], a[120:123], v[68:71], v[160:163]
ds_read_b64_tr_b16 v[94:95], v99 offset:62208
v_mul_f32 v244, s98, v244
v_mfma_f32_16x16x32_bf16 v[176:179], a[108:111], v[64:67], v[176:179]
buffer_load_dwordx4 v106, s[80:83], s48 offen offset:2048 lds
v_mul_f32 v245, s98, v245
v_mfma_f32_16x16x32_bf16 v[176:179], a[124:127], v[68:71], v[176:179]
s_cmp_lg_u32 %3, 0
s_cbranch_scc1 W0N11_%=
s_waitcnt vmcnt(11)
v_cmp_ne_u32 vcc, s54, v24
s_and_b32 s91, vcc_lo, 1
s_cbranch_scc1 HSP0N_%=
HOK0N_%=:
W0N11_%=:
s_waitcnt lgkmcnt(8)
v_mfma_f32_16x16x32_bf16 v[132:135], a[96:99], v[72:75], v[132:135]
v_cvt_pk_bf16_f32 v122, v244, v245
v_mfma_f32_16x16x32_bf16 v[132:135], a[112:115], v[76:79], v[132:135]
buffer_load_dwordx4 v107, s[80:83], s48 offen offset:3072 lds
v_mul_f32 v246, s98, v246
v_mfma_f32_16x16x32_bf16 v[148:151], a[100:103], v[72:75], v[148:151]
v_mul_f32 v247, s98, v247
v_mfma_f32_16x16x32_bf16 v[148:151], a[116:119], v[76:79], v[148:151]
s_add_u32 m0, s75, 36864
s_xor_b32 s48, s66, 2359296
v_mfma_f32_16x16x32_bf16 v[164:167], a[104:107], v[72:75], v[164:167]
v_cvt_pk_bf16_f32 v123, v246, v247
v_mfma_f32_16x16x32_bf16 v[164:167], a[120:123], v[76:79], v[164:167]
s_waitcnt vmcnt(16) lgkmcnt(0)
s_barrier
v_mfma_f32_16x16x32_bf16 v[180:183], a[108:111], v[72:75], v[180:183]
ds_read_b64_tr_b16 v[64:65], v96 offset:0
v_mul_f32 v248, s98, v248
v_mfma_f32_16x16x32_bf16 v[180:183], a[124:127], v[76:79], v[180:183]
ds_read_b64_tr_b16 v[66:67], v96 offset:256
v_mfma_f32_16x16x32_bf16 v[136:139], a[96:99], v[80:83], v[136:139]
ds_read_b64_tr_b16 v[68:69], v96 offset:4096
v_mul_f32 v249, s98, v249
v_mfma_f32_16x16x32_bf16 v[136:139], a[112:115], v[84:87], v[136:139]
ds_read_b64_tr_b16 v[70:71], v96 offset:4352
v_cvt_pk_bf16_f32 v124, v248, v249
v_mfma_f32_16x16x32_bf16 v[152:155], a[100:103], v[80:83], v[152:155]
ds_read_b64_tr_b16 v[72:73], v97 offset:0
v_mul_f32 v250, s98, v250
v_mfma_f32_16x16x32_bf16 v[152:155], a[116:119], v[84:87], v[152:155]
ds_read_b64_tr_b16 v[74:75], v97 offset:256
v_mul_f32 v251, s98, v251
v_mfma_f32_16x16x32_bf16 v[168:171], a[104:107], v[80:83], v[168:171]
ds_read_b64_tr_b16 v[76:77], v97 offset:4096
v_cvt_pk_bf16_f32 v125, v250, v251
v_mfma_f32_16x16x32_bf16 v[168:171], a[120:123], v[84:87], v[168:171]
ds_read_b64_tr_b16 v[78:79], v97 offset:4352
v_mul_f32 v252, s98, v252
v_mfma_f32_16x16x32_bf16 v[184:187], a[108:111], v[80:83], v[184:187]
buffer_load_dwordx4 v104, s[80:83], s48 offen offset:0 lds
v_mul_f32 v253, s98, v253
v_mfma_f32_16x16x32_bf16 v[184:187], a[124:127], v[84:87], v[184:187]
v_cvt_pk_bf16_f32 v126, v252, v253
v_mfma_f32_16x16x32_bf16 v[140:143], a[96:99], v[88:91], v[140:143]
ds_read_b64_tr_b16 v[80:81], v96 offset:512
v_mul_f32 v254, s98, v254
v_mfma_f32_16x16x32_bf16 v[140:143], a[112:115], v[92:95], v[140:143]
ds_read_b64_tr_b16 v[82:83], v96 offset:768
v_mul_f32 v255, s98, v255
v_mfma_f32_16x16x32_bf16 v[156:159], a[100:103], v[88:91], v[156:159]
buffer_load_dwordx4 v105, s[80:83], s48 offen offset:1024 lds
v_mfma_f32_16x16x32_bf16 v[156:159], a[116:119], v[92:95], v[156:159]
v_cvt_pk_bf16_f32 v127, v254, v255
v_mfma_f32_16x16x32_bf16 v[172:175], a[104:107], v[88:91], v[172:175]
ds_read_b64_tr_b16 v[84:85], v96 offset:4608
v_mfma_f32_16x16x32_bf16 v[172:175], a[120:123], v[92:95], v[172:175]
ds_read_b64_tr_b16 v[86:87], v96 offset:4864
v_mfma_f32_16x16x32_bf16 v[188:191], a[108:111], v[88:91], v[188:191]
buffer_load_dwordx4 v106, s[80:83], s48 offen offset:2048 lds
v_mfma_f32_16x16x32_bf16 v[188:191], a[124:127], v[92:95], v[188:191]
s_waitcnt lgkmcnt(8)
v_mfma_f32_16x16x32_bf16 v[192:195], a[0:3], v[64:67], 0
ds_read_b64_tr_b16 v[88:89], v97 offset:512
v_mfma_f32_16x16x32_bf16 v[192:195], a[16:19], v[68:71], v[192:195]
ds_read_b64_tr_b16 v[90:91], v97 offset:768
v_mfma_f32_16x16x32_bf16 v[208:211], a[4:7], v[64:67], 0
buffer_load_dwordx4 v107, s[80:83], s48 offen offset:3072 lds
v_mfma_f32_16x16x32_bf16 v[208:211], a[20:23], v[68:71], v[208:211]
v_mfma_f32_16x16x32_bf16 v[224:227], a[8:11], v[64:67], 0
ds_read_b64_tr_b16 v[92:93], v97 offset:4608
v_mfma_f32_16x16x32_bf16 v[224:227], a[24:27], v[68:71], v[224:227]
ds_read_b64_tr_b16 v[94:95], v97 offset:4864
v_mfma_f32_16x16x32_bf16 v[240:243], a[12:15], v[64:67], 0
global_store_dwordx4 v22, v[60:63], s[96:97] offset:1024 sc1
v_mfma_f32_16x16x32_bf16 v[240:243], a[28:31], v[68:71], v[240:243]
s_waitcnt lgkmcnt(8)
v_mfma_f32_16x16x32_bf16 v[196:199], a[0:3], v[72:75], 0
ds_read_b64_tr_b16 v[64:65], v96 offset:8192
v_mfma_f32_16x16x32_bf16 v[196:199], a[16:19], v[76:79], v[196:199]
ds_read_b64_tr_b16 v[66:67], v96 offset:8448
v_mfma_f32_16x16x32_bf16 v[212:215], a[4:7], v[72:75], 0
v_mfma_f32_16x16x32_bf16 v[212:215], a[20:23], v[76:79], v[212:215]
v_mfma_f32_16x16x32_bf16 v[228:231], a[8:11], v[72:75], 0
ds_read_b64_tr_b16 v[68:69], v96 offset:12288
v_mfma_f32_16x16x32_bf16 v[228:231], a[24:27], v[76:79], v[228:231]
ds_read_b64_tr_b16 v[70:71], v96 offset:12544
v_mfma_f32_16x16x32_bf16 v[244:247], a[12:15], v[72:75], 0
global_store_dwordx4 v22, v[2:5], s[94:95] offset:-2048 sc1
v_mfma_f32_16x16x32_bf16 v[244:247], a[28:31], v[76:79], v[244:247]
s_waitcnt lgkmcnt(8)
v_mfma_f32_16x16x32_bf16 v[200:203], a[0:3], v[80:83], 0
ds_read_b64_tr_b16 v[72:73], v97 offset:8192
v_mfma_f32_16x16x32_bf16 v[200:203], a[16:19], v[84:87], v[200:203]
ds_read_b64_tr_b16 v[74:75], v97 offset:8448
v_mfma_f32_16x16x32_bf16 v[216:219], a[4:7], v[80:83], 0
v_mfma_f32_16x16x32_bf16 v[216:219], a[20:23], v[84:87], v[216:219]
global_store_dwordx4 v22, v[6:9], s[94:95] offset:-1024 sc1
v_mfma_f32_16x16x32_bf16 v[232:235], a[8:11], v[80:83], 0
ds_read_b64_tr_b16 v[76:77], v97 offset:12288
v_mfma_f32_16x16x32_bf16 v[232:235], a[24:27], v[84:87], v[232:235]
ds_read_b64_tr_b16 v[78:79], v97 offset:12544
v_mfma_f32_16x16x32_bf16 v[248:251], a[12:15], v[80:83], 0
v_mfma_f32_16x16x32_bf16 v[248:251], a[28:31], v[84:87], v[248:251]
s_waitcnt lgkmcnt(8)
v_mfma_f32_16x16x32_bf16 v[204:207], a[0:3], v[88:91], 0
ds_read_b64_tr_b16 v[80:81], v96 offset:8704
v_mfma_f32_16x16x32_bf16 v[204:207], a[16:19], v[92:95], v[204:207]
ds_read_b64_tr_b16 v[82:83], v96 offset:8960
v_mfma_f32_16x16x32_bf16 v[220:223], a[4:7], v[88:91], 0
global_store_dwordx4 v22, v[10:13], s[94:95] offset:2048 sc1
v_mfma_f32_16x16x32_bf16 v[220:223], a[20:23], v[92:95], v[220:223]
v_mfma_f32_16x16x32_bf16 v[236:239], a[8:11], v[88:91], 0
ds_read_b64_tr_b16 v[84:85], v96 offset:12800
v_mfma_f32_16x16x32_bf16 v[236:239], a[24:27], v[92:95], v[236:239]
ds_read_b64_tr_b16 v[86:87], v96 offset:13056
s_add_u32 m0, s75, 65536
v_mfma_f32_16x16x32_bf16 v[252:255], a[12:15], v[88:91], 0
s_xor_b32 s48, s66, 2097280
v_mfma_f32_16x16x32_bf16 v[252:255], a[28:31], v[92:95], v[252:255]
s_waitcnt lgkmcnt(8)
v_mfma_f32_16x16x32_bf16 v[192:195], a[32:35], v[64:67], v[192:195]
ds_read_b64_tr_b16 v[88:89], v97 offset:8704
v_mfma_f32_16x16x32_bf16 v[192:195], a[48:51], v[68:71], v[192:195]
ds_read_b64_tr_b16 v[90:91], v97 offset:8960
v_mfma_f32_16x16x32_bf16 v[208:211], a[36:39], v[64:67], v[208:211]
buffer_load_dwordx4 v104, s[80:83], s48 offen offset:0 lds
v_mfma_f32_16x16x32_bf16 v[208:211], a[52:55], v[68:71], v[208:211]
v_mfma_f32_16x16x32_bf16 v[224:227], a[40:43], v[64:67], v[224:227]
ds_read_b64_tr_b16 v[92:93], v97 offset:12800
v_mfma_f32_16x16x32_bf16 v[224:227], a[56:59], v[68:71], v[224:227]
ds_read_b64_tr_b16 v[94:95], v97 offset:13056
v_mfma_f32_16x16x32_bf16 v[240:243], a[44:47], v[64:67], v[240:243]
buffer_load_dwordx4 v105, s[80:83], s48 offen offset:1024 lds
v_mfma_f32_16x16x32_bf16 v[240:243], a[60:63], v[68:71], v[240:243]
s_waitcnt lgkmcnt(8)
v_mfma_f32_16x16x32_bf16 v[196:199], a[32:35], v[72:75], v[196:199]
ds_read_b64_tr_b16 v[64:65], v96 offset:16384
v_mfma_f32_16x16x32_bf16 v[196:199], a[48:51], v[76:79], v[196:199]
ds_read_b64_tr_b16 v[66:67], v96 offset:16640
v_mfma_f32_16x16x32_bf16 v[212:215], a[36:39], v[72:75], v[212:215]
buffer_load_dwordx4 v106, s[80:83], s48 offen offset:2048 lds
v_mfma_f32_16x16x32_bf16 v[212:215], a[52:55], v[76:79], v[212:215]
v_mfma_f32_16x16x32_bf16 v[228:231], a[40:43], v[72:75], v[228:231]
ds_read_b64_tr_b16 v[68:69], v96 offset:20480
v_mfma_f32_16x16x32_bf16 v[228:231], a[56:59], v[76:79], v[228:231]
ds_read_b64_tr_b16 v[70:71], v96 offset:20736
v_mfma_f32_16x16x32_bf16 v[244:247], a[44:47], v[72:75], v[244:247]
buffer_load_dwordx4 v107, s[80:83], s48 offen offset:3072 lds
v_mfma_f32_16x16x32_bf16 v[244:247], a[60:63], v[76:79], v[244:247]
s_add_u32 m0, s75, 69632
s_waitcnt lgkmcnt(8)
v_mfma_f32_16x16x32_bf16 v[200:203], a[32:35], v[80:83], v[200:203]
ds_read_b64_tr_b16 v[72:73], v97 offset:16384
s_xor_b32 s48, s66, 2359424
v_mfma_f32_16x16x32_bf16 v[200:203], a[48:51], v[84:87], v[200:203]
ds_read_b64_tr_b16 v[74:75], v97 offset:16640
v_mfma_f32_16x16x32_bf16 v[216:219], a[36:39], v[80:83], v[216:219]
buffer_load_dwordx4 v104, s[80:83], s48 offen offset:0 lds
v_mfma_f32_16x16x32_bf16 v[216:219], a[52:55], v[84:87], v[216:219]
v_mfma_f32_16x16x32_bf16 v[232:235], a[40:43], v[80:83], v[232:235]
ds_read_b64_tr_b16 v[76:77], v97 offset:20480
v_mfma_f32_16x16x32_bf16 v[232:235], a[56:59], v[84:87], v[232:235]
ds_read_b64_tr_b16 v[78:79], v97 offset:20736
v_mfma_f32_16x16x32_bf16 v[248:251], a[44:47], v[80:83], v[248:251]
buffer_load_dwordx4 v105, s[80:83], s48 offen offset:1024 lds
v_mfma_f32_16x16x32_bf16 v[248:251], a[60:63], v[84:87], v[248:251]
s_waitcnt lgkmcnt(8)
v_mfma_f32_16x16x32_bf16 v[204:207], a[32:35], v[88:91], v[204:207]
ds_read_b64_tr_b16 v[80:81], v96 offset:16896
v_mfma_f32_16x16x32_bf16 v[204:207], a[48:51], v[92:95], v[204:207]
ds_read_b64_tr_b16 v[82:83], v96 offset:17152
v_mfma_f32_16x16x32_bf16 v[220:223], a[36:39], v[88:91], v[220:223]
buffer_load_dwordx4 v106, s[80:83], s48 offen offset:2048 lds
v_mfma_f32_16x16x32_bf16 v[220:223], a[52:55], v[92:95], v[220:223]
v_mfma_f32_16x16x32_bf16 v[236:239], a[40:43], v[88:91], v[236:239]
ds_read_b64_tr_b16 v[84:85], v96 offset:20992
v_mfma_f32_16x16x32_bf16 v[236:239], a[56:59], v[92:95], v[236:239]
ds_read_b64_tr_b16 v[86:87], v96 offset:21248
v_mfma_f32_16x16x32_bf16 v[252:255], a[44:47], v[88:91], v[252:255]
v_mfma_f32_16x16x32_bf16 v[252:255], a[60:63], v[92:95], v[252:255]
buffer_load_dwordx4 v107, s[80:83], s48 offen offset:3072 lds
s_waitcnt lgkmcnt(8)
v_mfma_f32_16x16x32_bf16 v[192:195], a[64:67], v[64:67], v[192:195]
ds_read_b64_tr_b16 v[88:89], v97 offset:16896
v_mfma_f32_16x16x32_bf16 v[192:195], a[80:83], v[68:71], v[192:195]
ds_read_b64_tr_b16 v[90:91], v97 offset:17152
v_mfma_f32_16x16x32_bf16 v[208:211], a[68:71], v[64:67], v[208:211]
v_mfma_f32_16x16x32_bf16 v[208:211], a[84:87], v[68:71], v[208:211]
global_store_dwordx4 v22, v[14:17], s[94:95] offset:3072 sc1
v_mfma_f32_16x16x32_bf16 v[224:227], a[72:75], v[64:67], v[224:227]
ds_read_b64_tr_b16 v[92:93], v97 offset:20992
v_mfma_f32_16x16x32_bf16 v[224:227], a[88:91], v[68:71], v[224:227]
ds_read_b64_tr_b16 v[94:95], v97 offset:21248
v_mfma_f32_16x16x32_bf16 v[240:243], a[76:79], v[64:67], v[240:243]
v_mfma_f32_16x16x32_bf16 v[240:243], a[92:95], v[68:71], v[240:243]
s_waitcnt lgkmcnt(8)
v_mfma_f32_16x16x32_bf16 v[196:199], a[64:67], v[72:75], v[196:199]
ds_read_b64_tr_b16 v[64:65], v96 offset:24576
v_mfma_f32_16x16x32_bf16 v[196:199], a[80:83], v[76:79], v[196:199]
ds_read_b64_tr_b16 v[66:67], v96 offset:24832
v_mfma_f32_16x16x32_bf16 v[212:215], a[68:71], v[72:75], v[212:215]
global_store_dwordx4 v22, v[18:21], s[96:97] offset:-2048 sc1
v_mfma_f32_16x16x32_bf16 v[212:215], a[84:87], v[76:79], v[212:215]
v_mfma_f32_16x16x32_bf16 v[228:231], a[72:75], v[72:75], v[228:231]
ds_read_b64_tr_b16 v[68:69], v96 offset:28672
v_mfma_f32_16x16x32_bf16 v[228:231], a[88:91], v[76:79], v[228:231]
ds_read_b64_tr_b16 v[70:71], v96 offset:28928
v_mfma_f32_16x16x32_bf16 v[244:247], a[76:79], v[72:75], v[244:247]
v_mfma_f32_16x16x32_bf16 v[244:247], a[92:95], v[76:79], v[244:247]
s_waitcnt lgkmcnt(8)
v_mfma_f32_16x16x32_bf16 v[200:203], a[64:67], v[80:83], v[200:203]
ds_read_b64_tr_b16 v[72:73], v97 offset:24576
v_mfma_f32_16x16x32_bf16 v[200:203], a[80:83], v[84:87], v[200:203]
ds_read_b64_tr_b16 v[74:75], v97 offset:24832
v_mfma_f32_16x16x32_bf16 v[216:219], a[68:71], v[80:83], v[216:219]
global_store_dwordx4 v22, v[28:31], s[96:97] offset:-1024 sc1
v_mfma_f32_16x16x32_bf16 v[216:219], a[84:87], v[84:87], v[216:219]
v_mfma_f32_16x16x32_bf16 v[232:235], a[72:75], v[80:83], v[232:235]
ds_read_b64_tr_b16 v[76:77], v97 offset:28672
v_mfma_f32_16x16x32_bf16 v[232:235], a[88:91], v[84:87], v[232:235]
ds_read_b64_tr_b16 v[78:79], v97 offset:28928
v_mfma_f32_16x16x32_bf16 v[248:251], a[76:79], v[80:83], v[248:251]
v_mfma_f32_16x16x32_bf16 v[248:251], a[92:95], v[84:87], v[248:251]
global_store_dwordx4 v22, v[120:123], s[96:97] offset:2048 sc1
s_waitcnt lgkmcnt(8)
v_mfma_f32_16x16x32_bf16 v[204:207], a[64:67], v[88:91], v[204:207]
ds_read_b64_tr_b16 v[80:81], v96 offset:25088
v_mfma_f32_16x16x32_bf16 v[204:207], a[80:83], v[92:95], v[204:207]
ds_read_b64_tr_b16 v[82:83], v96 offset:25344
v_mfma_f32_16x16x32_bf16 v[220:223], a[68:71], v[88:91], v[220:223]
v_mfma_f32_16x16x32_bf16 v[220:223], a[84:87], v[92:95], v[220:223]
v_mfma_f32_16x16x32_bf16 v[236:239], a[72:75], v[88:91], v[236:239]
ds_read_b64_tr_b16 v[84:85], v96 offset:29184
v_mfma_f32_16x16x32_bf16 v[236:239], a[88:91], v[92:95], v[236:239]
ds_read_b64_tr_b16 v[86:87], v96 offset:29440
v_mfma_f32_16x16x32_bf16 v[252:255], a[76:79], v[88:91], v[252:255]
global_store_dwordx4 v22, v[124:127], s[96:97] offset:3072 sc1
v_mfma_f32_16x16x32_bf16 v[252:255], a[92:95], v[92:95], v[252:255]
s_waitcnt lgkmcnt(8)
v_mfma_f32_16x16x32_bf16 v[192:195], a[96:99], v[64:67], v[192:195]
ds_read_b64_tr_b16 v[88:89], v97 offset:25088
v_mfma_f32_16x16x32_bf16 v[192:195], a[112:115], v[68:71], v[192:195]
ds_read_b64_tr_b16 v[90:91], v97 offset:25344
v_mfma_f32_16x16x32_bf16 v[208:211], a[100:103], v[64:67], v[208:211]
s_add_u32 s94, s94, 0x1000000
s_addc_u32 s95, s95, 0
v_mfma_f32_16x16x32_bf16 v[208:211], a[116:119], v[68:71], v[208:211]
s_add_u32 s96, s96, 0x1000000
s_addc_u32 s97, s97, 0
v_mfma_f32_16x16x32_bf16 v[224:227], a[104:107], v[64:67], v[224:227]
ds_read_b64_tr_b16 v[92:93], v97 offset:29184
v_mfma_f32_16x16x32_bf16 v[224:227], a[120:123], v[68:71], v[224:227]
ds_read_b64_tr_b16 v[94:95], v97 offset:29440
v_mfma_f32_16x16x32_bf16 v[240:243], a[108:111], v[64:67], v[240:243]
global_load_dwordx4 a[0:3], v112, s[44:45] offset:-4096 sc1
v_mfma_f32_16x16x32_bf16 v[240:243], a[124:127], v[68:71], v[240:243]
s_waitcnt lgkmcnt(8)
v_mfma_f32_16x16x32_bf16 v[196:199], a[96:99], v[72:75], v[196:199]
v_mfma_f32_16x16x32_bf16 v[196:199], a[112:115], v[76:79], v[196:199]
global_load_dwordx4 a[4:7], v114, s[44:45] offset:-4096 sc1
v_mfma_f32_16x16x32_bf16 v[212:215], a[100:103], v[72:75], v[212:215]
v_mfma_f32_16x16x32_bf16 v[212:215], a[116:119], v[76:79], v[212:215]
v_mfma_f32_16x16x32_bf16 v[228:231], a[104:107], v[72:75], v[228:231]
v_mfma_f32_16x16x32_bf16 v[228:231], a[120:123], v[76:79], v[228:231]
s_waitcnt vmcnt(19) lgkmcnt(0)
s_barrier
v_mfma_f32_16x16x32_bf16 v[244:247], a[108:111], v[72:75], v[244:247]
ds_read_b64_tr_b16 v[64:65], v96 offset:32768
v_mfma_f32_16x16x32_bf16 v[244:247], a[124:127], v[76:79], v[244:247]
ds_read_b64_tr_b16 v[66:67], v96 offset:33024
v_mfma_f32_16x16x32_bf16 v[200:203], a[96:99], v[80:83], v[200:203]
ds_read_b64_tr_b16 v[68:69], v96 offset:36864
v_mfma_f32_16x16x32_bf16 v[200:203], a[112:115], v[84:87], v[200:203]
ds_read_b64_tr_b16 v[70:71], v96 offset:37120
v_mfma_f32_16x16x32_bf16 v[216:219], a[100:103], v[80:83], v[216:219]
ds_read_b64_tr_b16 v[72:73], v97 offset:32768
v_mfma_f32_16x16x32_bf16 v[216:219], a[116:119], v[84:87], v[216:219]
ds_read_b64_tr_b16 v[74:75], v97 offset:33024
v_mfma_f32_16x16x32_bf16 v[232:235], a[104:107], v[80:83], v[232:235]
ds_read_b64_tr_b16 v[76:77], v97 offset:36864
v_mfma_f32_16x16x32_bf16 v[232:235], a[120:123], v[84:87], v[232:235]
ds_read_b64_tr_b16 v[78:79], v97 offset:37120
v_mfma_f32_16x16x32_bf16 v[248:251], a[108:111], v[80:83], v[248:251]
global_load_dwordx4 a[8:11], v112, s[46:47] offset:-4096 sc1
v_mfma_f32_16x16x32_bf16 v[248:251], a[124:127], v[84:87], v[248:251]
v_mfma_f32_16x16x32_bf16 v[204:207], a[96:99], v[88:91], v[204:207]
ds_read_b64_tr_b16 v[80:81], v96 offset:33280
v_mfma_f32_16x16x32_bf16 v[204:207], a[112:115], v[92:95], v[204:207]
ds_read_b64_tr_b16 v[82:83], v96 offset:33536
v_mfma_f32_16x16x32_bf16 v[220:223], a[100:103], v[88:91], v[220:223]
global_load_dwordx4 a[12:15], v114, s[46:47] offset:-4096 sc1
v_mfma_f32_16x16x32_bf16 v[220:223], a[116:119], v[92:95], v[220:223]
v_mfma_f32_16x16x32_bf16 v[236:239], a[104:107], v[88:91], v[236:239]
ds_read_b64_tr_b16 v[84:85], v96 offset:37376
v_mfma_f32_16x16x32_bf16 v[236:239], a[120:123], v[92:95], v[236:239]
ds_read_b64_tr_b16 v[86:87], v96 offset:37632
v_mfma_f32_16x16x32_bf16 v[252:255], a[108:111], v[88:91], v[252:255]
global_load_dwordx4 a[16:19], v112, s[44:45] offset:-3072 sc1
v_mfma_f32_16x16x32_bf16 v[252:255], a[124:127], v[92:95], v[252:255]
s_waitcnt lgkmcnt(8)
v_mfma_f32_16x16x32_bf16 v[128:131], a[128:131], v[64:67], v[128:131]
ds_read_b64_tr_b16 v[88:89], v97 offset:33280
v_mfma_f32_16x16x32_bf16 v[128:131], a[144:147], v[68:71], v[128:131]
ds_read_b64_tr_b16 v[90:91], v97 offset:33536
v_mfma_f32_16x16x32_bf16 v[144:147], a[132:135], v[64:67], v[144:147]
global_load_dwordx4 a[20:23], v114, s[44:45] offset:-3072 sc1
v_mfma_f32_16x16x32_bf16 v[144:147], a[148:151], v[68:71], v[144:147]
v_mfma_f32_16x16x32_bf16 v[160:163], a[136:139], v[64:67], v[160:163]
ds_read_b64_tr_b16 v[92:93], v97 offset:37376
v_mfma_f32_16x16x32_bf16 v[160:163], a[152:155], v[68:71], v[160:163]
ds_read_b64_tr_b16 v[94:95], v97 offset:37632
v_mfma_f32_16x16x32_bf16 v[176:179], a[140:143], v[64:67], v[176:179]
global_load_dwordx4 a[24:27], v112, s[46:47] offset:-3072 sc1
v_mfma_f32_16x16x32_bf16 v[176:179], a[156:159], v[68:71], v[176:179]
s_waitcnt lgkmcnt(8)
v_mfma_f32_16x16x32_bf16 v[132:135], a[128:131], v[72:75], v[132:135]
ds_read_b64_tr_b16 v[64:65], v96 offset:40960
v_mfma_f32_16x16x32_bf16 v[132:135], a[144:147], v[76:79], v[132:135]
ds_read_b64_tr_b16 v[66:67], v96 offset:41216
v_mfma_f32_16x16x32_bf16 v[148:151], a[132:135], v[72:75], v[148:151]
global_load_dwordx4 a[28:31], v114, s[46:47] offset:-3072 sc1
v_mfma_f32_16x16x32_bf16 v[148:151], a[148:151], v[76:79], v[148:151]
v_mfma_f32_16x16x32_bf16 v[164:167], a[136:139], v[72:75], v[164:167]
ds_read_b64_tr_b16 v[68:69], v96 offset:45056
v_mfma_f32_16x16x32_bf16 v[164:167], a[152:155], v[76:79], v[164:167]
ds_read_b64_tr_b16 v[70:71], v96 offset:45312
v_mfma_f32_16x16x32_bf16 v[180:183], a[140:143], v[72:75], v[180:183]
global_load_dwordx4 a[32:35], v112, s[44:45] offset:-2048 sc1
v_mfma_f32_16x16x32_bf16 v[180:183], a[156:159], v[76:79], v[180:183]
s_waitcnt lgkmcnt(8)
v_mfma_f32_16x16x32_bf16 v[136:139], a[128:131], v[80:83], v[136:139]
ds_read_b64_tr_b16 v[72:73], v97 offset:40960
v_mfma_f32_16x16x32_bf16 v[136:139], a[144:147], v[84:87], v[136:139]
ds_read_b64_tr_b16 v[74:75], v97 offset:41216
PAD3_%=:
s_cbranch_execz PAD11_%=
v_mfma_f32_16x16x32_bf16 v[152:155], a[132:135], v[80:83], v[152:155]
v_mfma_f32_16x16x32_bf16 v[152:155], a[148:151], v[84:87], v[152:155]
global_load_dwordx4 a[36:39], v114, s[44:45] offset:-2048 sc1
v_mfma_f32_16x16x32_bf16 v[168:171], a[136:139], v[80:83], v[168:171]
ds_read_b64_tr_b16 v[76:77], v97 offset:45056
v_mfma_f32_16x16x32_bf16 v[168:171], a[152:155], v[84:87], v[168:171]
ds_read_b64_tr_b16 v[78:79], v97 offset:45312
v_mfma_f32_16x16x32_bf16 v[184:187], a[140:143], v[80:83], v[184:187]
v_mfma_f32_16x16x32_bf16 v[184:187], a[156:159], v[84:87], v[184:187]
global_load_dwordx4 a[40:43], v112, s[46:47] offset:-2048 sc1
s_waitcnt lgkmcnt(8)
v_mfma_f32_16x16x32_bf16 v[140:143], a[128:131], v[88:91], v[140:143]
ds_read_b64_tr_b16 v[80:81], v96 offset:41472
v_mfma_f32_16x16x32_bf16 v[140:143], a[144:147], v[92:95], v[140:143]
ds_read_b64_tr_b16 v[82:83], v96 offset:41728
v_mfma_f32_16x16x32_bf16 v[156:159], a[132:135], v[88:91], v[156:159]
v_mfma_f32_16x16x32_bf16 v[156:159], a[148:151], v[92:95], v[156:159]
v_mfma_f32_16x16x32_bf16 v[172:175], a[136:139], v[88:91], v[172:175]
ds_read_b64_tr_b16 v[84:85], v96 offset:45568
v_mfma_f32_16x16x32_bf16 v[172:175], a[152:155], v[92:95], v[172:175]
ds_read_b64_tr_b16 v[86:87], v96 offset:45824
v_mfma_f32_16x16x32_bf16 v[188:191], a[140:143], v[88:91], v[188:191]
)"
R"(global_load_dwordx4 a[44:47], v114, s[46:47] offset:-2048 sc1
v_mfma_f32_16x16x32_bf16 v[188:191], a[156:159], v[92:95], v[188:191]
s_waitcnt lgkmcnt(8)
v_mfma_f32_16x16x32_bf16 v[128:131], a[160:163], v[64:67], v[128:131]
ds_read_b64_tr_b16 v[88:89], v97 offset:41472
v_mfma_f32_16x16x32_bf16 v[128:131], a[176:179], v[68:71], v[128:131]
ds_read_b64_tr_b16 v[90:91], v97 offset:41728
v_mfma_f32_16x16x32_bf16 v[144:147], a[164:167], v[64:67], v[144:147]
global_load_dwordx4 a[48:51], v112, s[44:45] offset:-1024 sc1
v_mfma_f32_16x16x32_bf16 v[144:147], a[180:183], v[68:71], v[144:147]
v_mfma_f32_16x16x32_bf16 v[160:163], a[168:171], v[64:67], v[160:163]
ds_read_b64_tr_b16 v[92:93], v97 offset:45568
v_mfma_f32_16x16x32_bf16 v[160:163], a[184:187], v[68:71], v[160:163]
ds_read_b64_tr_b16 v[94:95], v97 offset:45824
v_mfma_f32_16x16x32_bf16 v[176:179], a[172:175], v[64:67], v[176:179]
global_load_dwordx4 a[52:55], v114, s[44:45] offset:-1024 sc1
v_mfma_f32_16x16x32_bf16 v[176:179], a[188:191], v[68:71], v[176:179]
s_waitcnt lgkmcnt(8)
v_mfma_f32_16x16x32_bf16 v[132:135], a[160:163], v[72:75], v[132:135]
ds_read_b64_tr_b16 v[64:65], v96 offset:49152
v_mfma_f32_16x16x32_bf16 v[132:135], a[176:179], v[76:79], v[132:135]
ds_read_b64_tr_b16 v[66:67], v96 offset:49408
v_mfma_f32_16x16x32_bf16 v[148:151], a[164:167], v[72:75], v[148:151]
global_load_dwordx4 a[56:59], v112, s[46:47] offset:-1024 sc1
v_mfma_f32_16x16x32_bf16 v[148:151], a[180:183], v[76:79], v[148:151]
v_mfma_f32_16x16x32_bf16 v[164:167], a[168:171], v[72:75], v[164:167]
ds_read_b64_tr_b16 v[68:69], v96 offset:53248
v_mfma_f32_16x16x32_bf16 v[164:167], a[184:187], v[76:79], v[164:167]
ds_read_b64_tr_b16 v[70:71], v96 offset:53504
v_mfma_f32_16x16x32_bf16 v[180:183], a[172:175], v[72:75], v[180:183]
global_load_dwordx4 a[60:63], v114, s[46:47] offset:-1024 sc1
v_mfma_f32_16x16x32_bf16 v[180:183], a[188:191], v[76:79], v[180:183]
s_waitcnt lgkmcnt(8)
v_mfma_f32_16x16x32_bf16 v[136:139], a[160:163], v[80:83], v[136:139]
ds_read_b64_tr_b16 v[72:73], v97 offset:49152
v_mfma_f32_16x16x32_bf16 v[136:139], a[176:179], v[84:87], v[136:139]
ds_read_b64_tr_b16 v[74:75], v97 offset:49408
v_mfma_f32_16x16x32_bf16 v[152:155], a[164:167], v[80:83], v[152:155]
global_load_dwordx4 a[64:67], v112, s[44:45] offset:0 sc1
v_mfma_f32_16x16x32_bf16 v[152:155], a[180:183], v[84:87], v[152:155]
v_mfma_f32_16x16x32_bf16 v[168:171], a[168:171], v[80:83], v[168:171]
ds_read_b64_tr_b16 v[76:77], v97 offset:53248
v_mfma_f32_16x16x32_bf16 v[168:171], a[184:187], v[84:87], v[168:171]
ds_read_b64_tr_b16 v[78:79], v97 offset:53504
v_mfma_f32_16x16x32_bf16 v[184:187], a[172:175], v[80:83], v[184:187]
global_load_dwordx4 a[68:71], v114, s[44:45] offset:0 sc1
v_mfma_f32_16x16x32_bf16 v[184:187], a[188:191], v[84:87], v[184:187]
s_waitcnt lgkmcnt(8)
v_mfma_f32_16x16x32_bf16 v[140:143], a[160:163], v[88:91], v[140:143]
ds_read_b64_tr_b16 v[80:81], v96 offset:49664
v_mfma_f32_16x16x32_bf16 v[140:143], a[176:179], v[92:95], v[140:143]
ds_read_b64_tr_b16 v[82:83], v96 offset:49920
v_mfma_f32_16x16x32_bf16 v[156:159], a[164:167], v[88:91], v[156:159]
v_mfma_f32_16x16x32_bf16 v[156:159], a[180:183], v[92:95], v[156:159]
global_load_dwordx4 a[72:75], v112, s[46:47] offset:0 sc1
v_mfma_f32_16x16x32_bf16 v[172:175], a[168:171], v[88:91], v[172:175]
ds_read_b64_tr_b16 v[84:85], v96 offset:53760
v_mfma_f32_16x16x32_bf16 v[172:175], a[184:187], v[92:95], v[172:175]
ds_read_b64_tr_b16 v[86:87], v96 offset:54016
v_mfma_f32_16x16x32_bf16 v[188:191], a[172:175], v[88:91], v[188:191]
v_mfma_f32_16x16x32_bf16 v[188:191], a[188:191], v[92:95], v[188:191]
global_load_dwordx4 a[76:79], v114, s[46:47] offset:0 sc1
s_waitcnt lgkmcnt(8)
v_mfma_f32_16x16x32_bf16 v[128:131], a[192:195], v[64:67], v[128:131]
ds_read_b64_tr_b16 v[88:89], v97 offset:49664
v_mfma_f32_16x16x32_bf16 v[128:131], a[208:211], v[68:71], v[128:131]
ds_read_b64_tr_b16 v[90:91], v97 offset:49920
v_mfma_f32_16x16x32_bf16 v[144:147], a[196:199], v[64:67], v[144:147]
v_mfma_f32_16x16x32_bf16 v[144:147], a[212:215], v[68:71], v[144:147]
v_mfma_f32_16x16x32_bf16 v[160:163], a[200:203], v[64:67], v[160:163]
ds_read_b64_tr_b16 v[92:93], v97 offset:53760
v_mfma_f32_16x16x32_bf16 v[160:163], a[216:219], v[68:71], v[160:163]
ds_read_b64_tr_b16 v[94:95], v97 offset:54016
v_mfma_f32_16x16x32_bf16 v[176:179], a[204:207], v[64:67], v[176:179]
global_load_dwordx4 a[80:83], v112, s[44:45] offset:1024 sc1
v_mfma_f32_16x16x32_bf16 v[176:179], a[220:223], v[68:71], v[176:179]
s_waitcnt lgkmcnt(8)
v_mfma_f32_16x16x32_bf16 v[132:135], a[192:195], v[72:75], v[132:135]
ds_read_b64_tr_b16 v[64:65], v96 offset:57344
v_mfma_f32_16x16x32_bf16 v[132:135], a[208:211], v[76:79], v[132:135]
ds_read_b64_tr_b16 v[66:67], v96 offset:57600
v_mfma_f32_16x16x32_bf16 v[148:151], a[196:199], v[72:75], v[148:151]
global_load_dwordx4 a[84:87], v114, s[44:45] offset:1024 sc1
v_mfma_f32_16x16x32_bf16 v[148:151], a[212:215], v[76:79], v[148:151]
v_mfma_f32_16x16x32_bf16 v[164:167], a[200:203], v[72:75], v[164:167]
ds_read_b64_tr_b16 v[68:69], v96 offset:61440
v_mfma_f32_16x16x32_bf16 v[164:167], a[216:219], v[76:79], v[164:167]
ds_read_b64_tr_b16 v[70:71], v96 offset:61696
v_mfma_f32_16x16x32_bf16 v[180:183], a[204:207], v[72:75], v[180:183]
global_load_dwordx4 a[88:91], v112, s[46:47] offset:1024 sc1
v_mfma_f32_16x16x32_bf16 v[180:183], a[220:223], v[76:79], v[180:183]
s_waitcnt lgkmcnt(8)
v_mfma_f32_16x16x32_bf16 v[136:139], a[192:195], v[80:83], v[136:139]
ds_read_b64_tr_b16 v[72:73], v97 offset:57344
v_mfma_f32_16x16x32_bf16 v[136:139], a[208:211], v[84:87], v[136:139]
ds_read_b64_tr_b16 v[74:75], v97 offset:57600
s_mov_b64 s[80:81], s[16:17]
v_mfma_f32_16x16x32_bf16 v[152:155], a[196:199], v[80:83], v[152:155]
global_load_dwordx4 a[92:95], v114, s[46:47] offset:1024 sc1
v_mfma_f32_16x16x32_bf16 v[152:155], a[212:215], v[84:87], v[152:155]
s_add_u32 s80, s80, s99
s_addc_u32 s81, s81, 0
v_mfma_f32_16x16x32_bf16 v[168:171], a[200:203], v[80:83], v[168:171]
ds_read_b64_tr_b16 v[76:77], v97 offset:61440
s_and_b32 s81, s81, 0xffff
v_mfma_f32_16x16x32_bf16 v[168:171], a[216:219], v[84:87], v[168:171]
ds_read_b64_tr_b16 v[78:79], v97 offset:61696
s_sub_u32 s82, 1835008, s99
v_mfma_f32_16x16x32_bf16 v[184:187], a[204:207], v[80:83], v[184:187]
s_add_u32 m0, s75, 98304
s_xor_b32 s48, s67, 0
v_mfma_f32_16x16x32_bf16 v[184:187], a[220:223], v[84:87], v[184:187]
buffer_load_dwordx4 v108, s[80:83], s48 offen offset:0 lds
s_waitcnt lgkmcnt(8)
v_mfma_f32_16x16x32_bf16 v[140:143], a[192:195], v[88:91], v[140:143]
ds_read_b64_tr_b16 v[80:81], v96 offset:57856
v_mfma_f32_16x16x32_bf16 v[140:143], a[208:211], v[92:95], v[140:143]
ds_read_b64_tr_b16 v[82:83], v96 offset:58112
v_mfma_f32_16x16x32_bf16 v[156:159], a[196:199], v[88:91], v[156:159]
buffer_load_dwordx4 v109, s[80:83], s48 offen offset:1024 lds
v_mfma_f32_16x16x32_bf16 v[156:159], a[212:215], v[92:95], v[156:159]
v_mfma_f32_16x16x32_bf16 v[172:175], a[200:203], v[88:91], v[172:175]
ds_read_b64_tr_b16 v[84:85], v96 offset:61952
v_mfma_f32_16x16x32_bf16 v[172:175], a[216:219], v[92:95], v[172:175]
ds_read_b64_tr_b16 v[86:87], v96 offset:62208
v_mfma_f32_16x16x32_bf16 v[188:191], a[204:207], v[88:91], v[188:191]
buffer_load_dwordx4 v110, s[80:83], s48 offen offset:2048 lds
v_mfma_f32_16x16x32_bf16 v[188:191], a[220:223], v[92:95], v[188:191]
s_waitcnt lgkmcnt(8)
v_mfma_f32_16x16x32_bf16 v[128:131], a[224:227], v[64:67], v[128:131]
ds_read_b64_tr_b16 v[88:89], v97 offset:57856
v_mfma_f32_16x16x32_bf16 v[128:131], a[240:243], v[68:71], v[128:131]
ds_read_b64_tr_b16 v[90:91], v97 offset:58112
v_mfma_f32_16x16x32_bf16 v[144:147], a[228:231], v[64:67], v[144:147]
v_mfma_f32_16x16x32_bf16 v[144:147], a[244:247], v[68:71], v[144:147]
buffer_load_dwordx4 v111, s[80:83], s48 offen offset:3072 lds
s_mov_b64 s[40:41], s[18:19]
v_mfma_f32_16x16x32_bf16 v[160:163], a[232:235], v[64:67], v[160:163]
ds_read_b64_tr_b16 v[92:93], v97 offset:61952
v_mfma_f32_16x16x32_bf16 v[160:163], a[248:251], v[68:71], v[160:163]
ds_read_b64_tr_b16 v[94:95], v97 offset:62208
v_mfma_f32_16x16x32_bf16 v[176:179], a[236:239], v[64:67], v[176:179]
s_add_u32 s40, s40, s99
s_addc_u32 s41, s41, 0
v_mfma_f32_16x16x32_bf16 v[176:179], a[252:255], v[68:71], v[176:179]
s_and_b32 s41, s41, 0xffff
s_waitcnt lgkmcnt(8)
v_mfma_f32_16x16x32_bf16 v[132:135], a[224:227], v[72:75], v[132:135]
s_sub_u32 s42, 1835008, s99
s_add_u32 m0, s75, 102400
v_mfma_f32_16x16x32_bf16 v[132:135], a[240:243], v[76:79], v[132:135]
s_xor_b32 s48, s67, 0
v_mfma_f32_16x16x32_bf16 v[148:151], a[228:231], v[72:75], v[148:151]
buffer_load_dwordx4 v108, s[40:43], s48 offen offset:0 lds
v_mfma_f32_16x16x32_bf16 v[148:151], a[244:247], v[76:79], v[148:151]
v_mfma_f32_16x16x32_bf16 v[164:167], a[232:235], v[72:75], v[164:167]
buffer_load_dwordx4 v109, s[40:43], s48 offen offset:1024 lds
v_mfma_f32_16x16x32_bf16 v[164:167], a[248:251], v[76:79], v[164:167]
s_waitcnt vmcnt(35) lgkmcnt(0)
s_barrier
v_mfma_f32_16x16x32_bf16 v[180:183], a[236:239], v[72:75], v[180:183]
ds_read_b64_tr_b16 v[64:65], v98 offset:0
v_mfma_f32_16x16x32_bf16 v[180:183], a[252:255], v[76:79], v[180:183]
ds_read_b64_tr_b16 v[66:67], v98 offset:256
v_mfma_f32_16x16x32_bf16 v[136:139], a[224:227], v[80:83], v[136:139]
ds_read_b64_tr_b16 v[68:69], v98 offset:4096
s_and_b32 s12, s26, 0xfc
v_mfma_f32_16x16x32_bf16 v[136:139], a[240:243], v[84:87], v[136:139]
ds_read_b64_tr_b16 v[70:71], v98 offset:4352
s_lshl1_add_u32 s12, s12, %2
v_mfma_f32_16x16x32_bf16 v[152:155], a[228:231], v[80:83], v[152:155]
ds_read_b64_tr_b16 v[72:73], v99 offset:0
s_lshl_b32 s12, s12, 14
v_mfma_f32_16x16x32_bf16 v[152:155], a[244:247], v[84:87], v[152:155]
ds_read_b64_tr_b16 v[74:75], v99 offset:256
s_and_b32 s13, s26, 3
v_mfma_f32_16x16x32_bf16 v[168:171], a[232:235], v[80:83], v[168:171]
ds_read_b64_tr_b16 v[76:77], v99 offset:4096
s_lshl2_add_u32 s13, s13, s31
v_mfma_f32_16x16x32_bf16 v[168:171], a[248:251], v[84:87], v[168:171]
ds_read_b64_tr_b16 v[78:79], v99 offset:4352
s_lshl_b32 s13, s13, 6
v_mfma_f32_16x16x32_bf16 v[184:187], a[236:239], v[80:83], v[184:187]
buffer_load_dwordx4 v110, s[40:43], s48 offen offset:2048 lds
s_add_u32 s12, s12, s13
v_mfma_f32_16x16x32_bf16 v[184:187], a[252:255], v[84:87], v[184:187]
v_mfma_f32_16x16x32_bf16 v[140:143], a[224:227], v[88:91], v[140:143]
ds_read_b64_tr_b16 v[80:81], v98 offset:512
v_mfma_f32_16x16x32_bf16 v[140:143], a[240:243], v[92:95], v[140:143]
ds_read_b64_tr_b16 v[82:83], v98 offset:768
v_mfma_f32_16x16x32_bf16 v[156:159], a[228:231], v[88:91], v[156:159]
buffer_load_dwordx4 v111, s[40:43], s48 offen offset:3072 lds
v_mfma_f32_16x16x32_bf16 v[156:159], a[244:247], v[92:95], v[156:159]
s_add_u32 m0, s75, 0
s_xor_b32 s48, s67, 0
v_mfma_f32_16x16x32_bf16 v[172:175], a[232:235], v[88:91], v[172:175]
ds_read_b64_tr_b16 v[84:85], v98 offset:4608
s_add_u32 s48, s48, 458752
v_mfma_f32_16x16x32_bf16 v[172:175], a[248:251], v[92:95], v[172:175]
ds_read_b64_tr_b16 v[86:87], v98 offset:4864
v_mfma_f32_16x16x32_bf16 v[188:191], a[236:239], v[88:91], v[188:191]
buffer_load_dwordx4 v108, s[80:83], s48 offen offset:0 lds
v_mfma_f32_16x16x32_bf16 v[188:191], a[252:255], v[92:95], v[188:191]
s_waitcnt lgkmcnt(8)
v_mfma_f32_16x16x32_bf16 v[192:195], a[128:131], v[64:67], v[192:195]
ds_read_b64_tr_b16 v[88:89], v99 offset:512
v_mfma_f32_16x16x32_bf16 v[192:195], a[144:147], v[68:71], v[192:195]
ds_read_b64_tr_b16 v[90:91], v99 offset:768
v_mfma_f32_16x16x32_bf16 v[208:211], a[132:135], v[64:67], v[208:211]
buffer_load_dwordx4 v109, s[80:83], s48 offen offset:1024 lds
v_mfma_f32_16x16x32_bf16 v[208:211], a[148:151], v[68:71], v[208:211]
s_add_u32 s4, s4, s12
s_addc_u32 s5, s5, 0
v_mfma_f32_16x16x32_bf16 v[224:227], a[136:139], v[64:67], v[224:227]
ds_read_b64_tr_b16 v[92:93], v99 offset:4608
v_cvt_pk_bf16_f32 v32, v128, v129
v_mfma_f32_16x16x32_bf16 v[224:227], a[152:155], v[68:71], v[224:227]
ds_read_b64_tr_b16 v[94:95], v99 offset:4864
v_cvt_pk_bf16_f32 v33, v130, v131
v_mfma_f32_16x16x32_bf16 v[240:243], a[140:143], v[64:67], v[240:243]
buffer_load_dwordx4 v110, s[80:83], s48 offen offset:2048 lds
v_cvt_pk_bf16_f32 v34, v144, v145
v_mfma_f32_16x16x32_bf16 v[240:243], a[156:159], v[68:71], v[240:243]
v_cvt_pk_bf16_f32 v35, v146, v147
s_waitcnt lgkmcnt(8)
v_mfma_f32_16x16x32_bf16 v[196:199], a[128:131], v[72:75], v[196:199]
ds_read_b64_tr_b16 v[64:65], v98 offset:8192
v_mfma_f32_16x16x32_bf16 v[196:199], a[144:147], v[76:79], v[196:199]
ds_read_b64_tr_b16 v[66:67], v98 offset:8448
v_mfma_f32_16x16x32_bf16 v[212:215], a[132:135], v[72:75], v[212:215]
buffer_load_dwordx4 v111, s[80:83], s48 offen offset:3072 lds
v_mfma_f32_16x16x32_bf16 v[212:215], a[148:151], v[76:79], v[212:215]
s_add_u32 m0, s75, 4096
s_xor_b32 s48, s67, 0
v_mfma_f32_16x16x32_bf16 v[228:231], a[136:139], v[72:75], v[228:231]
ds_read_b64_tr_b16 v[68:69], v98 offset:12288
s_add_u32 s48, s48, 458752
v_mfma_f32_16x16x32_bf16 v[228:231], a[152:155], v[76:79], v[228:231]
ds_read_b64_tr_b16 v[70:71], v98 offset:12544
v_cvt_pk_bf16_f32 v36, v160, v161
v_mfma_f32_16x16x32_bf16 v[244:247], a[140:143], v[72:75], v[244:247]
buffer_load_dwordx4 v108, s[40:43], s48 offen offset:0 lds
v_mfma_f32_16x16x32_bf16 v[244:247], a[156:159], v[76:79], v[244:247]
v_cvt_pk_bf16_f32 v37, v162, v163
s_waitcnt lgkmcnt(8)
v_mfma_f32_16x16x32_bf16 v[200:203], a[128:131], v[80:83], v[200:203]
ds_read_b64_tr_b16 v[72:73], v99 offset:8192
v_mfma_f32_16x16x32_bf16 v[200:203], a[144:147], v[84:87], v[200:203]
ds_read_b64_tr_b16 v[74:75], v99 offset:8448
v_mfma_f32_16x16x32_bf16 v[216:219], a[132:135], v[80:83], v[216:219]
s_add_u32 s24, s24, s12
s_addc_u32 s25, s25, 0
v_mfma_f32_16x16x32_bf16 v[216:219], a[148:151], v[84:87], v[216:219]
buffer_load_dwordx4 v109, s[40:43], s48 offen offset:1024 lds
v_cvt_pk_bf16_f32 v38, v176, v177
v_mfma_f32_16x16x32_bf16 v[232:235], a[136:139], v[80:83], v[232:235]
ds_read_b64_tr_b16 v[76:77], v99 offset:12288
v_mfma_f32_16x16x32_bf16 v[232:235], a[152:155], v[84:87], v[232:235]
ds_read_b64_tr_b16 v[78:79], v99 offset:12544
v_mfma_f32_16x16x32_bf16 v[248:251], a[140:143], v[80:83], v[248:251]
s_add_u32 s6, s4, 0x10000
s_addc_u32 s7, s5, 0
v_mfma_f32_16x16x32_bf16 v[248:251], a[156:159], v[84:87], v[248:251]
buffer_load_dwordx4 v110, s[40:43], s48 offen offset:2048 lds
s_waitcnt lgkmcnt(8)
v_mfma_f32_16x16x32_bf16 v[204:207], a[128:131], v[88:91], v[204:207]
ds_read_b64_tr_b16 v[80:81], v98 offset:8704
v_cvt_pk_bf16_f32 v39, v178, v179
v_mfma_f32_16x16x32_bf16 v[204:207], a[144:147], v[92:95], v[204:207]
ds_read_b64_tr_b16 v[82:83], v98 offset:8960
v_mfma_f32_16x16x32_bf16 v[220:223], a[132:135], v[88:91], v[220:223]
s_add_u32 s8, s24, 0x10000
s_addc_u32 s9, s25, 0
v_mfma_f32_16x16x32_bf16 v[220:223], a[148:151], v[92:95], v[220:223]
v_cvt_pk_bf16_f32 v40, v132, v133
s_and_b32 s101, s26, 3
v_mfma_f32_16x16x32_bf16 v[236:239], a[136:139], v[88:91], v[236:239]
ds_read_b64_tr_b16 v[84:85], v98 offset:12800
s_lshl_b32 s101, s101, 8
v_mfma_f32_16x16x32_bf16 v[236:239], a[152:155], v[92:95], v[236:239]
ds_read_b64_tr_b16 v[86:87], v98 offset:13056
v_mfma_f32_16x16x32_bf16 v[252:255], a[140:143], v[88:91], v[252:255]
buffer_load_dwordx4 v111, s[40:43], s48 offen offset:3072 lds
v_cvt_pk_bf16_f32 v41, v134, v135
v_mfma_f32_16x16x32_bf16 v[252:255], a[156:159], v[92:95], v[252:255]
s_waitcnt lgkmcnt(8)
v_mfma_f32_16x16x32_bf16 v[192:195], a[160:163], v[64:67], v[192:195]
ds_read_b64_tr_b16 v[88:89], v99 offset:8704
v_mfma_f32_16x16x32_bf16 v[192:195], a[176:179], v[68:71], v[192:195]
ds_read_b64_tr_b16 v[90:91], v99 offset:8960
v_mfma_f32_16x16x32_bf16 v[208:211], a[164:167], v[64:67], v[208:211]
global_load_dwordx4 a[96:99], v112, s[44:45] offset:2048 sc1
v_cvt_pk_bf16_f32 v42, v148, v149
v_mfma_f32_16x16x32_bf16 v[208:211], a[180:183], v[68:71], v[208:211]
s_add_u32 s20, s20, s101
s_addc_u32 s21, s21, 0
v_mfma_f32_16x16x32_bf16 v[224:227], a[168:171], v[64:67], v[224:227]
ds_read_b64_tr_b16 v[92:93], v99 offset:12800
s_and_b32 s21, s21, 0xffff
v_mfma_f32_16x16x32_bf16 v[224:227], a[184:187], v[68:71], v[224:227]
ds_read_b64_tr_b16 v[94:95], v99 offset:13056
s_sub_u32 s22, 0x400000, s101
v_mfma_f32_16x16x32_bf16 v[240:243], a[172:175], v[64:67], v[240:243]
global_store_dwordx4 v117, v[32:35], s[94:95] offset:-4096 sc1
v_cvt_pk_bf16_f32 v43, v150, v151
v_mfma_f32_16x16x32_bf16 v[240:243], a[188:191], v[68:71], v[240:243]
s_mov_b32 s23, 0x20000
s_waitcnt lgkmcnt(8)
v_mfma_f32_16x16x32_bf16 v[196:199], a[160:163], v[72:75], v[196:199]
ds_read_b64_tr_b16 v[64:65], v98 offset:16384
s_and_b32 s101, s26, 0xfc
v_mfma_f32_16x16x32_bf16 v[196:199], a[176:179], v[76:79], v[196:199]
ds_read_b64_tr_b16 v[66:67], v98 offset:16640
s_add_u32 s101, s101, %2
v_mfma_f32_16x16x32_bf16 v[212:215], a[164:167], v[72:75], v[212:215]
v_cvt_pk_bf16_f32 v44, v164, v165
s_mul_i32 s101, s101, 376832
v_mfma_f32_16x16x32_bf16 v[212:215], a[180:183], v[76:79], v[212:215]
global_load_dwordx4 a[100:103], v114, s[44:45] offset:2048 sc1
s_mov_b64 s[12:13], s[10:11]
v_mfma_f32_16x16x32_bf16 v[228:231], a[168:171], v[72:75], v[228:231]
ds_read_b64_tr_b16 v[68:69], v98 offset:20480
v_mfma_f32_16x16x32_bf16 v[228:231], a[184:187], v[76:79], v[228:231]
ds_read_b64_tr_b16 v[70:71], v98 offset:20736
v_mfma_f32_16x16x32_bf16 v[244:247], a[172:175], v[72:75], v[244:247]
v_cvt_pk_bf16_f32 v45, v166, v167
v_mfma_f32_16x16x32_bf16 v[244:247], a[188:191], v[76:79], v[244:247]
s_waitcnt lgkmcnt(8)
v_mfma_f32_16x16x32_bf16 v[200:203], a[160:163], v[80:83], v[200:203]
ds_read_b64_tr_b16 v[72:73], v99 offset:16384
v_mfma_f32_16x16x32_bf16 v[200:203], a[176:179], v[84:87], v[200:203]
ds_read_b64_tr_b16 v[74:75], v99 offset:16640
v_mfma_f32_16x16x32_bf16 v[216:219], a[164:167], v[80:83], v[216:219]
global_store_dwordx4 v117, v[36:39], s[96:97] offset:-4096 sc1
v_cvt_pk_bf16_f32 v46, v180, v181
v_mfma_f32_16x16x32_bf16 v[216:219], a[180:183], v[84:87], v[216:219]
s_add_u32 s12, s12, 0x341c000
s_addc_u32 s13, s13, 0
v_mfma_f32_16x16x32_bf16 v[232:235], a[168:171], v[80:83], v[232:235]
ds_read_b64_tr_b16 v[76:77], v99 offset:20480
v_mfma_f32_16x16x32_bf16 v[232:235], a[184:187], v[84:87], v[232:235]
ds_read_b64_tr_b16 v[78:79], v99 offset:20736
v_cvt_pk_bf16_f32 v47, v182, v183
v_mfma_f32_16x16x32_bf16 v[248:251], a[172:175], v[80:83], v[248:251]
s_add_u32 s12, s12, s101
s_addc_u32 s13, s13, 0
v_mfma_f32_16x16x32_bf16 v[248:251], a[188:191], v[84:87], v[248:251]
global_load_dwordx4 a[104:107], v112, s[46:47] offset:2048 sc1
s_waitcnt lgkmcnt(8)
v_mfma_f32_16x16x32_bf16 v[204:207], a[160:163], v[88:91], v[204:207]
ds_read_b64_tr_b16 v[80:81], v98 offset:16896
s_and_b32 s13, s13, 0xffff
v_mfma_f32_16x16x32_bf16 v[204:207], a[176:179], v[92:95], v[204:207]
ds_read_b64_tr_b16 v[82:83], v98 offset:17152
v_cvt_pk_bf16_f32 v48, v136, v137
v_mfma_f32_16x16x32_bf16 v[220:223], a[164:167], v[88:91], v[220:223]
s_mov_b32 s14, 262144
s_mov_b32 s15, 0x20000
v_mfma_f32_16x16x32_bf16 v[220:223], a[180:183], v[92:95], v[220:223]
global_load_dwordx4 a[108:111], v114, s[46:47] offset:2048 sc1
s_mul_i32 s101, s31, 10
v_mfma_f32_16x16x32_bf16 v[236:239], a[168:171], v[88:91], v[236:239]
ds_read_b64_tr_b16 v[84:85], v98 offset:20992
s_add_u32 s100, s101, 10
v_mfma_f32_16x16x32_bf16 v[236:239], a[184:187], v[92:95], v[236:239]
ds_read_b64_tr_b16 v[86:87], v98 offset:21248
v_cvt_pk_bf16_f32 v49, v138, v139
v_mfma_f32_16x16x32_bf16 v[252:255], a[172:175], v[88:91], v[252:255]
s_cmp_eq_u32 s31, 2
s_cselect_b32 s100, 32, s100
v_mfma_f32_16x16x32_bf16 v[252:255], a[188:191], v[92:95], v[252:255]
global_store_dwordx4 v117, v[40:43], s[94:95] offset:-3072 sc1
s_waitcnt lgkmcnt(8)
v_mfma_f32_16x16x32_bf16 v[192:195], a[192:195], v[64:67], v[192:195]
ds_read_b64_tr_b16 v[88:89], v99 offset:16896
s_sub_u32 s59, s100, s101
v_mfma_f32_16x16x32_bf16 v[192:195], a[208:211], v[68:71], v[192:195]
ds_read_b64_tr_b16 v[90:91], v99 offset:17152
v_cvt_pk_bf16_f32 v50, v152, v153
v_mfma_f32_16x16x32_bf16 v[208:211], a[196:199], v[64:67], v[208:211]
s_bfm_b32 s99, s59, s101
s_lshr_b32 s59, s26, 5
v_mfma_f32_16x16x32_bf16 v[208:211], a[212:215], v[68:71], v[208:211]
s_lshl_b32 s89, 0x55555555, s59
s_and_b32 s89, s89, s99
v_mfma_f32_16x16x32_bf16 v[224:227], a[200:203], v[64:67], v[224:227]
ds_read_b64_tr_b16 v[92:93], v99 offset:20992
s_ff1_i32_b32 s101, s89
v_mfma_f32_16x16x32_bf16 v[224:227], a[216:219], v[68:71], v[224:227]
ds_read_b64_tr_b16 v[94:95], v99 offset:21248
v_cvt_pk_bf16_f32 v51, v154, v155
v_mfma_f32_16x16x32_bf16 v[240:243], a[204:207], v[64:67], v[240:243]
global_load_dwordx4 a[112:115], v112, s[44:45] offset:3072 sc1
s_lshr_b32 s59, s26, 2
v_mfma_f32_16x16x32_bf16 v[240:243], a[220:223], v[68:71], v[240:243]
s_lshl4_add_u32 s59, s31, s59
s_waitcnt lgkmcnt(8)
v_mfma_f32_16x16x32_bf16 v[196:199], a[192:195], v[72:75], v[196:199]
ds_read_b64_tr_b16 v[64:65], v98 offset:24576
s_lshl_b32 s59, s59, 7
)"
R"(v_mfma_f32_16x16x32_bf16 v[196:199], a[208:211], v[76:79], v[196:199]
ds_read_b64_tr_b16 v[66:67], v98 offset:24832
v_cvt_pk_bf16_f32 v52, v168, v169
v_mfma_f32_16x16x32_bf16 v[212:215], a[196:199], v[72:75], v[212:215]
global_load_dwordx4 a[116:119], v114, s[44:45] offset:3072 sc1
s_add_u32 s59, s59, 49152
v_mfma_f32_16x16x32_bf16 v[212:215], a[212:215], v[76:79], v[212:215]
v_mfma_f32_16x16x32_bf16 v[228:231], a[200:203], v[72:75], v[228:231]
ds_read_b64_tr_b16 v[68:69], v98 offset:28672
v_mfma_f32_16x16x32_bf16 v[228:231], a[216:219], v[76:79], v[228:231]
ds_read_b64_tr_b16 v[70:71], v98 offset:28928
v_cvt_pk_bf16_f32 v53, v170, v171
v_mfma_f32_16x16x32_bf16 v[244:247], a[204:207], v[72:75], v[244:247]
v_mfma_f32_16x16x32_bf16 v[244:247], a[220:223], v[76:79], v[244:247]
global_load_dwordx4 a[120:123], v112, s[46:47] offset:3072 sc1
s_waitcnt lgkmcnt(8)
v_mfma_f32_16x16x32_bf16 v[200:203], a[192:195], v[80:83], v[200:203]
ds_read_b64_tr_b16 v[72:73], v99 offset:24576
v_cvt_pk_bf16_f32 v54, v184, v185
v_mfma_f32_16x16x32_bf16 v[200:203], a[208:211], v[84:87], v[200:203]
ds_read_b64_tr_b16 v[74:75], v99 offset:24832
v_mfma_f32_16x16x32_bf16 v[216:219], a[196:199], v[80:83], v[216:219]
v_mfma_f32_16x16x32_bf16 v[216:219], a[212:215], v[84:87], v[216:219]
global_load_dwordx4 a[124:127], v114, s[46:47] offset:3072 sc1
v_mfma_f32_16x16x32_bf16 v[232:235], a[200:203], v[80:83], v[232:235]
ds_read_b64_tr_b16 v[76:77], v99 offset:28672
v_cvt_pk_bf16_f32 v55, v186, v187
v_mfma_f32_16x16x32_bf16 v[232:235], a[216:219], v[84:87], v[232:235]
ds_read_b64_tr_b16 v[78:79], v99 offset:28928
v_mfma_f32_16x16x32_bf16 v[248:251], a[204:207], v[80:83], v[248:251]
v_mfma_f32_16x16x32_bf16 v[248:251], a[220:223], v[84:87], v[248:251]
s_waitcnt lgkmcnt(8)
v_mfma_f32_16x16x32_bf16 v[204:207], a[192:195], v[88:91], v[204:207]
ds_read_b64_tr_b16 v[80:81], v98 offset:25088
v_cvt_pk_bf16_f32 v56, v140, v141
v_mfma_f32_16x16x32_bf16 v[204:207], a[208:211], v[92:95], v[204:207]
ds_read_b64_tr_b16 v[82:83], v98 offset:25344
v_mfma_f32_16x16x32_bf16 v[220:223], a[196:199], v[88:91], v[220:223]
global_store_dwordx4 v117, v[44:47], s[96:97] offset:-3072 sc1
v_mfma_f32_16x16x32_bf16 v[220:223], a[212:215], v[92:95], v[220:223]
v_mfma_f32_16x16x32_bf16 v[236:239], a[200:203], v[88:91], v[236:239]
ds_read_b64_tr_b16 v[84:85], v98 offset:29184
v_cvt_pk_bf16_f32 v57, v142, v143
v_mfma_f32_16x16x32_bf16 v[236:239], a[216:219], v[92:95], v[236:239]
ds_read_b64_tr_b16 v[86:87], v98 offset:29440
v_mfma_f32_16x16x32_bf16 v[252:255], a[204:207], v[88:91], v[252:255]
v_mfma_f32_16x16x32_bf16 v[252:255], a[220:223], v[92:95], v[252:255]
global_store_dwordx4 v117, v[48:51], s[94:95] offset:-2048 sc1
s_waitcnt lgkmcnt(8)
v_mfma_f32_16x16x32_bf16 v[192:195], a[224:227], v[64:67], v[192:195]
ds_read_b64_tr_b16 v[88:89], v99 offset:25088
v_cvt_pk_bf16_f32 v58, v156, v157
v_mfma_f32_16x16x32_bf16 v[192:195], a[240:243], v[68:71], v[192:195]
ds_read_b64_tr_b16 v[90:91], v99 offset:25344
v_mfma_f32_16x16x32_bf16 v[208:211], a[228:231], v[64:67], v[208:211]
s_add_u32 m0, s75, 32768
s_xor_b32 s48, s65, 2097152
v_mfma_f32_16x16x32_bf16 v[208:211], a[244:247], v[68:71], v[208:211]
v_mfma_f32_16x16x32_bf16 v[224:227], a[232:235], v[64:67], v[224:227]
ds_read_b64_tr_b16 v[92:93], v99 offset:29184
v_cvt_pk_bf16_f32 v59, v158, v159
v_mfma_f32_16x16x32_bf16 v[224:227], a[248:251], v[68:71], v[224:227]
ds_read_b64_tr_b16 v[94:95], v99 offset:29440
v_mfma_f32_16x16x32_bf16 v[240:243], a[236:239], v[64:67], v[240:243]
buffer_load_dwordx4 v104, s[84:87], s48 offen offset:0 lds
v_mfma_f32_16x16x32_bf16 v[240:243], a[252:255], v[68:71], v[240:243]
v_cvt_pk_bf16_f32 v60, v172, v173
s_waitcnt lgkmcnt(8)
v_mfma_f32_16x16x32_bf16 v[196:199], a[224:227], v[72:75], v[196:199]
v_mfma_f32_16x16x32_bf16 v[196:199], a[240:243], v[76:79], v[196:199]
v_mfma_f32_16x16x32_bf16 v[212:215], a[228:231], v[72:75], v[212:215]
buffer_load_dwordx4 v105, s[84:87], s48 offen offset:1024 lds
v_mfma_f32_16x16x32_bf16 v[212:215], a[244:247], v[76:79], v[212:215]
v_cvt_pk_bf16_f32 v61, v174, v175
v_mfma_f32_16x16x32_bf16 v[228:231], a[232:235], v[72:75], v[228:231]
v_mfma_f32_16x16x32_bf16 v[228:231], a[248:251], v[76:79], v[228:231]
s_waitcnt lgkmcnt(0)
s_barrier
v_mfma_f32_16x16x32_bf16 v[244:247], a[236:239], v[72:75], v[244:247]
ds_read_b64_tr_b16 v[64:65], v26 offset:0
v_mfma_f32_16x16x32_bf16 v[244:247], a[252:255], v[76:79], v[244:247]
ds_read_b64_tr_b16 v[66:67], v26 offset:256
v_mfma_f32_16x16x32_bf16 v[200:203], a[224:227], v[80:83], v[200:203]
ds_read_b64_tr_b16 v[68:69], v26 offset:4096
v_cvt_pk_bf16_f32 v62, v188, v189
v_mfma_f32_16x16x32_bf16 v[200:203], a[240:243], v[84:87], v[200:203]
ds_read_b64_tr_b16 v[70:71], v26 offset:4352
v_mfma_f32_16x16x32_bf16 v[216:219], a[228:231], v[80:83], v[216:219]
ds_read_b64_tr_b16 v[72:73], v27 offset:0
v_mfma_f32_16x16x32_bf16 v[216:219], a[244:247], v[84:87], v[216:219]
ds_read_b64_tr_b16 v[74:75], v27 offset:256
v_cvt_pk_bf16_f32 v63, v190, v191
v_mfma_f32_16x16x32_bf16 v[232:235], a[232:235], v[80:83], v[232:235]
ds_read_b64_tr_b16 v[76:77], v27 offset:4096
v_mfma_f32_16x16x32_bf16 v[232:235], a[248:251], v[84:87], v[232:235]
ds_read_b64_tr_b16 v[78:79], v27 offset:4352
v_mfma_f32_16x16x32_bf16 v[248:251], a[236:239], v[80:83], v[248:251]
global_store_dwordx4 v117, v[52:55], s[96:97] offset:-2048 sc1
v_mfma_f32_16x16x32_bf16 v[248:251], a[252:255], v[84:87], v[248:251]
v_mfma_f32_16x16x32_bf16 v[204:207], a[224:227], v[88:91], v[204:207]
ds_read_b64_tr_b16 v[80:81], v26 offset:512
v_mfma_f32_16x16x32_bf16 v[204:207], a[240:243], v[92:95], v[204:207]
ds_read_b64_tr_b16 v[82:83], v26 offset:768
v_mfma_f32_16x16x32_bf16 v[220:223], a[228:231], v[88:91], v[220:223]
v_mfma_f32_16x16x32_bf16 v[220:223], a[244:247], v[92:95], v[220:223]
buffer_load_dwordx4 v106, s[84:87], s48 offen offset:2048 lds
v_mfma_f32_16x16x32_bf16 v[236:239], a[232:235], v[88:91], v[236:239]
ds_read_b64_tr_b16 v[84:85], v26 offset:4608
v_mfma_f32_16x16x32_bf16 v[236:239], a[248:251], v[92:95], v[236:239]
ds_read_b64_tr_b16 v[86:87], v26 offset:4864
v_mfma_f32_16x16x32_bf16 v[252:255], a[236:239], v[88:91], v[252:255]
v_mfma_f32_16x16x32_bf16 v[252:255], a[252:255], v[92:95], v[252:255]
global_store_dwordx4 v117, v[56:59], s[94:95] offset:-1024 sc1
s_waitcnt vmcnt(50) lgkmcnt(8)
v_mfma_f32_16x16x32_bf16 v[128:131], v[64:67], a[0:3], 0
ds_read_b64_tr_b16 v[88:89], v27 offset:512
v_mfma_f32_16x16x32_bf16 v[128:131], v[68:71], a[16:19], v[128:131]
ds_read_b64_tr_b16 v[90:91], v27 offset:768
v_mfma_f32_16x16x32_bf16 v[144:147], v[64:67], a[4:7], 0
v_mfma_f32_16x16x32_bf16 v[144:147], v[68:71], a[20:23], v[144:147]
v_mfma_f32_16x16x32_bf16 v[160:163], v[64:67], a[8:11], 0
ds_read_b64_tr_b16 v[92:93], v27 offset:4608
v_cvt_pk_bf16_f32 v2, v192, v193
v_mfma_f32_16x16x32_bf16 v[160:163], v[68:71], a[24:27], v[160:163]
ds_read_b64_tr_b16 v[94:95], v27 offset:4864
v_cvt_pk_bf16_f32 v3, v194, v195
PAD4_%=:
s_cbranch_execz PAD12_%=
v_mfma_f32_16x16x32_bf16 v[176:179], v[64:67], a[12:15], 0
buffer_load_dwordx4 v107, s[84:87], s48 offen offset:3072 lds
v_cvt_pk_bf16_f32 v4, v208, v209
v_mfma_f32_16x16x32_bf16 v[176:179], v[68:71], a[28:31], v[176:179]
s_add_u32 m0, s75, 36864
s_waitcnt lgkmcnt(8)
v_mfma_f32_16x16x32_bf16 v[132:135], v[72:75], a[0:3], 0
ds_read_b64_tr_b16 v[64:65], v26 offset:8192
s_xor_b32 s48, s65, 2359296
v_mfma_f32_16x16x32_bf16 v[132:135], v[76:79], a[16:19], v[132:135]
ds_read_b64_tr_b16 v[66:67], v26 offset:8448
v_cvt_pk_bf16_f32 v5, v210, v211
v_mfma_f32_16x16x32_bf16 v[148:151], v[72:75], a[4:7], 0
buffer_load_dwordx4 v104, s[84:87], s48 offen offset:0 lds
v_mfma_f32_16x16x32_bf16 v[148:151], v[76:79], a[20:23], v[148:151]
v_cvt_pk_bf16_f32 v6, v224, v225
v_mfma_f32_16x16x32_bf16 v[164:167], v[72:75], a[8:11], 0
ds_read_b64_tr_b16 v[68:69], v26 offset:12288
v_mfma_f32_16x16x32_bf16 v[164:167], v[76:79], a[24:27], v[164:167]
ds_read_b64_tr_b16 v[70:71], v26 offset:12544
v_mfma_f32_16x16x32_bf16 v[180:183], v[72:75], a[12:15], 0
buffer_load_dwordx4 v105, s[84:87], s48 offen offset:1024 lds
v_mfma_f32_16x16x32_bf16 v[180:183], v[76:79], a[28:31], v[180:183]
v_cvt_pk_bf16_f32 v7, v226, v227
s_waitcnt lgkmcnt(8)
v_mfma_f32_16x16x32_bf16 v[136:139], v[80:83], a[0:3], 0
ds_read_b64_tr_b16 v[72:73], v27 offset:8192
v_mfma_f32_16x16x32_bf16 v[136:139], v[84:87], a[16:19], v[136:139]
ds_read_b64_tr_b16 v[74:75], v27 offset:8448
v_mfma_f32_16x16x32_bf16 v[152:155], v[80:83], a[4:7], 0
v_mfma_f32_16x16x32_bf16 v[152:155], v[84:87], a[20:23], v[152:155]
buffer_load_dwordx4 v106, s[84:87], s48 offen offset:2048 lds
v_cvt_pk_bf16_f32 v8, v240, v241
v_mfma_f32_16x16x32_bf16 v[168:171], v[80:83], a[8:11], 0
ds_read_b64_tr_b16 v[76:77], v27 offset:12288
v_mfma_f32_16x16x32_bf16 v[168:171], v[84:87], a[24:27], v[168:171]
ds_read_b64_tr_b16 v[78:79], v27 offset:12544
v_mfma_f32_16x16x32_bf16 v[184:187], v[80:83], a[12:15], 0
v_mfma_f32_16x16x32_bf16 v[184:187], v[84:87], a[28:31], v[184:187]
buffer_load_dwordx4 v107, s[84:87], s48 offen offset:3072 lds
s_waitcnt lgkmcnt(8)
v_mfma_f32_16x16x32_bf16 v[140:143], v[88:91], a[0:3], 0
ds_read_b64_tr_b16 v[80:81], v26 offset:8704
v_cvt_pk_bf16_f32 v9, v242, v243
v_mfma_f32_16x16x32_bf16 v[140:143], v[92:95], a[16:19], v[140:143]
ds_read_b64_tr_b16 v[82:83], v26 offset:8960
v_mfma_f32_16x16x32_bf16 v[156:159], v[88:91], a[4:7], 0
v_mfma_f32_16x16x32_bf16 v[156:159], v[92:95], a[20:23], v[156:159]
v_cvt_pk_bf16_f32 v10, v196, v197
v_mfma_f32_16x16x32_bf16 v[172:175], v[88:91], a[8:11], 0
ds_read_b64_tr_b16 v[84:85], v26 offset:12800
v_mfma_f32_16x16x32_bf16 v[172:175], v[92:95], a[24:27], v[172:175]
ds_read_b64_tr_b16 v[86:87], v26 offset:13056
v_mfma_f32_16x16x32_bf16 v[188:191], v[88:91], a[12:15], 0
global_store_dwordx4 v117, v[60:63], s[96:97] offset:-1024 sc1
s_nop 0
v_mfma_f32_16x16x32_bf16 v[188:191], v[92:95], a[28:31], v[188:191]
s_nop 0
s_waitcnt vmcnt(48) lgkmcnt(8)
v_mfma_f32_16x16x32_bf16 v[128:131], v[64:67], a[32:35], v[128:131]
ds_read_b64_tr_b16 v[88:89], v27 offset:8704
v_xor_b32 v117, 0x1000, v117
v_mfma_f32_16x16x32_bf16 v[128:131], v[68:71], a[48:51], v[128:131]
ds_read_b64_tr_b16 v[90:91], v27 offset:8960
v_cvt_pk_bf16_f32 v11, v198, v199
v_mfma_f32_16x16x32_bf16 v[144:147], v[64:67], a[36:39], v[144:147]
v_cvt_pk_bf16_f32 v12, v212, v213
v_mfma_f32_16x16x32_bf16 v[144:147], v[68:71], a[52:55], v[144:147]
global_load_dwordx4 a[128:131], v113, s[44:45] offset:-4096 sc1
v_mfma_f32_16x16x32_bf16 v[160:163], v[64:67], a[40:43], v[160:163]
ds_read_b64_tr_b16 v[92:93], v27 offset:12800
v_mfma_f32_16x16x32_bf16 v[160:163], v[68:71], a[56:59], v[160:163]
ds_read_b64_tr_b16 v[94:95], v27 offset:13056
v_mfma_f32_16x16x32_bf16 v[176:179], v[64:67], a[44:47], v[176:179]
v_cvt_pk_bf16_f32 v13, v214, v215
v_mfma_f32_16x16x32_bf16 v[176:179], v[68:71], a[60:63], v[176:179]
global_load_dwordx4 a[132:135], v115, s[44:45] offset:-4096 sc1
s_waitcnt lgkmcnt(8)
v_mfma_f32_16x16x32_bf16 v[132:135], v[72:75], a[32:35], v[132:135]
ds_read_b64_tr_b16 v[64:65], v26 offset:16384
v_mfma_f32_16x16x32_bf16 v[132:135], v[76:79], a[48:51], v[132:135]
ds_read_b64_tr_b16 v[66:67], v26 offset:16640
v_mfma_f32_16x16x32_bf16 v[148:151], v[72:75], a[36:39], v[148:151]
v_cvt_pk_bf16_f32 v14, v228, v229
v_mfma_f32_16x16x32_bf16 v[148:151], v[76:79], a[52:55], v[148:151]
global_load_dwordx4 a[136:139], v113, s[46:47] offset:-4096 sc1
v_mfma_f32_16x16x32_bf16 v[164:167], v[72:75], a[40:43], v[164:167]
ds_read_b64_tr_b16 v[68:69], v26 offset:20480
v_mfma_f32_16x16x32_bf16 v[164:167], v[76:79], a[56:59], v[164:167]
ds_read_b64_tr_b16 v[70:71], v26 offset:20736
v_mfma_f32_16x16x32_bf16 v[180:183], v[72:75], a[44:47], v[180:183]
v_cvt_pk_bf16_f32 v15, v230, v231
v_mfma_f32_16x16x32_bf16 v[180:183], v[76:79], a[60:63], v[180:183]
s_waitcnt lgkmcnt(8)
v_mfma_f32_16x16x32_bf16 v[136:139], v[80:83], a[32:35], v[136:139]
ds_read_b64_tr_b16 v[72:73], v27 offset:16384
v_mfma_f32_16x16x32_bf16 v[136:139], v[84:87], a[48:51], v[136:139]
ds_read_b64_tr_b16 v[74:75], v27 offset:16640
v_mfma_f32_16x16x32_bf16 v[152:155], v[80:83], a[36:39], v[152:155]
global_load_dwordx4 a[140:143], v115, s[46:47] offset:-4096 sc1
v_cvt_pk_bf16_f32 v16, v244, v245
v_mfma_f32_16x16x32_bf16 v[152:155], v[84:87], a[52:55], v[152:155]
v_mfma_f32_16x16x32_bf16 v[168:171], v[80:83], a[40:43], v[168:171]
ds_read_b64_tr_b16 v[76:77], v27 offset:20480
v_mfma_f32_16x16x32_bf16 v[168:171], v[84:87], a[56:59], v[168:171]
ds_read_b64_tr_b16 v[78:79], v27 offset:20736
v_cvt_pk_bf16_f32 v17, v246, v247
v_mfma_f32_16x16x32_bf16 v[184:187], v[80:83], a[44:47], v[184:187]
global_load_dwordx4 a[144:147], v113, s[44:45] offset:-3072 sc1
v_mfma_f32_16x16x32_bf16 v[184:187], v[84:87], a[60:63], v[184:187]
s_waitcnt lgkmcnt(8)
v_mfma_f32_16x16x32_bf16 v[140:143], v[88:91], a[32:35], v[140:143]
ds_read_b64_tr_b16 v[80:81], v26 offset:16896
v_mfma_f32_16x16x32_bf16 v[140:143], v[92:95], a[48:51], v[140:143]
ds_read_b64_tr_b16 v[82:83], v26 offset:17152
v_cvt_pk_bf16_f32 v18, v200, v201
v_mfma_f32_16x16x32_bf16 v[156:159], v[88:91], a[36:39], v[156:159]
global_load_dwordx4 a[148:151], v115, s[44:45] offset:-3072 sc1
v_mfma_f32_16x16x32_bf16 v[156:159], v[92:95], a[52:55], v[156:159]
v_mfma_f32_16x16x32_bf16 v[172:175], v[88:91], a[40:43], v[172:175]
ds_read_b64_tr_b16 v[84:85], v26 offset:20992
v_mfma_f32_16x16x32_bf16 v[172:175], v[92:95], a[56:59], v[172:175]
ds_read_b64_tr_b16 v[86:87], v26 offset:21248
v_cvt_pk_bf16_f32 v19, v202, v203
v_mfma_f32_16x16x32_bf16 v[188:191], v[88:91], a[44:47], v[188:191]
global_load_dwordx4 a[152:155], v113, s[46:47] offset:-3072 sc1
v_mfma_f32_16x16x32_bf16 v[188:191], v[92:95], a[60:63], v[188:191]
s_waitcnt vmcnt(47) lgkmcnt(8)
v_mfma_f32_16x16x32_bf16 v[128:131], v[64:67], a[64:67], v[128:131]
ds_read_b64_tr_b16 v[88:89], v27 offset:16896
v_mfma_f32_16x16x32_bf16 v[128:131], v[68:71], a[80:83], v[128:131]
ds_read_b64_tr_b16 v[90:91], v27 offset:17152
v_cvt_pk_bf16_f32 v20, v216, v217
v_mfma_f32_16x16x32_bf16 v[144:147], v[64:67], a[68:71], v[144:147]
global_load_dwordx4 a[156:159], v115, s[46:47] offset:-3072 sc1
v_mfma_f32_16x16x32_bf16 v[144:147], v[68:71], a[84:87], v[144:147]
v_mfma_f32_16x16x32_bf16 v[160:163], v[64:67], a[72:75], v[160:163]
ds_read_b64_tr_b16 v[92:93], v27 offset:20992
v_mfma_f32_16x16x32_bf16 v[160:163], v[68:71], a[88:91], v[160:163]
ds_read_b64_tr_b16 v[94:95], v27 offset:21248
v_cvt_pk_bf16_f32 v21, v218, v219
v_mfma_f32_16x16x32_bf16 v[176:179], v[64:67], a[76:79], v[176:179]
global_store_dwordx4 v117, v[2:5], s[94:95] offset:-4096 sc1
v_mfma_f32_16x16x32_bf16 v[176:179], v[68:71], a[92:95], v[176:179]
s_waitcnt lgkmcnt(8)
v_mfma_f32_16x16x32_bf16 v[132:135], v[72:75], a[64:67], v[132:135]
ds_read_b64_tr_b16 v[64:65], v26 offset:24576
v_bfe_u32 v5, v22, 6, 2
v_mfma_f32_16x16x32_bf16 v[132:135], v[76:79], a[80:83], v[132:135]
ds_read_b64_tr_b16 v[66:67], v26 offset:24832
v_cvt_pk_bf16_f32 v28, v232, v233
v_mfma_f32_16x16x32_bf16 v[148:151], v[72:75], a[68:71], v[148:151]
v_mfma_f32_16x16x32_bf16 v[148:151], v[76:79], a[84:87], v[148:151]
v_mfma_f32_16x16x32_bf16 v[164:167], v[72:75], a[72:75], v[164:167]
ds_read_b64_tr_b16 v[68:69], v26 offset:28672
v_mfma_f32_16x16x32_bf16 v[164:167], v[76:79], a[88:91], v[164:167]
ds_read_b64_tr_b16 v[70:71], v26 offset:28928
v_cvt_pk_bf16_f32 v29, v234, v235
v_mfma_f32_16x16x32_bf16 v[180:183], v[72:75], a[76:79], v[180:183]
global_store_dwordx4 v117, v[6:9], s[96:97] offset:-4096 sc1
v_mfma_f32_16x16x32_bf16 v[180:183], v[76:79], a[92:95], v[180:183]
s_waitcnt lgkmcnt(8)
v_mfma_f32_16x16x32_bf16 v[136:139], v[80:83], a[64:67], v[136:139]
ds_read_b64_tr_b16 v[72:73], v27 offset:24576
v_cvt_pk_bf16_f32 v30, v248, v249
v_mfma_f32_16x16x32_bf16 v[136:139], v[84:87], a[80:83], v[136:139]
ds_read_b64_tr_b16 v[74:75], v27 offset:24832
v_lshrrev_b32 v6, 8, v22
v_mfma_f32_16x16x32_bf16 v[152:155], v[80:83], a[68:71], v[152:155]
v_lshrrev_b32 v9, 1, v6
v_lshl_add_u32 v9, v9, 6, v5
v_mfma_f32_16x16x32_bf16 v[152:155], v[84:87], a[84:87], v[152:155]
global_load_dwordx4 a[160:163], v113, s[44:45] offset:-2048 sc1
v_lshlrev_b32 v9, 6, v9
v_mfma_f32_16x16x32_bf16 v[168:171], v[80:83], a[72:75], v[168:171]
ds_read_b64_tr_b16 v[76:77], v27 offset:28672
v_cvt_pk_bf16_f32 v31, v250, v251
v_mfma_f32_16x16x32_bf16 v[168:171], v[84:87], a[88:91], v[168:171]
ds_read_b64_tr_b16 v[78:79], v27 offset:28928
v_and_b32 v7, 32, v22
v_mfma_f32_16x16x32_bf16 v[184:187], v[80:83], a[76:79], v[184:187]
v_bfe_u32 v8, v22, 4, 1
v_lshl_add_u32 v7, v8, 3, v7
v_mfma_f32_16x16x32_bf16 v[184:187], v[84:87], a[92:95], v[184:187]
v_and_b32 v8, 1, v6
s_waitcnt lgkmcnt(8)
v_mfma_f32_16x16x32_bf16 v[140:143], v[88:91], a[64:67], v[140:143]
ds_read_b64_tr_b16 v[80:81], v26 offset:25088
v_cvt_pk_bf16_f32 v120, v204, v205
v_mfma_f32_16x16x32_bf16 v[140:143], v[92:95], a[80:83], v[140:143]
ds_read_b64_tr_b16 v[82:83], v26 offset:25344
v_mul_u32_u24 v8, 1040, v8
v_mfma_f32_16x16x32_bf16 v[156:159], v[88:91], a[68:71], v[156:159]
global_load_dwordx4 a[164:167], v115, s[44:45] offset:-2048 sc1
v_add3_u32 v9, v9, v7, v8
v_mfma_f32_16x16x32_bf16 v[156:159], v[92:95], a[84:87], v[156:159]
v_lshl_add_u32 v9, v9, 2, %2
v_lshl_add_u32 v9, v9, 5, v5
v_mfma_f32_16x16x32_bf16 v[172:175], v[88:91], a[72:75], v[172:175]
ds_read_b64_tr_b16 v[84:85], v26 offset:29184
v_cvt_pk_bf16_f32 v121, v206, v207
v_mfma_f32_16x16x32_bf16 v[172:175], v[92:95], a[88:91], v[172:175]
ds_read_b64_tr_b16 v[86:87], v26 offset:29440
v_xor_b32 v6, s31, v6
v_mfma_f32_16x16x32_bf16 v[188:191], v[88:91], a[76:79], v[188:191]
global_load_dwordx4 a[168:171], v113, s[46:47] offset:-2048 sc1
v_lshl_add_u32 v9, v9, 4, v6
v_mfma_f32_16x16x32_bf16 v[188:191], v[92:95], a[92:95], v[188:191]
v_and_b32 v7, 48, v22
s_waitcnt vmcnt(26) lgkmcnt(8)
v_mfma_f32_16x16x32_bf16 v[128:131], v[64:67], a[96:99], v[128:131]
ds_read_b64_tr_b16 v[88:89], v27 offset:25088
v_cvt_pk_bf16_f32 v122, v220, v221
v_mfma_f32_16x16x32_bf16 v[128:131], v[68:71], a[112:115], v[128:131]
ds_read_b64_tr_b16 v[90:91], v27 offset:25344
v_mfma_f32_16x16x32_bf16 v[144:147], v[64:67], a[100:103], v[144:147]
global_load_dwordx4 a[172:175], v115, s[46:47] offset:-2048 sc1
v_mfma_f32_16x16x32_bf16 v[144:147], v[68:71], a[116:119], v[144:147]
v_mfma_f32_16x16x32_bf16 v[160:163], v[64:67], a[104:107], v[160:163]
ds_read_b64_tr_b16 v[92:93], v27 offset:29184
v_cvt_pk_bf16_f32 v123, v222, v223
v_mfma_f32_16x16x32_bf16 v[160:163], v[68:71], a[120:123], v[160:163]
ds_read_b64_tr_b16 v[94:95], v27 offset:29440
v_mfma_f32_16x16x32_bf16 v[176:179], v[64:67], a[108:111], v[176:179]
global_load_dwordx4 a[176:179], v113, s[44:45] offset:-1024 sc1
v_mfma_f32_16x16x32_bf16 v[176:179], v[68:71], a[124:127], v[176:179]
v_cvt_pk_bf16_f32 v124, v236, v237
s_waitcnt lgkmcnt(8)
v_mfma_f32_16x16x32_bf16 v[132:135], v[72:75], a[96:99], v[132:135]
v_mfma_f32_16x16x32_bf16 v[132:135], v[76:79], a[112:115], v[132:135]
v_mfma_f32_16x16x32_bf16 v[148:151], v[72:75], a[100:103], v[148:151]
global_load_dwordx4 a[180:183], v115, s[44:45] offset:-1024 sc1
v_mfma_f32_16x16x32_bf16 v[148:151], v[76:79], a[116:119], v[148:151]
v_cvt_pk_bf16_f32 v125, v238, v239
v_mfma_f32_16x16x32_bf16 v[164:167], v[72:75], a[104:107], v[164:167]
v_mfma_f32_16x16x32_bf16 v[164:167], v[76:79], a[120:123], v[164:167]
s_waitcnt lgkmcnt(0)
s_barrier
v_mfma_f32_16x16x32_bf16 v[180:183], v[72:75], a[108:111], v[180:183]
ds_read_b64_tr_b16 v[64:65], v98 offset:32768
v_mfma_f32_16x16x32_bf16 v[180:183], v[76:79], a[124:127], v[180:183]
ds_read_b64_tr_b16 v[66:67], v98 offset:33024
v_mfma_f32_16x16x32_bf16 v[136:139], v[80:83], a[96:99], v[136:139]
ds_read_b64_tr_b16 v[68:69], v98 offset:40960
v_cvt_pk_bf16_f32 v126, v252, v253
v_mfma_f32_16x16x32_bf16 v[136:139], v[84:87], a[112:115], v[136:139]
ds_read_b64_tr_b16 v[70:71], v98 offset:41216
v_mfma_f32_16x16x32_bf16 v[152:155], v[80:83], a[100:103], v[152:155]
ds_read_b64_tr_b16 v[72:73], v99 offset:32768
v_mfma_f32_16x16x32_bf16 v[152:155], v[84:87], a[116:119], v[152:155]
ds_read_b64_tr_b16 v[74:75], v99 offset:33024
v_cvt_pk_bf16_f32 v127, v254, v255
v_mfma_f32_16x16x32_bf16 v[168:171], v[80:83], a[104:107], v[168:171]
ds_read_b64_tr_b16 v[76:77], v99 offset:40960
v_mfma_f32_16x16x32_bf16 v[168:171], v[84:87], a[120:123], v[168:171]
ds_read_b64_tr_b16 v[78:79], v99 offset:41216
v_mfma_f32_16x16x32_bf16 v[184:187], v[80:83], a[108:111], v[184:187]
global_load_dwordx4 a[184:187], v113, s[46:47] offset:-1024 sc1
v_mfma_f32_16x16x32_bf16 v[184:187], v[84:87], a[124:127], v[184:187]
v_mfma_f32_16x16x32_bf16 v[140:143], v[88:91], a[96:99], v[140:143]
ds_read_b64_tr_b16 v[80:81], v98 offset:36864
v_mfma_f32_16x16x32_bf16 v[140:143], v[92:95], a[112:115], v[140:143]
ds_read_b64_tr_b16 v[82:83], v98 offset:37120
v_mfma_f32_16x16x32_bf16 v[156:159], v[88:91], a[100:103], v[156:159]
global_load_dwordx4 a[188:191], v115, s[46:47] offset:-1024 sc1
v_mfma_f32_16x16x32_bf16 v[156:159], v[92:95], a[116:119], v[156:159]
v_mfma_f32_16x16x32_bf16 v[172:175], v[88:91], a[104:107], v[172:175]
ds_read_b64_tr_b16 v[84:85], v98 offset:45056
v_mfma_f32_16x16x32_bf16 v[172:175], v[92:95], a[120:123], v[172:175]
ds_read_b64_tr_b16 v[86:87], v98 offset:45312
v_mfma_f32_16x16x32_bf16 v[188:191], v[88:91], a[108:111], v[188:191]
global_store_dwordx4 v117, v[10:13], s[94:95] offset:-3072 sc1
)"
R"(v_mfma_f32_16x16x32_bf16 v[188:191], v[92:95], a[124:127], v[188:191]
s_waitcnt lgkmcnt(8)
v_mfma_f32_16x16x32_bf16 v[192:195], v[64:67], a[0:3], 0
ds_read_b64_tr_b16 v[88:89], v99 offset:36864
v_mfma_f32_16x16x32_bf16 v[192:195], v[68:71], a[16:19], v[192:195]
ds_read_b64_tr_b16 v[90:91], v99 offset:37120
v_mfma_f32_16x16x32_bf16 v[208:211], v[64:67], a[4:7], 0
v_mfma_f32_16x16x32_bf16 v[208:211], v[68:71], a[20:23], v[208:211]
global_load_dwordx4 a[192:195], v113, s[44:45] offset:0 sc1
v_mfma_f32_16x16x32_bf16 v[224:227], v[64:67], a[8:11], 0
ds_read_b64_tr_b16 v[92:93], v99 offset:45056
v_mfma_f32_16x16x32_bf16 v[224:227], v[68:71], a[24:27], v[224:227]
ds_read_b64_tr_b16 v[94:95], v99 offset:45312
v_mfma_f32_16x16x32_bf16 v[240:243], v[64:67], a[12:15], 0
v_mfma_f32_16x16x32_bf16 v[240:243], v[68:71], a[28:31], v[240:243]
s_waitcnt lgkmcnt(8)
v_mfma_f32_16x16x32_bf16 v[196:199], v[72:75], a[0:3], 0
ds_read_b64_tr_b16 v[64:65], v98 offset:49152
v_mfma_f32_16x16x32_bf16 v[196:199], v[76:79], a[16:19], v[196:199]
ds_read_b64_tr_b16 v[66:67], v98 offset:49408
v_mfma_f32_16x16x32_bf16 v[212:215], v[72:75], a[4:7], 0
global_load_dwordx4 a[196:199], v115, s[44:45] offset:0 sc1
v_mfma_f32_16x16x32_bf16 v[212:215], v[76:79], a[20:23], v[212:215]
v_mfma_f32_16x16x32_bf16 v[228:231], v[72:75], a[8:11], 0
ds_read_b64_tr_b16 v[68:69], v98 offset:57344
v_mfma_f32_16x16x32_bf16 v[228:231], v[76:79], a[24:27], v[228:231]
ds_read_b64_tr_b16 v[70:71], v98 offset:57600
v_mfma_f32_16x16x32_bf16 v[244:247], v[72:75], a[12:15], 0
global_load_dwordx4 a[200:203], v113, s[46:47] offset:0 sc1
v_mfma_f32_16x16x32_bf16 v[244:247], v[76:79], a[28:31], v[244:247]
s_waitcnt lgkmcnt(8)
v_mfma_f32_16x16x32_bf16 v[200:203], v[80:83], a[0:3], 0
ds_read_b64_tr_b16 v[72:73], v99 offset:49152
v_mfma_f32_16x16x32_bf16 v[200:203], v[84:87], a[16:19], v[200:203]
ds_read_b64_tr_b16 v[74:75], v99 offset:49408
v_mfma_f32_16x16x32_bf16 v[216:219], v[80:83], a[4:7], 0
global_load_dwordx4 a[204:207], v115, s[46:47] offset:0 sc1
v_mfma_f32_16x16x32_bf16 v[216:219], v[84:87], a[20:23], v[216:219]
v_mfma_f32_16x16x32_bf16 v[232:235], v[80:83], a[8:11], 0
ds_read_b64_tr_b16 v[76:77], v99 offset:57344
v_mfma_f32_16x16x32_bf16 v[232:235], v[84:87], a[24:27], v[232:235]
ds_read_b64_tr_b16 v[78:79], v99 offset:57600
v_mfma_f32_16x16x32_bf16 v[248:251], v[80:83], a[12:15], 0
global_load_dwordx4 a[208:211], v113, s[44:45] offset:1024 sc1
v_mfma_f32_16x16x32_bf16 v[248:251], v[84:87], a[28:31], v[248:251]
s_waitcnt lgkmcnt(8)
v_mfma_f32_16x16x32_bf16 v[204:207], v[88:91], a[0:3], 0
ds_read_b64_tr_b16 v[80:81], v98 offset:53248
v_mfma_f32_16x16x32_bf16 v[204:207], v[92:95], a[16:19], v[204:207]
ds_read_b64_tr_b16 v[82:83], v98 offset:53504
v_mfma_f32_16x16x32_bf16 v[220:223], v[88:91], a[4:7], 0
global_load_dwordx4 a[212:215], v115, s[44:45] offset:1024 sc1
v_mfma_f32_16x16x32_bf16 v[220:223], v[92:95], a[20:23], v[220:223]
v_mfma_f32_16x16x32_bf16 v[236:239], v[88:91], a[8:11], 0
ds_read_b64_tr_b16 v[84:85], v98 offset:61440
v_mfma_f32_16x16x32_bf16 v[236:239], v[92:95], a[24:27], v[236:239]
ds_read_b64_tr_b16 v[86:87], v98 offset:61696
v_mfma_f32_16x16x32_bf16 v[252:255], v[88:91], a[12:15], 0
global_load_dwordx4 a[216:219], v113, s[46:47] offset:1024 sc1
v_mfma_f32_16x16x32_bf16 v[252:255], v[92:95], a[28:31], v[252:255]
s_waitcnt lgkmcnt(8)
v_mfma_f32_16x16x32_bf16 v[192:195], v[64:67], a[32:35], v[192:195]
ds_read_b64_tr_b16 v[88:89], v99 offset:53248
v_mfma_f32_16x16x32_bf16 v[192:195], v[68:71], a[48:51], v[192:195]
ds_read_b64_tr_b16 v[90:91], v99 offset:53504
v_mfma_f32_16x16x32_bf16 v[208:211], v[64:67], a[36:39], v[208:211]
global_load_dwordx4 a[220:223], v115, s[46:47] offset:1024 sc1
v_mfma_f32_16x16x32_bf16 v[208:211], v[68:71], a[52:55], v[208:211]
v_mfma_f32_16x16x32_bf16 v[224:227], v[64:67], a[40:43], v[224:227]
ds_read_b64_tr_b16 v[92:93], v99 offset:61440
s_add_u32 m0, s75, 65536
v_mfma_f32_16x16x32_bf16 v[224:227], v[68:71], a[56:59], v[224:227]
ds_read_b64_tr_b16 v[94:95], v99 offset:61696
s_xor_b32 s48, s67, 917504
v_mfma_f32_16x16x32_bf16 v[240:243], v[64:67], a[44:47], v[240:243]
v_mfma_f32_16x16x32_bf16 v[240:243], v[68:71], a[60:63], v[240:243]
buffer_load_dwordx4 v108, s[80:83], s48 offen offset:0 lds
s_waitcnt lgkmcnt(8)
v_mfma_f32_16x16x32_bf16 v[196:199], v[72:75], a[32:35], v[196:199]
ds_read_b64_tr_b16 v[64:65], v96 offset:0
v_mfma_f32_16x16x32_bf16 v[196:199], v[76:79], a[48:51], v[196:199]
ds_read_b64_tr_b16 v[66:67], v96 offset:256
v_mfma_f32_16x16x32_bf16 v[212:215], v[72:75], a[36:39], v[212:215]
v_mfma_f32_16x16x32_bf16 v[212:215], v[76:79], a[52:55], v[212:215]
buffer_load_dwordx4 v109, s[80:83], s48 offen offset:1024 lds
v_mfma_f32_16x16x32_bf16 v[228:231], v[72:75], a[40:43], v[228:231]
ds_read_b64_tr_b16 v[68:69], v96 offset:8192
v_mfma_f32_16x16x32_bf16 v[228:231], v[76:79], a[56:59], v[228:231]
ds_read_b64_tr_b16 v[70:71], v96 offset:8448
v_mfma_f32_16x16x32_bf16 v[244:247], v[72:75], a[44:47], v[244:247]
v_mfma_f32_16x16x32_bf16 v[244:247], v[76:79], a[60:63], v[244:247]
s_waitcnt lgkmcnt(8)
v_mfma_f32_16x16x32_bf16 v[200:203], v[80:83], a[32:35], v[200:203]
ds_read_b64_tr_b16 v[72:73], v97 offset:0
v_mfma_f32_16x16x32_bf16 v[200:203], v[84:87], a[48:51], v[200:203]
ds_read_b64_tr_b16 v[74:75], v97 offset:256
v_mfma_f32_16x16x32_bf16 v[216:219], v[80:83], a[36:39], v[216:219]
buffer_load_dwordx4 v110, s[80:83], s48 offen offset:2048 lds
v_mfma_f32_16x16x32_bf16 v[216:219], v[84:87], a[52:55], v[216:219]
v_mfma_f32_16x16x32_bf16 v[232:235], v[80:83], a[40:43], v[232:235]
ds_read_b64_tr_b16 v[76:77], v97 offset:8192
v_mfma_f32_16x16x32_bf16 v[232:235], v[84:87], a[56:59], v[232:235]
ds_read_b64_tr_b16 v[78:79], v97 offset:8448
v_mfma_f32_16x16x32_bf16 v[248:251], v[80:83], a[44:47], v[248:251]
buffer_load_dwordx4 v111, s[80:83], s48 offen offset:3072 lds
s_add_u32 m0, s75, 69632
v_mfma_f32_16x16x32_bf16 v[248:251], v[84:87], a[60:63], v[248:251]
s_xor_b32 s48, s67, 917504
s_waitcnt lgkmcnt(8)
v_mfma_f32_16x16x32_bf16 v[204:207], v[88:91], a[32:35], v[204:207]
ds_read_b64_tr_b16 v[80:81], v96 offset:4096
v_mfma_f32_16x16x32_bf16 v[204:207], v[92:95], a[48:51], v[204:207]
ds_read_b64_tr_b16 v[82:83], v96 offset:4352
v_mfma_f32_16x16x32_bf16 v[220:223], v[88:91], a[36:39], v[220:223]
buffer_load_dwordx4 v108, s[40:43], s48 offen offset:0 lds
v_mfma_f32_16x16x32_bf16 v[220:223], v[92:95], a[52:55], v[220:223]
v_mfma_f32_16x16x32_bf16 v[236:239], v[88:91], a[40:43], v[236:239]
ds_read_b64_tr_b16 v[84:85], v96 offset:12288
v_mfma_f32_16x16x32_bf16 v[236:239], v[92:95], a[56:59], v[236:239]
ds_read_b64_tr_b16 v[86:87], v96 offset:12544
v_mfma_f32_16x16x32_bf16 v[252:255], v[88:91], a[44:47], v[252:255]
buffer_load_dwordx4 v109, s[40:43], s48 offen offset:1024 lds
v_mfma_f32_16x16x32_bf16 v[252:255], v[92:95], a[60:63], v[252:255]
s_waitcnt lgkmcnt(8)
v_mfma_f32_16x16x32_bf16 v[192:195], v[64:67], a[64:67], v[192:195]
ds_read_b64_tr_b16 v[88:89], v97 offset:4096
v_mfma_f32_16x16x32_bf16 v[192:195], v[68:71], a[80:83], v[192:195]
ds_read_b64_tr_b16 v[90:91], v97 offset:4352
v_mfma_f32_16x16x32_bf16 v[208:211], v[64:67], a[68:71], v[208:211]
buffer_load_dwordx4 v110, s[40:43], s48 offen offset:2048 lds
v_mfma_f32_16x16x32_bf16 v[208:211], v[68:71], a[84:87], v[208:211]
v_mfma_f32_16x16x32_bf16 v[224:227], v[64:67], a[72:75], v[224:227]
ds_read_b64_tr_b16 v[92:93], v97 offset:12288
v_mfma_f32_16x16x32_bf16 v[224:227], v[68:71], a[88:91], v[224:227]
ds_read_b64_tr_b16 v[94:95], v97 offset:12544
v_mfma_f32_16x16x32_bf16 v[240:243], v[64:67], a[76:79], v[240:243]
buffer_load_dwordx4 v111, s[40:43], s48 offen offset:3072 lds
v_mfma_f32_16x16x32_bf16 v[240:243], v[68:71], a[92:95], v[240:243]
s_add_u32 m0, s75, 131072
s_waitcnt lgkmcnt(8)
v_mfma_f32_16x16x32_bf16 v[196:199], v[72:75], a[64:67], v[196:199]
ds_read_b64_tr_b16 v[64:65], v96 offset:16384
s_xor_b32 s48, s67, 917504
v_mfma_f32_16x16x32_bf16 v[196:199], v[76:79], a[80:83], v[196:199]
ds_read_b64_tr_b16 v[66:67], v96 offset:16640
s_add_u32 s48, s48, 458752
v_mfma_f32_16x16x32_bf16 v[212:215], v[72:75], a[68:71], v[212:215]
buffer_load_dwordx4 v108, s[80:83], s48 offen offset:0 lds
v_mfma_f32_16x16x32_bf16 v[212:215], v[76:79], a[84:87], v[212:215]
v_mfma_f32_16x16x32_bf16 v[228:231], v[72:75], a[72:75], v[228:231]
ds_read_b64_tr_b16 v[68:69], v96 offset:24576
v_mfma_f32_16x16x32_bf16 v[228:231], v[76:79], a[88:91], v[228:231]
ds_read_b64_tr_b16 v[70:71], v96 offset:24832
v_mfma_f32_16x16x32_bf16 v[244:247], v[72:75], a[76:79], v[244:247]
v_mfma_f32_16x16x32_bf16 v[244:247], v[76:79], a[92:95], v[244:247]
buffer_load_dwordx4 v109, s[80:83], s48 offen offset:1024 lds
s_waitcnt lgkmcnt(8)
v_mfma_f32_16x16x32_bf16 v[200:203], v[80:83], a[64:67], v[200:203]
ds_read_b64_tr_b16 v[72:73], v97 offset:16384
v_mfma_f32_16x16x32_bf16 v[200:203], v[84:87], a[80:83], v[200:203]
ds_read_b64_tr_b16 v[74:75], v97 offset:16640
v_mfma_f32_16x16x32_bf16 v[216:219], v[80:83], a[68:71], v[216:219]
v_mfma_f32_16x16x32_bf16 v[216:219], v[84:87], a[84:87], v[216:219]
buffer_load_dwordx4 v110, s[80:83], s48 offen offset:2048 lds
v_mfma_f32_16x16x32_bf16 v[232:235], v[80:83], a[72:75], v[232:235]
ds_read_b64_tr_b16 v[76:77], v97 offset:24576
v_mfma_f32_16x16x32_bf16 v[232:235], v[84:87], a[88:91], v[232:235]
ds_read_b64_tr_b16 v[78:79], v97 offset:24832
v_mfma_f32_16x16x32_bf16 v[248:251], v[80:83], a[76:79], v[248:251]
v_mfma_f32_16x16x32_bf16 v[248:251], v[84:87], a[92:95], v[248:251]
s_waitcnt lgkmcnt(8)
v_mfma_f32_16x16x32_bf16 v[204:207], v[88:91], a[64:67], v[204:207]
ds_read_b64_tr_b16 v[80:81], v96 offset:20480
v_mfma_f32_16x16x32_bf16 v[204:207], v[92:95], a[80:83], v[204:207]
ds_read_b64_tr_b16 v[82:83], v96 offset:20736
v_mfma_f32_16x16x32_bf16 v[220:223], v[88:91], a[68:71], v[220:223]
buffer_load_dwordx4 v111, s[80:83], s48 offen offset:3072 lds
s_add_u32 m0, s75, 135168
v_mfma_f32_16x16x32_bf16 v[220:223], v[92:95], a[84:87], v[220:223]
s_xor_b32 s48, s67, 917504
s_add_u32 s48, s48, 458752
v_mfma_f32_16x16x32_bf16 v[236:239], v[88:91], a[72:75], v[236:239]
ds_read_b64_tr_b16 v[84:85], v96 offset:28672
v_mfma_f32_16x16x32_bf16 v[236:239], v[92:95], a[88:91], v[236:239]
ds_read_b64_tr_b16 v[86:87], v96 offset:28928
v_mfma_f32_16x16x32_bf16 v[252:255], v[88:91], a[76:79], v[252:255]
buffer_load_dwordx4 v108, s[40:43], s48 offen offset:0 lds
v_mfma_f32_16x16x32_bf16 v[252:255], v[92:95], a[92:95], v[252:255]
s_waitcnt lgkmcnt(8)
v_mfma_f32_16x16x32_bf16 v[192:195], v[64:67], a[96:99], v[192:195]
ds_read_b64_tr_b16 v[88:89], v97 offset:20480
v_mfma_f32_16x16x32_bf16 v[192:195], v[68:71], a[112:115], v[192:195]
ds_read_b64_tr_b16 v[90:91], v97 offset:20736
v_mfma_f32_16x16x32_bf16 v[208:211], v[64:67], a[100:103], v[208:211]
buffer_load_dwordx4 v109, s[40:43], s48 offen offset:1024 lds
v_mfma_f32_16x16x32_bf16 v[208:211], v[68:71], a[116:119], v[208:211]
v_mfma_f32_16x16x32_bf16 v[224:227], v[64:67], a[104:107], v[224:227]
ds_read_b64_tr_b16 v[92:93], v97 offset:28672
v_mfma_f32_16x16x32_bf16 v[224:227], v[68:71], a[120:123], v[224:227]
ds_read_b64_tr_b16 v[94:95], v97 offset:28928
v_mfma_f32_16x16x32_bf16 v[240:243], v[64:67], a[108:111], v[240:243]
buffer_load_dwordx4 v110, s[40:43], s48 offen offset:2048 lds
v_mfma_f32_16x16x32_bf16 v[240:243], v[68:71], a[124:127], v[240:243]
s_waitcnt lgkmcnt(8)
v_mfma_f32_16x16x32_bf16 v[196:199], v[72:75], a[96:99], v[196:199]
v_mfma_f32_16x16x32_bf16 v[196:199], v[76:79], a[112:115], v[196:199]
v_mfma_f32_16x16x32_bf16 v[212:215], v[72:75], a[100:103], v[212:215]
buffer_load_dwordx4 v111, s[40:43], s48 offen offset:3072 lds
v_mfma_f32_16x16x32_bf16 v[212:215], v[76:79], a[116:119], v[212:215]
s_mov_b32 s55, s29
s_lshl_b32 s79, s28, 19
v_mfma_f32_16x16x32_bf16 v[228:231], v[72:75], a[104:107], v[228:231]
s_mov_b64 s[36:37], s[10:11]
v_mfma_f32_16x16x32_bf16 v[228:231], v[76:79], a[120:123], v[228:231]
s_waitcnt vmcnt(44) lgkmcnt(0)
s_barrier
v_mfma_f32_16x16x32_bf16 v[244:247], v[72:75], a[108:111], v[244:247]
ds_read_b64_tr_b16 v[64:65], v96 offset:32768
v_mfma_f32_16x16x32_bf16 v[244:247], v[76:79], a[124:127], v[244:247]
ds_read_b64_tr_b16 v[66:67], v96 offset:33024
v_mfma_f32_16x16x32_bf16 v[200:203], v[80:83], a[96:99], v[200:203]
ds_read_b64_tr_b16 v[68:69], v96 offset:36864
v_mfma_f32_16x16x32_bf16 v[200:203], v[84:87], a[112:115], v[200:203]
ds_read_b64_tr_b16 v[70:71], v96 offset:37120
v_mfma_f32_16x16x32_bf16 v[216:219], v[80:83], a[100:103], v[216:219]
ds_read_b64_tr_b16 v[72:73], v97 offset:32768
v_mfma_f32_16x16x32_bf16 v[216:219], v[84:87], a[116:119], v[216:219]
ds_read_b64_tr_b16 v[74:75], v97 offset:33024
v_mfma_f32_16x16x32_bf16 v[232:235], v[80:83], a[104:107], v[232:235]
ds_read_b64_tr_b16 v[76:77], v97 offset:36864
v_mfma_f32_16x16x32_bf16 v[232:235], v[84:87], a[120:123], v[232:235]
ds_read_b64_tr_b16 v[78:79], v97 offset:37120
v_mfma_f32_16x16x32_bf16 v[248:251], v[80:83], a[108:111], v[248:251]
s_add_u32 s36, s36, 0x1400000
s_addc_u32 s37, s37, 0
v_mfma_f32_16x16x32_bf16 v[248:251], v[84:87], a[124:127], v[248:251]
global_store_dwordx4 v117, v[14:17], s[96:97] offset:-3072 sc1
v_mfma_f32_16x16x32_bf16 v[204:207], v[88:91], a[96:99], v[204:207]
ds_read_b64_tr_b16 v[80:81], v96 offset:33280
v_mfma_f32_16x16x32_bf16 v[204:207], v[92:95], a[112:115], v[204:207]
ds_read_b64_tr_b16 v[82:83], v96 offset:33536
v_mfma_f32_16x16x32_bf16 v[220:223], v[88:91], a[100:103], v[220:223]
s_add_u32 s36, s36, s79
s_addc_u32 s37, s37, 0
v_mfma_f32_16x16x32_bf16 v[220:223], v[92:95], a[116:119], v[220:223]
s_mov_b64 s[40:41], s[10:11]
v_mfma_f32_16x16x32_bf16 v[236:239], v[88:91], a[104:107], v[236:239]
ds_read_b64_tr_b16 v[84:85], v96 offset:37376
v_mfma_f32_16x16x32_bf16 v[236:239], v[92:95], a[120:123], v[236:239]
ds_read_b64_tr_b16 v[86:87], v96 offset:37632
v_mfma_f32_16x16x32_bf16 v[252:255], v[88:91], a[108:111], v[252:255]
s_add_u32 s40, s40, 0x2400000
s_addc_u32 s41, s41, 0
v_mfma_f32_16x16x32_bf16 v[252:255], v[92:95], a[124:127], v[252:255]
global_load_dwordx4 a[224:227], v113, s[44:45] offset:2048 sc1
s_waitcnt vmcnt(37) lgkmcnt(8)
v_mfma_f32_16x16x32_bf16 v[128:131], v[64:67], a[128:131], v[128:131]
ds_read_b64_tr_b16 v[88:89], v97 offset:33280
v_mfma_f32_16x16x32_bf16 v[128:131], v[68:71], a[144:147], v[128:131]
ds_read_b64_tr_b16 v[90:91], v97 offset:33536
v_mfma_f32_16x16x32_bf16 v[144:147], v[64:67], a[132:135], v[144:147]
s_add_u32 s40, s40, s79
s_addc_u32 s41, s41, 0
v_mfma_f32_16x16x32_bf16 v[144:147], v[68:71], a[148:151], v[144:147]
s_and_b32 s37, s37, 0xffff
s_mov_b32 s38, 0x80000
PAD5_%=:
s_cbranch_execz PAD13_%=
v_mfma_f32_16x16x32_bf16 v[160:163], v[64:67], a[136:139], v[160:163]
ds_read_b64_tr_b16 v[92:93], v97 offset:37376
s_mov_b32 s39, 0x20000
v_mfma_f32_16x16x32_bf16 v[160:163], v[68:71], a[152:155], v[160:163]
ds_read_b64_tr_b16 v[94:95], v97 offset:37632
s_and_b32 s41, s41, 0xffff
v_mfma_f32_16x16x32_bf16 v[176:179], v[64:67], a[140:143], v[176:179]
s_mov_b32 s42, 0x80000
s_mov_b32 s43, 0x20000
v_mfma_f32_16x16x32_bf16 v[176:179], v[68:71], a[156:159], v[176:179]
s_lshl_b32 s79, s28, 13
s_waitcnt lgkmcnt(8)
v_mfma_f32_16x16x32_bf16 v[132:135], v[72:75], a[128:131], v[132:135]
ds_read_b64_tr_b16 v[64:65], v96 offset:40960
s_lshl_b32 s54, s55, 2
v_mfma_f32_16x16x32_bf16 v[132:135], v[76:79], a[144:147], v[132:135]
ds_read_b64_tr_b16 v[66:67], v96 offset:41216
s_add_u32 s76, s54, %2
v_mfma_f32_16x16x32_bf16 v[148:151], v[72:75], a[132:135], v[148:151]
s_mul_i32 s76, s76, 376832
s_add_u32 s76, s76, s79
v_mfma_f32_16x16x32_bf16 v[148:151], v[76:79], a[148:151], v[148:151]
global_load_dwordx4 a[228:231], v115, s[44:45] offset:2048 sc1
s_mov_b64 s[50:51], s[10:11]
v_mfma_f32_16x16x32_bf16 v[164:167], v[72:75], a[136:139], v[164:167]
ds_read_b64_tr_b16 v[68:69], v96 offset:45056
v_mfma_f32_16x16x32_bf16 v[164:167], v[76:79], a[152:155], v[164:167]
ds_read_b64_tr_b16 v[70:71], v96 offset:45312
v_mfma_f32_16x16x32_bf16 v[180:183], v[72:75], a[140:143], v[180:183]
s_add_u32 s50, s50, 0x341d000
s_addc_u32 s51, s51, 0
v_mfma_f32_16x16x32_bf16 v[180:183], v[76:79], a[156:159], v[180:183]
global_load_dwordx4 a[232:235], v113, s[46:47] offset:2048 sc1
s_waitcnt lgkmcnt(8)
v_mfma_f32_16x16x32_bf16 v[136:139], v[80:83], a[128:131], v[136:139]
ds_read_b64_tr_b16 v[72:73], v97 offset:40960
v_mfma_f32_16x16x32_bf16 v[136:139], v[84:87], a[144:147], v[136:139]
ds_read_b64_tr_b16 v[74:75], v97 offset:41216
v_mfma_f32_16x16x32_bf16 v[152:155], v[80:83], a[132:135], v[152:155]
s_add_u32 s50, s50, s76
s_addc_u32 s51, s51, 0
v_mfma_f32_16x16x32_bf16 v[152:155], v[84:87], a[148:151], v[152:155]
s_sub_u32 s54, 60, s54
s_add_u32 s76, s54, %2
v_mfma_f32_16x16x32_bf16 v[168:171], v[80:83], a[136:139], v[168:171]
ds_read_b64_tr_b16 v[76:77], v97 offset:45056
s_mul_i32 s76, s76, 376832
v_mfma_f32_16x16x32_bf16 v[168:171], v[84:87], a[152:155], v[168:171]
ds_read_b64_tr_b16 v[78:79], v97 offset:45312
s_add_u32 s76, s76, s79
v_mfma_f32_16x16x32_bf16 v[184:187], v[80:83], a[140:143], v[184:187]
global_load_dwordx4 a[236:239], v115, s[46:47] offset:2048 sc1
s_mov_b64 s[52:53], s[10:11]
v_mfma_f32_16x16x32_bf16 v[184:187], v[84:87], a[156:159], v[184:187]
s_waitcnt lgkmcnt(8)
v_mfma_f32_16x16x32_bf16 v[140:143], v[88:91], a[128:131], v[140:143]
ds_read_b64_tr_b16 v[80:81], v96 offset:41472
v_mfma_f32_16x16x32_bf16 v[140:143], v[92:95], a[144:147], v[140:143]
ds_read_b64_tr_b16 v[82:83], v96 offset:41728
v_mfma_f32_16x16x32_bf16 v[156:159], v[88:91], a[132:135], v[156:159]
s_add_u32 s52, s52, 0x341d000
s_addc_u32 s53, s53, 0
v_mfma_f32_16x16x32_bf16 v[156:159], v[92:95], a[148:151], v[156:159]
s_add_u32 s52, s52, s76
s_addc_u32 s53, s53, 0
v_mfma_f32_16x16x32_bf16 v[172:175], v[88:91], a[136:139], v[172:175]
ds_read_b64_tr_b16 v[84:85], v96 offset:45568
s_add_u32 s74, s55, 1
v_mfma_f32_16x16x32_bf16 v[172:175], v[92:95], a[152:155], v[172:175]
ds_read_b64_tr_b16 v[86:87], v96 offset:45824
s_lshl_b32 s70, s55, 15
v_mfma_f32_16x16x32_bf16 v[188:191], v[88:91], a[140:143], v[188:191]
s_lshl_b32 s71, s55, 15
s_mov_b32 s69, 0
v_mfma_f32_16x16x32_bf16 v[188:191], v[92:95], a[156:159], v[188:191]
s_mov_b32 s88, 0
s_waitcnt vmcnt(30) lgkmcnt(8)
v_mfma_f32_16x16x32_bf16 v[128:131], v[64:67], a[160:163], v[128:131]
ds_read_b64_tr_b16 v[88:89], v97 offset:41472
s_sub_u32 s72, s55, 1
v_mfma_f32_16x16x32_bf16 v[128:131], v[68:71], a[176:179], v[128:131]
ds_read_b64_tr_b16 v[90:91], v97 offset:41728
s_mov_b32 s54, -1
v_mfma_f32_16x16x32_bf16 v[144:147], v[64:67], a[164:167], v[144:147]
global_load_dwordx4 a[240:243], v113, s[44:45] offset:3072 sc1
s_mov_b32 s56, s55
v_mfma_f32_16x16x32_bf16 v[144:147], v[68:71], a[180:183], v[144:147]
s_add_u32 s73, s55, 2
v_mfma_f32_16x16x32_bf16 v[160:163], v[64:67], a[168:171], v[160:163]
ds_read_b64_tr_b16 v[92:93], v97 offset:45568
v_mfma_f32_16x16x32_bf16 v[160:163], v[68:71], a[184:187], v[160:163]
ds_read_b64_tr_b16 v[94:95], v97 offset:45824
v_mfma_f32_16x16x32_bf16 v[176:179], v[64:67], a[172:175], v[176:179]
global_load_dwordx4 a[244:247], v115, s[44:45] offset:3072 sc1
v_mfma_f32_16x16x32_bf16 v[176:179], v[68:71], a[188:191], v[176:179]
s_waitcnt lgkmcnt(8)
v_mfma_f32_16x16x32_bf16 v[132:135], v[72:75], a[160:163], v[132:135]
ds_read_b64_tr_b16 v[64:65], v96 offset:49152
v_mfma_f32_16x16x32_bf16 v[132:135], v[76:79], a[176:179], v[132:135]
ds_read_b64_tr_b16 v[66:67], v96 offset:49408
v_mfma_f32_16x16x32_bf16 v[148:151], v[72:75], a[164:167], v[148:151]
global_load_dwordx4 a[248:251], v113, s[46:47] offset:3072 sc1
v_mfma_f32_16x16x32_bf16 v[148:151], v[76:79], a[180:183], v[148:151]
s_cmp_le_u32 s73, 12
s_cselect_b32 s79, s73, 2
v_mfma_f32_16x16x32_bf16 v[164:167], v[72:75], a[168:171], v[164:167]
ds_read_b64_tr_b16 v[68:69], v96 offset:53248
s_lshl_b32 s27, 6, s55
v_mfma_f32_16x16x32_bf16 v[164:167], v[76:79], a[184:187], v[164:167]
ds_read_b64_tr_b16 v[70:71], v96 offset:53504
s_and_b32 s27, s27, 0xffff
v_mfma_f32_16x16x32_bf16 v[180:183], v[72:75], a[172:175], v[180:183]
global_load_dwordx4 a[252:255], v115, s[46:47] offset:3072 sc1
s_bitset1_b32 s27, s74
v_mfma_f32_16x16x32_bf16 v[180:183], v[76:79], a[188:191], v[180:183]
s_bitset1_b32 s27, s79
s_waitcnt lgkmcnt(8)
v_mfma_f32_16x16x32_bf16 v[136:139], v[80:83], a[160:163], v[136:139]
ds_read_b64_tr_b16 v[72:73], v97 offset:49152
s_or_b32 s57, s27, 0x8000
v_mfma_f32_16x16x32_bf16 v[136:139], v[84:87], a[176:179], v[136:139]
ds_read_b64_tr_b16 v[74:75], v97 offset:49408
s_lshl_b32 s76, s55, 5
v_mfma_f32_16x16x32_bf16 v[152:155], v[80:83], a[164:167], v[152:155]
global_store_dwordx4 v117, v[18:21], s[94:95] offset:-2048 sc1
s_add_u32 s76, s76, s28
v_mfma_f32_16x16x32_bf16 v[152:155], v[84:87], a[180:183], v[152:155]
s_lshl2_add_u32 s76, s76, 49152
s_min_u32 s66, s28, 20
v_mfma_f32_16x16x32_bf16 v[168:171], v[80:83], a[168:171], v[168:171]
ds_read_b64_tr_b16 v[76:77], v97 offset:53248
s_mul_i32 s66, s66, 205
)"
R"(v_mfma_f32_16x16x32_bf16 v[168:171], v[84:87], a[184:187], v[168:171]
ds_read_b64_tr_b16 v[78:79], v97 offset:53504
s_and_b32 s66, s66, 0x1800
v_mfma_f32_16x16x32_bf16 v[184:187], v[80:83], a[172:175], v[184:187]
s_add_u32 s76, s76, s66
s_lshr_b32 s47, s30, 8
v_mfma_f32_16x16x32_bf16 v[184:187], v[84:87], a[188:191], v[184:187]
s_and_b32 s47, s47, 14
s_waitcnt lgkmcnt(8)
v_mfma_f32_16x16x32_bf16 v[140:143], v[88:91], a[160:163], v[140:143]
ds_read_b64_tr_b16 v[80:81], v96 offset:49664
s_add_u32 s47, s101, s47
v_mfma_f32_16x16x32_bf16 v[140:143], v[92:95], a[176:179], v[140:143]
ds_read_b64_tr_b16 v[82:83], v96 offset:49920
s_mov_b32 s46, s47
v_mfma_f32_16x16x32_bf16 v[156:159], v[88:91], a[164:167], v[156:159]
global_store_dwordx4 v117, v[28:31], s[96:97] offset:-2048 sc1
v_mfma_f32_16x16x32_bf16 v[156:159], v[92:95], a[180:183], v[156:159]
s_add_u32 s66, s92, s76
s_addc_u32 s67, s93, 0
v_mfma_f32_16x16x32_bf16 v[172:175], v[88:91], a[168:171], v[172:175]
ds_read_b64_tr_b16 v[84:85], v96 offset:53760
s_xor_b32 s76, s76, 1920
v_mfma_f32_16x16x32_bf16 v[172:175], v[92:95], a[184:187], v[172:175]
ds_read_b64_tr_b16 v[86:87], v96 offset:54016
v_mfma_f32_16x16x32_bf16 v[188:191], v[88:91], a[172:175], v[188:191]
s_add_u32 s16, s92, s76
s_addc_u32 s17, s93, 0
v_mfma_f32_16x16x32_bf16 v[188:191], v[92:95], a[188:191], v[188:191]
global_store_dwordx4 v117, v[120:123], s[94:95] offset:-1024 sc1
s_waitcnt vmcnt(28) lgkmcnt(8)
v_mfma_f32_16x16x32_bf16 v[128:131], v[64:67], a[192:195], v[128:131]
ds_read_b64_tr_b16 v[88:89], v97 offset:49664
s_and_b32 s76, s26, 7
v_mfma_f32_16x16x32_bf16 v[128:131], v[68:71], a[208:211], v[128:131]
ds_read_b64_tr_b16 v[90:91], v97 offset:49920
s_lshl_b32 s76, s76, 7
v_mfma_f32_16x16x32_bf16 v[144:147], v[64:67], a[196:199], v[144:147]
s_add_u32 s76, s76, 4096
v_mfma_f32_16x16x32_bf16 v[144:147], v[68:71], a[212:215], v[144:147]
s_add_u32 s18, s92, s76
s_addc_u32 s19, s93, 0
v_mfma_f32_16x16x32_bf16 v[160:163], v[64:67], a[200:203], v[160:163]
ds_read_b64_tr_b16 v[92:93], v97 offset:53760
s_mov_b32 s78, 0x41a00000
v_mfma_f32_16x16x32_bf16 v[160:163], v[68:71], a[216:219], v[160:163]
ds_read_b64_tr_b16 v[94:95], v97 offset:54016
s_mov_b32 s68, 1
v_mfma_f32_16x16x32_bf16 v[176:179], v[64:67], a[204:207], v[176:179]
global_store_dwordx4 v117, v[124:127], s[96:97] offset:-1024 sc1
s_lshl_b32 s98, s28, 1
v_mfma_f32_16x16x32_bf16 v[176:179], v[68:71], a[220:223], v[176:179]
s_add_u32 s91, s28, 24
s_waitcnt lgkmcnt(8)
v_mfma_f32_16x16x32_bf16 v[132:135], v[72:75], a[192:195], v[132:135]
ds_read_b64_tr_b16 v[64:65], v96 offset:57344
s_min_u32 s98, s98, s91
v_mfma_f32_16x16x32_bf16 v[132:135], v[76:79], a[208:211], v[132:135]
ds_read_b64_tr_b16 v[66:67], v96 offset:57600
s_lshl_b32 s98, s98, 11
v_mfma_f32_16x16x32_bf16 v[148:151], v[72:75], a[196:199], v[148:151]
s_lshl_b32 s91, s29, 2
s_add_u32 s91, s91, %2
v_mfma_f32_16x16x32_bf16 v[148:151], v[76:79], a[212:215], v[148:151]
s_mul_i32 s91, s91, 376832
s_add_u32 s91, s91, s98
v_mfma_f32_16x16x32_bf16 v[164:167], v[72:75], a[200:203], v[164:167]
ds_read_b64_tr_b16 v[68:69], v96 offset:61440
s_mov_b64 s[94:95], s[10:11]
v_mfma_f32_16x16x32_bf16 v[164:167], v[76:79], a[216:219], v[164:167]
ds_read_b64_tr_b16 v[70:71], v96 offset:61696
s_and_b32 s76, %3, 4
v_mfma_f32_16x16x32_bf16 v[180:183], v[72:75], a[204:207], v[180:183]
s_add_u32 s94, s94, 0x3400000
s_addc_u32 s95, s95, 0
v_mfma_f32_16x16x32_bf16 v[180:183], v[76:79], a[220:223], v[180:183]
s_lshl_b32 s76, s76, 14
s_waitcnt lgkmcnt(8)
v_mfma_f32_16x16x32_bf16 v[136:139], v[80:83], a[192:195], v[136:139]
ds_read_b64_tr_b16 v[72:73], v97 offset:57344
v_mfma_f32_16x16x32_bf16 v[136:139], v[84:87], a[208:211], v[136:139]
ds_read_b64_tr_b16 v[74:75], v97 offset:57600
v_mfma_f32_16x16x32_bf16 v[152:155], v[80:83], a[196:199], v[152:155]
s_add_u32 s94, s94, s91
s_addc_u32 s95, s95, 0
v_mfma_f32_16x16x32_bf16 v[152:155], v[84:87], a[212:215], v[152:155]
s_lshl_b32 s91, s29, 2
s_sub_u32 s91, 60, s91
v_mfma_f32_16x16x32_bf16 v[168:171], v[80:83], a[200:203], v[168:171]
ds_read_b64_tr_b16 v[76:77], v97 offset:61440
s_add_u32 s91, s91, %2
v_mfma_f32_16x16x32_bf16 v[168:171], v[84:87], a[216:219], v[168:171]
ds_read_b64_tr_b16 v[78:79], v97 offset:61696
s_mul_i32 s91, s91, 376832
v_mfma_f32_16x16x32_bf16 v[184:187], v[80:83], a[204:207], v[184:187]
s_add_u32 s91, s91, s98
s_mov_b64 s[96:97], s[10:11]
v_mfma_f32_16x16x32_bf16 v[184:187], v[84:87], a[220:223], v[184:187]
s_mov_b32 s29, 2
s_waitcnt lgkmcnt(8)
v_mfma_f32_16x16x32_bf16 v[140:143], v[88:91], a[192:195], v[140:143]
ds_read_b64_tr_b16 v[80:81], v96 offset:57856
s_mov_b32 s98, 4
v_mfma_f32_16x16x32_bf16 v[140:143], v[92:95], a[208:211], v[140:143]
ds_read_b64_tr_b16 v[82:83], v96 offset:58112
s_add_u32 s46, s46, s29
v_mfma_f32_16x16x32_bf16 v[156:159], v[88:91], a[196:199], v[156:159]
s_add_u32 s96, s96, 0x3400000
s_addc_u32 s97, s97, 0
v_mfma_f32_16x16x32_bf16 v[156:159], v[92:95], a[212:215], v[156:159]
s_add_u32 s96, s96, s91
s_addc_u32 s97, s97, 0
v_mfma_f32_16x16x32_bf16 v[172:175], v[88:91], a[200:203], v[172:175]
ds_read_b64_tr_b16 v[84:85], v96 offset:61952
v_xor_b32 v117, 0x800, v22
v_mfma_f32_16x16x32_bf16 v[172:175], v[92:95], a[216:219], v[172:175]
ds_read_b64_tr_b16 v[86:87], v96 offset:62208
v_bfe_u32 v21, v22, 4, 4
v_mfma_f32_16x16x32_bf16 v[188:191], v[88:91], a[204:207], v[188:191]
v_lshlrev_b32 v21, 10, v21
v_lshrrev_b32 v20, 8, v22
v_mfma_f32_16x16x32_bf16 v[188:191], v[92:95], a[220:223], v[188:191]
v_lshl_add_u32 v21, v20, 4, v21
s_waitcnt vmcnt(4) lgkmcnt(8)
v_mfma_f32_16x16x32_bf16 v[128:131], v[64:67], a[224:227], v[128:131]
ds_read_b64_tr_b16 v[88:89], v97 offset:57856
v_add_u32 v3, s75, v22
v_mfma_f32_16x16x32_bf16 v[128:131], v[68:71], a[240:243], v[128:131]
ds_read_b64_tr_b16 v[90:91], v97 offset:58112
v_add_u32 v4, 0x1000, v3
v_mfma_f32_16x16x32_bf16 v[144:147], v[64:67], a[228:231], v[144:147]
v_and_b32 v1, 0x2000, v3
v_lshl_add_u32 v1, v1, 1, v22
v_mfma_f32_16x16x32_bf16 v[144:147], v[68:71], a[244:247], v[144:147]
v_add_u32 v2, 0x10000, v1
v_bfe_u32 v18, v22, 4, 4
v_mfma_f32_16x16x32_bf16 v[160:163], v[64:67], a[232:235], v[160:163]
ds_read_b64_tr_b16 v[92:93], v97 offset:61952
v_lshrrev_b32 v19, 8, v22
v_mfma_f32_16x16x32_bf16 v[160:163], v[68:71], a[248:251], v[160:163]
ds_read_b64_tr_b16 v[94:95], v97 offset:62208
v_lshlrev_b32 v19, 2, v19
v_mfma_f32_16x16x32_bf16 v[176:179], v[64:67], a[236:239], v[176:179]
v_sub_u32 v18, v18, v19
v_mov_b32 v20, 0xff800000
v_mfma_f32_16x16x32_bf16 v[176:179], v[68:71], a[252:255], v[176:179]
v_xor_b32 v23, 0x22000, v3
s_waitcnt lgkmcnt(8)
v_mfma_f32_16x16x32_bf16 v[132:135], v[72:75], a[224:227], v[132:135]
v_mov_b32 v10, 0
v_mov_b32 v11, 0
v_mfma_f32_16x16x32_bf16 v[132:135], v[76:79], a[240:243], v[132:135]
v_mov_b32 v12, 0
v_mov_b32 v13, 0
v_mfma_f32_16x16x32_bf16 v[148:151], v[72:75], a[228:231], v[148:151]
v_mov_b32 v14, 0
v_mov_b32 v15, 0
v_mfma_f32_16x16x32_bf16 v[148:151], v[76:79], a[244:247], v[148:151]
v_mov_b32 v16, 0
v_mov_b32 v17, 0
v_mfma_f32_16x16x32_bf16 v[164:167], v[72:75], a[232:235], v[164:167]
v_lshl_add_u32 v19, v9, 6, v7
v_mfma_f32_16x16x32_bf16 v[164:167], v[76:79], a[248:251], v[164:167]
s_waitcnt lgkmcnt(0)
s_barrier
v_mfma_f32_16x16x32_bf16 v[180:183], v[72:75], a[236:239], v[180:183]
ds_read_b64_tr_b16 v[64:65], v98 offset:0
v_mfma_f32_16x16x32_bf16 v[180:183], v[76:79], a[252:255], v[180:183]
ds_read_b64_tr_b16 v[66:67], v98 offset:256
v_mfma_f32_16x16x32_bf16 v[136:139], v[80:83], a[224:227], v[136:139]
ds_read_b64_tr_b16 v[68:69], v98 offset:8192
s_add_u32 m0, s75, 32768
v_mfma_f32_16x16x32_bf16 v[136:139], v[84:87], a[240:243], v[136:139]
ds_read_b64_tr_b16 v[70:71], v98 offset:8448
s_xor_b32 s48, s65, 128
v_mfma_f32_16x16x32_bf16 v[152:155], v[80:83], a[228:231], v[152:155]
ds_read_b64_tr_b16 v[72:73], v99 offset:0
v_mfma_f32_16x16x32_bf16 v[152:155], v[84:87], a[244:247], v[152:155]
ds_read_b64_tr_b16 v[74:75], v99 offset:256
v_mfma_f32_16x16x32_bf16 v[168:171], v[80:83], a[232:235], v[168:171]
ds_read_b64_tr_b16 v[76:77], v99 offset:8192
v_mfma_f32_16x16x32_bf16 v[168:171], v[84:87], a[248:251], v[168:171]
ds_read_b64_tr_b16 v[78:79], v99 offset:8448
v_mfma_f32_16x16x32_bf16 v[184:187], v[80:83], a[236:239], v[184:187]
buffer_load_dwordx4 v104, s[84:87], s48 offen offset:0 lds
v_mfma_f32_16x16x32_bf16 v[184:187], v[84:87], a[252:255], v[184:187]
v_mfma_f32_16x16x32_bf16 v[140:143], v[88:91], a[224:227], v[140:143]
ds_read_b64_tr_b16 v[80:81], v98 offset:4096
v_mfma_f32_16x16x32_bf16 v[140:143], v[92:95], a[240:243], v[140:143]
ds_read_b64_tr_b16 v[82:83], v98 offset:4352
v_mfma_f32_16x16x32_bf16 v[156:159], v[88:91], a[228:231], v[156:159]
buffer_load_dwordx4 v105, s[84:87], s48 offen offset:1024 lds
v_mfma_f32_16x16x32_bf16 v[156:159], v[92:95], a[244:247], v[156:159]
s_add_u32 s18, s18, s76
s_addc_u32 s19, s19, 0
v_mfma_f32_16x16x32_bf16 v[172:175], v[88:91], a[232:235], v[172:175]
ds_read_b64_tr_b16 v[84:85], v98 offset:12288
s_and_b32 s76, %3, 16
v_mfma_f32_16x16x32_bf16 v[172:175], v[92:95], a[248:251], v[172:175]
ds_read_b64_tr_b16 v[86:87], v98 offset:12544
s_lshl_b32 s76, s76, 12
v_mfma_f32_16x16x32_bf16 v[188:191], v[88:91], a[236:239], v[188:191]
buffer_load_dwordx4 v106, s[84:87], s48 offen offset:2048 lds
v_mfma_f32_16x16x32_bf16 v[188:191], v[92:95], a[252:255], v[188:191]
s_waitcnt lgkmcnt(8)
v_mfma_f32_16x16x32_bf16 v[192:195], v[64:67], a[128:131], v[192:195]
ds_read_b64_tr_b16 v[88:89], v99 offset:4096
v_mfma_f32_16x16x32_bf16 v[192:195], v[68:71], a[144:147], v[192:195]
ds_read_b64_tr_b16 v[90:91], v99 offset:4352
v_mfma_f32_16x16x32_bf16 v[208:211], v[64:67], a[132:135], v[208:211]
buffer_load_dwordx4 v107, s[84:87], s48 offen offset:3072 lds
v_mfma_f32_16x16x32_bf16 v[208:211], v[68:71], a[148:151], v[208:211]
s_add_u32 m0, s75, 36864
s_xor_b32 s48, s65, 262272
v_mfma_f32_16x16x32_bf16 v[224:227], v[64:67], a[136:139], v[224:227]
ds_read_b64_tr_b16 v[92:93], v99 offset:12288
v_mul_f32 v128, 0x3e0293ee, v128
v_mfma_f32_16x16x32_bf16 v[224:227], v[68:71], a[152:155], v[224:227]
ds_read_b64_tr_b16 v[94:95], v99 offset:12544
v_mul_f32 v129, 0x3e0293ee, v129
v_mfma_f32_16x16x32_bf16 v[240:243], v[64:67], a[140:143], v[240:243]
buffer_load_dwordx4 v104, s[84:87], s48 offen offset:0 lds
v_cvt_pk_bf16_f32 v32, v128, v129
v_mfma_f32_16x16x32_bf16 v[240:243], v[68:71], a[156:159], v[240:243]
v_mul_f32 v130, 0x3e0293ee, v130
s_waitcnt lgkmcnt(8)
v_mfma_f32_16x16x32_bf16 v[196:199], v[72:75], a[128:131], v[196:199]
ds_read_b64_tr_b16 v[64:65], v98 offset:16384
v_mul_f32 v131, 0x3e0293ee, v131
v_mfma_f32_16x16x32_bf16 v[196:199], v[76:79], a[144:147], v[196:199]
ds_read_b64_tr_b16 v[66:67], v98 offset:16640
v_cvt_pk_bf16_f32 v33, v130, v131
v_mfma_f32_16x16x32_bf16 v[212:215], v[72:75], a[132:135], v[212:215]
buffer_load_dwordx4 v105, s[84:87], s48 offen offset:1024 lds
v_mul_f32 v132, 0x3e0293ee, v132
v_mfma_f32_16x16x32_bf16 v[212:215], v[76:79], a[148:151], v[212:215]
v_mul_f32 v133, 0x3e0293ee, v133
v_cvt_pk_bf16_f32 v34, v132, v133
v_mfma_f32_16x16x32_bf16 v[228:231], v[72:75], a[136:139], v[228:231]
ds_read_b64_tr_b16 v[68:69], v98 offset:24576
v_mul_f32 v134, 0x3e0293ee, v134
v_mfma_f32_16x16x32_bf16 v[228:231], v[76:79], a[152:155], v[228:231]
ds_read_b64_tr_b16 v[70:71], v98 offset:24832
v_mfma_f32_16x16x32_bf16 v[244:247], v[72:75], a[140:143], v[244:247]
buffer_load_dwordx4 v106, s[84:87], s48 offen offset:2048 lds
v_mul_f32 v135, 0x3e0293ee, v135
v_mfma_f32_16x16x32_bf16 v[244:247], v[76:79], a[156:159], v[244:247]
v_cvt_pk_bf16_f32 v35, v134, v135
s_waitcnt lgkmcnt(8)
v_mfma_f32_16x16x32_bf16 v[200:203], v[80:83], a[128:131], v[200:203]
ds_read_b64_tr_b16 v[72:73], v99 offset:16384
v_mul_f32 v136, 0x3e0293ee, v136
v_mfma_f32_16x16x32_bf16 v[200:203], v[84:87], a[144:147], v[200:203]
ds_read_b64_tr_b16 v[74:75], v99 offset:16640
v_mfma_f32_16x16x32_bf16 v[216:219], v[80:83], a[132:135], v[216:219]
v_mul_f32 v137, 0x3e0293ee, v137
v_mfma_f32_16x16x32_bf16 v[216:219], v[84:87], a[148:151], v[216:219]
buffer_load_dwordx4 v107, s[84:87], s48 offen offset:3072 lds
v_cvt_pk_bf16_f32 v36, v136, v137
v_mfma_f32_16x16x32_bf16 v[232:235], v[80:83], a[136:139], v[232:235]
ds_read_b64_tr_b16 v[76:77], v99 offset:24576
v_mul_f32 v138, 0x3e0293ee, v138
v_mfma_f32_16x16x32_bf16 v[232:235], v[84:87], a[152:155], v[232:235]
ds_read_b64_tr_b16 v[78:79], v99 offset:24832
v_mul_f32 v139, 0x3e0293ee, v139
v_mfma_f32_16x16x32_bf16 v[248:251], v[80:83], a[140:143], v[248:251]
s_add_u32 s66, s66, s76
s_addc_u32 s67, s67, 0
v_mfma_f32_16x16x32_bf16 v[248:251], v[84:87], a[156:159], v[248:251]
v_cvt_pk_bf16_f32 v37, v138, v139
s_waitcnt lgkmcnt(8)
v_mfma_f32_16x16x32_bf16 v[204:207], v[88:91], a[128:131], v[204:207]
ds_read_b64_tr_b16 v[80:81], v98 offset:20480
v_mul_f32 v140, 0x3e0293ee, v140
v_mfma_f32_16x16x32_bf16 v[204:207], v[92:95], a[144:147], v[204:207]
ds_read_b64_tr_b16 v[82:83], v98 offset:20736
v_mul_f32 v141, 0x3e0293ee, v141
v_mfma_f32_16x16x32_bf16 v[220:223], v[88:91], a[132:135], v[220:223]
s_and_b32 s76, %3, 16
s_lshl_b32 s76, s76, 22
v_mfma_f32_16x16x32_bf16 v[220:223], v[92:95], a[148:151], v[220:223]
v_cvt_pk_bf16_f32 v38, v140, v141
v_mfma_f32_16x16x32_bf16 v[236:239], v[88:91], a[136:139], v[236:239]
ds_read_b64_tr_b16 v[84:85], v98 offset:28672
v_mul_f32 v142, 0x3e0293ee, v142
v_mfma_f32_16x16x32_bf16 v[236:239], v[92:95], a[152:155], v[236:239]
ds_read_b64_tr_b16 v[86:87], v98 offset:28928
v_mul_f32 v143, 0x3e0293ee, v143
v_mfma_f32_16x16x32_bf16 v[252:255], v[88:91], a[140:143], v[252:255]
v_cvt_pk_bf16_f32 v39, v142, v143
v_mfma_f32_16x16x32_bf16 v[252:255], v[92:95], a[156:159], v[252:255]
s_waitcnt lgkmcnt(8)
v_mfma_f32_16x16x32_bf16 v[192:195], v[64:67], a[160:163], v[192:195]
ds_read_b64_tr_b16 v[88:89], v99 offset:20480
v_mul_f32 v144, 0x3e0293ee, v144
v_mfma_f32_16x16x32_bf16 v[192:195], v[68:71], a[176:179], v[192:195]
ds_read_b64_tr_b16 v[90:91], v99 offset:20736
v_mul_f32 v145, 0x3e0293ee, v145
v_mfma_f32_16x16x32_bf16 v[208:211], v[64:67], a[164:167], v[208:211]
v_cvt_pk_bf16_f32 v40, v144, v145
v_mfma_f32_16x16x32_bf16 v[208:211], v[68:71], a[180:183], v[208:211]
s_add_u32 s50, s50, s76
s_addc_u32 s51, s51, 0
v_mfma_f32_16x16x32_bf16 v[224:227], v[64:67], a[168:171], v[224:227]
ds_read_b64_tr_b16 v[92:93], v99 offset:28672
v_mul_f32 v146, 0x3e0293ee, v146
v_mfma_f32_16x16x32_bf16 v[224:227], v[68:71], a[184:187], v[224:227]
ds_read_b64_tr_b16 v[94:95], v99 offset:28928
v_mul_f32 v147, 0x3e0293ee, v147
v_mfma_f32_16x16x32_bf16 v[240:243], v[64:67], a[172:175], v[240:243]
v_cvt_pk_bf16_f32 v41, v146, v147
s_mov_b32 s58, 0x41c00000
v_mfma_f32_16x16x32_bf16 v[240:243], v[68:71], a[188:191], v[240:243]
v_mul_f32 v148, 0x3e0293ee, v148
s_waitcnt lgkmcnt(8)
v_mfma_f32_16x16x32_bf16 v[196:199], v[72:75], a[160:163], v[196:199]
ds_read_b64_tr_b16 v[64:65], v26 offset:0
v_mfma_f32_16x16x32_bf16 v[196:199], v[76:79], a[176:179], v[196:199]
ds_read_b64_tr_b16 v[66:67], v26 offset:256
v_mul_f32 v149, 0x3e0293ee, v149
v_mfma_f32_16x16x32_bf16 v[212:215], v[72:75], a[164:167], v[212:215]
v_cvt_pk_bf16_f32 v42, v148, v149
v_mfma_f32_16x16x32_bf16 v[212:215], v[76:79], a[180:183], v[212:215]
v_mul_f32 v150, 0x3e0293ee, v150
v_mfma_f32_16x16x32_bf16 v[228:231], v[72:75], a[168:171], v[228:231]
ds_read_b64_tr_b16 v[68:69], v26 offset:8192
v_mul_f32 v151, 0x3e0293ee, v151
v_mfma_f32_16x16x32_bf16 v[228:231], v[76:79], a[184:187], v[228:231]
ds_read_b64_tr_b16 v[70:71], v26 offset:8448
v_mfma_f32_16x16x32_bf16 v[244:247], v[72:75], a[172:175], v[244:247]
v_cvt_pk_bf16_f32 v43, v150, v151
v_mfma_f32_16x16x32_bf16 v[244:247], v[76:79], a[188:191], v[244:247]
v_mul_f32 v152, 0x3e0293ee, v152
s_waitcnt lgkmcnt(8)
v_mfma_f32_16x16x32_bf16 v[200:203], v[80:83], a[160:163], v[200:203]
ds_read_b64_tr_b16 v[72:73], v27 offset:0
v_mul_f32 v153, 0x3e0293ee, v153
v_mfma_f32_16x16x32_bf16 v[200:203], v[84:87], a[176:179], v[200:203]
ds_read_b64_tr_b16 v[74:75], v27 offset:256
v_mfma_f32_16x16x32_bf16 v[216:219], v[80:83], a[164:167], v[216:219]
v_cvt_pk_bf16_f32 v44, v152, v153
v_mfma_f32_16x16x32_bf16 v[216:219], v[84:87], a[180:183], v[216:219]
v_mul_f32 v154, 0x3e0293ee, v154
v_mfma_f32_16x16x32_bf16 v[232:235], v[80:83], a[168:171], v[232:235]
ds_read_b64_tr_b16 v[76:77], v27 offset:8192
v_mul_f32 v155, 0x3e0293ee, v155
v_mfma_f32_16x16x32_bf16 v[232:235], v[84:87], a[184:187], v[232:235]
ds_read_b64_tr_b16 v[78:79], v27 offset:8448
v_cvt_pk_bf16_f32 v45, v154, v155
v_mfma_f32_16x16x32_bf16 v[248:251], v[80:83], a[172:175], v[248:251]
s_bitcmp1_b32 %3, 2
s_cselect_b32 s58, 0xff800000, s58
v_mfma_f32_16x16x32_bf16 v[248:251], v[84:87], a[188:191], v[248:251]
v_mul_f32 v156, 0x3e0293ee, v156
s_waitcnt lgkmcnt(8)
v_mfma_f32_16x16x32_bf16 v[204:207], v[88:91], a[160:163], v[204:207]
ds_read_b64_tr_b16 v[80:81], v26 offset:4096
v_mul_f32 v157, 0x3e0293ee, v157
v_mfma_f32_16x16x32_bf16 v[204:207], v[92:95], a[176:179], v[204:207]
ds_read_b64_tr_b16 v[82:83], v26 offset:4352
v_cvt_pk_bf16_f32 v46, v156, v157
v_mfma_f32_16x16x32_bf16 v[220:223], v[88:91], a[164:167], v[220:223]
s_mov_b32 s76, 0x5d800000
v_mfma_f32_16x16x32_bf16 v[220:223], v[92:95], a[180:183], v[220:223]
v_mul_f32 v158, 0x3e0293ee, v158
v_mfma_f32_16x16x32_bf16 v[236:239], v[88:91], a[168:171], v[236:239]
ds_read_b64_tr_b16 v[84:85], v26 offset:12288
v_mul_f32 v159, 0x3e0293ee, v159
v_mfma_f32_16x16x32_bf16 v[236:239], v[92:95], a[184:187], v[236:239]
ds_read_b64_tr_b16 v[86:87], v26 offset:12544
v_cvt_pk_bf16_f32 v47, v158, v159
v_mfma_f32_16x16x32_bf16 v[252:255], v[88:91], a[172:175], v[252:255]
v_mul_f32 v160, 0x3e0293ee, v160
v_mfma_f32_16x16x32_bf16 v[252:255], v[92:95], a[188:191], v[252:255]
s_waitcnt lgkmcnt(8)
v_mfma_f32_16x16x32_bf16 v[192:195], v[64:67], a[192:195], v[192:195]
ds_read_b64_tr_b16 v[88:89], v27 offset:4096
v_mul_f32 v161, 0x3e0293ee, v161
v_mfma_f32_16x16x32_bf16 v[192:195], v[68:71], a[208:211], v[192:195]
ds_read_b64_tr_b16 v[90:91], v27 offset:4352
v_cvt_pk_bf16_f32 v48, v160, v161
v_mfma_f32_16x16x32_bf16 v[208:211], v[64:67], a[196:199], v[208:211]
v_mul_f32 v162, 0x3e0293ee, v162
v_mfma_f32_16x16x32_bf16 v[208:211], v[68:71], a[212:215], v[208:211]
s_bitcmp1_b32 %3, 2
s_cselect_b32 s76, 0x7f800000, s76
v_mfma_f32_16x16x32_bf16 v[224:227], v[64:67], a[200:203], v[224:227]
ds_read_b64_tr_b16 v[92:93], v27 offset:12288
v_mul_f32 v163, 0x3e0293ee, v163
v_mfma_f32_16x16x32_bf16 v[224:227], v[68:71], a[216:219], v[224:227]
ds_read_b64_tr_b16 v[94:95], v27 offset:12544
v_cvt_pk_bf16_f32 v49, v162, v163
v_mfma_f32_16x16x32_bf16 v[240:243], v[64:67], a[204:207], v[240:243]
v_mul_f32 v164, 0x3e0293ee, v164
v_mfma_f32_16x16x32_bf16 v[240:243], v[68:71], a[220:223], v[240:243]
v_mul_f32 v165, 0x3e0293ee, v165
s_waitcnt lgkmcnt(8)
v_mfma_f32_16x16x32_bf16 v[196:199], v[72:75], a[192:195], v[196:199]
ds_read_b64_tr_b16 v[64:65], v26 offset:16384
v_mfma_f32_16x16x32_bf16 v[196:199], v[76:79], a[208:211], v[196:199]
)"
R"(ds_read_b64_tr_b16 v[66:67], v26 offset:16640
v_cvt_pk_bf16_f32 v50, v164, v165
v_mfma_f32_16x16x32_bf16 v[212:215], v[72:75], a[196:199], v[212:215]
v_mul_f32 v166, 0x3e0293ee, v166
v_mfma_f32_16x16x32_bf16 v[212:215], v[76:79], a[212:215], v[212:215]
v_mul_f32 v167, 0x3e0293ee, v167
v_mfma_f32_16x16x32_bf16 v[228:231], v[72:75], a[200:203], v[228:231]
ds_read_b64_tr_b16 v[68:69], v26 offset:24576
v_mfma_f32_16x16x32_bf16 v[228:231], v[76:79], a[216:219], v[228:231]
ds_read_b64_tr_b16 v[70:71], v26 offset:24832
v_cvt_pk_bf16_f32 v51, v166, v167
v_mfma_f32_16x16x32_bf16 v[244:247], v[72:75], a[204:207], v[244:247]
v_mul_f32 v168, 0x3e0293ee, v168
v_mfma_f32_16x16x32_bf16 v[244:247], v[76:79], a[220:223], v[244:247]
v_mov_b32 v28, -1
s_waitcnt lgkmcnt(8)
v_mfma_f32_16x16x32_bf16 v[200:203], v[80:83], a[192:195], v[200:203]
ds_read_b64_tr_b16 v[72:73], v27 offset:16384
v_mov_b32 v29, -1
v_mfma_f32_16x16x32_bf16 v[200:203], v[84:87], a[208:211], v[200:203]
ds_read_b64_tr_b16 v[74:75], v27 offset:16640
v_mov_b32 v30, -1
v_mfma_f32_16x16x32_bf16 v[216:219], v[80:83], a[196:199], v[216:219]
v_mov_b32 v31, -1
s_lshl_b32 s77, s26, 2
v_mfma_f32_16x16x32_bf16 v[216:219], v[84:87], a[212:215], v[216:219]
s_add_u32 s77, s77, s31
s_lshl_b32 s77, s77, 2
v_mfma_f32_16x16x32_bf16 v[232:235], v[80:83], a[200:203], v[232:235]
ds_read_b64_tr_b16 v[76:77], v27 offset:24576
s_add_u32 s77, s77, %2
v_mfma_f32_16x16x32_bf16 v[232:235], v[84:87], a[216:219], v[232:235]
ds_read_b64_tr_b16 v[78:79], v27 offset:24832
s_lshl_b32 s77, s77, 13
v_mfma_f32_16x16x32_bf16 v[248:251], v[80:83], a[204:207], v[248:251]
s_and_b32 s44, %3, 4
s_lshl_b32 s44, s44, 22
v_mfma_f32_16x16x32_bf16 v[248:251], v[84:87], a[220:223], v[248:251]
s_add_u32 s77, s77, s44
s_waitcnt lgkmcnt(8)
v_mfma_f32_16x16x32_bf16 v[204:207], v[88:91], a[192:195], v[204:207]
ds_read_b64_tr_b16 v[80:81], v26 offset:20480
s_mov_b64 s[44:45], s[10:11]
v_mfma_f32_16x16x32_bf16 v[204:207], v[92:95], a[208:211], v[204:207]
ds_read_b64_tr_b16 v[82:83], v26 offset:20736
v_mul_f32 v169, 0x3e0293ee, v169
v_mfma_f32_16x16x32_bf16 v[220:223], v[88:91], a[196:199], v[220:223]
s_add_u32 s44, s44, 0x5001000
s_addc_u32 s45, s45, 0
PAD6_%=:
s_cbranch_execz PAD14_%=
v_mfma_f32_16x16x32_bf16 v[220:223], v[92:95], a[212:215], v[220:223]
s_add_u32 s44, s44, s77
s_addc_u32 s45, s45, 0
v_mfma_f32_16x16x32_bf16 v[236:239], v[88:91], a[200:203], v[236:239]
ds_read_b64_tr_b16 v[84:85], v26 offset:28672
v_cvt_pk_bf16_f32 v52, v168, v169
v_mfma_f32_16x16x32_bf16 v[236:239], v[92:95], a[216:219], v[236:239]
ds_read_b64_tr_b16 v[86:87], v26 offset:28928
v_mul_f32 v170, 0x3e0293ee, v170
v_mfma_f32_16x16x32_bf16 v[252:255], v[88:91], a[204:207], v[252:255]
v_mul_f32 v171, 0x3e0293ee, v171
v_cvt_pk_bf16_f32 v53, v170, v171
v_mfma_f32_16x16x32_bf16 v[252:255], v[92:95], a[220:223], v[252:255]
v_mul_f32 v172, 0x3e0293ee, v172
s_waitcnt lgkmcnt(8)
v_mfma_f32_16x16x32_bf16 v[192:195], v[64:67], a[224:227], v[192:195]
ds_read_b64_tr_b16 v[88:89], v27 offset:20480
v_mul_f32 v173, 0x3e0293ee, v173
v_mfma_f32_16x16x32_bf16 v[192:195], v[68:71], a[240:243], v[192:195]
ds_read_b64_tr_b16 v[90:91], v27 offset:20736
v_cvt_pk_bf16_f32 v54, v172, v173
v_mfma_f32_16x16x32_bf16 v[208:211], v[64:67], a[228:231], v[208:211]
v_mul_f32 v174, 0x3e0293ee, v174
v_mul_f32 v175, 0x3e0293ee, v175
v_mfma_f32_16x16x32_bf16 v[208:211], v[68:71], a[244:247], v[208:211]
v_cvt_pk_bf16_f32 v55, v174, v175
v_mul_f32 v176, 0x3e0293ee, v176
v_mfma_f32_16x16x32_bf16 v[224:227], v[64:67], a[232:235], v[224:227]
ds_read_b64_tr_b16 v[92:93], v27 offset:28672
v_mul_f32 v177, 0x3e0293ee, v177
v_mfma_f32_16x16x32_bf16 v[224:227], v[68:71], a[248:251], v[224:227]
ds_read_b64_tr_b16 v[94:95], v27 offset:28928
v_cvt_pk_bf16_f32 v56, v176, v177
v_mfma_f32_16x16x32_bf16 v[240:243], v[64:67], a[236:239], v[240:243]
v_mul_f32 v178, 0x3e0293ee, v178
v_mul_f32 v179, 0x3e0293ee, v179
v_mfma_f32_16x16x32_bf16 v[240:243], v[68:71], a[252:255], v[240:243]
v_cvt_pk_bf16_f32 v57, v178, v179
s_waitcnt lgkmcnt(8)
v_mfma_f32_16x16x32_bf16 v[196:199], v[72:75], a[224:227], v[196:199]
v_mul_f32 v180, 0x3e0293ee, v180
v_mul_f32 v181, 0x3e0293ee, v181
v_mfma_f32_16x16x32_bf16 v[196:199], v[76:79], a[240:243], v[196:199]
v_cvt_pk_bf16_f32 v58, v180, v181
v_mul_f32 v182, 0x3e0293ee, v182
v_mfma_f32_16x16x32_bf16 v[212:215], v[72:75], a[228:231], v[212:215]
v_mul_f32 v183, 0x3e0293ee, v183
v_mfma_f32_16x16x32_bf16 v[212:215], v[76:79], a[244:247], v[212:215]
v_cvt_pk_bf16_f32 v59, v182, v183
v_mfma_f32_16x16x32_bf16 v[228:231], v[72:75], a[232:235], v[228:231]
s_cmp_ge_u32 s46, s100
s_cselect_b32 s46, s101, s46
v_mfma_f32_16x16x32_bf16 v[228:231], v[76:79], a[248:251], v[228:231]
s_waitcnt vmcnt(0) lgkmcnt(0)
s_barrier
v_mfma_f32_16x16x32_bf16 v[244:247], v[72:75], a[236:239], v[244:247]
ds_read_b64_tr_b16 v[64:65], v98 offset:33280
s_cmp_lg_u32 %3, 0
s_cbranch_scc1 W0N12_%=
v_mov_b32 v24, 1
s_mov_b64 exec, 2
global_atomic_add v101, v24, s[92:93] sc1
s_mov_b64 exec, -1
W0N12_%=:
v_mfma_f32_16x16x32_bf16 v[244:247], v[76:79], a[252:255], v[244:247]
ds_read_b64_tr_b16 v[66:67], v98 offset:33536
v_mfma_f32_16x16x32_bf16 v[200:203], v[80:83], a[224:227], v[200:203]
ds_read_b64_tr_b16 v[68:69], v98 offset:41472
v_mul_f32 v184, 0x3e0293ee, v184
v_mfma_f32_16x16x32_bf16 v[200:203], v[84:87], a[240:243], v[200:203]
ds_read_b64_tr_b16 v[70:71], v98 offset:41728
v_mul_f32 v185, 0x3e0293ee, v185
v_mfma_f32_16x16x32_bf16 v[216:219], v[80:83], a[228:231], v[216:219]
ds_read_b64_tr_b16 v[72:73], v99 offset:33280
v_cvt_pk_bf16_f32 v60, v184, v185
v_mfma_f32_16x16x32_bf16 v[216:219], v[84:87], a[244:247], v[216:219]
ds_read_b64_tr_b16 v[74:75], v99 offset:33536
v_mul_f32 v186, 0x3e0293ee, v186
v_mfma_f32_16x16x32_bf16 v[232:235], v[80:83], a[232:235], v[232:235]
ds_read_b64_tr_b16 v[76:77], v99 offset:41472
v_mul_f32 v187, 0x3e0293ee, v187
v_mfma_f32_16x16x32_bf16 v[232:235], v[84:87], a[248:251], v[232:235]
ds_read_b64_tr_b16 v[78:79], v99 offset:41728
v_cvt_pk_bf16_f32 v61, v186, v187
v_mfma_f32_16x16x32_bf16 v[248:251], v[80:83], a[236:239], v[248:251]
global_store_dwordx4 v22, v[28:31], s[44:45] offset:-4096 sc1
v_mul_f32 v188, 0x3e0293ee, v188
v_mfma_f32_16x16x32_bf16 v[248:251], v[84:87], a[252:255], v[248:251]
v_mul_f32 v189, 0x3e0293ee, v189
v_mfma_f32_16x16x32_bf16 v[204:207], v[88:91], a[224:227], v[204:207]
ds_read_b64_tr_b16 v[80:81], v98 offset:37376
v_cvt_pk_bf16_f32 v62, v188, v189
v_mfma_f32_16x16x32_bf16 v[204:207], v[92:95], a[240:243], v[204:207]
ds_read_b64_tr_b16 v[82:83], v98 offset:37632
v_mul_f32 v190, 0x3e0293ee, v190
v_mfma_f32_16x16x32_bf16 v[220:223], v[88:91], a[228:231], v[220:223]
global_store_dwordx4 v22, v[28:31], s[44:45] offset:-3072 sc1
v_mul_f32 v191, 0x3e0293ee, v191
v_mfma_f32_16x16x32_bf16 v[220:223], v[92:95], a[244:247], v[220:223]
v_cvt_pk_bf16_f32 v63, v190, v191
s_add_u32 s46, s46, s29
v_mfma_f32_16x16x32_bf16 v[236:239], v[88:91], a[232:235], v[236:239]
ds_read_b64_tr_b16 v[84:85], v98 offset:45568
v_mfma_f32_16x16x32_bf16 v[236:239], v[92:95], a[248:251], v[236:239]
ds_read_b64_tr_b16 v[86:87], v98 offset:45824
v_mfma_f32_16x16x32_bf16 v[252:255], v[88:91], a[236:239], v[252:255]
global_store_dwordx4 v22, v[28:31], s[44:45] offset:-2048 sc1
v_mfma_f32_16x16x32_bf16 v[252:255], v[92:95], a[252:255], v[252:255]
s_waitcnt lgkmcnt(8)
v_mfma_f32_16x16x32_bf16 v[128:131], v[64:67], a[0:3], 0
ds_read_b64_tr_b16 v[88:89], v99 offset:37376
v_mfma_f32_16x16x32_bf16 v[128:131], v[68:71], a[16:19], v[128:131]
ds_read_b64_tr_b16 v[90:91], v99 offset:37632
v_mfma_f32_16x16x32_bf16 v[144:147], v[64:67], a[4:7], 0
global_store_dwordx4 v22, v[28:31], s[44:45] offset:-1024 sc1
v_mfma_f32_16x16x32_bf16 v[144:147], v[68:71], a[20:23], v[144:147]
s_cmp_ge_u32 s46, s100
s_cselect_b32 s46, s101, s46
v_mfma_f32_16x16x32_bf16 v[160:163], v[64:67], a[8:11], 0
ds_read_b64_tr_b16 v[92:93], v99 offset:45568
v_mul_f32 v124, v196, v196
v_mul_f32 v125, v197, v197
v_mfma_f32_16x16x32_bf16 v[160:163], v[68:71], a[24:27], v[160:163]
ds_read_b64_tr_b16 v[94:95], v99 offset:45824
v_mul_f32 v126, v198, v198
v_mul_f32 v127, v199, v199
v_mfma_f32_16x16x32_bf16 v[176:179], v[64:67], a[12:15], 0
global_store_dwordx4 v22, v[28:31], s[44:45] offset:0 sc1
v_fma_f32 v124, v124, s49, v119
v_fma_f32 v125, v125, s49, v119
v_mfma_f32_16x16x32_bf16 v[176:179], v[68:71], a[28:31], v[176:179]
v_fma_f32 v126, v126, s49, v119
v_fma_f32 v127, v127, s49, v119
s_waitcnt lgkmcnt(8)
v_mfma_f32_16x16x32_bf16 v[132:135], v[72:75], a[0:3], 0
ds_read_b64_tr_b16 v[64:65], v98 offset:49664
v_mul_f32 v124, v124, v196
v_mul_f32 v125, v125, v197
v_mfma_f32_16x16x32_bf16 v[132:135], v[76:79], a[16:19], v[132:135]
ds_read_b64_tr_b16 v[66:67], v98 offset:49920
v_exp_f32 v124, v124
v_mfma_f32_16x16x32_bf16 v[148:151], v[72:75], a[4:7], 0
global_store_dwordx4 v22, v[28:31], s[44:45] offset:1024 sc1
v_exp_f32 v125, v125
v_mfma_f32_16x16x32_bf16 v[148:151], v[76:79], a[20:23], v[148:151]
v_mul_f32 v126, v126, v198
v_exp_f32 v126, v126
v_mfma_f32_16x16x32_bf16 v[164:167], v[72:75], a[8:11], 0
ds_read_b64_tr_b16 v[68:69], v98 offset:57856
v_mul_f32 v127, v127, v199
v_mul_f32 v204, v204, v196
v_mfma_f32_16x16x32_bf16 v[164:167], v[76:79], a[24:27], v[164:167]
ds_read_b64_tr_b16 v[70:71], v98 offset:58112
v_exp_f32 v127, v127
v_mfma_f32_16x16x32_bf16 v[180:183], v[72:75], a[12:15], 0
v_mul_f32 v205, v205, v197
v_mul_f32 v206, v206, v198
v_mul_f32 v207, v207, v199
v_mfma_f32_16x16x32_bf16 v[180:183], v[76:79], a[28:31], v[180:183]
v_add_f32 v124, 1.0, v124
v_add_f32 v125, 1.0, v125
s_waitcnt lgkmcnt(8)
v_mfma_f32_16x16x32_bf16 v[136:139], v[80:83], a[0:3], 0
ds_read_b64_tr_b16 v[72:73], v99 offset:49664
v_rcp_f32 v124, v124
v_mfma_f32_16x16x32_bf16 v[136:139], v[84:87], a[16:19], v[136:139]
ds_read_b64_tr_b16 v[74:75], v99 offset:49920
v_rcp_f32 v125, v125
v_mfma_f32_16x16x32_bf16 v[152:155], v[80:83], a[4:7], 0
v_add_f32 v126, 1.0, v126
v_rcp_f32 v126, v126
v_mfma_f32_16x16x32_bf16 v[152:155], v[84:87], a[20:23], v[152:155]
v_add_f32 v127, 1.0, v127
v_rcp_f32 v127, v127
v_mfma_f32_16x16x32_bf16 v[168:171], v[80:83], a[8:11], 0
ds_read_b64_tr_b16 v[76:77], v99 offset:57856
v_mul_f32 v204, v204, v124
v_mfma_f32_16x16x32_bf16 v[168:171], v[84:87], a[24:27], v[168:171]
ds_read_b64_tr_b16 v[78:79], v99 offset:58112
v_mul_f32 v205, v205, v125
v_mfma_f32_16x16x32_bf16 v[184:187], v[80:83], a[12:15], 0
v_mul_f32 v206, v206, v126
v_mul_f32 v207, v207, v127
v_mfma_f32_16x16x32_bf16 v[184:187], v[84:87], a[28:31], v[184:187]
v_cvt_pk_bf16_f32 v207, v206, v207
s_waitcnt lgkmcnt(8)
v_mfma_f32_16x16x32_bf16 v[140:143], v[88:91], a[0:3], 0
ds_read_b64_tr_b16 v[80:81], v98 offset:53760
v_cvt_pk_bf16_f32 v206, v204, v205
v_mfma_f32_16x16x32_bf16 v[140:143], v[92:95], a[16:19], v[140:143]
ds_read_b64_tr_b16 v[82:83], v98 offset:54016
v_mul_f32 v120, v192, v192
v_mfma_f32_16x16x32_bf16 v[156:159], v[88:91], a[4:7], 0
v_mul_f32 v121, v193, v193
v_mul_f32 v122, v194, v194
v_mfma_f32_16x16x32_bf16 v[156:159], v[92:95], a[20:23], v[156:159]
v_mul_f32 v123, v195, v195
v_fma_f32 v120, v120, s49, v119
v_mfma_f32_16x16x32_bf16 v[172:175], v[88:91], a[8:11], 0
ds_read_b64_tr_b16 v[84:85], v98 offset:61952
v_fma_f32 v121, v121, s49, v119
v_mfma_f32_16x16x32_bf16 v[172:175], v[92:95], a[24:27], v[172:175]
ds_read_b64_tr_b16 v[86:87], v98 offset:62208
v_fma_f32 v122, v122, s49, v119
v_mfma_f32_16x16x32_bf16 v[188:191], v[88:91], a[12:15], 0
v_fma_f32 v123, v123, s49, v119
v_mul_f32 v120, v120, v192
v_mfma_f32_16x16x32_bf16 v[188:191], v[92:95], a[28:31], v[188:191]
v_mul_f32 v121, v121, v193
s_waitcnt lgkmcnt(8)
v_mfma_f32_16x16x32_bf16 v[128:131], v[64:67], a[32:35], v[128:131]
ds_read_b64_tr_b16 v[88:89], v99 offset:53760
v_mul_f32 v122, v122, v194
v_mfma_f32_16x16x32_bf16 v[128:131], v[68:71], a[48:51], v[128:131]
ds_read_b64_tr_b16 v[90:91], v99 offset:54016
v_mul_f32 v123, v123, v195
v_mfma_f32_16x16x32_bf16 v[144:147], v[64:67], a[36:39], v[144:147]
v_exp_f32 v120, v120
v_mfma_f32_16x16x32_bf16 v[144:147], v[68:71], a[52:55], v[144:147]
v_exp_f32 v121, v121
v_mfma_f32_16x16x32_bf16 v[160:163], v[64:67], a[40:43], v[160:163]
ds_read_b64_tr_b16 v[92:93], v99 offset:61952
v_mul_f32 v200, v200, v192
v_mfma_f32_16x16x32_bf16 v[160:163], v[68:71], a[56:59], v[160:163]
ds_read_b64_tr_b16 v[94:95], v99 offset:62208
v_mul_f32 v201, v201, v193
v_mfma_f32_16x16x32_bf16 v[176:179], v[64:67], a[44:47], v[176:179]
v_exp_f32 v122, v122
v_mfma_f32_16x16x32_bf16 v[176:179], v[68:71], a[60:63], v[176:179]
v_mul_f32 v202, v202, v194
s_waitcnt lgkmcnt(8)
v_mfma_f32_16x16x32_bf16 v[132:135], v[72:75], a[32:35], v[132:135]
ds_read_b64_tr_b16 v[64:65], v96 offset:512
v_mul_f32 v203, v203, v195
v_mfma_f32_16x16x32_bf16 v[132:135], v[76:79], a[48:51], v[132:135]
ds_read_b64_tr_b16 v[66:67], v96 offset:768
v_add_f32 v120, 1.0, v120
v_mfma_f32_16x16x32_bf16 v[148:151], v[72:75], a[36:39], v[148:151]
v_exp_f32 v123, v123
v_mfma_f32_16x16x32_bf16 v[148:151], v[76:79], a[52:55], v[148:151]
v_rcp_f32 v120, v120
v_mfma_f32_16x16x32_bf16 v[164:167], v[72:75], a[40:43], v[164:167]
ds_read_b64_tr_b16 v[68:69], v96 offset:8704
v_add_f32 v121, 1.0, v121
v_mfma_f32_16x16x32_bf16 v[164:167], v[76:79], a[56:59], v[164:167]
ds_read_b64_tr_b16 v[70:71], v96 offset:8960
v_add_f32 v122, 1.0, v122
v_mfma_f32_16x16x32_bf16 v[180:183], v[72:75], a[44:47], v[180:183]
v_rcp_f32 v121, v121
v_mfma_f32_16x16x32_bf16 v[180:183], v[76:79], a[60:63], v[180:183]
v_add_f32 v123, 1.0, v123
s_waitcnt lgkmcnt(8)
v_mfma_f32_16x16x32_bf16 v[136:139], v[80:83], a[32:35], v[136:139]
ds_read_b64_tr_b16 v[72:73], v97 offset:512
v_mul_f32 v200, v200, v120
v_mfma_f32_16x16x32_bf16 v[136:139], v[84:87], a[48:51], v[136:139]
ds_read_b64_tr_b16 v[74:75], v97 offset:768
v_mul_f32 v201, v201, v121
v_mfma_f32_16x16x32_bf16 v[152:155], v[80:83], a[36:39], v[152:155]
v_rcp_f32 v122, v122
v_mfma_f32_16x16x32_bf16 v[152:155], v[84:87], a[52:55], v[152:155]
v_rcp_f32 v123, v123
v_mfma_f32_16x16x32_bf16 v[168:171], v[80:83], a[40:43], v[168:171]
ds_read_b64_tr_b16 v[76:77], v97 offset:8704
v_mul_f32 v202, v202, v122
v_mfma_f32_16x16x32_bf16 v[168:171], v[84:87], a[56:59], v[168:171]
ds_read_b64_tr_b16 v[78:79], v97 offset:8960
v_mul_f32 v203, v203, v123
v_mfma_f32_16x16x32_bf16 v[184:187], v[80:83], a[44:47], v[184:187]
v_cvt_pk_bf16_f32 v205, v202, v203
v_cvt_pk_bf16_f32 v204, v200, v201
v_mfma_f32_16x16x32_bf16 v[184:187], v[84:87], a[60:63], v[184:187]
global_store_dwordx4 v22, v[204:207], s[94:95] offset:0 sc1
v_mul_f32 v124, v212, v212
s_waitcnt lgkmcnt(8)
v_mfma_f32_16x16x32_bf16 v[140:143], v[88:91], a[32:35], v[140:143]
ds_read_b64_tr_b16 v[80:81], v96 offset:4608
v_mul_f32 v125, v213, v213
v_mfma_f32_16x16x32_bf16 v[140:143], v[92:95], a[48:51], v[140:143]
ds_read_b64_tr_b16 v[82:83], v96 offset:4864
v_mul_f32 v126, v214, v214
v_mfma_f32_16x16x32_bf16 v[156:159], v[88:91], a[36:39], v[156:159]
v_mul_f32 v127, v215, v215
v_fma_f32 v124, v124, s49, v119
v_mfma_f32_16x16x32_bf16 v[156:159], v[92:95], a[52:55], v[156:159]
v_fma_f32 v125, v125, s49, v119
v_fma_f32 v126, v126, s49, v119
v_mfma_f32_16x16x32_bf16 v[172:175], v[88:91], a[40:43], v[172:175]
ds_read_b64_tr_b16 v[84:85], v96 offset:12800
v_fma_f32 v127, v127, s49, v119
v_mfma_f32_16x16x32_bf16 v[172:175], v[92:95], a[56:59], v[172:175]
ds_read_b64_tr_b16 v[86:87], v96 offset:13056
v_mul_f32 v124, v124, v212
v_mfma_f32_16x16x32_bf16 v[188:191], v[88:91], a[44:47], v[188:191]
v_exp_f32 v124, v124
v_mfma_f32_16x16x32_bf16 v[188:191], v[92:95], a[60:63], v[188:191]
v_mul_f32 v125, v125, v213
s_waitcnt lgkmcnt(8)
v_mfma_f32_16x16x32_bf16 v[128:131], v[64:67], a[64:67], v[128:131]
ds_read_b64_tr_b16 v[88:89], v97 offset:4608
v_mul_f32 v126, v126, v214
v_mfma_f32_16x16x32_bf16 v[128:131], v[68:71], a[80:83], v[128:131]
ds_read_b64_tr_b16 v[90:91], v97 offset:4864
v_mul_f32 v127, v127, v215
v_mfma_f32_16x16x32_bf16 v[144:147], v[64:67], a[68:71], v[144:147]
v_exp_f32 v125, v125
v_mfma_f32_16x16x32_bf16 v[144:147], v[68:71], a[84:87], v[144:147]
v_exp_f32 v126, v126
v_mfma_f32_16x16x32_bf16 v[160:163], v[64:67], a[72:75], v[160:163]
ds_read_b64_tr_b16 v[92:93], v97 offset:12800
v_mul_f32 v220, v220, v212
v_mfma_f32_16x16x32_bf16 v[160:163], v[68:71], a[88:91], v[160:163]
ds_read_b64_tr_b16 v[94:95], v97 offset:13056
v_mul_f32 v221, v221, v213
v_mfma_f32_16x16x32_bf16 v[176:179], v[64:67], a[76:79], v[176:179]
v_exp_f32 v127, v127
v_mfma_f32_16x16x32_bf16 v[176:179], v[68:71], a[92:95], v[176:179]
v_mul_f32 v222, v222, v214
s_waitcnt lgkmcnt(8)
v_mfma_f32_16x16x32_bf16 v[132:135], v[72:75], a[64:67], v[132:135]
ds_read_b64_tr_b16 v[64:65], v96 offset:16896
v_mul_f32 v223, v223, v215
v_mfma_f32_16x16x32_bf16 v[132:135], v[76:79], a[80:83], v[132:135]
ds_read_b64_tr_b16 v[66:67], v96 offset:17152
v_add_f32 v124, 1.0, v124
v_mfma_f32_16x16x32_bf16 v[148:151], v[72:75], a[68:71], v[148:151]
v_rcp_f32 v124, v124
v_mfma_f32_16x16x32_bf16 v[148:151], v[76:79], a[84:87], v[148:151]
v_add_f32 v125, 1.0, v125
v_add_f32 v126, 1.0, v126
v_mfma_f32_16x16x32_bf16 v[164:167], v[72:75], a[72:75], v[164:167]
ds_read_b64_tr_b16 v[68:69], v96 offset:25088
v_add_f32 v127, 1.0, v127
v_mfma_f32_16x16x32_bf16 v[164:167], v[76:79], a[88:91], v[164:167]
ds_read_b64_tr_b16 v[70:71], v96 offset:25344
v_mul_f32 v220, v220, v124
v_mfma_f32_16x16x32_bf16 v[180:183], v[72:75], a[76:79], v[180:183]
v_rcp_f32 v125, v125
v_mfma_f32_16x16x32_bf16 v[180:183], v[76:79], a[92:95], v[180:183]
v_mul_f32 v221, v221, v125
s_waitcnt lgkmcnt(8)
v_mfma_f32_16x16x32_bf16 v[136:139], v[80:83], a[64:67], v[136:139]
ds_read_b64_tr_b16 v[72:73], v97 offset:16896
v_mul_f32 v120, v208, v208
v_mfma_f32_16x16x32_bf16 v[136:139], v[84:87], a[80:83], v[136:139]
ds_read_b64_tr_b16 v[74:75], v97 offset:17152
v_mul_f32 v121, v209, v209
v_mfma_f32_16x16x32_bf16 v[152:155], v[80:83], a[68:71], v[152:155]
v_rcp_f32 v126, v126
)"
R"(v_mfma_f32_16x16x32_bf16 v[152:155], v[84:87], a[84:87], v[152:155]
v_rcp_f32 v127, v127
v_mfma_f32_16x16x32_bf16 v[168:171], v[80:83], a[72:75], v[168:171]
ds_read_b64_tr_b16 v[76:77], v97 offset:25088
v_mul_f32 v222, v222, v126
v_mfma_f32_16x16x32_bf16 v[168:171], v[84:87], a[88:91], v[168:171]
ds_read_b64_tr_b16 v[78:79], v97 offset:25344
v_mul_f32 v223, v223, v127
v_mfma_f32_16x16x32_bf16 v[184:187], v[80:83], a[76:79], v[184:187]
v_cvt_pk_bf16_f32 v223, v222, v223
v_cvt_pk_bf16_f32 v222, v220, v221
v_mfma_f32_16x16x32_bf16 v[184:187], v[84:87], a[92:95], v[184:187]
v_mul_f32 v122, v210, v210
s_waitcnt lgkmcnt(8)
v_mfma_f32_16x16x32_bf16 v[140:143], v[88:91], a[64:67], v[140:143]
ds_read_b64_tr_b16 v[80:81], v96 offset:20992
v_mul_f32 v123, v211, v211
v_mfma_f32_16x16x32_bf16 v[140:143], v[92:95], a[80:83], v[140:143]
ds_read_b64_tr_b16 v[82:83], v96 offset:21248
v_fma_f32 v120, v120, s49, v119
v_mfma_f32_16x16x32_bf16 v[156:159], v[88:91], a[68:71], v[156:159]
v_fma_f32 v121, v121, s49, v119
v_fma_f32 v122, v122, s49, v119
v_mfma_f32_16x16x32_bf16 v[156:159], v[92:95], a[84:87], v[156:159]
v_fma_f32 v123, v123, s49, v119
v_mul_f32 v120, v120, v208
v_mfma_f32_16x16x32_bf16 v[172:175], v[88:91], a[72:75], v[172:175]
ds_read_b64_tr_b16 v[84:85], v96 offset:29184
v_mul_f32 v121, v121, v209
v_mfma_f32_16x16x32_bf16 v[172:175], v[92:95], a[88:91], v[172:175]
ds_read_b64_tr_b16 v[86:87], v96 offset:29440
v_mul_f32 v122, v122, v210
v_mfma_f32_16x16x32_bf16 v[188:191], v[88:91], a[76:79], v[188:191]
v_exp_f32 v120, v120
v_mfma_f32_16x16x32_bf16 v[188:191], v[92:95], a[92:95], v[188:191]
v_mul_f32 v123, v123, v211
s_waitcnt lgkmcnt(8)
v_mfma_f32_16x16x32_bf16 v[128:131], v[64:67], a[96:99], v[128:131]
ds_read_b64_tr_b16 v[88:89], v97 offset:20992
v_mul_f32 v216, v216, v208
v_mfma_f32_16x16x32_bf16 v[128:131], v[68:71], a[112:115], v[128:131]
ds_read_b64_tr_b16 v[90:91], v97 offset:21248
v_mul_f32 v217, v217, v209
v_mfma_f32_16x16x32_bf16 v[144:147], v[64:67], a[100:103], v[144:147]
v_exp_f32 v121, v121
v_mfma_f32_16x16x32_bf16 v[144:147], v[68:71], a[116:119], v[144:147]
v_exp_f32 v122, v122
v_mfma_f32_16x16x32_bf16 v[160:163], v[64:67], a[104:107], v[160:163]
ds_read_b64_tr_b16 v[92:93], v97 offset:29184
v_mul_f32 v218, v218, v210
v_mfma_f32_16x16x32_bf16 v[160:163], v[68:71], a[120:123], v[160:163]
ds_read_b64_tr_b16 v[94:95], v97 offset:29440
v_mul_f32 v219, v219, v211
v_mfma_f32_16x16x32_bf16 v[176:179], v[64:67], a[108:111], v[176:179]
v_exp_f32 v123, v123
v_mfma_f32_16x16x32_bf16 v[176:179], v[68:71], a[124:127], v[176:179]
v_add_f32 v120, 1.0, v120
s_waitcnt lgkmcnt(8)
v_mfma_f32_16x16x32_bf16 v[132:135], v[72:75], a[96:99], v[132:135]
v_rcp_f32 v120, v120
v_mfma_f32_16x16x32_bf16 v[132:135], v[76:79], a[112:115], v[132:135]
v_add_f32 v121, 1.0, v121
v_add_f32 v122, 1.0, v122
v_mfma_f32_16x16x32_bf16 v[148:151], v[72:75], a[100:103], v[148:151]
v_rcp_f32 v121, v121
v_mfma_f32_16x16x32_bf16 v[148:151], v[76:79], a[116:119], v[148:151]
v_rcp_f32 v122, v122
v_mfma_f32_16x16x32_bf16 v[164:167], v[72:75], a[104:107], v[164:167]
v_add_f32 v123, 1.0, v123
v_mul_f32 v216, v216, v120
v_mfma_f32_16x16x32_bf16 v[164:167], v[76:79], a[120:123], v[164:167]
s_waitcnt lgkmcnt(0)
s_barrier
v_mfma_f32_16x16x32_bf16 v[180:183], v[72:75], a[108:111], v[180:183]
ds_read_b64_tr_b16 v[64:65], v98 offset:512
s_cmp_lg_u32 %3, 0
s_cbranch_scc1 W0N13_%=
s_mov_b64 exec, 2
global_load_dword v24, v101, s[92:93] offset:0 sc1
s_mov_b64 exec, -1
W0N13_%=:
v_mfma_f32_16x16x32_bf16 v[180:183], v[76:79], a[124:127], v[180:183]
ds_read_b64_tr_b16 v[66:67], v98 offset:768
v_mfma_f32_16x16x32_bf16 v[136:139], v[80:83], a[96:99], v[136:139]
ds_read_b64_tr_b16 v[68:69], v98 offset:8704
s_add_u32 m0, s75, 98304
v_mfma_f32_16x16x32_bf16 v[136:139], v[84:87], a[112:115], v[136:139]
ds_read_b64_tr_b16 v[70:71], v98 offset:8960
s_xor_b32 s48, s65, 2097280
v_mfma_f32_16x16x32_bf16 v[152:155], v[80:83], a[100:103], v[152:155]
ds_read_b64_tr_b16 v[72:73], v99 offset:512
v_mul_f32 v217, v217, v121
v_mfma_f32_16x16x32_bf16 v[152:155], v[84:87], a[116:119], v[152:155]
ds_read_b64_tr_b16 v[74:75], v99 offset:768
v_mul_f32 v218, v218, v122
v_mfma_f32_16x16x32_bf16 v[168:171], v[80:83], a[104:107], v[168:171]
ds_read_b64_tr_b16 v[76:77], v99 offset:8704
v_cvt_pk_bf16_f32 v220, v216, v217
v_mfma_f32_16x16x32_bf16 v[168:171], v[84:87], a[120:123], v[168:171]
ds_read_b64_tr_b16 v[78:79], v99 offset:8960
v_mul_f32 v124, v228, v228
v_mfma_f32_16x16x32_bf16 v[184:187], v[80:83], a[108:111], v[184:187]
buffer_load_dwordx4 v104, s[84:87], s48 offen offset:0 lds
v_mul_f32 v125, v229, v229
v_mfma_f32_16x16x32_bf16 v[184:187], v[84:87], a[124:127], v[184:187]
v_mul_f32 v126, v230, v230
v_mfma_f32_16x16x32_bf16 v[140:143], v[88:91], a[96:99], v[140:143]
ds_read_b64_tr_b16 v[80:81], v98 offset:4608
v_mul_f32 v127, v231, v231
v_mfma_f32_16x16x32_bf16 v[140:143], v[92:95], a[112:115], v[140:143]
ds_read_b64_tr_b16 v[82:83], v98 offset:4864
v_fma_f32 v124, v124, s49, v119
v_mfma_f32_16x16x32_bf16 v[156:159], v[88:91], a[100:103], v[156:159]
buffer_load_dwordx4 v105, s[84:87], s48 offen offset:1024 lds
v_fma_f32 v125, v125, s49, v119
v_mfma_f32_16x16x32_bf16 v[156:159], v[92:95], a[116:119], v[156:159]
v_rcp_f32 v123, v123
v_mfma_f32_16x16x32_bf16 v[172:175], v[88:91], a[104:107], v[172:175]
ds_read_b64_tr_b16 v[84:85], v98 offset:12800
v_mul_f32 v219, v219, v123
v_mfma_f32_16x16x32_bf16 v[172:175], v[92:95], a[120:123], v[172:175]
ds_read_b64_tr_b16 v[86:87], v98 offset:13056
v_cvt_pk_bf16_f32 v221, v218, v219
v_mfma_f32_16x16x32_bf16 v[188:191], v[88:91], a[108:111], v[188:191]
buffer_load_dwordx4 v106, s[84:87], s48 offen offset:2048 lds
v_fma_f32 v126, v126, s49, v119
v_mfma_f32_16x16x32_bf16 v[188:191], v[92:95], a[124:127], v[188:191]
global_store_dwordx4 v22, v[220:223], s[94:95] offset:1024 sc1
v_fma_f32 v127, v127, s49, v119
s_waitcnt lgkmcnt(8)
v_mfma_f32_16x16x32_bf16 v[128:131], v[64:67], a[128:131], v[128:131]
ds_read_b64_tr_b16 v[88:89], v99 offset:4608
v_mul_f32 v124, v124, v228
v_mfma_f32_16x16x32_bf16 v[128:131], v[68:71], a[144:147], v[128:131]
ds_read_b64_tr_b16 v[90:91], v99 offset:4864
v_mul_f32 v125, v125, v229
v_mfma_f32_16x16x32_bf16 v[144:147], v[64:67], a[132:135], v[144:147]
buffer_load_dwordx4 v107, s[84:87], s48 offen offset:3072 lds
v_mul_f32 v126, v126, v230
v_mfma_f32_16x16x32_bf16 v[144:147], v[68:71], a[148:151], v[144:147]
s_add_u32 m0, s75, 102400
s_xor_b32 s48, s65, 2359424
v_mfma_f32_16x16x32_bf16 v[160:163], v[64:67], a[136:139], v[160:163]
ds_read_b64_tr_b16 v[92:93], v99 offset:12800
v_mul_f32 v127, v127, v231
v_mfma_f32_16x16x32_bf16 v[160:163], v[68:71], a[152:155], v[160:163]
ds_read_b64_tr_b16 v[94:95], v99 offset:13056
v_mul_f32 v236, v236, v228
v_mfma_f32_16x16x32_bf16 v[176:179], v[64:67], a[140:143], v[176:179]
buffer_load_dwordx4 v104, s[84:87], s48 offen offset:0 lds
v_mul_f32 v237, v237, v229
v_mfma_f32_16x16x32_bf16 v[176:179], v[68:71], a[156:159], v[176:179]
v_mul_f32 v238, v238, v230
s_waitcnt lgkmcnt(8)
v_mfma_f32_16x16x32_bf16 v[132:135], v[72:75], a[128:131], v[132:135]
ds_read_b64_tr_b16 v[64:65], v98 offset:16896
v_mul_f32 v239, v239, v231
v_mfma_f32_16x16x32_bf16 v[132:135], v[76:79], a[144:147], v[132:135]
ds_read_b64_tr_b16 v[66:67], v98 offset:17152
v_mul_f32 v120, v224, v224
v_mfma_f32_16x16x32_bf16 v[148:151], v[72:75], a[132:135], v[148:151]
buffer_load_dwordx4 v105, s[84:87], s48 offen offset:1024 lds
v_mul_f32 v121, v225, v225
v_mfma_f32_16x16x32_bf16 v[148:151], v[76:79], a[148:151], v[148:151]
v_exp_f32 v124, v124
v_mfma_f32_16x16x32_bf16 v[164:167], v[72:75], a[136:139], v[164:167]
ds_read_b64_tr_b16 v[68:69], v98 offset:25088
v_add_f32 v124, 1.0, v124
v_mfma_f32_16x16x32_bf16 v[164:167], v[76:79], a[152:155], v[164:167]
ds_read_b64_tr_b16 v[70:71], v98 offset:25344
v_mul_f32 v122, v226, v226
v_mfma_f32_16x16x32_bf16 v[180:183], v[72:75], a[140:143], v[180:183]
buffer_load_dwordx4 v106, s[84:87], s48 offen offset:2048 lds
v_mul_f32 v123, v227, v227
v_mfma_f32_16x16x32_bf16 v[180:183], v[76:79], a[156:159], v[180:183]
v_fma_f32 v120, v120, s49, v119
s_waitcnt lgkmcnt(8)
v_mfma_f32_16x16x32_bf16 v[136:139], v[80:83], a[128:131], v[136:139]
ds_read_b64_tr_b16 v[72:73], v99 offset:16896
v_fma_f32 v121, v121, s49, v119
v_mfma_f32_16x16x32_bf16 v[136:139], v[84:87], a[144:147], v[136:139]
ds_read_b64_tr_b16 v[74:75], v99 offset:17152
v_fma_f32 v122, v122, s49, v119
v_mfma_f32_16x16x32_bf16 v[152:155], v[80:83], a[132:135], v[152:155]
v_exp_f32 v125, v125
v_mfma_f32_16x16x32_bf16 v[152:155], v[84:87], a[148:151], v[152:155]
buffer_load_dwordx4 v107, s[84:87], s48 offen offset:3072 lds
v_add_f32 v125, 1.0, v125
v_mfma_f32_16x16x32_bf16 v[168:171], v[80:83], a[136:139], v[168:171]
ds_read_b64_tr_b16 v[76:77], v99 offset:25088
v_fma_f32 v123, v123, s49, v119
v_mfma_f32_16x16x32_bf16 v[168:171], v[84:87], a[152:155], v[168:171]
ds_read_b64_tr_b16 v[78:79], v99 offset:25344
v_mul_f32 v120, v120, v224
v_mfma_f32_16x16x32_bf16 v[184:187], v[80:83], a[140:143], v[184:187]
v_exp_f32 v126, v126
v_mfma_f32_16x16x32_bf16 v[184:187], v[84:87], a[156:159], v[184:187]
v_add_f32 v126, 1.0, v126
s_waitcnt lgkmcnt(8)
v_mfma_f32_16x16x32_bf16 v[140:143], v[88:91], a[128:131], v[140:143]
ds_read_b64_tr_b16 v[80:81], v98 offset:20992
v_mul_f32 v121, v121, v225
v_mfma_f32_16x16x32_bf16 v[140:143], v[92:95], a[144:147], v[140:143]
ds_read_b64_tr_b16 v[82:83], v98 offset:21248
v_mul_f32 v122, v122, v226
v_mfma_f32_16x16x32_bf16 v[156:159], v[88:91], a[132:135], v[156:159]
v_exp_f32 v127, v127
v_mfma_f32_16x16x32_bf16 v[156:159], v[92:95], a[148:151], v[156:159]
v_rcp_f32 v124, v124
v_mfma_f32_16x16x32_bf16 v[172:175], v[88:91], a[136:139], v[172:175]
ds_read_b64_tr_b16 v[84:85], v98 offset:29184
s_cmp_lg_u32 %3, 0
s_cbranch_scc1 W0N14_%=
s_waitcnt vmcnt(9)
v_cmp_ne_u32 vcc, s90, v24
s_and_b32 s91, vcc_lo, 2
s_cbranch_scc1 HSP1N_%=
HOK1N_%=:
buffer_inv sc1
W0N14_%=:
v_mfma_f32_16x16x32_bf16 v[172:175], v[92:95], a[152:155], v[172:175]
ds_read_b64_tr_b16 v[86:87], v98 offset:29440
v_add_f32 v127, 1.0, v127
v_mfma_f32_16x16x32_bf16 v[188:191], v[88:91], a[140:143], v[188:191]
v_rcp_f32 v125, v125
v_mfma_f32_16x16x32_bf16 v[188:191], v[92:95], a[156:159], v[188:191]
v_mul_f32 v236, v236, v124
s_waitcnt lgkmcnt(8)
v_mfma_f32_16x16x32_bf16 v[128:131], v[64:67], a[160:163], v[128:131]
ds_read_b64_tr_b16 v[88:89], v99 offset:20992
v_mul_f32 v237, v237, v125
v_mfma_f32_16x16x32_bf16 v[128:131], v[68:71], a[176:179], v[128:131]
ds_read_b64_tr_b16 v[90:91], v99 offset:21248
v_cvt_pk_bf16_f32 v110, v236, v237
v_mfma_f32_16x16x32_bf16 v[144:147], v[64:67], a[164:167], v[144:147]
v_rcp_f32 v126, v126
v_mfma_f32_16x16x32_bf16 v[144:147], v[68:71], a[180:183], v[144:147]
v_rcp_f32 v127, v127
v_mfma_f32_16x16x32_bf16 v[160:163], v[64:67], a[168:171], v[160:163]
ds_read_b64_tr_b16 v[92:93], v99 offset:29184
v_mul_f32 v238, v238, v126
v_mfma_f32_16x16x32_bf16 v[160:163], v[68:71], a[184:187], v[160:163]
ds_read_b64_tr_b16 v[94:95], v99 offset:29440
v_mul_f32 v239, v239, v127
v_mfma_f32_16x16x32_bf16 v[176:179], v[64:67], a[172:175], v[176:179]
v_exp_f32 v120, v120
v_mfma_f32_16x16x32_bf16 v[176:179], v[68:71], a[188:191], v[176:179]
v_cvt_pk_bf16_f32 v111, v238, v239
s_waitcnt lgkmcnt(8)
v_mfma_f32_16x16x32_bf16 v[132:135], v[72:75], a[160:163], v[132:135]
ds_read_b64_tr_b16 v[64:65], v26 offset:512
v_mul_f32 v123, v123, v227
v_mfma_f32_16x16x32_bf16 v[132:135], v[76:79], a[176:179], v[132:135]
ds_read_b64_tr_b16 v[66:67], v26 offset:768
v_mul_f32 v232, v232, v224
v_mfma_f32_16x16x32_bf16 v[148:151], v[72:75], a[164:167], v[148:151]
v_exp_f32 v121, v121
v_mfma_f32_16x16x32_bf16 v[148:151], v[76:79], a[180:183], v[148:151]
v_exp_f32 v122, v122
v_mfma_f32_16x16x32_bf16 v[164:167], v[72:75], a[168:171], v[164:167]
ds_read_b64_tr_b16 v[68:69], v26 offset:8704
v_mul_f32 v233, v233, v225
v_mfma_f32_16x16x32_bf16 v[164:167], v[76:79], a[184:187], v[164:167]
ds_read_b64_tr_b16 v[70:71], v26 offset:8960
v_mul_f32 v234, v234, v226
v_mfma_f32_16x16x32_bf16 v[180:183], v[72:75], a[172:175], v[180:183]
v_exp_f32 v123, v123
PAD7_%=:
s_cbranch_execz PAD15_%=
v_mfma_f32_16x16x32_bf16 v[180:183], v[76:79], a[188:191], v[180:183]
v_mul_f32 v235, v235, v227
s_waitcnt lgkmcnt(8)
v_mfma_f32_16x16x32_bf16 v[136:139], v[80:83], a[160:163], v[136:139]
ds_read_b64_tr_b16 v[72:73], v27 offset:512
v_add_f32 v120, 1.0, v120
v_mfma_f32_16x16x32_bf16 v[136:139], v[84:87], a[176:179], v[136:139]
ds_read_b64_tr_b16 v[74:75], v27 offset:768
v_add_f32 v121, 1.0, v121
v_mfma_f32_16x16x32_bf16 v[152:155], v[80:83], a[164:167], v[152:155]
v_rcp_f32 v120, v120
v_mfma_f32_16x16x32_bf16 v[152:155], v[84:87], a[180:183], v[152:155]
v_rcp_f32 v121, v121
v_mfma_f32_16x16x32_bf16 v[168:171], v[80:83], a[168:171], v[168:171]
ds_read_b64_tr_b16 v[76:77], v27 offset:8704
v_add_f32 v122, 1.0, v122
v_mfma_f32_16x16x32_bf16 v[168:171], v[84:87], a[184:187], v[168:171]
ds_read_b64_tr_b16 v[78:79], v27 offset:8960
v_add_f32 v123, 1.0, v123
v_mfma_f32_16x16x32_bf16 v[184:187], v[80:83], a[172:175], v[184:187]
v_rcp_f32 v122, v122
v_mfma_f32_16x16x32_bf16 v[184:187], v[84:87], a[188:191], v[184:187]
v_mul_f32 v232, v232, v120
s_waitcnt lgkmcnt(8)
v_mfma_f32_16x16x32_bf16 v[140:143], v[88:91], a[160:163], v[140:143]
ds_read_b64_tr_b16 v[80:81], v26 offset:4608
v_mul_f32 v233, v233, v121
v_mfma_f32_16x16x32_bf16 v[140:143], v[92:95], a[176:179], v[140:143]
ds_read_b64_tr_b16 v[82:83], v26 offset:4864
v_mul_f32 v234, v234, v122
v_mfma_f32_16x16x32_bf16 v[156:159], v[88:91], a[164:167], v[156:159]
v_rcp_f32 v123, v123
v_mfma_f32_16x16x32_bf16 v[156:159], v[92:95], a[180:183], v[156:159]
v_mul_f32 v235, v235, v123
v_cvt_pk_bf16_f32 v109, v234, v235
v_mfma_f32_16x16x32_bf16 v[172:175], v[88:91], a[168:171], v[172:175]
ds_read_b64_tr_b16 v[84:85], v26 offset:12800
v_cvt_pk_bf16_f32 v108, v232, v233
v_mfma_f32_16x16x32_bf16 v[172:175], v[92:95], a[184:187], v[172:175]
ds_read_b64_tr_b16 v[86:87], v26 offset:13056
v_mul_f32 v124, v244, v244
v_mfma_f32_16x16x32_bf16 v[188:191], v[88:91], a[172:175], v[188:191]
v_mul_f32 v125, v245, v245
v_mul_f32 v126, v246, v246
v_mfma_f32_16x16x32_bf16 v[188:191], v[92:95], a[188:191], v[188:191]
v_mul_f32 v127, v247, v247
s_waitcnt lgkmcnt(8)
v_mfma_f32_16x16x32_bf16 v[128:131], v[64:67], a[192:195], v[128:131]
ds_read_b64_tr_b16 v[88:89], v27 offset:4608
v_fma_f32 v124, v124, s49, v119
v_mfma_f32_16x16x32_bf16 v[128:131], v[68:71], a[208:211], v[128:131]
ds_read_b64_tr_b16 v[90:91], v27 offset:4864
v_fma_f32 v125, v125, s49, v119
v_mfma_f32_16x16x32_bf16 v[144:147], v[64:67], a[196:199], v[144:147]
v_fma_f32 v126, v126, s49, v119
v_fma_f32 v127, v127, s49, v119
v_mfma_f32_16x16x32_bf16 v[144:147], v[68:71], a[212:215], v[144:147]
v_mul_f32 v124, v124, v244
v_mul_f32 v125, v125, v245
v_mfma_f32_16x16x32_bf16 v[160:163], v[64:67], a[200:203], v[160:163]
ds_read_b64_tr_b16 v[92:93], v27 offset:12800
v_mul_f32 v126, v126, v246
v_mfma_f32_16x16x32_bf16 v[160:163], v[68:71], a[216:219], v[160:163]
ds_read_b64_tr_b16 v[94:95], v27 offset:13056
v_mul_f32 v127, v127, v247
v_mfma_f32_16x16x32_bf16 v[176:179], v[64:67], a[204:207], v[176:179]
v_exp_f32 v124, v124
v_mfma_f32_16x16x32_bf16 v[176:179], v[68:71], a[220:223], v[176:179]
v_mul_f32 v252, v252, v244
s_waitcnt lgkmcnt(8)
v_mfma_f32_16x16x32_bf16 v[132:135], v[72:75], a[192:195], v[132:135]
ds_read_b64_tr_b16 v[64:65], v26 offset:16896
v_mul_f32 v253, v253, v245
v_mfma_f32_16x16x32_bf16 v[132:135], v[76:79], a[208:211], v[132:135]
ds_read_b64_tr_b16 v[66:67], v26 offset:17152
v_mul_f32 v254, v254, v246
v_mfma_f32_16x16x32_bf16 v[148:151], v[72:75], a[196:199], v[148:151]
v_exp_f32 v125, v125
v_mfma_f32_16x16x32_bf16 v[148:151], v[76:79], a[212:215], v[148:151]
v_exp_f32 v126, v126
v_mfma_f32_16x16x32_bf16 v[164:167], v[72:75], a[200:203], v[164:167]
ds_read_b64_tr_b16 v[68:69], v26 offset:25088
v_mul_f32 v255, v255, v247
v_mfma_f32_16x16x32_bf16 v[164:167], v[76:79], a[216:219], v[164:167]
ds_read_b64_tr_b16 v[70:71], v26 offset:25344
v_add_f32 v124, 1.0, v124
v_mfma_f32_16x16x32_bf16 v[180:183], v[72:75], a[204:207], v[180:183]
v_exp_f32 v127, v127
v_mfma_f32_16x16x32_bf16 v[180:183], v[76:79], a[220:223], v[180:183]
v_add_f32 v125, 1.0, v125
s_waitcnt lgkmcnt(8)
v_mfma_f32_16x16x32_bf16 v[136:139], v[80:83], a[192:195], v[136:139]
ds_read_b64_tr_b16 v[72:73], v27 offset:16896
v_add_f32 v126, 1.0, v126
v_mfma_f32_16x16x32_bf16 v[136:139], v[84:87], a[208:211], v[136:139]
ds_read_b64_tr_b16 v[74:75], v27 offset:17152
v_add_f32 v127, 1.0, v127
v_mfma_f32_16x16x32_bf16 v[152:155], v[80:83], a[196:199], v[152:155]
v_rcp_f32 v124, v124
v_mfma_f32_16x16x32_bf16 v[152:155], v[84:87], a[212:215], v[152:155]
v_rcp_f32 v125, v125
v_mfma_f32_16x16x32_bf16 v[168:171], v[80:83], a[200:203], v[168:171]
ds_read_b64_tr_b16 v[76:77], v27 offset:25088
v_mul_f32 v252, v252, v124
v_mfma_f32_16x16x32_bf16 v[168:171], v[84:87], a[216:219], v[168:171]
ds_read_b64_tr_b16 v[78:79], v27 offset:25344
v_mul_f32 v253, v253, v125
v_mfma_f32_16x16x32_bf16 v[184:187], v[80:83], a[204:207], v[184:187]
v_rcp_f32 v126, v126
v_mfma_f32_16x16x32_bf16 v[184:187], v[84:87], a[220:223], v[184:187]
v_mul_f32 v254, v254, v126
s_waitcnt lgkmcnt(8)
v_mfma_f32_16x16x32_bf16 v[140:143], v[88:91], a[192:195], v[140:143]
ds_read_b64_tr_b16 v[80:81], v26 offset:20992
v_cvt_pk_bf16_f32 v30, v252, v253
v_mfma_f32_16x16x32_bf16 v[140:143], v[92:95], a[208:211], v[140:143]
ds_read_b64_tr_b16 v[82:83], v26 offset:21248
v_mul_f32 v120, v240, v240
v_mfma_f32_16x16x32_bf16 v[156:159], v[88:91], a[196:199], v[156:159]
v_rcp_f32 v127, v127
v_mfma_f32_16x16x32_bf16 v[156:159], v[92:95], a[212:215], v[156:159]
v_mul_f32 v255, v255, v127
v_cvt_pk_bf16_f32 v31, v254, v255
v_mfma_f32_16x16x32_bf16 v[172:175], v[88:91], a[200:203], v[172:175]
ds_read_b64_tr_b16 v[84:85], v26 offset:29184
v_mul_f32 v121, v241, v241
v_mfma_f32_16x16x32_bf16 v[172:175], v[92:95], a[216:219], v[172:175]
ds_read_b64_tr_b16 v[86:87], v26 offset:29440
v_mul_f32 v122, v242, v242
v_mfma_f32_16x16x32_bf16 v[188:191], v[88:91], a[204:207], v[188:191]
v_mul_f32 v123, v243, v243
)"
R"(v_fma_f32 v120, v120, s49, v119
v_mfma_f32_16x16x32_bf16 v[188:191], v[92:95], a[220:223], v[188:191]
v_fma_f32 v121, v121, s49, v119
s_waitcnt lgkmcnt(8)
v_mfma_f32_16x16x32_bf16 v[128:131], v[64:67], a[224:227], v[128:131]
ds_read_b64_tr_b16 v[88:89], v27 offset:20992
v_fma_f32 v122, v122, s49, v119
v_mfma_f32_16x16x32_bf16 v[128:131], v[68:71], a[240:243], v[128:131]
ds_read_b64_tr_b16 v[90:91], v27 offset:21248
v_fma_f32 v123, v123, s49, v119
v_mfma_f32_16x16x32_bf16 v[144:147], v[64:67], a[228:231], v[144:147]
v_mul_f32 v120, v120, v240
v_mul_f32 v121, v121, v241
v_mfma_f32_16x16x32_bf16 v[144:147], v[68:71], a[244:247], v[144:147]
v_exp_f32 v120, v120
v_mfma_f32_16x16x32_bf16 v[160:163], v[64:67], a[232:235], v[160:163]
ds_read_b64_tr_b16 v[92:93], v27 offset:29184
v_mul_f32 v122, v122, v242
v_mfma_f32_16x16x32_bf16 v[160:163], v[68:71], a[248:251], v[160:163]
ds_read_b64_tr_b16 v[94:95], v27 offset:29440
v_mul_f32 v123, v123, v243
v_mfma_f32_16x16x32_bf16 v[176:179], v[64:67], a[236:239], v[176:179]
v_exp_f32 v121, v121
v_mfma_f32_16x16x32_bf16 v[176:179], v[68:71], a[252:255], v[176:179]
v_mul_f32 v248, v248, v240
s_waitcnt lgkmcnt(8)
v_mfma_f32_16x16x32_bf16 v[132:135], v[72:75], a[224:227], v[132:135]
v_exp_f32 v122, v122
v_mfma_f32_16x16x32_bf16 v[132:135], v[76:79], a[240:243], v[132:135]
v_exp_f32 v123, v123
v_mfma_f32_16x16x32_bf16 v[148:151], v[72:75], a[228:231], v[148:151]
v_mul_f32 v249, v249, v241
v_mul_f32 v250, v250, v242
v_mfma_f32_16x16x32_bf16 v[148:151], v[76:79], a[244:247], v[148:151]
v_mul_f32 v251, v251, v243
v_add_f32 v120, 1.0, v120
v_mfma_f32_16x16x32_bf16 v[164:167], v[72:75], a[232:235], v[164:167]
v_rcp_f32 v120, v120
v_mfma_f32_16x16x32_bf16 v[164:167], v[76:79], a[248:251], v[164:167]
s_waitcnt lgkmcnt(0)
s_barrier
v_mfma_f32_16x16x32_bf16 v[180:183], v[72:75], a[236:239], v[180:183]
ds_read_b64_tr_b16 v[64:65], v96 offset:32768
v_add_f32 v121, 1.0, v121
v_mfma_f32_16x16x32_bf16 v[180:183], v[76:79], a[252:255], v[180:183]
ds_read_b64_tr_b16 v[66:67], v96 offset:33024
v_mfma_f32_16x16x32_bf16 v[136:139], v[80:83], a[224:227], v[136:139]
ds_read_b64_tr_b16 v[68:69], v96 offset:36864
v_add_f32 v122, 1.0, v122
v_mfma_f32_16x16x32_bf16 v[136:139], v[84:87], a[240:243], v[136:139]
ds_read_b64_tr_b16 v[70:71], v96 offset:37120
v_add_f32 v123, 1.0, v123
v_mfma_f32_16x16x32_bf16 v[152:155], v[80:83], a[228:231], v[152:155]
ds_read_b64_tr_b16 v[72:73], v97 offset:32768
v_mul_f32 v248, v248, v120
v_mfma_f32_16x16x32_bf16 v[152:155], v[84:87], a[244:247], v[152:155]
ds_read_b64_tr_b16 v[74:75], v97 offset:33024
s_bcnt1_i32_b32 s44, s99
v_mfma_f32_16x16x32_bf16 v[168:171], v[80:83], a[232:235], v[168:171]
ds_read_b64_tr_b16 v[76:77], v97 offset:36864
v_rcp_f32 v121, v121
v_mfma_f32_16x16x32_bf16 v[168:171], v[84:87], a[248:251], v[168:171]
ds_read_b64_tr_b16 v[78:79], v97 offset:37120
v_mul_f32 v249, v249, v121
v_mfma_f32_16x16x32_bf16 v[184:187], v[80:83], a[236:239], v[184:187]
ds_write_b128 v118, v[32:35] offset:4096
v_cvt_pk_bf16_f32 v28, v248, v249
v_mfma_f32_16x16x32_bf16 v[184:187], v[84:87], a[252:255], v[184:187]
ds_write_b128 v118, v[36:39] offset:6144
v_mfma_f32_16x16x32_bf16 v[140:143], v[88:91], a[224:227], v[140:143]
ds_read_b64_tr_b16 v[80:81], v96 offset:33280
s_bcnt1_i32_b32 s45, s89
v_mfma_f32_16x16x32_bf16 v[140:143], v[92:95], a[240:243], v[140:143]
ds_read_b64_tr_b16 v[82:83], v96 offset:33536
v_rcp_f32 v122, v122
v_mfma_f32_16x16x32_bf16 v[156:159], v[88:91], a[228:231], v[156:159]
ds_write_b128 v118, v[40:43] offset:5120
v_mul_f32 v250, v250, v122
v_mfma_f32_16x16x32_bf16 v[156:159], v[92:95], a[244:247], v[156:159]
ds_write_b128 v118, v[44:47] offset:7168
s_sub_u32 s45, s44, s45
v_mfma_f32_16x16x32_bf16 v[172:175], v[88:91], a[232:235], v[172:175]
ds_read_b64_tr_b16 v[84:85], v96 offset:37376
v_rcp_f32 v123, v123
v_mfma_f32_16x16x32_bf16 v[172:175], v[92:95], a[248:251], v[172:175]
ds_read_b64_tr_b16 v[86:87], v96 offset:37632
v_mul_f32 v251, v251, v123
v_mfma_f32_16x16x32_bf16 v[188:191], v[88:91], a[236:239], v[188:191]
ds_write_b128 v118, v[48:51] offset:0
v_cvt_pk_bf16_f32 v29, v250, v251
v_mfma_f32_16x16x32_bf16 v[188:191], v[92:95], a[252:255], v[188:191]
ds_write_b128 v118, v[52:55] offset:2048
s_waitcnt lgkmcnt(14)
v_mfma_f32_16x16x32_bf16 v[192:195], v[64:67], a[0:3], 0
ds_read_b64_tr_b16 v[88:89], v97 offset:33280
s_add_u32 s45, s45, 3
v_mfma_f32_16x16x32_bf16 v[192:195], v[68:71], a[16:19], v[192:195]
ds_read_b64_tr_b16 v[90:91], v97 offset:33536
s_sub_u32 s65, s47, s101
v_mfma_f32_16x16x32_bf16 v[208:211], v[64:67], a[4:7], 0
ds_write_b128 v118, v[56:59] offset:1024
s_xor_b32 s48, s99, s89
v_mfma_f32_16x16x32_bf16 v[208:211], v[68:71], a[20:23], v[208:211]
ds_write_b128 v118, v[60:63] offset:3072
s_ff1_i32_b32 s48, s48
v_mfma_f32_16x16x32_bf16 v[224:227], v[64:67], a[8:11], 0
ds_read_b64_tr_b16 v[92:93], v97 offset:37376
v_add_u32 v112, 0x20000, v3
v_mfma_f32_16x16x32_bf16 v[224:227], v[68:71], a[24:27], v[224:227]
ds_read_b64_tr_b16 v[94:95], v97 offset:37632
v_xor_b32 v113, 0x2000, v112
v_mfma_f32_16x16x32_bf16 v[240:243], v[64:67], a[12:15], 0
v_mul_f32 v124, v132, v132
v_mul_f32 v125, v133, v133
v_mfma_f32_16x16x32_bf16 v[240:243], v[68:71], a[28:31], v[240:243]
v_mul_f32 v126, v134, v134
s_waitcnt lgkmcnt(14)
v_mfma_f32_16x16x32_bf16 v[196:199], v[72:75], a[0:3], 0
ds_read_b64_tr_b16 v[64:65], v96 offset:40960
v_mul_f32 v127, v135, v135
v_mfma_f32_16x16x32_bf16 v[196:199], v[76:79], a[16:19], v[196:199]
ds_read_b64_tr_b16 v[66:67], v96 offset:41216
v_fma_f32 v124, v124, s49, v119
v_mfma_f32_16x16x32_bf16 v[212:215], v[72:75], a[4:7], 0
v_fma_f32 v125, v125, s49, v119
v_fma_f32 v126, v126, s49, v119
v_mfma_f32_16x16x32_bf16 v[212:215], v[76:79], a[20:23], v[212:215]
v_fma_f32 v127, v127, s49, v119
v_mul_f32 v124, v124, v132
v_mfma_f32_16x16x32_bf16 v[228:231], v[72:75], a[8:11], 0
ds_read_b64_tr_b16 v[68:69], v96 offset:45056
v_mul_f32 v125, v125, v133
v_mfma_f32_16x16x32_bf16 v[228:231], v[76:79], a[24:27], v[228:231]
ds_read_b64_tr_b16 v[70:71], v96 offset:45312
v_mul_f32 v126, v126, v134
v_mfma_f32_16x16x32_bf16 v[244:247], v[72:75], a[12:15], 0
v_exp_f32 v124, v124
v_mfma_f32_16x16x32_bf16 v[244:247], v[76:79], a[28:31], v[244:247]
v_mul_f32 v127, v127, v135
s_waitcnt lgkmcnt(12)
v_mfma_f32_16x16x32_bf16 v[200:203], v[80:83], a[0:3], 0
ds_read_b64_tr_b16 v[72:73], v97 offset:40960
v_mul_f32 v140, v140, v132
v_mfma_f32_16x16x32_bf16 v[200:203], v[84:87], a[16:19], v[200:203]
ds_read_b64_tr_b16 v[74:75], v97 offset:41216
v_mul_f32 v141, v141, v133
v_mfma_f32_16x16x32_bf16 v[216:219], v[80:83], a[4:7], 0
v_exp_f32 v125, v125
v_mfma_f32_16x16x32_bf16 v[216:219], v[84:87], a[20:23], v[216:219]
v_exp_f32 v126, v126
v_mfma_f32_16x16x32_bf16 v[232:235], v[80:83], a[8:11], 0
ds_read_b64_tr_b16 v[76:77], v97 offset:45056
v_mul_f32 v142, v142, v134
v_mfma_f32_16x16x32_bf16 v[232:235], v[84:87], a[24:27], v[232:235]
ds_read_b64_tr_b16 v[78:79], v97 offset:45312
v_mul_f32 v143, v143, v135
v_mfma_f32_16x16x32_bf16 v[248:251], v[80:83], a[12:15], 0
v_exp_f32 v127, v127
v_mfma_f32_16x16x32_bf16 v[248:251], v[84:87], a[28:31], v[248:251]
v_add_f32 v124, 1.0, v124
s_waitcnt lgkmcnt(8)
v_mfma_f32_16x16x32_bf16 v[204:207], v[88:91], a[0:3], 0
ds_read_b64_tr_b16 v[80:81], v96 offset:41472
v_add_f32 v125, 1.0, v125
v_mfma_f32_16x16x32_bf16 v[204:207], v[92:95], a[16:19], v[204:207]
ds_read_b64_tr_b16 v[82:83], v96 offset:41728
v_add_f32 v126, 1.0, v126
v_mfma_f32_16x16x32_bf16 v[220:223], v[88:91], a[4:7], 0
v_rcp_f32 v124, v124
v_mfma_f32_16x16x32_bf16 v[220:223], v[92:95], a[20:23], v[220:223]
v_rcp_f32 v125, v125
v_mfma_f32_16x16x32_bf16 v[236:239], v[88:91], a[8:11], 0
ds_read_b64_tr_b16 v[84:85], v96 offset:45568
v_add_f32 v127, 1.0, v127
v_mfma_f32_16x16x32_bf16 v[236:239], v[92:95], a[24:27], v[236:239]
ds_read_b64_tr_b16 v[86:87], v96 offset:45824
v_mul_f32 v140, v140, v124
v_mfma_f32_16x16x32_bf16 v[252:255], v[88:91], a[12:15], 0
v_rcp_f32 v126, v126
v_mfma_f32_16x16x32_bf16 v[252:255], v[92:95], a[28:31], v[252:255]
v_mul_f32 v141, v141, v125
s_waitcnt lgkmcnt(8)
v_mfma_f32_16x16x32_bf16 v[192:195], v[64:67], a[32:35], v[192:195]
ds_read_b64_tr_b16 v[88:89], v97 offset:41472
v_mul_f32 v142, v142, v126
v_mfma_f32_16x16x32_bf16 v[192:195], v[68:71], a[48:51], v[192:195]
ds_read_b64_tr_b16 v[90:91], v97 offset:41728
v_mul_f32 v120, v128, v128
v_mfma_f32_16x16x32_bf16 v[208:211], v[64:67], a[36:39], v[208:211]
v_rcp_f32 v127, v127
v_mfma_f32_16x16x32_bf16 v[208:211], v[68:71], a[52:55], v[208:211]
v_mul_f32 v143, v143, v127
v_cvt_pk_bf16_f32 v143, v142, v143
v_mfma_f32_16x16x32_bf16 v[224:227], v[64:67], a[40:43], v[224:227]
ds_read_b64_tr_b16 v[92:93], v97 offset:45568
v_cvt_pk_bf16_f32 v142, v140, v141
v_mfma_f32_16x16x32_bf16 v[224:227], v[68:71], a[56:59], v[224:227]
ds_read_b64_tr_b16 v[94:95], v97 offset:45824
v_mul_f32 v121, v129, v129
v_mfma_f32_16x16x32_bf16 v[240:243], v[64:67], a[44:47], v[240:243]
v_mul_f32 v122, v130, v130
v_mul_f32 v123, v131, v131
v_mfma_f32_16x16x32_bf16 v[240:243], v[68:71], a[60:63], v[240:243]
v_fma_f32 v120, v120, s49, v119
s_waitcnt lgkmcnt(8)
v_mfma_f32_16x16x32_bf16 v[196:199], v[72:75], a[32:35], v[196:199]
ds_read_b64_tr_b16 v[64:65], v96 offset:49152
v_fma_f32 v121, v121, s49, v119
v_mfma_f32_16x16x32_bf16 v[196:199], v[76:79], a[48:51], v[196:199]
ds_read_b64_tr_b16 v[66:67], v96 offset:49408
v_fma_f32 v122, v122, s49, v119
v_mfma_f32_16x16x32_bf16 v[212:215], v[72:75], a[36:39], v[212:215]
v_fma_f32 v123, v123, s49, v119
v_mul_f32 v120, v120, v128
v_mfma_f32_16x16x32_bf16 v[212:215], v[76:79], a[52:55], v[212:215]
v_exp_f32 v120, v120
v_mfma_f32_16x16x32_bf16 v[228:231], v[72:75], a[40:43], v[228:231]
ds_read_b64_tr_b16 v[68:69], v96 offset:53248
v_mul_f32 v121, v121, v129
v_mfma_f32_16x16x32_bf16 v[228:231], v[76:79], a[56:59], v[228:231]
ds_read_b64_tr_b16 v[70:71], v96 offset:53504
v_mul_f32 v122, v122, v130
v_mfma_f32_16x16x32_bf16 v[244:247], v[72:75], a[44:47], v[244:247]
v_exp_f32 v121, v121
v_mfma_f32_16x16x32_bf16 v[244:247], v[76:79], a[60:63], v[244:247]
v_mul_f32 v123, v123, v131
s_waitcnt lgkmcnt(8)
v_mfma_f32_16x16x32_bf16 v[200:203], v[80:83], a[32:35], v[200:203]
ds_read_b64_tr_b16 v[72:73], v97 offset:49152
v_mul_f32 v136, v136, v128
v_mfma_f32_16x16x32_bf16 v[200:203], v[84:87], a[48:51], v[200:203]
ds_read_b64_tr_b16 v[74:75], v97 offset:49408
v_mul_f32 v137, v137, v129
v_mfma_f32_16x16x32_bf16 v[216:219], v[80:83], a[36:39], v[216:219]
v_exp_f32 v122, v122
v_mfma_f32_16x16x32_bf16 v[216:219], v[84:87], a[52:55], v[216:219]
s_add_u32 s65, s65, s48
s_add_u32 s90, s45, 1
v_mfma_f32_16x16x32_bf16 v[232:235], v[80:83], a[40:43], v[232:235]
ds_read_b64_tr_b16 v[76:77], v97 offset:53248
v_mul_f32 v138, v138, v130
v_mfma_f32_16x16x32_bf16 v[232:235], v[84:87], a[56:59], v[232:235]
ds_read_b64_tr_b16 v[78:79], v97 offset:53504
v_mul_f32 v139, v139, v131
v_mfma_f32_16x16x32_bf16 v[248:251], v[80:83], a[44:47], v[248:251]
v_exp_f32 v123, v123
v_mfma_f32_16x16x32_bf16 v[248:251], v[84:87], a[60:63], v[248:251]
v_add_f32 v120, 1.0, v120
s_waitcnt lgkmcnt(8)
v_mfma_f32_16x16x32_bf16 v[204:207], v[88:91], a[32:35], v[204:207]
ds_read_b64_tr_b16 v[80:81], v96 offset:49664
v_add_f32 v121, 1.0, v121
v_mfma_f32_16x16x32_bf16 v[204:207], v[92:95], a[48:51], v[204:207]
ds_read_b64_tr_b16 v[82:83], v96 offset:49920
v_add_f32 v122, 1.0, v122
v_mfma_f32_16x16x32_bf16 v[220:223], v[88:91], a[36:39], v[220:223]
v_mfma_f32_16x16x32_bf16 v[220:223], v[92:95], a[52:55], v[220:223]
v_rcp_f32 v120, v120
v_mfma_f32_16x16x32_bf16 v[236:239], v[88:91], a[40:43], v[236:239]
ds_read_b64_tr_b16 v[84:85], v96 offset:53760
v_add_f32 v123, 1.0, v123
v_mfma_f32_16x16x32_bf16 v[236:239], v[92:95], a[56:59], v[236:239]
ds_read_b64_tr_b16 v[86:87], v96 offset:54016
v_mul_f32 v136, v136, v120
v_mfma_f32_16x16x32_bf16 v[252:255], v[88:91], a[44:47], v[252:255]
v_rcp_f32 v121, v121
v_mfma_f32_16x16x32_bf16 v[252:255], v[92:95], a[60:63], v[252:255]
v_mul_f32 v137, v137, v121
s_waitcnt lgkmcnt(8)
v_mfma_f32_16x16x32_bf16 v[192:195], v[64:67], a[64:67], v[192:195]
ds_read_b64_tr_b16 v[88:89], v97 offset:49664
v_cvt_pk_bf16_f32 v140, v136, v137
v_mfma_f32_16x16x32_bf16 v[192:195], v[68:71], a[80:83], v[192:195]
ds_read_b64_tr_b16 v[90:91], v97 offset:49920
v_mfma_f32_16x16x32_bf16 v[208:211], v[64:67], a[68:71], v[208:211]
v_rcp_f32 v122, v122
v_mfma_f32_16x16x32_bf16 v[208:211], v[68:71], a[84:87], v[208:211]
v_rcp_f32 v123, v123
v_mfma_f32_16x16x32_bf16 v[224:227], v[64:67], a[72:75], v[224:227]
ds_read_b64_tr_b16 v[92:93], v97 offset:53760
v_mfma_f32_16x16x32_bf16 v[224:227], v[68:71], a[88:91], v[224:227]
ds_read_b64_tr_b16 v[94:95], v97 offset:54016
v_mul_f32 v138, v138, v122
v_mfma_f32_16x16x32_bf16 v[240:243], v[64:67], a[76:79], v[240:243]
v_mul_f32 v139, v139, v123
v_mfma_f32_16x16x32_bf16 v[240:243], v[68:71], a[92:95], v[240:243]
v_cvt_pk_bf16_f32 v141, v138, v139
s_waitcnt lgkmcnt(8)
v_mfma_f32_16x16x32_bf16 v[196:199], v[72:75], a[64:67], v[196:199]
ds_read_b64_tr_b16 v[64:65], v96 offset:57344
v_mul_f32 v124, v148, v148
v_mfma_f32_16x16x32_bf16 v[196:199], v[76:79], a[80:83], v[196:199]
ds_read_b64_tr_b16 v[66:67], v96 offset:57600
v_mul_f32 v125, v149, v149
v_mfma_f32_16x16x32_bf16 v[212:215], v[72:75], a[68:71], v[212:215]
v_mul_f32 v126, v150, v150
v_mfma_f32_16x16x32_bf16 v[212:215], v[76:79], a[84:87], v[212:215]
v_mul_f32 v127, v151, v151
v_fma_f32 v124, v124, s49, v119
v_mfma_f32_16x16x32_bf16 v[228:231], v[72:75], a[72:75], v[228:231]
ds_read_b64_tr_b16 v[68:69], v96 offset:61440
v_fma_f32 v125, v125, s49, v119
v_mfma_f32_16x16x32_bf16 v[228:231], v[76:79], a[88:91], v[228:231]
ds_read_b64_tr_b16 v[70:71], v96 offset:61696
v_fma_f32 v126, v126, s49, v119
v_mfma_f32_16x16x32_bf16 v[244:247], v[72:75], a[76:79], v[244:247]
v_fma_f32 v127, v127, s49, v119
v_mfma_f32_16x16x32_bf16 v[244:247], v[76:79], a[92:95], v[244:247]
v_mul_f32 v124, v124, v148
s_waitcnt lgkmcnt(8)
v_mfma_f32_16x16x32_bf16 v[200:203], v[80:83], a[64:67], v[200:203]
ds_read_b64_tr_b16 v[72:73], v97 offset:57344
v_mul_f32 v125, v125, v149
v_mfma_f32_16x16x32_bf16 v[200:203], v[84:87], a[80:83], v[200:203]
ds_read_b64_tr_b16 v[74:75], v97 offset:57600
v_mul_f32 v126, v126, v150
v_mfma_f32_16x16x32_bf16 v[216:219], v[80:83], a[68:71], v[216:219]
v_exp_f32 v124, v124
v_mfma_f32_16x16x32_bf16 v[216:219], v[84:87], a[84:87], v[216:219]
v_mfma_f32_16x16x32_bf16 v[232:235], v[80:83], a[72:75], v[232:235]
ds_read_b64_tr_b16 v[76:77], v97 offset:61440
v_mul_f32 v127, v127, v151
v_mfma_f32_16x16x32_bf16 v[232:235], v[84:87], a[88:91], v[232:235]
ds_read_b64_tr_b16 v[78:79], v97 offset:61696
v_mul_f32 v156, v156, v148
v_mfma_f32_16x16x32_bf16 v[248:251], v[80:83], a[76:79], v[248:251]
v_exp_f32 v125, v125
v_mfma_f32_16x16x32_bf16 v[248:251], v[84:87], a[92:95], v[248:251]
v_mul_f32 v157, v157, v149
s_waitcnt lgkmcnt(8)
v_mfma_f32_16x16x32_bf16 v[204:207], v[88:91], a[64:67], v[204:207]
ds_read_b64_tr_b16 v[80:81], v96 offset:57856
v_mul_f32 v158, v158, v150
v_mfma_f32_16x16x32_bf16 v[204:207], v[92:95], a[80:83], v[204:207]
ds_read_b64_tr_b16 v[82:83], v96 offset:58112
v_mul_f32 v159, v159, v151
v_mfma_f32_16x16x32_bf16 v[220:223], v[88:91], a[68:71], v[220:223]
v_exp_f32 v126, v126
v_mfma_f32_16x16x32_bf16 v[220:223], v[92:95], a[84:87], v[220:223]
v_mfma_f32_16x16x32_bf16 v[236:239], v[88:91], a[72:75], v[236:239]
ds_read_b64_tr_b16 v[84:85], v96 offset:61952
v_add_f32 v124, 1.0, v124
v_mfma_f32_16x16x32_bf16 v[236:239], v[92:95], a[88:91], v[236:239]
ds_read_b64_tr_b16 v[86:87], v96 offset:62208
v_add_f32 v125, 1.0, v125
v_mfma_f32_16x16x32_bf16 v[252:255], v[88:91], a[76:79], v[252:255]
v_exp_f32 v127, v127
v_mfma_f32_16x16x32_bf16 v[252:255], v[92:95], a[92:95], v[252:255]
v_add_f32 v126, 1.0, v126
s_waitcnt lgkmcnt(8)
v_mfma_f32_16x16x32_bf16 v[192:195], v[64:67], a[96:99], v[192:195]
ds_read_b64_tr_b16 v[88:89], v97 offset:57856
v_add_f32 v127, 1.0, v127
v_mfma_f32_16x16x32_bf16 v[192:195], v[68:71], a[112:115], v[192:195]
ds_read_b64_tr_b16 v[90:91], v97 offset:58112
v_mul_f32 v120, v144, v144
v_mfma_f32_16x16x32_bf16 v[208:211], v[64:67], a[100:103], v[208:211]
v_mfma_f32_16x16x32_bf16 v[208:211], v[68:71], a[116:119], v[208:211]
v_rcp_f32 v124, v124
v_mfma_f32_16x16x32_bf16 v[224:227], v[64:67], a[104:107], v[224:227]
ds_read_b64_tr_b16 v[92:93], v97 offset:61952
v_mul_f32 v156, v156, v124
v_mfma_f32_16x16x32_bf16 v[224:227], v[68:71], a[120:123], v[224:227]
ds_read_b64_tr_b16 v[94:95], v97 offset:62208
v_mul_f32 v121, v145, v145
v_mfma_f32_16x16x32_bf16 v[240:243], v[64:67], a[108:111], v[240:243]
v_rcp_f32 v125, v125
v_mfma_f32_16x16x32_bf16 v[240:243], v[68:71], a[124:127], v[240:243]
v_mul_f32 v157, v157, v125
s_waitcnt lgkmcnt(8)
v_mfma_f32_16x16x32_bf16 v[196:199], v[72:75], a[96:99], v[196:199]
v_rcp_f32 v126, v126
v_mfma_f32_16x16x32_bf16 v[196:199], v[76:79], a[112:115], v[196:199]
v_mfma_f32_16x16x32_bf16 v[212:215], v[72:75], a[100:103], v[212:215]
v_rcp_f32 v127, v127
v_mfma_f32_16x16x32_bf16 v[212:215], v[76:79], a[116:119], v[212:215]
v_mul_f32 v158, v158, v126
v_mfma_f32_16x16x32_bf16 v[228:231], v[72:75], a[104:107], v[228:231]
v_mul_f32 v159, v159, v127
v_cvt_pk_bf16_f32 v159, v158, v159
v_mfma_f32_16x16x32_bf16 v[228:231], v[76:79], a[120:123], v[228:231]
s_waitcnt vmcnt(0) lgkmcnt(0)
s_barrier
v_mfma_f32_16x16x32_bf16 v[244:247], v[72:75], a[108:111], v[244:247]
ds_read_b64_tr_b16 v[64:65], v98 offset:32768
s_cmp_lg_u32 %3, 0
s_cbranch_scc1 W0N4_%=
s_lshl_b32 s91, s26, 5
s_add_u32 s91, s91, s31
s_lshl_b32 s91, s91, 2
s_add_u32 s91, s91, 12320
v_lshl_add_u32 v116, v22, 9, s91
v_mov_b32 v24, 1
s_mov_b64 exec, 15
global_store_dword v116, v24, s[92:93] sc1
s_mov_b64 exec, -1
W0N4_%=:
v_mfma_f32_16x16x32_bf16 v[244:247], v[76:79], a[124:127], v[244:247]
ds_read_b64_tr_b16 v[66:67], v98 offset:33024
v_mfma_f32_16x16x32_bf16 v[200:203], v[80:83], a[96:99], v[200:203]
)"
R"(ds_read_b64_tr_b16 v[68:69], v98 offset:36864
s_add_u32 m0, s75, 0
v_mfma_f32_16x16x32_bf16 v[200:203], v[84:87], a[112:115], v[200:203]
ds_read_b64_tr_b16 v[70:71], v98 offset:37120
v_cvt_pk_bf16_f32 v158, v156, v157
v_mfma_f32_16x16x32_bf16 v[216:219], v[80:83], a[100:103], v[216:219]
ds_read_b64_tr_b16 v[72:73], v99 offset:32768
v_mul_f32 v122, v146, v146
v_mfma_f32_16x16x32_bf16 v[216:219], v[84:87], a[116:119], v[216:219]
ds_read_b64_tr_b16 v[74:75], v99 offset:33024
v_mul_f32 v123, v147, v147
v_mfma_f32_16x16x32_bf16 v[232:235], v[80:83], a[104:107], v[232:235]
ds_read_b64_tr_b16 v[76:77], v99 offset:36864
v_fma_f32 v120, v120, s49, v119
v_mfma_f32_16x16x32_bf16 v[232:235], v[84:87], a[120:123], v[232:235]
ds_read_b64_tr_b16 v[78:79], v99 offset:37120
v_mov_b32 v40, v119
v_mfma_f32_16x16x32_bf16 v[248:251], v[80:83], a[108:111], v[248:251]
global_store_dwordx4 v22, v[108:111], s[96:97] offset:0 sc1
v_mov_b32 v41, v117
v_mov_b32 v42, v118
v_mfma_f32_16x16x32_bf16 v[248:251], v[84:87], a[124:127], v[248:251]
global_store_dwordx4 v22, v[28:31], s[96:97] offset:1024 sc1
v_subrev_u32 v43, 0x18000, v118
v_mfma_f32_16x16x32_bf16 v[204:207], v[88:91], a[96:99], v[204:207]
ds_read_b64_tr_b16 v[80:81], v98 offset:33280
v_xor_b32 v44, 0x2000, v3
v_mfma_f32_16x16x32_bf16 v[204:207], v[92:95], a[112:115], v[204:207]
ds_read_b64_tr_b16 v[82:83], v98 offset:33536
v_fma_f32 v121, v121, s49, v119
v_mfma_f32_16x16x32_bf16 v[220:223], v[88:91], a[100:103], v[220:223]
buffer_load_dwordx4 v3, s[36:39], s70 offen offset:0 lds
v_fma_f32 v122, v122, s49, v119
v_mfma_f32_16x16x32_bf16 v[220:223], v[92:95], a[116:119], v[220:223]
global_store_dwordx4 v117, v[140:143], s[94:95] offset:0 sc1
s_lshl_b32 s86, %2, 3
v_mfma_f32_16x16x32_bf16 v[236:239], v[88:91], a[104:107], v[236:239]
ds_read_b64_tr_b16 v[84:85], v98 offset:37376
v_fma_f32 v123, v123, s49, v119
v_mfma_f32_16x16x32_bf16 v[236:239], v[92:95], a[120:123], v[236:239]
ds_read_b64_tr_b16 v[86:87], v98 offset:37632
v_mul_f32 v120, v120, v144
v_mfma_f32_16x16x32_bf16 v[252:255], v[88:91], a[108:111], v[252:255]
buffer_load_dwordx4 v3, s[36:39], s70 offen offset:1024 lds
v_mul_f32 v121, v121, v145
v_mfma_f32_16x16x32_bf16 v[252:255], v[92:95], a[124:127], v[252:255]
v_mul_f32 v122, v122, v146
s_waitcnt lgkmcnt(8)
v_mfma_f32_16x16x32_bf16 v[192:195], v[64:67], a[128:131], v[192:195]
ds_read_b64_tr_b16 v[88:89], v99 offset:33280
v_mul_f32 v123, v123, v147
v_mfma_f32_16x16x32_bf16 v[192:195], v[68:71], a[144:147], v[192:195]
ds_read_b64_tr_b16 v[90:91], v99 offset:33536
v_mul_f32 v152, v152, v144
v_mfma_f32_16x16x32_bf16 v[208:211], v[64:67], a[132:135], v[208:211]
buffer_load_dwordx4 v3, s[36:39], s70 offen offset:2048 lds
v_mul_f32 v153, v153, v145
v_mfma_f32_16x16x32_bf16 v[208:211], v[68:71], a[148:151], v[208:211]
v_exp_f32 v120, v120
v_mfma_f32_16x16x32_bf16 v[224:227], v[64:67], a[136:139], v[224:227]
ds_read_b64_tr_b16 v[92:93], v99 offset:37376
s_lshr_b32 s86, 0x1020f000, s86
v_mfma_f32_16x16x32_bf16 v[224:227], v[68:71], a[152:155], v[224:227]
ds_read_b64_tr_b16 v[94:95], v99 offset:37632
v_mul_f32 v154, v154, v146
v_mfma_f32_16x16x32_bf16 v[240:243], v[64:67], a[140:143], v[240:243]
buffer_load_dwordx4 v3, s[36:39], s70 offen offset:3072 lds
v_mul_f32 v155, v155, v147
v_mfma_f32_16x16x32_bf16 v[240:243], v[68:71], a[156:159], v[240:243]
v_add_f32 v120, 1.0, v120
s_waitcnt lgkmcnt(8)
v_mfma_f32_16x16x32_bf16 v[196:199], v[72:75], a[128:131], v[196:199]
ds_read_b64_tr_b16 v[64:65], v98 offset:40960
s_add_u32 m0, s75, 4096
v_mfma_f32_16x16x32_bf16 v[196:199], v[76:79], a[144:147], v[196:199]
ds_read_b64_tr_b16 v[66:67], v98 offset:41216
s_sext_i32_i8 s86, s86
v_mfma_f32_16x16x32_bf16 v[212:215], v[72:75], a[132:135], v[212:215]
buffer_load_dwordx4 v4, s[36:39], s70 offen offset:0 lds
v_mul_f32 v124, v164, v164
v_mfma_f32_16x16x32_bf16 v[212:215], v[76:79], a[148:151], v[212:215]
v_exp_f32 v121, v121
v_mfma_f32_16x16x32_bf16 v[228:231], v[72:75], a[136:139], v[228:231]
ds_read_b64_tr_b16 v[68:69], v98 offset:45056
v_add_f32 v121, 1.0, v121
v_mfma_f32_16x16x32_bf16 v[228:231], v[76:79], a[152:155], v[228:231]
ds_read_b64_tr_b16 v[70:71], v98 offset:45312
v_add_u32 v5, s86, v18
v_mfma_f32_16x16x32_bf16 v[244:247], v[72:75], a[140:143], v[244:247]
buffer_load_dwordx4 v4, s[36:39], s70 offen offset:1024 lds
v_mul_f32 v125, v165, v165
v_mfma_f32_16x16x32_bf16 v[244:247], v[76:79], a[156:159], v[244:247]
v_mul_f32 v126, v166, v166
s_waitcnt lgkmcnt(8)
PAD8_%=:
s_cbranch_execz PAD16_%=
v_mfma_f32_16x16x32_bf16 v[200:203], v[80:83], a[128:131], v[200:203]
ds_read_b64_tr_b16 v[72:73], v99 offset:40960
v_mul_f32 v127, v167, v167
v_mfma_f32_16x16x32_bf16 v[200:203], v[84:87], a[144:147], v[200:203]
ds_read_b64_tr_b16 v[74:75], v99 offset:41216
v_fma_f32 v124, v124, s49, v119
v_mfma_f32_16x16x32_bf16 v[216:219], v[80:83], a[132:135], v[216:219]
buffer_load_dwordx4 v4, s[36:39], s70 offen offset:2048 lds
s_lshl_b32 s86, %2, 3
v_mfma_f32_16x16x32_bf16 v[216:219], v[84:87], a[148:151], v[216:219]
v_exp_f32 v122, v122
v_mfma_f32_16x16x32_bf16 v[232:235], v[80:83], a[136:139], v[232:235]
ds_read_b64_tr_b16 v[76:77], v99 offset:45056
v_add_f32 v122, 1.0, v122
v_mfma_f32_16x16x32_bf16 v[232:235], v[84:87], a[152:155], v[232:235]
ds_read_b64_tr_b16 v[78:79], v99 offset:45312
v_fma_f32 v125, v125, s49, v119
v_mfma_f32_16x16x32_bf16 v[248:251], v[80:83], a[140:143], v[248:251]
s_lshr_b32 s86, 0x30e010, s86
s_sext_i32_i8 s86, s86
v_mfma_f32_16x16x32_bf16 v[248:251], v[84:87], a[156:159], v[248:251]
buffer_load_dwordx4 v4, s[36:39], s70 offen offset:3072 lds
s_waitcnt lgkmcnt(8)
v_mfma_f32_16x16x32_bf16 v[204:207], v[88:91], a[128:131], v[204:207]
ds_read_b64_tr_b16 v[80:81], v98 offset:41472
s_mov_b32 s70, 0
v_mfma_f32_16x16x32_bf16 v[204:207], v[92:95], a[144:147], v[204:207]
ds_read_b64_tr_b16 v[82:83], v98 offset:41728
s_add_u32 m0, s75, 65536
v_mfma_f32_16x16x32_bf16 v[220:223], v[88:91], a[132:135], v[220:223]
v_exp_f32 v123, v123
v_mfma_f32_16x16x32_bf16 v[220:223], v[92:95], a[148:151], v[220:223]
buffer_load_dwordx4 v3, s[36:39], s70 offen offset:0 lds
v_add_f32 v123, 1.0, v123
v_mfma_f32_16x16x32_bf16 v[236:239], v[88:91], a[136:139], v[236:239]
ds_read_b64_tr_b16 v[84:85], v98 offset:45568
v_fma_f32 v126, v126, s49, v119
v_mfma_f32_16x16x32_bf16 v[236:239], v[92:95], a[152:155], v[236:239]
ds_read_b64_tr_b16 v[86:87], v98 offset:45824
v_fma_f32 v127, v127, s49, v119
v_mfma_f32_16x16x32_bf16 v[252:255], v[88:91], a[140:143], v[252:255]
v_rcp_f32 v120, v120
v_mfma_f32_16x16x32_bf16 v[252:255], v[92:95], a[156:159], v[252:255]
ds_read_b128 a[128:131], v112 offset:4096
s_waitcnt lgkmcnt(9)
v_mfma_f32_16x16x32_bf16 v[192:195], v[64:67], a[160:163], v[192:195]
ds_read_b64_tr_b16 v[88:89], v99 offset:41472
v_mul_f32 v152, v152, v120
v_mfma_f32_16x16x32_bf16 v[192:195], v[68:71], a[176:179], v[192:195]
ds_read_b64_tr_b16 v[90:91], v99 offset:41728
v_add_u32 v6, s86, v18
v_mfma_f32_16x16x32_bf16 v[208:211], v[64:67], a[164:167], v[208:211]
ds_read_b128 a[132:135], v112 offset:5120
v_mul_f32 v124, v124, v164
v_mfma_f32_16x16x32_bf16 v[208:211], v[68:71], a[180:183], v[208:211]
ds_read_b128 a[136:139], v112 offset:6144
v_mul_f32 v125, v125, v165
v_mfma_f32_16x16x32_bf16 v[224:227], v[64:67], a[168:171], v[224:227]
ds_read_b64_tr_b16 v[92:93], v99 offset:45568
v_mul_f32 v126, v126, v166
v_mfma_f32_16x16x32_bf16 v[224:227], v[68:71], a[184:187], v[224:227]
ds_read_b64_tr_b16 v[94:95], v99 offset:45824
v_mul_f32 v127, v127, v167
v_mfma_f32_16x16x32_bf16 v[240:243], v[64:67], a[172:175], v[240:243]
ds_read_b128 a[140:143], v112 offset:7168
v_mul_f32 v172, v172, v164
v_mfma_f32_16x16x32_bf16 v[240:243], v[68:71], a[188:191], v[240:243]
buffer_load_dwordx4 v3, s[36:39], s70 offen offset:1024 lds
s_waitcnt lgkmcnt(12)
v_mfma_f32_16x16x32_bf16 v[196:199], v[72:75], a[160:163], v[196:199]
ds_read_b64_tr_b16 v[64:65], v98 offset:49152
v_mul_f32 v173, v173, v165
v_mfma_f32_16x16x32_bf16 v[196:199], v[76:79], a[176:179], v[196:199]
ds_read_b64_tr_b16 v[66:67], v98 offset:49408
v_mul_f32 v174, v174, v166
v_mfma_f32_16x16x32_bf16 v[212:215], v[72:75], a[164:167], v[212:215]
buffer_load_dwordx4 v3, s[36:39], s70 offen offset:2048 lds
v_mul_f32 v175, v175, v167
v_mfma_f32_16x16x32_bf16 v[212:215], v[76:79], a[180:183], v[212:215]
v_rcp_f32 v121, v121
v_mfma_f32_16x16x32_bf16 v[228:231], v[72:75], a[168:171], v[228:231]
ds_read_b64_tr_b16 v[68:69], v98 offset:53248
v_mul_f32 v153, v153, v121
v_mfma_f32_16x16x32_bf16 v[228:231], v[76:79], a[184:187], v[228:231]
ds_read_b64_tr_b16 v[70:71], v98 offset:53504
v_cvt_pk_bf16_f32 v156, v152, v153
v_mfma_f32_16x16x32_bf16 v[244:247], v[72:75], a[172:175], v[244:247]
buffer_load_dwordx4 v3, s[36:39], s70 offen offset:3072 lds
v_mul_f32 v120, v160, v160
v_mfma_f32_16x16x32_bf16 v[244:247], v[76:79], a[188:191], v[244:247]
v_mul_f32 v121, v161, v161
s_waitcnt lgkmcnt(12)
v_mfma_f32_16x16x32_bf16 v[200:203], v[80:83], a[160:163], v[200:203]
ds_read_b64_tr_b16 v[72:73], v99 offset:49152
s_add_u32 m0, s75, 69632
v_mfma_f32_16x16x32_bf16 v[200:203], v[84:87], a[176:179], v[200:203]
ds_read_b64_tr_b16 v[74:75], v99 offset:49408
v_fma_f32 v120, v120, s49, v119
v_mfma_f32_16x16x32_bf16 v[216:219], v[80:83], a[164:167], v[216:219]
buffer_load_dwordx4 v4, s[36:39], s70 offen offset:0 lds
v_fma_f32 v121, v121, s49, v119
v_mfma_f32_16x16x32_bf16 v[216:219], v[84:87], a[180:183], v[216:219]
v_rcp_f32 v122, v122
v_mfma_f32_16x16x32_bf16 v[232:235], v[80:83], a[168:171], v[232:235]
ds_read_b64_tr_b16 v[76:77], v99 offset:53248
v_mul_f32 v154, v154, v122
v_mfma_f32_16x16x32_bf16 v[232:235], v[84:87], a[184:187], v[232:235]
ds_read_b64_tr_b16 v[78:79], v99 offset:53504
v_mul_f32 v122, v162, v162
v_mfma_f32_16x16x32_bf16 v[248:251], v[80:83], a[172:175], v[248:251]
v_rcp_f32 v123, v123
v_mfma_f32_16x16x32_bf16 v[248:251], v[84:87], a[188:191], v[248:251]
buffer_load_dwordx4 v4, s[36:39], s70 offen offset:1024 lds
s_waitcnt lgkmcnt(9)
v_mfma_f32_16x16x32_bf16 v[204:207], v[88:91], a[160:163], v[204:207]
ds_read_b64_tr_b16 v[80:81], v98 offset:49664
v_mul_f32 v155, v155, v123
v_mfma_f32_16x16x32_bf16 v[204:207], v[92:95], a[176:179], v[204:207]
ds_read_b64_tr_b16 v[82:83], v98 offset:49920
v_cvt_pk_bf16_f32 v157, v154, v155
v_mfma_f32_16x16x32_bf16 v[220:223], v[88:91], a[164:167], v[220:223]
global_store_dwordx4 v117, v[156:159], s[94:95] offset:1024 sc1
v_exp_f32 v124, v124
v_mfma_f32_16x16x32_bf16 v[220:223], v[92:95], a[180:183], v[220:223]
buffer_load_dwordx4 v4, s[36:39], s70 offen offset:2048 lds
v_add_f32 v124, 1.0, v124
v_mfma_f32_16x16x32_bf16 v[236:239], v[88:91], a[168:171], v[236:239]
ds_read_b64_tr_b16 v[84:85], v98 offset:53760
v_mul_f32 v123, v163, v163
v_mfma_f32_16x16x32_bf16 v[236:239], v[92:95], a[184:187], v[236:239]
ds_read_b64_tr_b16 v[86:87], v98 offset:54016
v_fma_f32 v122, v122, s49, v119
v_mfma_f32_16x16x32_bf16 v[252:255], v[88:91], a[172:175], v[252:255]
v_exp_f32 v125, v125
v_mfma_f32_16x16x32_bf16 v[252:255], v[92:95], a[188:191], v[252:255]
ds_read_b128 a[160:163], v113 offset:4096
s_waitcnt lgkmcnt(9)
v_mfma_f32_16x16x32_bf16 v[192:195], v[64:67], a[192:195], v[192:195]
ds_read_b64_tr_b16 v[88:89], v99 offset:49664
v_add_f32 v125, 1.0, v125
v_mfma_f32_16x16x32_bf16 v[192:195], v[68:71], a[208:211], v[192:195]
ds_read_b64_tr_b16 v[90:91], v99 offset:49920
v_fma_f32 v123, v123, s49, v119
v_mfma_f32_16x16x32_bf16 v[208:211], v[64:67], a[196:199], v[208:211]
ds_read_b128 a[164:167], v113 offset:5120
v_mul_f32 v120, v120, v160
v_mfma_f32_16x16x32_bf16 v[208:211], v[68:71], a[212:215], v[208:211]
ds_read_b128 a[168:171], v113 offset:6144
v_mul_f32 v121, v121, v161
v_mfma_f32_16x16x32_bf16 v[224:227], v[64:67], a[200:203], v[224:227]
ds_read_b64_tr_b16 v[92:93], v99 offset:53760
v_mul_f32 v122, v122, v162
v_mfma_f32_16x16x32_bf16 v[224:227], v[68:71], a[216:219], v[224:227]
ds_read_b64_tr_b16 v[94:95], v99 offset:54016
v_mul_f32 v123, v123, v163
v_mfma_f32_16x16x32_bf16 v[240:243], v[64:67], a[204:207], v[240:243]
ds_read_b128 a[172:175], v113 offset:7168
v_mul_f32 v168, v168, v160
v_mfma_f32_16x16x32_bf16 v[240:243], v[68:71], a[220:223], v[240:243]
buffer_load_dwordx4 v4, s[36:39], s70 offen offset:3072 lds
s_waitcnt lgkmcnt(12)
v_mfma_f32_16x16x32_bf16 v[196:199], v[72:75], a[192:195], v[196:199]
ds_read_b64_tr_b16 v[64:65], v98 offset:57344
v_mul_f32 v169, v169, v161
v_mfma_f32_16x16x32_bf16 v[196:199], v[76:79], a[208:211], v[196:199]
ds_read_b64_tr_b16 v[66:67], v98 offset:57600
v_mul_f32 v170, v170, v162
v_mfma_f32_16x16x32_bf16 v[212:215], v[72:75], a[196:199], v[212:215]
v_exp_f32 v126, v126
v_mfma_f32_16x16x32_bf16 v[212:215], v[76:79], a[212:215], v[212:215]
v_exp_f32 v127, v127
v_mfma_f32_16x16x32_bf16 v[228:231], v[72:75], a[200:203], v[228:231]
ds_read_b64_tr_b16 v[68:69], v98 offset:61440
v_cmp_gt_i32 s[80:81], 16, v5
v_mfma_f32_16x16x32_bf16 v[228:231], v[76:79], a[216:219], v[228:231]
ds_read_b64_tr_b16 v[70:71], v98 offset:61696
v_add_f32 v126, 1.0, v126
v_mfma_f32_16x16x32_bf16 v[244:247], v[72:75], a[204:207], v[244:247]
v_rcp_f32 v124, v124
v_mfma_f32_16x16x32_bf16 v[244:247], v[76:79], a[220:223], v[244:247]
v_cmp_gt_i32 s[82:83], 17, v5
s_waitcnt lgkmcnt(12)
v_mfma_f32_16x16x32_bf16 v[200:203], v[80:83], a[192:195], v[200:203]
ds_read_b64_tr_b16 v[72:73], v99 offset:57344
v_add_f32 v127, 1.0, v127
v_mfma_f32_16x16x32_bf16 v[200:203], v[84:87], a[208:211], v[200:203]
ds_read_b64_tr_b16 v[74:75], v99 offset:57600
v_mul_f32 v172, v172, v124
v_mfma_f32_16x16x32_bf16 v[216:219], v[80:83], a[196:199], v[216:219]
v_rcp_f32 v125, v125
v_mfma_f32_16x16x32_bf16 v[216:219], v[84:87], a[212:215], v[216:219]
v_cmp_gt_i32 s[84:85], 18, v5
v_cmp_gt_i32 s[86:87], 19, v5
v_mfma_f32_16x16x32_bf16 v[232:235], v[80:83], a[200:203], v[232:235]
ds_read_b64_tr_b16 v[76:77], v99 offset:61440
v_mul_f32 v173, v173, v125
v_mfma_f32_16x16x32_bf16 v[232:235], v[84:87], a[216:219], v[232:235]
ds_read_b64_tr_b16 v[78:79], v99 offset:61696
v_mul_f32 v171, v171, v163
v_mfma_f32_16x16x32_bf16 v[248:251], v[80:83], a[204:207], v[248:251]
v_rcp_f32 v126, v126
v_mfma_f32_16x16x32_bf16 v[248:251], v[84:87], a[220:223], v[248:251]
v_mul_f32 v174, v174, v126
s_waitcnt lgkmcnt(9)
v_mfma_f32_16x16x32_bf16 v[204:207], v[88:91], a[192:195], v[204:207]
ds_read_b64_tr_b16 v[80:81], v98 offset:57856
v_cndmask_b32 v100, 0, v20, s[80:81]
v_mfma_f32_16x16x32_bf16 v[204:207], v[92:95], a[208:211], v[204:207]
ds_read_b64_tr_b16 v[82:83], v98 offset:58112
v_cndmask_b32 v101, 0, v20, s[82:83]
v_mfma_f32_16x16x32_bf16 v[220:223], v[88:91], a[196:199], v[220:223]
v_rcp_f32 v127, v127
v_mfma_f32_16x16x32_bf16 v[220:223], v[92:95], a[212:215], v[220:223]
v_exp_f32 v120, v120
v_mfma_f32_16x16x32_bf16 v[236:239], v[88:91], a[200:203], v[236:239]
ds_read_b64_tr_b16 v[84:85], v98 offset:61952
v_mul_f32 v175, v175, v127
v_mfma_f32_16x16x32_bf16 v[236:239], v[92:95], a[216:219], v[236:239]
ds_read_b64_tr_b16 v[86:87], v98 offset:62208
v_cvt_pk_bf16_f32 v175, v174, v175
v_mfma_f32_16x16x32_bf16 v[252:255], v[88:91], a[204:207], v[252:255]
v_exp_f32 v121, v121
v_mfma_f32_16x16x32_bf16 v[252:255], v[92:95], a[220:223], v[252:255]
v_cndmask_b32 v102, 0, v20, s[84:85]
s_waitcnt lgkmcnt(8)
v_mfma_f32_16x16x32_bf16 v[192:195], v[64:67], a[224:227], v[192:195]
ds_read_b64_tr_b16 v[88:89], v99 offset:57856
v_cvt_pk_bf16_f32 v174, v172, v173
v_mfma_f32_16x16x32_bf16 v[192:195], v[68:71], a[240:243], v[192:195]
ds_read_b64_tr_b16 v[90:91], v99 offset:58112
v_add_f32 v120, 1.0, v120
v_mfma_f32_16x16x32_bf16 v[208:211], v[64:67], a[228:231], v[208:211]
v_exp_f32 v122, v122
v_mfma_f32_16x16x32_bf16 v[208:211], v[68:71], a[244:247], v[208:211]
v_cndmask_b32 v103, 0, v20, s[86:87]
v_cmp_gt_i32 s[80:81], 0, v6
v_mfma_f32_16x16x32_bf16 v[224:227], v[64:67], a[232:235], v[224:227]
ds_read_b64_tr_b16 v[92:93], v99 offset:61952
v_add_f32 v121, 1.0, v121
v_mfma_f32_16x16x32_bf16 v[224:227], v[68:71], a[248:251], v[224:227]
ds_read_b64_tr_b16 v[94:95], v99 offset:62208
v_add_f32 v122, 1.0, v122
v_mfma_f32_16x16x32_bf16 v[240:243], v[64:67], a[236:239], v[240:243]
v_exp_f32 v123, v123
v_mfma_f32_16x16x32_bf16 v[240:243], v[68:71], a[252:255], v[240:243]
v_cmp_gt_i32 s[82:83], 1, v6
s_waitcnt lgkmcnt(8)
v_mfma_f32_16x16x32_bf16 v[196:199], v[72:75], a[224:227], v[196:199]
v_rcp_f32 v120, v120
v_mfma_f32_16x16x32_bf16 v[196:199], v[76:79], a[240:243], v[196:199]
v_cmp_gt_i32 s[84:85], 2, v6
v_cmp_gt_i32 s[86:87], 3, v6
v_mfma_f32_16x16x32_bf16 v[212:215], v[72:75], a[228:231], v[212:215]
v_rcp_f32 v121, v121
v_mfma_f32_16x16x32_bf16 v[212:215], v[76:79], a[244:247], v[212:215]
v_cndmask_b32 v112, 0, v20, s[80:81]
v_cndmask_b32 v113, 0, v20, s[82:83]
v_mfma_f32_16x16x32_bf16 v[228:231], v[72:75], a[232:235], v[228:231]
v_rcp_f32 v122, v122
v_mfma_f32_16x16x32_bf16 v[228:231], v[76:79], a[248:251], v[228:231]
s_waitcnt vmcnt(9) lgkmcnt(0)
s_barrier
v_mfma_f32_16x16x32_bf16 v[244:247], v[72:75], a[236:239], v[244:247]
ds_read_b128 a[192:195], v1 offset:0
v_add_f32 v123, 1.0, v123
v_mfma_f32_16x16x32_bf16 v[244:247], v[76:79], a[252:255], v[244:247]
ds_read_b128 a[196:199], v1 offset:1024
v_mfma_f32_16x16x32_bf16 v[200:203], v[80:83], a[224:227], v[200:203]
ds_read_b128 a[200:203], v1 offset:4096
v_mul_f32 v168, v168, v120
v_mfma_f32_16x16x32_bf16 v[200:203], v[84:87], a[240:243], v[200:203]
ds_read_b128 a[204:207], v1 offset:5120
v_mul_f32 v169, v169, v121
v_mfma_f32_16x16x32_bf16 v[216:219], v[80:83], a[228:231], v[216:219]
v_rcp_f32 v123, v123
v_mfma_f32_16x16x32_bf16 v[216:219], v[84:87], a[244:247], v[216:219]
v_mul_f32 v170, v170, v122
v_cndmask_b32 v114, 0, v20, s[84:85]
v_mfma_f32_16x16x32_bf16 v[232:235], v[80:83], a[232:235], v[232:235]
v_mul_f32 v171, v171, v123
v_cndmask_b32 v115, 0, v20, s[86:87]
v_mfma_f32_16x16x32_bf16 v[232:235], v[84:87], a[248:251], v[232:235]
v_cvt_pk_bf16_f32 v173, v170, v171
v_cmp_gt_i32 s[80:81], 0, v5
v_mfma_f32_16x16x32_bf16 v[248:251], v[80:83], a[236:239], v[248:251]
v_cvt_pk_bf16_f32 v172, v168, v169
v_cmp_gt_i32 s[82:83], 1, v5
v_mfma_f32_16x16x32_bf16 v[248:251], v[84:87], a[252:255], v[248:251]
global_store_dwordx4 v117, v[172:175], s[96:97] offset:0 sc1
v_mfma_f32_16x16x32_bf16 v[204:207], v[88:91], a[224:227], v[204:207]
v_cmp_gt_i32 s[84:85], 2, v5
v_cmp_gt_i32 s[86:87], 3, v5
v_mfma_f32_16x16x32_bf16 v[204:207], v[92:95], a[240:243], v[204:207]
v_cndmask_b32 v96, 0, v20, s[80:81]
v_cndmask_b32 v97, 0, v20, s[82:83]
v_mfma_f32_16x16x32_bf16 v[220:223], v[88:91], a[228:231], v[220:223]
v_cndmask_b32 v98, 0, v20, s[84:85]
v_cndmask_b32 v99, 0, v20, s[86:87]
v_mfma_f32_16x16x32_bf16 v[220:223], v[92:95], a[244:247], v[220:223]
v_cmp_gt_i32 s[80:81], 16, v6
v_cmp_gt_i32 s[82:83], 17, v6
v_mfma_f32_16x16x32_bf16 v[236:239], v[88:91], a[232:235], v[236:239]
v_cmp_gt_i32 s[84:85], 18, v6
v_cmp_gt_i32 s[86:87], 19, v6
)"
R"(v_mfma_f32_16x16x32_bf16 v[236:239], v[92:95], a[248:251], v[236:239]
v_cndmask_b32 v116, 0, v20, s[80:81]
v_cndmask_b32 v117, 0, v20, s[82:83]
v_mfma_f32_16x16x32_bf16 v[252:255], v[88:91], a[236:239], v[252:255]
v_cndmask_b32 v118, 0, v20, s[84:85]
v_cndmask_b32 v119, 0, v20, s[86:87]
v_mfma_f32_16x16x32_bf16 v[252:255], v[92:95], a[252:255], v[252:255]
PAD9_%=:
s_cbranch_execz PAD34_%=
P2_%=:
s_waitcnt lgkmcnt(0)
v_mfma_f32_16x16x32_bf16 v[64:67], a[192:195], a[128:131], v[96:99]
ds_read_b128 a[208:211], v1 offset:8192
s_add_u32 s54, s54, 1
v_mfma_f32_16x16x32_bf16 v[64:67], a[196:199], a[136:139], v[64:67]
v_cvt_pk_bf16_f32 v192, v192, v193
v_cvt_pk_bf16_f32 v193, v194, v195
v_mfma_f32_16x16x32_bf16 v[72:75], a[192:195], a[132:135], 0
ds_read_b128 a[212:215], v1 offset:9216
v_cvt_pk_bf16_f32 v194, v196, v197
v_mfma_f32_16x16x32_bf16 v[72:75], a[196:199], a[140:143], v[72:75]
v_cvt_pk_bf16_f32 v195, v198, v199
v_cvt_pk_bf16_f32 v200, v200, v201
v_mfma_f32_16x16x32_bf16 v[80:83], a[192:195], a[160:163], v[112:115]
ds_read_b128 a[216:219], v1 offset:12288
v_cvt_pk_bf16_f32 v201, v202, v203
v_mfma_f32_16x16x32_bf16 v[80:83], a[196:199], a[168:171], v[80:83]
ds_write_b128 v43, v[192:195] offset:4096
v_cvt_pk_bf16_f32 v202, v204, v205
v_mfma_f32_16x16x32_bf16 v[88:91], a[192:195], a[164:167], 0
ds_read_b128 a[220:223], v1 offset:13312
v_cvt_pk_bf16_f32 v203, v206, v207
v_mfma_f32_16x16x32_bf16 v[88:91], a[196:199], a[172:175], v[88:91]
ds_write_b128 v43, v[200:203] offset:6144
v_cvt_pk_bf16_f32 v208, v208, v209
v_mfma_f32_16x16x32_bf16 v[68:71], a[200:203], a[128:131], v[100:103]
v_cvt_pk_bf16_f32 v209, v210, v211
v_cvt_pk_bf16_f32 v210, v212, v213
v_mfma_f32_16x16x32_bf16 v[68:71], a[204:207], a[136:139], v[68:71]
v_cvt_pk_bf16_f32 v211, v214, v215
v_cvt_pk_bf16_f32 v216, v216, v217
v_mfma_f32_16x16x32_bf16 v[76:79], a[200:203], a[132:135], 0
ds_write_b128 v43, v[208:211] offset:5120
v_cvt_pk_bf16_f32 v217, v218, v219
v_mfma_f32_16x16x32_bf16 v[76:79], a[204:207], a[140:143], v[76:79]
v_cvt_pk_bf16_f32 v218, v220, v221
v_cvt_pk_bf16_f32 v219, v222, v223
v_mfma_f32_16x16x32_bf16 v[84:87], a[200:203], a[160:163], v[116:119]
ds_write_b128 v43, v[216:219] offset:7168
v_mfma_f32_16x16x32_bf16 v[84:87], a[204:207], a[168:171], v[84:87]
s_waitcnt lgkmcnt(0)
s_barrier
v_mfma_f32_16x16x32_bf16 v[92:95], a[200:203], a[164:167], 0
ds_read_b128 a[144:147], v3 offset:36864
ds_read_b128 a[176:179], v44 offset:36864
v_mfma_f32_16x16x32_bf16 v[92:95], a[204:207], a[172:175], v[92:95]
ds_read_b128 a[148:151], v3 offset:37888
v_mfma_f32_16x16x32_bf16 v[104:107], a[208:211], a[132:135], v[96:99]
ds_read_b128 a[192:195], v1 offset:2048
ds_read_b128 a[180:183], v44 offset:37888
v_mfma_f32_16x16x32_bf16 v[104:107], a[212:215], a[140:143], v[104:107]
ds_read_b128 a[196:199], v1 offset:3072
ds_read_b128 a[152:155], v3 offset:38912
v_mfma_f32_16x16x32_bf16 v[120:123], a[208:211], a[164:167], v[112:115]
ds_read_b128 a[200:203], v1 offset:6144
ds_read_b128 a[184:187], v44 offset:38912
v_mfma_f32_16x16x32_bf16 v[120:123], a[212:215], a[172:175], v[120:123]
ds_read_b128 a[204:207], v1 offset:7168
ds_read_b128 a[156:159], v3 offset:39936
v_mfma_f32_16x16x32_bf16 v[108:111], a[216:219], a[132:135], v[100:103]
ds_read_b128 a[188:191], v44 offset:39936
v_cvt_pk_bf16_f32 v224, v224, v225
v_mfma_f32_16x16x32_bf16 v[108:111], a[220:223], a[140:143], v[108:111]
v_cvt_pk_bf16_f32 v225, v226, v227
v_cvt_pk_bf16_f32 v226, v228, v229
v_mfma_f32_16x16x32_bf16 v[124:127], a[216:219], a[164:167], v[116:119]
v_cvt_pk_bf16_f32 v227, v230, v231
v_cvt_pk_bf16_f32 v232, v232, v233
v_mfma_f32_16x16x32_bf16 v[124:127], a[220:223], a[172:175], v[124:127]
s_waitcnt lgkmcnt(0)
v_mfma_f32_16x16x32_bf16 v[64:67], a[192:195], a[144:147], v[64:67]
ds_read_b128 a[208:211], v1 offset:10240
v_cvt_pk_bf16_f32 v233, v234, v235
v_mfma_f32_16x16x32_bf16 v[64:67], a[196:199], a[152:155], v[64:67]
ds_write_b128 v42, v[224:227] offset:4096
v_cvt_pk_bf16_f32 v234, v236, v237
v_mfma_f32_16x16x32_bf16 v[72:75], a[192:195], a[148:151], v[72:75]
ds_read_b128 a[212:215], v1 offset:11264
s_add_u32 m0, s75, 98304
v_mfma_f32_16x16x32_bf16 v[72:75], a[196:199], a[156:159], v[72:75]
buffer_load_dwordx4 v3, s[40:43], s71 offen offset:0 lds
v_cvt_pk_bf16_f32 v235, v238, v239
v_mfma_f32_16x16x32_bf16 v[80:83], a[192:195], a[176:179], v[80:83]
ds_read_b128 a[216:219], v1 offset:14336
v_cvt_pk_bf16_f32 v240, v240, v241
v_mfma_f32_16x16x32_bf16 v[80:83], a[196:199], a[184:187], v[80:83]
ds_write_b128 v42, v[232:235] offset:6144
v_cvt_pk_bf16_f32 v241, v242, v243
v_mfma_f32_16x16x32_bf16 v[88:91], a[192:195], a[180:183], v[88:91]
ds_read_b128 a[220:223], v1 offset:15360
v_cvt_pk_bf16_f32 v242, v244, v245
v_mfma_f32_16x16x32_bf16 v[88:91], a[196:199], a[188:191], v[88:91]
buffer_load_dwordx4 v3, s[40:43], s71 offen offset:1024 lds
v_cvt_pk_bf16_f32 v243, v246, v247
v_mfma_f32_16x16x32_bf16 v[68:71], a[200:203], a[144:147], v[68:71]
ds_write_b128 v42, v[240:243] offset:5120
v_cvt_pk_bf16_f32 v248, v248, v249
v_mfma_f32_16x16x32_bf16 v[68:71], a[204:207], a[152:155], v[68:71]
v_cvt_pk_bf16_f32 v249, v250, v251
v_cvt_pk_bf16_f32 v250, v252, v253
v_mfma_f32_16x16x32_bf16 v[76:79], a[200:203], a[148:151], v[76:79]
buffer_load_dwordx4 v3, s[40:43], s71 offen offset:2048 lds
v_cvt_pk_bf16_f32 v251, v254, v255
v_mfma_f32_16x16x32_bf16 v[76:79], a[204:207], a[156:159], v[76:79]
ds_write_b128 v42, v[248:251] offset:7168
v_mul_f32 v36, v180, v180
v_mfma_f32_16x16x32_bf16 v[84:87], a[200:203], a[176:179], v[84:87]
buffer_load_dwordx4 v3, s[40:43], s71 offen offset:3072 lds
s_add_u32 m0, s75, 102400
v_mfma_f32_16x16x32_bf16 v[84:87], a[204:207], a[184:187], v[84:87]
v_mul_f32 v37, v181, v181
v_mul_f32 v38, v182, v182
v_mfma_f32_16x16x32_bf16 v[92:95], a[200:203], a[180:183], v[92:95]
buffer_load_dwordx4 v4, s[40:43], s71 offen offset:0 lds
v_mul_f32 v39, v183, v183
v_mfma_f32_16x16x32_bf16 v[92:95], a[204:207], a[188:191], v[92:95]
v_fma_f32 v36, v36, s49, v40
s_waitcnt lgkmcnt(2)
v_mfma_f32_16x16x32_bf16 v[104:107], a[208:211], a[148:151], v[104:107]
v_fma_f32 v37, v37, s49, v40
v_fma_f32 v38, v38, s49, v40
v_mfma_f32_16x16x32_bf16 v[104:107], a[212:215], a[156:159], v[104:107]
buffer_load_dwordx4 v4, s[40:43], s71 offen offset:1024 lds
v_fma_f32 v39, v39, s49, v40
v_mfma_f32_16x16x32_bf16 v[120:123], a[208:211], a[180:183], v[120:123]
v_mul_f32 v36, v36, v180
v_mul_f32 v37, v37, v181
v_mfma_f32_16x16x32_bf16 v[120:123], a[212:215], a[188:191], v[120:123]
v_exp_f32 v36, v36
v_mfma_f32_16x16x32_bf16 v[108:111], a[216:219], a[148:151], v[108:111]
buffer_load_dwordx4 v4, s[40:43], s71 offen offset:2048 lds
v_mul_f32 v38, v38, v182
v_mfma_f32_16x16x32_bf16 v[108:111], a[220:223], a[156:159], v[108:111]
v_exp_f32 v37, v37
v_mfma_f32_16x16x32_bf16 v[124:127], a[216:219], a[180:183], v[124:127]
v_exp_f32 v38, v38
v_mfma_f32_16x16x32_bf16 v[124:127], a[220:223], a[188:191], v[124:127]
buffer_load_dwordx4 v4, s[40:43], s71 offen offset:3072 lds
v_mul_f32 v39, v39, v183
v_exp_f32 v39, v39
v_mul_f32 v188, v188, v180
v_mul_f32 v189, v189, v181
v_mul_f32 v190, v190, v182
v_mul_f32 v191, v191, v183
v_add_f32 v36, 1.0, v36
v_add_f32 v37, 1.0, v37
v_rcp_f32 v36, v36
v_rcp_f32 v37, v37
v_add_f32 v38, 1.0, v38
v_add_f32 v39, 1.0, v39
v_rcp_f32 v38, v38
v_rcp_f32 v39, v39
v_mul_f32 v188, v188, v36
v_mul_f32 v189, v189, v37
v_mul_f32 v190, v190, v38
v_mul_f32 v191, v191, v39
v_cvt_pk_bf16_f32 v191, v190, v191
v_cvt_pk_bf16_f32 v190, v188, v189
v_mul_f32 v32, v176, v176
v_mul_f32 v33, v177, v177
v_mul_f32 v34, v178, v178
v_mul_f32 v35, v179, v179
v_fma_f32 v32, v32, s49, v40
v_fma_f32 v33, v33, s49, v40
v_fma_f32 v34, v34, s49, v40
v_fma_f32 v35, v35, s49, v40
v_mul_f32 v32, v32, v176
v_mul_f32 v33, v33, v177
v_exp_f32 v32, v32
v_exp_f32 v33, v33
v_mul_f32 v34, v34, v178
v_mul_f32 v35, v35, v179
v_exp_f32 v34, v34
v_exp_f32 v35, v35
v_mul_f32 v184, v184, v176
v_mul_f32 v185, v185, v177
v_mul_f32 v186, v186, v178
v_mul_f32 v187, v187, v179
v_add_f32 v32, 1.0, v32
v_add_f32 v33, 1.0, v33
v_rcp_f32 v32, v32
v_rcp_f32 v33, v33
v_add_f32 v34, 1.0, v34
v_add_f32 v35, 1.0, v35
v_rcp_f32 v34, v34
v_rcp_f32 v35, v35
v_mul_f32 v184, v184, v32
v_mul_f32 v185, v185, v33
v_mul_f32 v186, v186, v34
v_mul_f32 v187, v187, v35
v_cvt_pk_bf16_f32 v189, v186, v187
v_cvt_pk_bf16_f32 v188, v184, v185
global_store_dwordx4 v41, v[188:191], s[96:97] offset:1024 sc1
v_mov_b32 v24, 0x3f803f80
v_accvgpr_write_b32 a240, v24
v_accvgpr_write_b32 a241, v24
v_accvgpr_write_b32 a242, v24
v_accvgpr_write_b32 a243, v24
v_bfe_i32 v14, v22, 4, 1
v_and_b32 v14, 0xaaaa, v14
v_or_b32 v14, 0x4444, v14
global_load_dwordx4 a[244:247], v21, s[4:5]
global_load_dwordx4 a[248:251], v21, s[6:7]
s_waitcnt vmcnt(12) lgkmcnt(0)
s_barrier
v_mfma_f32_32x32x16_bf16 a[224:239], v[10:13], v[10:13], 0
v_max3_f32 v9, abs(v72), abs(v73), abs(v74)
v_max3_f32 v9, v9, abs(v75), abs(v88)
v_max3_f32 v9, v9, abs(v89), abs(v90)
v_max3_f32 v9, v9, abs(v91), abs(v76)
v_max3_f32 v9, v9, abs(v77), abs(v78)
v_max3_f32 v9, v9, abs(v79), abs(v92)
v_max3_f32 v9, v9, abs(v93), abs(v94)
v_max3_f32 v9, v9, abs(v95), abs(v72)
v_cmp_le_f32 vcc, s58, v9
s_cbranch_vccnz RARE0_%=
s_cmp_eq_u32 s54, s56
s_cbranch_scc1 MASK0_%=
MASKR0_%=:
v_exp_f32 v64, v64
v_exp_f32 v65, v65
s_mov_b32 s71, s70
v_exp_f32 v66, v66
s_add_u32 s70, s70, 32768
v_exp_f32 v67, v67
s_cmp_eq_u32 s54, s72
v_exp_f32 v68, v68
s_cselect_b32 s70, 0, s70
v_exp_f32 v69, v69
v_exp_f32 v70, v70
v_exp_f32 v71, v71
v_cvt_pk_bf16_f32 v64, v64, v65
v_cvt_pk_bf16_f32 v65, v66, v67
v_exp_f32 v72, v72
v_exp_f32 v73, v73
v_exp_f32 v74, v74
v_exp_f32 v75, v75
ds_read_b128 a[192:195], v2 offset:0
v_cvt_pk_bf16_f32 v66, v68, v69
v_exp_f32 v76, v76
ds_read_b128 a[196:199], v2 offset:1024
v_cvt_pk_bf16_f32 v67, v70, v71
v_exp_f32 v77, v77
ds_read_b128 a[200:203], v2 offset:2048
v_cvt_pk_bf16_f32 v72, v72, v73
v_exp_f32 v78, v78
ds_read_b128 a[204:207], v2 offset:3072
v_cvt_pk_bf16_f32 v73, v74, v75
v_exp_f32 v79, v79
v_exp_f32 v80, v80
v_exp_f32 v81, v81
v_exp_f32 v82, v82
v_exp_f32 v83, v83
v_cvt_pk_bf16_f32 v74, v76, v77
v_cvt_pk_bf16_f32 v75, v78, v79
v_exp_f32 v84, v84
v_cvt_pk_bf16_f32 v80, v80, v81
v_mov_b32 v192, v96
v_mov_b32 v193, v97
v_mov_b32 v194, v98
v_mov_b32 v195, v99
v_mov_b32 v196, v100
v_mov_b32 v197, v101
v_mov_b32 v198, v102
v_mov_b32 v199, v103
v_mov_b32 v200, v112
v_mov_b32 v201, v113
v_mov_b32 v202, v114
v_mov_b32 v203, v115
v_mov_b32 v204, v116
v_mov_b32 v205, v117
v_mov_b32 v206, v118
v_mov_b32 v207, v119
s_waitcnt lgkmcnt(0)
v_mfma_f32_16x16x32_bf16 v[128:131], a[192:195], a[128:131], 0
ds_read_b128 a[208:211], v2 offset:4096
s_add_u32 s54, s54, 1
v_mfma_f32_16x16x32_bf16 v[128:131], a[196:199], a[136:139], v[128:131]
v_exp_f32 v85, v85
v_mfma_f32_16x16x32_bf16 v[128:131], a[200:203], a[144:147], v[128:131]
ds_read_b128 a[212:215], v2 offset:5120
s_add_u32 m0, s75, 32768
v_mfma_f32_16x16x32_bf16 v[128:131], a[204:207], a[152:155], v[128:131]
buffer_load_dwordx4 v3, s[40:43], s71 offen offset:0 lds
v_mfma_f32_16x16x32_bf16 v[136:139], a[192:195], a[132:135], 0
ds_read_b128 a[216:219], v2 offset:6144
v_cvt_pk_bf16_f32 v81, v82, v83
v_mfma_f32_16x16x32_bf16 v[136:139], a[196:199], a[140:143], v[136:139]
v_exp_f32 v86, v86
v_mfma_f32_16x16x32_bf16 v[136:139], a[200:203], a[148:151], v[136:139]
ds_read_b128 a[220:223], v2 offset:7168
v_cvt_pk_bf16_f32 v82, v84, v85
v_mfma_f32_16x16x32_bf16 v[136:139], a[204:207], a[156:159], v[136:139]
buffer_load_dwordx4 v3, s[40:43], s71 offen offset:1024 lds
v_mfma_f32_16x16x32_bf16 v[144:147], a[192:195], a[160:163], 0
v_exp_f32 v87, v87
v_mfma_f32_16x16x32_bf16 v[144:147], a[196:199], a[168:171], v[144:147]
v_exp_f32 v88, v88
PAD10_%=:
s_cbranch_execz PAD18_%=
v_mfma_f32_16x16x32_bf16 v[144:147], a[200:203], a[176:179], v[144:147]
v_exp_f32 v89, v89
v_mfma_f32_16x16x32_bf16 v[144:147], a[204:207], a[184:187], v[144:147]
buffer_load_dwordx4 v3, s[40:43], s71 offen offset:2048 lds
v_mfma_f32_16x16x32_bf16 v[152:155], a[192:195], a[164:167], 0
v_exp_f32 v90, v90
v_mfma_f32_16x16x32_bf16 v[152:155], a[196:199], a[172:175], v[152:155]
v_exp_f32 v91, v91
v_mfma_f32_16x16x32_bf16 v[152:155], a[200:203], a[180:183], v[152:155]
buffer_load_dwordx4 v3, s[40:43], s71 offen offset:3072 lds
v_mfma_f32_16x16x32_bf16 v[152:155], a[204:207], a[188:191], v[152:155]
v_cvt_pk_bf16_f32 v83, v86, v87
s_waitcnt lgkmcnt(0)
v_mfma_f32_16x16x32_bf16 v[132:135], a[208:211], a[128:131], 0
ds_read_b128 a[192:195], v2 offset:8192
v_cvt_pk_bf16_f32 v88, v88, v89
v_mfma_f32_16x16x32_bf16 v[132:135], a[212:215], a[136:139], v[132:135]
v_exp_f32 v92, v92
v_mfma_f32_16x16x32_bf16 v[132:135], a[216:219], a[144:147], v[132:135]
ds_read_b128 a[196:199], v2 offset:9216
s_add_u32 m0, s75, 36864
v_mfma_f32_16x16x32_bf16 v[132:135], a[220:223], a[152:155], v[132:135]
buffer_load_dwordx4 v4, s[40:43], s71 offen offset:0 lds
v_mfma_f32_16x16x32_bf16 v[140:143], a[208:211], a[132:135], 0
ds_read_b128 a[200:203], v2 offset:10240
v_cvt_pk_bf16_f32 v89, v90, v91
v_mfma_f32_16x16x32_bf16 v[140:143], a[212:215], a[140:143], v[140:143]
v_exp_f32 v93, v93
v_mfma_f32_16x16x32_bf16 v[140:143], a[216:219], a[148:151], v[140:143]
ds_read_b128 a[204:207], v2 offset:11264
v_cvt_pk_bf16_f32 v90, v92, v93
v_mfma_f32_16x16x32_bf16 v[140:143], a[220:223], a[156:159], v[140:143]
buffer_load_dwordx4 v4, s[40:43], s71 offen offset:1024 lds
v_mfma_f32_16x16x32_bf16 v[148:151], a[208:211], a[160:163], 0
v_exp_f32 v94, v94
v_mfma_f32_16x16x32_bf16 v[148:151], a[212:215], a[168:171], v[148:151]
v_exp_f32 v95, v95
v_mfma_f32_16x16x32_bf16 v[148:151], a[216:219], a[176:179], v[148:151]
v_exp_f32 v104, v104
v_mfma_f32_16x16x32_bf16 v[148:151], a[220:223], a[184:187], v[148:151]
buffer_load_dwordx4 v4, s[40:43], s71 offen offset:2048 lds
v_mfma_f32_16x16x32_bf16 v[156:159], a[208:211], a[164:167], 0
v_exp_f32 v105, v105
v_mfma_f32_16x16x32_bf16 v[156:159], a[212:215], a[172:175], v[156:159]
v_exp_f32 v106, v106
v_mfma_f32_16x16x32_bf16 v[156:159], a[216:219], a[180:183], v[156:159]
buffer_load_dwordx4 v4, s[40:43], s71 offen offset:3072 lds
v_mfma_f32_16x16x32_bf16 v[156:159], a[220:223], a[188:191], v[156:159]
v_cvt_pk_bf16_f32 v91, v94, v95
s_waitcnt lgkmcnt(0)
v_mfma_f32_16x16x32_bf16 v[160:163], a[192:195], a[128:131], 0
ds_read_b128 a[208:211], v2 offset:12288
v_cvt_pk_bf16_f32 v76, v104, v105
v_mfma_f32_16x16x32_bf16 v[160:163], a[196:199], a[136:139], v[160:163]
v_exp_f32 v107, v107
v_mfma_f32_16x16x32_bf16 v[160:163], a[200:203], a[144:147], v[160:163]
ds_read_b128 a[212:215], v2 offset:13312
s_add_u32 m0, s75, 0
v_mfma_f32_16x16x32_bf16 v[160:163], a[204:207], a[152:155], v[160:163]
buffer_load_dwordx4 v3, s[36:39], s70 offen offset:0 lds
v_mfma_f32_16x16x32_bf16 v[168:171], a[192:195], a[132:135], 0
ds_read_b128 a[216:219], v2 offset:14336
v_cvt_pk_bf16_f32 v77, v106, v107
v_mfma_f32_16x16x32_bf16 v[168:171], a[196:199], a[140:143], v[168:171]
v_exp_f32 v108, v108
v_mfma_f32_16x16x32_bf16 v[168:171], a[200:203], a[148:151], v[168:171]
ds_read_b128 a[220:223], v2 offset:15360
v_mfma_f32_16x16x32_bf16 v[168:171], a[204:207], a[156:159], v[168:171]
buffer_load_dwordx4 v3, s[36:39], s70 offen offset:1024 lds
v_mfma_f32_16x16x32_bf16 v[176:179], a[192:195], a[160:163], 0
v_exp_f32 v109, v109
v_mfma_f32_16x16x32_bf16 v[176:179], a[196:199], a[168:171], v[176:179]
v_exp_f32 v110, v110
v_mfma_f32_16x16x32_bf16 v[176:179], a[200:203], a[176:179], v[176:179]
v_exp_f32 v111, v111
v_mfma_f32_16x16x32_bf16 v[176:179], a[204:207], a[184:187], v[176:179]
buffer_load_dwordx4 v3, s[36:39], s70 offen offset:2048 lds
v_mfma_f32_16x16x32_bf16 v[184:187], a[192:195], a[164:167], 0
v_exp_f32 v120, v120
v_mfma_f32_16x16x32_bf16 v[184:187], a[196:199], a[172:175], v[184:187]
v_exp_f32 v121, v121
v_mfma_f32_16x16x32_bf16 v[184:187], a[200:203], a[180:183], v[184:187]
buffer_load_dwordx4 v3, s[36:39], s70 offen offset:3072 lds
v_mfma_f32_16x16x32_bf16 v[184:187], a[204:207], a[188:191], v[184:187]
v_cvt_pk_bf16_f32 v78, v108, v109
s_waitcnt vmcnt(15) lgkmcnt(0)
s_barrier
v_mfma_f32_16x16x32_bf16 v[164:167], a[208:211], a[128:131], 0
ds_read_b128 a[192:195], v2 offset:32768
v_cvt_pk_bf16_f32 v79, v110, v111
)"
R"(v_mfma_f32_16x16x32_bf16 v[164:167], a[212:215], a[136:139], v[164:167]
v_exp_f32 v122, v122
v_mfma_f32_16x16x32_bf16 v[164:167], a[216:219], a[144:147], v[164:167]
ds_read_b128 a[196:199], v2 offset:40960
s_add_u32 m0, s75, 4096
v_mfma_f32_16x16x32_bf16 v[164:167], a[220:223], a[152:155], v[164:167]
buffer_load_dwordx4 v4, s[36:39], s70 offen offset:0 lds
v_mfma_f32_16x16x32_bf16 v[172:175], a[208:211], a[132:135], 0
ds_read_b128 a[200:203], v2 offset:33792
v_cvt_pk_bf16_f32 v92, v120, v121
v_mfma_f32_16x16x32_bf16 v[172:175], a[212:215], a[140:143], v[172:175]
v_exp_f32 v123, v123
v_mfma_f32_16x16x32_bf16 v[172:175], a[216:219], a[148:151], v[172:175]
ds_read_b128 a[204:207], v2 offset:41984
v_cvt_pk_bf16_f32 v93, v122, v123
v_mfma_f32_16x16x32_bf16 v[172:175], a[220:223], a[156:159], v[172:175]
buffer_load_dwordx4 v4, s[36:39], s70 offen offset:1024 lds
v_mfma_f32_16x16x32_bf16 v[180:183], a[208:211], a[160:163], 0
v_exp_f32 v124, v124
v_mfma_f32_16x16x32_bf16 v[180:183], a[212:215], a[168:171], v[180:183]
v_exp_f32 v125, v125
v_mfma_f32_16x16x32_bf16 v[180:183], a[216:219], a[176:179], v[180:183]
v_exp_f32 v126, v126
v_mfma_f32_16x16x32_bf16 v[180:183], a[220:223], a[184:187], v[180:183]
buffer_load_dwordx4 v4, s[36:39], s70 offen offset:2048 lds
v_mfma_f32_16x16x32_bf16 v[188:191], a[208:211], a[164:167], 0
v_exp_f32 v127, v127
v_mfma_f32_16x16x32_bf16 v[188:191], a[212:215], a[172:175], v[188:191]
v_cvt_pk_bf16_f32 v94, v124, v125
v_cvt_pk_bf16_f32 v95, v126, v127
v_mfma_f32_16x16x32_bf16 v[188:191], a[216:219], a[180:183], v[188:191]
buffer_load_dwordx4 v4, s[36:39], s70 offen offset:3072 lds
v_mfma_f32_16x16x32_bf16 v[188:191], a[220:223], a[188:191], v[188:191]
s_waitcnt lgkmcnt(0)
v_mfma_f32_16x16x32_bf16 a[0:3], a[192:195], v[64:67], 0
ds_read_b128 a[208:211], v2 offset:34816
s_cmp_eq_u32 s54, s56
s_cbranch_scc1 MASK5_%=
MASKR5_%=:
v_smfmac_f32_16x16x32_bf16 a[224:227], a[240:241], v[64:67], v14
v_smfmac_f32_16x16x64_bf16 a[228:231], a[240:243], v[72:79], v14
ds_read_b128 a[212:215], v2 offset:43008
v_mfma_f32_16x16x32_bf16 a[4:7], a[200:203], v[64:67], 0
v_exp_f32 v128, v128
v_smfmac_f32_16x16x32_bf16 a[232:235], a[240:241], v[80:83], v14
ds_read_b128 a[216:219], v2 offset:35840
v_smfmac_f32_16x16x64_bf16 a[236:239], a[240:243], v[88:95], v14
v_exp_f32 v129, v129
v_mfma_f32_16x16x32_bf16 a[32:35], a[192:195], v[72:75], 0
ds_read_b128 a[220:223], v2 offset:44032
v_exp_f32 v130, v130
v_mfma_f32_16x16x32_bf16 a[32:35], a[196:199], v[76:79], a[32:35]
v_exp_f32 v131, v131
RARER5_%=:
v_mfma_f32_16x16x32_bf16 a[36:39], a[200:203], v[72:75], 0
v_exp_f32 v132, v132
v_mfma_f32_16x16x32_bf16 a[36:39], a[204:207], v[76:79], a[36:39]
v_exp_f32 v133, v133
v_mfma_f32_16x16x32_bf16 a[64:67], a[192:195], v[80:83], 0
v_exp_f32 v134, v134
v_mfma_f32_16x16x32_bf16 a[68:71], a[200:203], v[80:83], 0
v_exp_f32 v135, v135
v_mfma_f32_16x16x32_bf16 a[96:99], a[192:195], v[88:91], 0
v_cvt_pk_bf16_f32 v128, v128, v129
v_cvt_pk_bf16_f32 v129, v130, v131
v_mfma_f32_16x16x32_bf16 a[96:99], a[196:199], v[92:95], a[96:99]
v_cvt_pk_bf16_f32 v130, v132, v133
v_exp_f32 v136, v136
v_mfma_f32_16x16x32_bf16 a[100:103], a[200:203], v[88:91], 0
v_cvt_pk_bf16_f32 v131, v134, v135
v_exp_f32 v137, v137
v_mfma_f32_16x16x32_bf16 a[100:103], a[204:207], v[92:95], a[100:103]
v_exp_f32 v138, v138
s_waitcnt lgkmcnt(0)
v_mfma_f32_16x16x32_bf16 a[8:11], a[208:211], v[64:67], 0
ds_read_b128 a[192:195], v2 offset:36864
v_cvt_pk_bf16_f32 v136, v136, v137
v_mfma_f32_16x16x32_bf16 a[12:15], a[216:219], v[64:67], 0
ds_read_b128 a[196:199], v2 offset:45056
v_exp_f32 v139, v139
v_mfma_f32_16x16x32_bf16 a[40:43], a[208:211], v[72:75], 0
v_exp_f32 v140, v140
v_mfma_f32_16x16x32_bf16 a[40:43], a[212:215], v[76:79], a[40:43]
ds_read_b128 a[200:203], v2 offset:37888
v_exp_f32 v141, v141
v_mfma_f32_16x16x32_bf16 a[44:47], a[216:219], v[72:75], 0
ds_read_b128 a[204:207], v2 offset:46080
v_exp_f32 v142, v142
v_mfma_f32_16x16x32_bf16 a[44:47], a[220:223], v[76:79], a[44:47]
v_exp_f32 v143, v143
v_mfma_f32_16x16x32_bf16 a[72:75], a[208:211], v[80:83], 0
v_exp_f32 v144, v144
v_mfma_f32_16x16x32_bf16 a[76:79], a[216:219], v[80:83], 0
v_exp_f32 v145, v145
v_mfma_f32_16x16x32_bf16 a[104:107], a[208:211], v[88:91], 0
v_cvt_pk_bf16_f32 v137, v138, v139
v_cvt_pk_bf16_f32 v138, v140, v141
v_mfma_f32_16x16x32_bf16 a[104:107], a[212:215], v[92:95], a[104:107]
s_mov_b32 s71, s70
v_cvt_pk_bf16_f32 v139, v142, v143
v_mfma_f32_16x16x32_bf16 a[108:111], a[216:219], v[88:91], 0
v_exp_f32 v146, v146
v_mfma_f32_16x16x32_bf16 a[108:111], a[220:223], v[92:95], a[108:111]
s_add_u32 s70, s70, 32768
s_waitcnt lgkmcnt(0)
v_mfma_f32_16x16x32_bf16 a[16:19], a[192:195], v[64:67], 0
ds_read_b128 a[208:211], v2 offset:38912
v_exp_f32 v147, v147
v_mfma_f32_16x16x32_bf16 a[20:23], a[200:203], v[64:67], 0
ds_read_b128 a[212:215], v2 offset:47104
s_cmp_eq_u32 s54, s72
v_mfma_f32_16x16x32_bf16 a[48:51], a[192:195], v[72:75], 0
v_exp_f32 v148, v148
s_cselect_b32 s70, 0, s70
v_mfma_f32_16x16x32_bf16 a[48:51], a[196:199], v[76:79], a[48:51]
ds_read_b128 a[216:219], v2 offset:39936
v_exp_f32 v149, v149
v_mfma_f32_16x16x32_bf16 a[52:55], a[200:203], v[72:75], 0
ds_read_b128 a[220:223], v2 offset:48128
v_exp_f32 v150, v150
v_mfma_f32_16x16x32_bf16 a[52:55], a[204:207], v[76:79], a[52:55]
v_exp_f32 v151, v151
v_mfma_f32_16x16x32_bf16 a[80:83], a[192:195], v[80:83], 0
v_exp_f32 v152, v152
v_mfma_f32_16x16x32_bf16 a[84:87], a[200:203], v[80:83], 0
v_cvt_pk_bf16_f32 v144, v144, v145
v_cvt_pk_bf16_f32 v145, v146, v147
v_mfma_f32_16x16x32_bf16 a[112:115], a[192:195], v[88:91], 0
v_exp_f32 v153, v153
v_mfma_f32_16x16x32_bf16 a[112:115], a[196:199], v[92:95], a[112:115]
v_exp_f32 v154, v154
v_mfma_f32_16x16x32_bf16 a[116:119], a[200:203], v[88:91], 0
v_exp_f32 v155, v155
v_mfma_f32_16x16x32_bf16 a[116:119], a[204:207], v[92:95], a[116:119]
s_waitcnt vmcnt(0) lgkmcnt(0)
s_barrier
v_mfma_f32_16x16x32_bf16 a[24:27], a[208:211], v[64:67], 0
ds_read_b128 a[192:195], v1 offset:0
v_exp_f32 v156, v156
v_mfma_f32_16x16x32_bf16 a[28:31], a[216:219], v[64:67], 0
ds_read_b128 a[196:199], v1 offset:1024
v_cvt_pk_bf16_f32 v146, v148, v149
v_mfma_f32_16x16x32_bf16 a[56:59], a[208:211], v[72:75], 0
v_exp_f32 v157, v157
v_cvt_pk_bf16_f32 v147, v150, v151
v_mfma_f32_16x16x32_bf16 a[56:59], a[212:215], v[76:79], a[56:59]
ds_read_b128 a[200:203], v1 offset:2048
v_exp_f32 v158, v158
v_mfma_f32_16x16x32_bf16 a[60:63], a[216:219], v[72:75], 0
ds_read_b128 a[204:207], v1 offset:3072
v_cvt_pk_bf16_f32 v152, v152, v153
v_mfma_f32_16x16x32_bf16 a[60:63], a[220:223], v[76:79], a[60:63]
v_exp_f32 v159, v159
v_cvt_pk_bf16_f32 v153, v154, v155
v_mfma_f32_16x16x32_bf16 a[88:91], a[208:211], v[80:83], 0
v_exp_f32 v160, v160
v_mfma_f32_16x16x32_bf16 a[92:95], a[216:219], v[80:83], 0
v_exp_f32 v161, v161
v_mfma_f32_16x16x32_bf16 a[120:123], a[208:211], v[88:91], 0
v_exp_f32 v162, v162
v_exp_f32 v163, v163
v_mfma_f32_16x16x32_bf16 a[120:123], a[212:215], v[92:95], a[120:123]
v_exp_f32 v164, v164
v_cvt_pk_bf16_f32 v154, v156, v157
v_mfma_f32_16x16x32_bf16 a[124:127], a[216:219], v[88:91], 0
v_cvt_pk_bf16_f32 v155, v158, v159
v_cvt_pk_bf16_f32 v132, v160, v161
s_bitcmp1_b32 s57, s54
v_mfma_f32_16x16x32_bf16 a[124:127], a[220:223], v[92:95], a[124:127]
s_cbranch_scc1 END1_%=
LOOPE_%=:
s_waitcnt lgkmcnt(0)
v_mfma_f32_16x16x32_bf16 v[64:67], a[192:195], a[128:131], 0
ds_read_b128 a[208:211], v1 offset:4096
s_add_u32 s54, s54, 1
v_mfma_f32_16x16x32_bf16 v[64:67], a[196:199], a[136:139], v[64:67]
v_exp_f32 v165, v165
v_mfma_f32_16x16x32_bf16 v[64:67], a[200:203], a[144:147], v[64:67]
ds_read_b128 a[212:215], v1 offset:5120
s_add_u32 m0, s75, 98304
v_mfma_f32_16x16x32_bf16 v[64:67], a[204:207], a[152:155], v[64:67]
buffer_load_dwordx4 v3, s[40:43], s71 offen offset:0 lds
v_mfma_f32_16x16x32_bf16 v[72:75], a[192:195], a[132:135], 0
ds_read_b128 a[216:219], v1 offset:6144
v_cvt_pk_bf16_f32 v133, v162, v163
v_mfma_f32_16x16x32_bf16 v[72:75], a[196:199], a[140:143], v[72:75]
v_exp_f32 v166, v166
v_mfma_f32_16x16x32_bf16 v[72:75], a[200:203], a[148:151], v[72:75]
ds_read_b128 a[220:223], v1 offset:7168
v_cvt_pk_bf16_f32 v134, v164, v165
v_mfma_f32_16x16x32_bf16 v[72:75], a[204:207], a[156:159], v[72:75]
buffer_load_dwordx4 v3, s[40:43], s71 offen offset:1024 lds
v_mfma_f32_16x16x32_bf16 v[80:83], a[192:195], a[160:163], 0
v_exp_f32 v167, v167
v_mfma_f32_16x16x32_bf16 v[80:83], a[196:199], a[168:171], v[80:83]
v_exp_f32 v168, v168
v_mfma_f32_16x16x32_bf16 v[80:83], a[200:203], a[176:179], v[80:83]
v_exp_f32 v169, v169
v_mfma_f32_16x16x32_bf16 v[80:83], a[204:207], a[184:187], v[80:83]
buffer_load_dwordx4 v3, s[40:43], s71 offen offset:2048 lds
v_mfma_f32_16x16x32_bf16 v[88:91], a[192:195], a[164:167], 0
v_exp_f32 v170, v170
v_mfma_f32_16x16x32_bf16 v[88:91], a[196:199], a[172:175], v[88:91]
v_exp_f32 v171, v171
v_mfma_f32_16x16x32_bf16 v[88:91], a[200:203], a[180:183], v[88:91]
buffer_load_dwordx4 v3, s[40:43], s71 offen offset:3072 lds
v_mfma_f32_16x16x32_bf16 v[88:91], a[204:207], a[188:191], v[88:91]
v_cvt_pk_bf16_f32 v135, v166, v167
s_waitcnt lgkmcnt(0)
v_mfma_f32_16x16x32_bf16 v[68:71], a[208:211], a[128:131], 0
ds_read_b128 a[192:195], v1 offset:8192
v_cvt_pk_bf16_f32 v140, v168, v169
v_mfma_f32_16x16x32_bf16 v[68:71], a[212:215], a[136:139], v[68:71]
v_exp_f32 v172, v172
v_mfma_f32_16x16x32_bf16 v[68:71], a[216:219], a[144:147], v[68:71]
ds_read_b128 a[196:199], v1 offset:9216
s_add_u32 m0, s75, 102400
v_mfma_f32_16x16x32_bf16 v[68:71], a[220:223], a[152:155], v[68:71]
buffer_load_dwordx4 v4, s[40:43], s71 offen offset:0 lds
v_mfma_f32_16x16x32_bf16 v[76:79], a[208:211], a[132:135], 0
ds_read_b128 a[200:203], v1 offset:10240
v_cvt_pk_bf16_f32 v141, v170, v171
v_mfma_f32_16x16x32_bf16 v[76:79], a[212:215], a[140:143], v[76:79]
v_exp_f32 v173, v173
v_mfma_f32_16x16x32_bf16 v[76:79], a[216:219], a[148:151], v[76:79]
ds_read_b128 a[204:207], v1 offset:11264
v_cvt_pk_bf16_f32 v142, v172, v173
v_mfma_f32_16x16x32_bf16 v[76:79], a[220:223], a[156:159], v[76:79]
buffer_load_dwordx4 v4, s[40:43], s71 offen offset:1024 lds
v_mfma_f32_16x16x32_bf16 v[84:87], a[208:211], a[160:163], 0
v_exp_f32 v174, v174
v_mfma_f32_16x16x32_bf16 v[84:87], a[212:215], a[168:171], v[84:87]
v_exp_f32 v175, v175
v_mfma_f32_16x16x32_bf16 v[84:87], a[216:219], a[176:179], v[84:87]
v_exp_f32 v176, v176
v_mfma_f32_16x16x32_bf16 v[84:87], a[220:223], a[184:187], v[84:87]
buffer_load_dwordx4 v4, s[40:43], s71 offen offset:2048 lds
v_mfma_f32_16x16x32_bf16 v[92:95], a[208:211], a[164:167], 0
v_exp_f32 v177, v177
v_mfma_f32_16x16x32_bf16 v[92:95], a[212:215], a[172:175], v[92:95]
v_exp_f32 v178, v178
v_mfma_f32_16x16x32_bf16 v[92:95], a[216:219], a[180:183], v[92:95]
buffer_load_dwordx4 v4, s[40:43], s71 offen offset:3072 lds
v_mfma_f32_16x16x32_bf16 v[92:95], a[220:223], a[188:191], v[92:95]
v_cvt_pk_bf16_f32 v143, v174, v175
s_waitcnt lgkmcnt(0)
v_mfma_f32_16x16x32_bf16 v[96:99], a[192:195], a[128:131], 0
ds_read_b128 a[208:211], v1 offset:12288
v_cvt_pk_bf16_f32 v148, v176, v177
v_mfma_f32_16x16x32_bf16 v[96:99], a[196:199], a[136:139], v[96:99]
v_exp_f32 v179, v179
v_mfma_f32_16x16x32_bf16 v[96:99], a[200:203], a[144:147], v[96:99]
ds_read_b128 a[212:215], v1 offset:13312
s_add_u32 m0, s75, 65536
v_mfma_f32_16x16x32_bf16 v[96:99], a[204:207], a[152:155], v[96:99]
buffer_load_dwordx4 v3, s[36:39], s70 offen offset:0 lds
v_mfma_f32_16x16x32_bf16 v[104:107], a[192:195], a[132:135], 0
ds_read_b128 a[216:219], v1 offset:14336
v_cvt_pk_bf16_f32 v149, v178, v179
v_mfma_f32_16x16x32_bf16 v[104:107], a[196:199], a[140:143], v[104:107]
v_exp_f32 v180, v180
v_mfma_f32_16x16x32_bf16 v[104:107], a[200:203], a[148:151], v[104:107]
ds_read_b128 a[220:223], v1 offset:15360
v_mfma_f32_16x16x32_bf16 v[104:107], a[204:207], a[156:159], v[104:107]
buffer_load_dwordx4 v3, s[36:39], s70 offen offset:1024 lds
v_mfma_f32_16x16x32_bf16 v[112:115], a[192:195], a[160:163], 0
v_exp_f32 v181, v181
v_mfma_f32_16x16x32_bf16 v[112:115], a[196:199], a[168:171], v[112:115]
v_exp_f32 v182, v182
v_mfma_f32_16x16x32_bf16 v[112:115], a[200:203], a[176:179], v[112:115]
v_exp_f32 v183, v183
v_mfma_f32_16x16x32_bf16 v[112:115], a[204:207], a[184:187], v[112:115]
buffer_load_dwordx4 v3, s[36:39], s70 offen offset:2048 lds
v_mfma_f32_16x16x32_bf16 v[120:123], a[192:195], a[164:167], 0
v_exp_f32 v184, v184
v_mfma_f32_16x16x32_bf16 v[120:123], a[196:199], a[172:175], v[120:123]
v_exp_f32 v185, v185
v_mfma_f32_16x16x32_bf16 v[120:123], a[200:203], a[180:183], v[120:123]
buffer_load_dwordx4 v3, s[36:39], s70 offen offset:3072 lds
v_mfma_f32_16x16x32_bf16 v[120:123], a[204:207], a[188:191], v[120:123]
v_cvt_pk_bf16_f32 v150, v180, v181
s_waitcnt lgkmcnt(0)
v_mfma_f32_16x16x32_bf16 v[100:103], a[208:211], a[128:131], 0
ds_read_b128 a[192:195], v1 offset:32768
v_cvt_pk_bf16_f32 v151, v182, v183
v_mfma_f32_16x16x32_bf16 v[100:103], a[212:215], a[136:139], v[100:103]
v_exp_f32 v186, v186
v_mfma_f32_16x16x32_bf16 v[100:103], a[216:219], a[144:147], v[100:103]
ds_read_b128 a[196:199], v1 offset:40960
s_add_u32 m0, s75, 69632
v_mfma_f32_16x16x32_bf16 v[100:103], a[220:223], a[152:155], v[100:103]
buffer_load_dwordx4 v4, s[36:39], s70 offen offset:0 lds
v_mfma_f32_16x16x32_bf16 v[108:111], a[208:211], a[132:135], 0
ds_read_b128 a[200:203], v1 offset:33792
v_cvt_pk_bf16_f32 v156, v184, v185
v_mfma_f32_16x16x32_bf16 v[108:111], a[212:215], a[140:143], v[108:111]
v_exp_f32 v187, v187
v_mfma_f32_16x16x32_bf16 v[108:111], a[216:219], a[148:151], v[108:111]
ds_read_b128 a[204:207], v1 offset:41984
v_cvt_pk_bf16_f32 v157, v186, v187
v_mfma_f32_16x16x32_bf16 v[108:111], a[220:223], a[156:159], v[108:111]
buffer_load_dwordx4 v4, s[36:39], s70 offen offset:1024 lds
v_mfma_f32_16x16x32_bf16 v[116:119], a[208:211], a[160:163], 0
v_exp_f32 v188, v188
v_mfma_f32_16x16x32_bf16 v[116:119], a[212:215], a[168:171], v[116:119]
v_exp_f32 v189, v189
v_mfma_f32_16x16x32_bf16 v[116:119], a[216:219], a[176:179], v[116:119]
v_exp_f32 v190, v190
v_mfma_f32_16x16x32_bf16 v[116:119], a[220:223], a[184:187], v[116:119]
buffer_load_dwordx4 v4, s[36:39], s70 offen offset:2048 lds
v_mfma_f32_16x16x32_bf16 v[124:127], a[208:211], a[164:167], 0
v_exp_f32 v191, v191
v_mfma_f32_16x16x32_bf16 v[124:127], a[212:215], a[172:175], v[124:127]
v_cvt_pk_bf16_f32 v158, v188, v189
v_cvt_pk_bf16_f32 v159, v190, v191
v_mfma_f32_16x16x32_bf16 v[124:127], a[216:219], a[180:183], v[124:127]
buffer_load_dwordx4 v4, s[36:39], s70 offen offset:3072 lds
v_mfma_f32_16x16x32_bf16 v[124:127], a[220:223], a[188:191], v[124:127]
s_waitcnt lgkmcnt(0)
v_mfma_f32_16x16x32_bf16 a[0:3], a[192:195], v[128:131], a[0:3]
ds_read_b128 a[208:211], v1 offset:34816
v_mfma_f32_16x16x32_bf16 a[0:3], a[196:199], v[132:135], a[0:3]
s_cmp_eq_u32 s54, s56
s_cbranch_scc1 MASK2_%=
MASKR2_%=:
v_smfmac_f32_16x16x64_bf16 a[224:227], a[240:243], v[128:135], v14
ds_read_b128 a[212:215], v1 offset:43008
v_smfmac_f32_16x16x64_bf16 a[228:231], a[240:243], v[136:143], v14
v_mfma_f32_16x16x32_bf16 a[4:7], a[200:203], v[128:131], a[4:7]
ds_read_b128 a[216:219], v1 offset:35840
v_mfma_f32_16x16x32_bf16 a[4:7], a[204:207], v[132:135], a[4:7]
v_smfmac_f32_16x16x64_bf16 a[232:235], a[240:243], v[144:151], v14
ds_read_b128 a[220:223], v1 offset:44032
v_smfmac_f32_16x16x64_bf16 a[236:239], a[240:243], v[152:159], v14
v_mfma_f32_16x16x32_bf16 a[32:35], a[192:195], v[136:139], a[32:35]
v_mfma_f32_16x16x32_bf16 a[32:35], a[196:199], v[140:143], a[32:35]
RARER2_%=:
v_mfma_f32_16x16x32_bf16 a[36:39], a[200:203], v[136:139], a[36:39]
v_exp_f32 v64, v64
v_mfma_f32_16x16x32_bf16 a[36:39], a[204:207], v[140:143], a[36:39]
v_exp_f32 v65, v65
v_mfma_f32_16x16x32_bf16 a[64:67], a[192:195], v[144:147], a[64:67]
v_exp_f32 v66, v66
v_mfma_f32_16x16x32_bf16 a[64:67], a[196:199], v[148:151], a[64:67]
v_exp_f32 v67, v67
v_mfma_f32_16x16x32_bf16 a[68:71], a[200:203], v[144:147], a[68:71]
v_exp_f32 v68, v68
v_mfma_f32_16x16x32_bf16 a[68:71], a[204:207], v[148:151], a[68:71]
v_exp_f32 v69, v69
v_mfma_f32_16x16x32_bf16 a[96:99], a[192:195], v[152:155], a[96:99]
v_exp_f32 v70, v70
v_mfma_f32_16x16x32_bf16 a[96:99], a[196:199], v[156:159], a[96:99]
v_exp_f32 v71, v71
v_mfma_f32_16x16x32_bf16 a[100:103], a[200:203], v[152:155], a[100:103]
v_cvt_pk_bf16_f32 v64, v64, v65
v_cvt_pk_bf16_f32 v65, v66, v67
v_mfma_f32_16x16x32_bf16 a[100:103], a[204:207], v[156:159], a[100:103]
v_cvt_pk_bf16_f32 v66, v68, v69
s_waitcnt lgkmcnt(0)
v_mfma_f32_16x16x32_bf16 a[8:11], a[208:211], v[128:131], a[8:11]
ds_read_b128 a[192:195], v1 offset:36864
v_cvt_pk_bf16_f32 v67, v70, v71
v_mfma_f32_16x16x32_bf16 a[8:11], a[212:215], v[132:135], a[8:11]
v_exp_f32 v72, v72
v_mfma_f32_16x16x32_bf16 a[12:15], a[216:219], v[128:131], a[12:15]
ds_read_b128 a[196:199], v1 offset:45056
v_mfma_f32_16x16x32_bf16 a[12:15], a[220:223], v[132:135], a[12:15]
v_exp_f32 v73, v73
v_mfma_f32_16x16x32_bf16 a[40:43], a[208:211], v[136:139], a[40:43]
ds_read_b128 a[200:203], v1 offset:37888
v_cvt_pk_bf16_f32 v72, v72, v73
v_mfma_f32_16x16x32_bf16 a[40:43], a[212:215], v[140:143], a[40:43]
v_exp_f32 v74, v74
v_mfma_f32_16x16x32_bf16 a[44:47], a[216:219], v[136:139], a[44:47]
ds_read_b128 a[204:207], v1 offset:46080
v_mfma_f32_16x16x32_bf16 a[44:47], a[220:223], v[140:143], a[44:47]
v_exp_f32 v75, v75
v_mfma_f32_16x16x32_bf16 a[72:75], a[208:211], v[144:147], a[72:75]
v_exp_f32 v76, v76
v_mfma_f32_16x16x32_bf16 a[72:75], a[212:215], v[148:151], a[72:75]
v_exp_f32 v77, v77
v_mfma_f32_16x16x32_bf16 a[76:79], a[216:219], v[144:147], a[76:79]
v_exp_f32 v78, v78
v_mfma_f32_16x16x32_bf16 a[76:79], a[220:223], v[148:151], a[76:79]
v_exp_f32 v79, v79
v_mfma_f32_16x16x32_bf16 a[104:107], a[208:211], v[152:155], a[104:107]
v_exp_f32 v80, v80
v_mfma_f32_16x16x32_bf16 a[104:107], a[212:215], v[156:159], a[104:107]
v_exp_f32 v81, v81
v_mfma_f32_16x16x32_bf16 a[108:111], a[216:219], v[152:155], a[108:111]
v_cvt_pk_bf16_f32 v73, v74, v75
v_cvt_pk_bf16_f32 v74, v76, v77
v_mfma_f32_16x16x32_bf16 a[108:111], a[220:223], v[156:159], a[108:111]
v_cvt_pk_bf16_f32 v75, v78, v79
s_waitcnt lgkmcnt(0)
v_mfma_f32_16x16x32_bf16 a[16:19], a[192:195], v[128:131], a[16:19]
ds_read_b128 a[208:211], v1 offset:38912
s_mov_b32 s71, s70
v_mfma_f32_16x16x32_bf16 a[16:19], a[196:199], v[132:135], a[16:19]
v_exp_f32 v82, v82
)"
R"(v_mfma_f32_16x16x32_bf16 a[20:23], a[200:203], v[128:131], a[20:23]
ds_read_b128 a[212:215], v1 offset:47104
s_add_u32 s70, s70, 32768
v_mfma_f32_16x16x32_bf16 a[20:23], a[204:207], v[132:135], a[20:23]
v_exp_f32 v83, v83
v_mfma_f32_16x16x32_bf16 a[48:51], a[192:195], v[136:139], a[48:51]
ds_read_b128 a[216:219], v1 offset:39936
s_cmp_eq_u32 s54, s72
v_mfma_f32_16x16x32_bf16 a[48:51], a[196:199], v[140:143], a[48:51]
v_exp_f32 v84, v84
v_mfma_f32_16x16x32_bf16 a[52:55], a[200:203], v[136:139], a[52:55]
ds_read_b128 a[220:223], v1 offset:48128
s_cselect_b32 s70, 0, s70
v_mfma_f32_16x16x32_bf16 a[52:55], a[204:207], v[140:143], a[52:55]
v_exp_f32 v85, v85
v_mfma_f32_16x16x32_bf16 a[80:83], a[192:195], v[144:147], a[80:83]
v_exp_f32 v86, v86
v_mfma_f32_16x16x32_bf16 a[80:83], a[196:199], v[148:151], a[80:83]
v_exp_f32 v87, v87
v_mfma_f32_16x16x32_bf16 a[84:87], a[200:203], v[144:147], a[84:87]
v_cvt_pk_bf16_f32 v80, v80, v81
v_cvt_pk_bf16_f32 v81, v82, v83
v_mfma_f32_16x16x32_bf16 a[84:87], a[204:207], v[148:151], a[84:87]
v_exp_f32 v88, v88
v_mfma_f32_16x16x32_bf16 a[112:115], a[192:195], v[152:155], a[112:115]
v_exp_f32 v89, v89
v_mfma_f32_16x16x32_bf16 a[112:115], a[196:199], v[156:159], a[112:115]
v_exp_f32 v90, v90
v_mfma_f32_16x16x32_bf16 a[116:119], a[200:203], v[152:155], a[116:119]
v_exp_f32 v91, v91
v_mfma_f32_16x16x32_bf16 a[116:119], a[204:207], v[156:159], a[116:119]
s_waitcnt vmcnt(0) lgkmcnt(0)
s_barrier
v_mfma_f32_16x16x32_bf16 a[24:27], a[208:211], v[128:131], a[24:27]
ds_read_b128 a[192:195], v2 offset:0
v_cvt_pk_bf16_f32 v82, v84, v85
v_mfma_f32_16x16x32_bf16 a[24:27], a[212:215], v[132:135], a[24:27]
v_exp_f32 v92, v92
v_mfma_f32_16x16x32_bf16 a[28:31], a[216:219], v[128:131], a[28:31]
ds_read_b128 a[196:199], v2 offset:1024
v_cvt_pk_bf16_f32 v83, v86, v87
v_mfma_f32_16x16x32_bf16 a[28:31], a[220:223], v[132:135], a[28:31]
v_exp_f32 v93, v93
v_mfma_f32_16x16x32_bf16 a[56:59], a[208:211], v[136:139], a[56:59]
ds_read_b128 a[200:203], v2 offset:2048
v_cvt_pk_bf16_f32 v88, v88, v89
v_mfma_f32_16x16x32_bf16 a[56:59], a[212:215], v[140:143], a[56:59]
v_exp_f32 v94, v94
v_mfma_f32_16x16x32_bf16 a[60:63], a[216:219], v[136:139], a[60:63]
ds_read_b128 a[204:207], v2 offset:3072
v_cvt_pk_bf16_f32 v89, v90, v91
v_mfma_f32_16x16x32_bf16 a[60:63], a[220:223], v[140:143], a[60:63]
v_exp_f32 v95, v95
v_mfma_f32_16x16x32_bf16 a[88:91], a[208:211], v[144:147], a[88:91]
v_exp_f32 v96, v96
v_mfma_f32_16x16x32_bf16 a[88:91], a[212:215], v[148:151], a[88:91]
v_exp_f32 v97, v97
v_mfma_f32_16x16x32_bf16 a[92:95], a[216:219], v[144:147], a[92:95]
v_exp_f32 v98, v98
v_mfma_f32_16x16x32_bf16 a[92:95], a[220:223], v[148:151], a[92:95]
v_exp_f32 v99, v99
v_mfma_f32_16x16x32_bf16 a[120:123], a[208:211], v[152:155], a[120:123]
v_cvt_pk_bf16_f32 v90, v92, v93
v_cvt_pk_bf16_f32 v91, v94, v95
v_mfma_f32_16x16x32_bf16 a[120:123], a[212:215], v[156:159], a[120:123]
v_exp_f32 v100, v100
v_mfma_f32_16x16x32_bf16 a[124:127], a[216:219], v[152:155], a[124:127]
v_cvt_pk_bf16_f32 v68, v96, v97
s_bitcmp1_b32 s57, s54
v_mfma_f32_16x16x32_bf16 a[124:127], a[220:223], v[156:159], a[124:127]
s_cbranch_scc1 END2_%=
ENDR2_%=:
PAD11_%=:
s_cbranch_execz PAD20_%=
LOOP_%=:
s_waitcnt lgkmcnt(0)
v_mfma_f32_16x16x32_bf16 v[128:131], a[192:195], a[128:131], 0
ds_read_b128 a[208:211], v2 offset:4096
s_add_u32 s54, s54, 1
v_mfma_f32_16x16x32_bf16 v[128:131], a[196:199], a[136:139], v[128:131]
v_exp_f32 v101, v101
v_mfma_f32_16x16x32_bf16 v[128:131], a[200:203], a[144:147], v[128:131]
ds_read_b128 a[212:215], v2 offset:5120
s_add_u32 m0, s75, 32768
v_mfma_f32_16x16x32_bf16 v[128:131], a[204:207], a[152:155], v[128:131]
buffer_load_dwordx4 v3, s[40:43], s71 offen offset:0 lds
v_mfma_f32_16x16x32_bf16 v[136:139], a[192:195], a[132:135], 0
ds_read_b128 a[216:219], v2 offset:6144
v_cvt_pk_bf16_f32 v69, v98, v99
v_mfma_f32_16x16x32_bf16 v[136:139], a[196:199], a[140:143], v[136:139]
v_exp_f32 v102, v102
v_mfma_f32_16x16x32_bf16 v[136:139], a[200:203], a[148:151], v[136:139]
ds_read_b128 a[220:223], v2 offset:7168
v_cvt_pk_bf16_f32 v70, v100, v101
v_mfma_f32_16x16x32_bf16 v[136:139], a[204:207], a[156:159], v[136:139]
buffer_load_dwordx4 v3, s[40:43], s71 offen offset:1024 lds
v_mfma_f32_16x16x32_bf16 v[144:147], a[192:195], a[160:163], 0
v_exp_f32 v103, v103
v_mfma_f32_16x16x32_bf16 v[144:147], a[196:199], a[168:171], v[144:147]
v_exp_f32 v104, v104
v_mfma_f32_16x16x32_bf16 v[144:147], a[200:203], a[176:179], v[144:147]
v_exp_f32 v105, v105
v_mfma_f32_16x16x32_bf16 v[144:147], a[204:207], a[184:187], v[144:147]
buffer_load_dwordx4 v3, s[40:43], s71 offen offset:2048 lds
v_mfma_f32_16x16x32_bf16 v[152:155], a[192:195], a[164:167], 0
v_exp_f32 v106, v106
v_mfma_f32_16x16x32_bf16 v[152:155], a[196:199], a[172:175], v[152:155]
v_exp_f32 v107, v107
v_mfma_f32_16x16x32_bf16 v[152:155], a[200:203], a[180:183], v[152:155]
buffer_load_dwordx4 v3, s[40:43], s71 offen offset:3072 lds
v_mfma_f32_16x16x32_bf16 v[152:155], a[204:207], a[188:191], v[152:155]
v_cvt_pk_bf16_f32 v71, v102, v103
s_waitcnt lgkmcnt(0)
v_mfma_f32_16x16x32_bf16 v[132:135], a[208:211], a[128:131], 0
ds_read_b128 a[192:195], v2 offset:8192
v_cvt_pk_bf16_f32 v76, v104, v105
PAD12_%=:
s_cbranch_execz PAD21_%=
v_mfma_f32_16x16x32_bf16 v[132:135], a[212:215], a[136:139], v[132:135]
v_exp_f32 v108, v108
v_mfma_f32_16x16x32_bf16 v[132:135], a[216:219], a[144:147], v[132:135]
ds_read_b128 a[196:199], v2 offset:9216
s_add_u32 m0, s75, 36864
v_mfma_f32_16x16x32_bf16 v[132:135], a[220:223], a[152:155], v[132:135]
buffer_load_dwordx4 v4, s[40:43], s71 offen offset:0 lds
v_mfma_f32_16x16x32_bf16 v[140:143], a[208:211], a[132:135], 0
ds_read_b128 a[200:203], v2 offset:10240
v_cvt_pk_bf16_f32 v77, v106, v107
v_mfma_f32_16x16x32_bf16 v[140:143], a[212:215], a[140:143], v[140:143]
v_exp_f32 v109, v109
v_mfma_f32_16x16x32_bf16 v[140:143], a[216:219], a[148:151], v[140:143]
ds_read_b128 a[204:207], v2 offset:11264
v_cvt_pk_bf16_f32 v78, v108, v109
v_mfma_f32_16x16x32_bf16 v[140:143], a[220:223], a[156:159], v[140:143]
buffer_load_dwordx4 v4, s[40:43], s71 offen offset:1024 lds
v_mfma_f32_16x16x32_bf16 v[148:151], a[208:211], a[160:163], 0
v_exp_f32 v110, v110
v_mfma_f32_16x16x32_bf16 v[148:151], a[212:215], a[168:171], v[148:151]
v_exp_f32 v111, v111
v_mfma_f32_16x16x32_bf16 v[148:151], a[216:219], a[176:179], v[148:151]
v_exp_f32 v112, v112
v_mfma_f32_16x16x32_bf16 v[148:151], a[220:223], a[184:187], v[148:151]
buffer_load_dwordx4 v4, s[40:43], s71 offen offset:2048 lds
v_mfma_f32_16x16x32_bf16 v[156:159], a[208:211], a[164:167], 0
v_exp_f32 v113, v113
v_mfma_f32_16x16x32_bf16 v[156:159], a[212:215], a[172:175], v[156:159]
v_exp_f32 v114, v114
v_mfma_f32_16x16x32_bf16 v[156:159], a[216:219], a[180:183], v[156:159]
buffer_load_dwordx4 v4, s[40:43], s71 offen offset:3072 lds
v_mfma_f32_16x16x32_bf16 v[156:159], a[220:223], a[188:191], v[156:159]
v_cvt_pk_bf16_f32 v79, v110, v111
s_waitcnt lgkmcnt(0)
v_mfma_f32_16x16x32_bf16 v[160:163], a[192:195], a[128:131], 0
ds_read_b128 a[208:211], v2 offset:12288
v_cvt_pk_bf16_f32 v84, v112, v113
v_mfma_f32_16x16x32_bf16 v[160:163], a[196:199], a[136:139], v[160:163]
v_exp_f32 v115, v115
v_mfma_f32_16x16x32_bf16 v[160:163], a[200:203], a[144:147], v[160:163]
ds_read_b128 a[212:215], v2 offset:13312
s_add_u32 m0, s75, 0
v_mfma_f32_16x16x32_bf16 v[160:163], a[204:207], a[152:155], v[160:163]
buffer_load_dwordx4 v3, s[36:39], s70 offen offset:0 lds
v_mfma_f32_16x16x32_bf16 v[168:171], a[192:195], a[132:135], 0
ds_read_b128 a[216:219], v2 offset:14336
v_cvt_pk_bf16_f32 v85, v114, v115
v_mfma_f32_16x16x32_bf16 v[168:171], a[196:199], a[140:143], v[168:171]
v_exp_f32 v116, v116
v_mfma_f32_16x16x32_bf16 v[168:171], a[200:203], a[148:151], v[168:171]
ds_read_b128 a[220:223], v2 offset:15360
v_mfma_f32_16x16x32_bf16 v[168:171], a[204:207], a[156:159], v[168:171]
buffer_load_dwordx4 v3, s[36:39], s70 offen offset:1024 lds
v_mfma_f32_16x16x32_bf16 v[176:179], a[192:195], a[160:163], 0
v_exp_f32 v117, v117
v_mfma_f32_16x16x32_bf16 v[176:179], a[196:199], a[168:171], v[176:179]
v_exp_f32 v118, v118
v_mfma_f32_16x16x32_bf16 v[176:179], a[200:203], a[176:179], v[176:179]
v_exp_f32 v119, v119
v_mfma_f32_16x16x32_bf16 v[176:179], a[204:207], a[184:187], v[176:179]
buffer_load_dwordx4 v3, s[36:39], s70 offen offset:2048 lds
v_mfma_f32_16x16x32_bf16 v[184:187], a[192:195], a[164:167], 0
v_exp_f32 v120, v120
v_mfma_f32_16x16x32_bf16 v[184:187], a[196:199], a[172:175], v[184:187]
v_exp_f32 v121, v121
v_mfma_f32_16x16x32_bf16 v[184:187], a[200:203], a[180:183], v[184:187]
buffer_load_dwordx4 v3, s[36:39], s70 offen offset:3072 lds
v_mfma_f32_16x16x32_bf16 v[184:187], a[204:207], a[188:191], v[184:187]
v_cvt_pk_bf16_f32 v86, v116, v117
s_waitcnt lgkmcnt(0)
v_mfma_f32_16x16x32_bf16 v[164:167], a[208:211], a[128:131], 0
ds_read_b128 a[192:195], v2 offset:32768
v_cvt_pk_bf16_f32 v87, v118, v119
v_mfma_f32_16x16x32_bf16 v[164:167], a[212:215], a[136:139], v[164:167]
v_exp_f32 v122, v122
v_mfma_f32_16x16x32_bf16 v[164:167], a[216:219], a[144:147], v[164:167]
ds_read_b128 a[196:199], v2 offset:40960
s_add_u32 m0, s75, 4096
v_mfma_f32_16x16x32_bf16 v[164:167], a[220:223], a[152:155], v[164:167]
buffer_load_dwordx4 v4, s[36:39], s70 offen offset:0 lds
v_mfma_f32_16x16x32_bf16 v[172:175], a[208:211], a[132:135], 0
ds_read_b128 a[200:203], v2 offset:33792
v_cvt_pk_bf16_f32 v92, v120, v121
v_mfma_f32_16x16x32_bf16 v[172:175], a[212:215], a[140:143], v[172:175]
v_exp_f32 v123, v123
v_mfma_f32_16x16x32_bf16 v[172:175], a[216:219], a[148:151], v[172:175]
ds_read_b128 a[204:207], v2 offset:41984
v_cvt_pk_bf16_f32 v93, v122, v123
v_mfma_f32_16x16x32_bf16 v[172:175], a[220:223], a[156:159], v[172:175]
buffer_load_dwordx4 v4, s[36:39], s70 offen offset:1024 lds
v_mfma_f32_16x16x32_bf16 v[180:183], a[208:211], a[160:163], 0
v_exp_f32 v124, v124
v_mfma_f32_16x16x32_bf16 v[180:183], a[212:215], a[168:171], v[180:183]
v_exp_f32 v125, v125
v_mfma_f32_16x16x32_bf16 v[180:183], a[216:219], a[176:179], v[180:183]
v_exp_f32 v126, v126
v_mfma_f32_16x16x32_bf16 v[180:183], a[220:223], a[184:187], v[180:183]
buffer_load_dwordx4 v4, s[36:39], s70 offen offset:2048 lds
v_mfma_f32_16x16x32_bf16 v[188:191], a[208:211], a[164:167], 0
v_exp_f32 v127, v127
v_mfma_f32_16x16x32_bf16 v[188:191], a[212:215], a[172:175], v[188:191]
v_cvt_pk_bf16_f32 v94, v124, v125
v_cvt_pk_bf16_f32 v95, v126, v127
v_mfma_f32_16x16x32_bf16 v[188:191], a[216:219], a[180:183], v[188:191]
buffer_load_dwordx4 v4, s[36:39], s70 offen offset:3072 lds
v_mfma_f32_16x16x32_bf16 v[188:191], a[220:223], a[188:191], v[188:191]
s_waitcnt lgkmcnt(0)
v_mfma_f32_16x16x32_bf16 a[0:3], a[192:195], v[64:67], a[0:3]
ds_read_b128 a[208:211], v2 offset:34816
v_mfma_f32_16x16x32_bf16 a[0:3], a[196:199], v[68:71], a[0:3]
s_cmp_eq_u32 s54, s56
s_cbranch_scc1 MASK1_%=
MASKR1_%=:
v_smfmac_f32_16x16x64_bf16 a[224:227], a[240:243], v[64:71], v14
ds_read_b128 a[212:215], v2 offset:43008
v_smfmac_f32_16x16x64_bf16 a[228:231], a[240:243], v[72:79], v14
v_mfma_f32_16x16x32_bf16 a[4:7], a[200:203], v[64:67], a[4:7]
ds_read_b128 a[216:219], v2 offset:35840
v_mfma_f32_16x16x32_bf16 a[4:7], a[204:207], v[68:71], a[4:7]
v_smfmac_f32_16x16x64_bf16 a[232:235], a[240:243], v[80:87], v14
ds_read_b128 a[220:223], v2 offset:44032
v_smfmac_f32_16x16x64_bf16 a[236:239], a[240:243], v[88:95], v14
v_mfma_f32_16x16x32_bf16 a[32:35], a[192:195], v[72:75], a[32:35]
v_mfma_f32_16x16x32_bf16 a[32:35], a[196:199], v[76:79], a[32:35]
RARER1_%=:
v_mfma_f32_16x16x32_bf16 a[36:39], a[200:203], v[72:75], a[36:39]
v_exp_f32 v128, v128
v_mfma_f32_16x16x32_bf16 a[36:39], a[204:207], v[76:79], a[36:39]
v_exp_f32 v129, v129
v_mfma_f32_16x16x32_bf16 a[64:67], a[192:195], v[80:83], a[64:67]
v_exp_f32 v130, v130
v_mfma_f32_16x16x32_bf16 a[64:67], a[196:199], v[84:87], a[64:67]
v_exp_f32 v131, v131
v_mfma_f32_16x16x32_bf16 a[68:71], a[200:203], v[80:83], a[68:71]
v_exp_f32 v132, v132
v_mfma_f32_16x16x32_bf16 a[68:71], a[204:207], v[84:87], a[68:71]
v_exp_f32 v133, v133
v_mfma_f32_16x16x32_bf16 a[96:99], a[192:195], v[88:91], a[96:99]
v_exp_f32 v134, v134
v_mfma_f32_16x16x32_bf16 a[96:99], a[196:199], v[92:95], a[96:99]
v_exp_f32 v135, v135
v_mfma_f32_16x16x32_bf16 a[100:103], a[200:203], v[88:91], a[100:103]
v_cvt_pk_bf16_f32 v128, v128, v129
v_cvt_pk_bf16_f32 v129, v130, v131
v_mfma_f32_16x16x32_bf16 a[100:103], a[204:207], v[92:95], a[100:103]
v_cvt_pk_bf16_f32 v130, v132, v133
s_waitcnt lgkmcnt(0)
v_mfma_f32_16x16x32_bf16 a[8:11], a[208:211], v[64:67], a[8:11]
ds_read_b128 a[192:195], v2 offset:36864
v_cvt_pk_bf16_f32 v131, v134, v135
v_mfma_f32_16x16x32_bf16 a[8:11], a[212:215], v[68:71], a[8:11]
v_exp_f32 v136, v136
v_mfma_f32_16x16x32_bf16 a[12:15], a[216:219], v[64:67], a[12:15]
ds_read_b128 a[196:199], v2 offset:45056
v_mfma_f32_16x16x32_bf16 a[12:15], a[220:223], v[68:71], a[12:15]
v_exp_f32 v137, v137
v_mfma_f32_16x16x32_bf16 a[40:43], a[208:211], v[72:75], a[40:43]
ds_read_b128 a[200:203], v2 offset:37888
v_cvt_pk_bf16_f32 v136, v136, v137
v_mfma_f32_16x16x32_bf16 a[40:43], a[212:215], v[76:79], a[40:43]
v_exp_f32 v138, v138
v_mfma_f32_16x16x32_bf16 a[44:47], a[216:219], v[72:75], a[44:47]
ds_read_b128 a[204:207], v2 offset:46080
v_mfma_f32_16x16x32_bf16 a[44:47], a[220:223], v[76:79], a[44:47]
v_exp_f32 v139, v139
v_mfma_f32_16x16x32_bf16 a[72:75], a[208:211], v[80:83], a[72:75]
v_exp_f32 v140, v140
v_mfma_f32_16x16x32_bf16 a[72:75], a[212:215], v[84:87], a[72:75]
v_exp_f32 v141, v141
v_mfma_f32_16x16x32_bf16 a[76:79], a[216:219], v[80:83], a[76:79]
v_exp_f32 v142, v142
v_mfma_f32_16x16x32_bf16 a[76:79], a[220:223], v[84:87], a[76:79]
v_exp_f32 v143, v143
v_mfma_f32_16x16x32_bf16 a[104:107], a[208:211], v[88:91], a[104:107]
v_exp_f32 v144, v144
v_mfma_f32_16x16x32_bf16 a[104:107], a[212:215], v[92:95], a[104:107]
v_exp_f32 v145, v145
v_mfma_f32_16x16x32_bf16 a[108:111], a[216:219], v[88:91], a[108:111]
v_cvt_pk_bf16_f32 v137, v138, v139
v_cvt_pk_bf16_f32 v138, v140, v141
v_mfma_f32_16x16x32_bf16 a[108:111], a[220:223], v[92:95], a[108:111]
v_cvt_pk_bf16_f32 v139, v142, v143
s_waitcnt lgkmcnt(0)
v_mfma_f32_16x16x32_bf16 a[16:19], a[192:195], v[64:67], a[16:19]
ds_read_b128 a[208:211], v2 offset:38912
s_mov_b32 s71, s70
v_mfma_f32_16x16x32_bf16 a[16:19], a[196:199], v[68:71], a[16:19]
v_exp_f32 v146, v146
v_mfma_f32_16x16x32_bf16 a[20:23], a[200:203], v[64:67], a[20:23]
ds_read_b128 a[212:215], v2 offset:47104
s_add_u32 s70, s70, 32768
v_mfma_f32_16x16x32_bf16 a[20:23], a[204:207], v[68:71], a[20:23]
v_exp_f32 v147, v147
v_mfma_f32_16x16x32_bf16 a[48:51], a[192:195], v[72:75], a[48:51]
ds_read_b128 a[216:219], v2 offset:39936
s_cmp_eq_u32 s54, s72
v_mfma_f32_16x16x32_bf16 a[48:51], a[196:199], v[76:79], a[48:51]
v_exp_f32 v148, v148
v_mfma_f32_16x16x32_bf16 a[52:55], a[200:203], v[72:75], a[52:55]
ds_read_b128 a[220:223], v2 offset:48128
s_cselect_b32 s70, 0, s70
v_mfma_f32_16x16x32_bf16 a[52:55], a[204:207], v[76:79], a[52:55]
v_exp_f32 v149, v149
v_mfma_f32_16x16x32_bf16 a[80:83], a[192:195], v[80:83], a[80:83]
v_exp_f32 v150, v150
v_mfma_f32_16x16x32_bf16 a[80:83], a[196:199], v[84:87], a[80:83]
v_exp_f32 v151, v151
v_mfma_f32_16x16x32_bf16 a[84:87], a[200:203], v[80:83], a[84:87]
v_cvt_pk_bf16_f32 v144, v144, v145
v_cvt_pk_bf16_f32 v145, v146, v147
v_mfma_f32_16x16x32_bf16 a[84:87], a[204:207], v[84:87], a[84:87]
v_exp_f32 v152, v152
v_mfma_f32_16x16x32_bf16 a[112:115], a[192:195], v[88:91], a[112:115]
v_exp_f32 v153, v153
v_mfma_f32_16x16x32_bf16 a[112:115], a[196:199], v[92:95], a[112:115]
v_exp_f32 v154, v154
v_mfma_f32_16x16x32_bf16 a[116:119], a[200:203], v[88:91], a[116:119]
v_exp_f32 v155, v155
v_mfma_f32_16x16x32_bf16 a[116:119], a[204:207], v[92:95], a[116:119]
s_waitcnt vmcnt(0) lgkmcnt(0)
s_barrier
v_mfma_f32_16x16x32_bf16 a[24:27], a[208:211], v[64:67], a[24:27]
ds_read_b128 a[192:195], v1 offset:0
v_cvt_pk_bf16_f32 v146, v148, v149
v_mfma_f32_16x16x32_bf16 a[24:27], a[212:215], v[68:71], a[24:27]
v_exp_f32 v156, v156
v_mfma_f32_16x16x32_bf16 a[28:31], a[216:219], v[64:67], a[28:31]
ds_read_b128 a[196:199], v1 offset:1024
v_cvt_pk_bf16_f32 v147, v150, v151
v_mfma_f32_16x16x32_bf16 a[28:31], a[220:223], v[68:71], a[28:31]
v_exp_f32 v157, v157
v_mfma_f32_16x16x32_bf16 a[56:59], a[208:211], v[72:75], a[56:59]
ds_read_b128 a[200:203], v1 offset:2048
v_cvt_pk_bf16_f32 v152, v152, v153
v_mfma_f32_16x16x32_bf16 a[56:59], a[212:215], v[76:79], a[56:59]
v_exp_f32 v158, v158
v_mfma_f32_16x16x32_bf16 a[60:63], a[216:219], v[72:75], a[60:63]
ds_read_b128 a[204:207], v1 offset:3072
v_cvt_pk_bf16_f32 v153, v154, v155
v_mfma_f32_16x16x32_bf16 a[60:63], a[220:223], v[76:79], a[60:63]
v_exp_f32 v159, v159
v_mfma_f32_16x16x32_bf16 a[88:91], a[208:211], v[80:83], a[88:91]
v_exp_f32 v160, v160
v_mfma_f32_16x16x32_bf16 a[88:91], a[212:215], v[84:87], a[88:91]
v_exp_f32 v161, v161
v_mfma_f32_16x16x32_bf16 a[92:95], a[216:219], v[80:83], a[92:95]
v_exp_f32 v162, v162
v_mfma_f32_16x16x32_bf16 a[92:95], a[220:223], v[84:87], a[92:95]
v_exp_f32 v163, v163
v_mfma_f32_16x16x32_bf16 a[120:123], a[208:211], v[88:91], a[120:123]
v_cvt_pk_bf16_f32 v154, v156, v157
v_cvt_pk_bf16_f32 v155, v158, v159
v_mfma_f32_16x16x32_bf16 a[120:123], a[212:215], v[92:95], a[120:123]
v_exp_f32 v164, v164
v_mfma_f32_16x16x32_bf16 a[124:127], a[216:219], v[88:91], a[124:127]
v_cvt_pk_bf16_f32 v132, v160, v161
s_bitcmp1_b32 s57, s54
v_mfma_f32_16x16x32_bf16 a[124:127], a[220:223], v[92:95], a[124:127]
s_cbranch_scc0 LOOPE_%=
s_bitcmp1_b32 s27, s54
s_cbranch_scc1 END1_%=
ENDR1_%=:
s_waitcnt lgkmcnt(0)
v_mfma_f32_16x16x32_bf16 v[64:67], a[192:195], a[128:131], v[192:195]
ds_read_b128 a[208:211], v1 offset:4096
s_add_u32 s54, s54, 1
s_add_u32 m0, s75, 98304
v_mfma_f32_16x16x32_bf16 v[64:67], a[196:199], a[136:139], v[64:67]
v_exp_f32 v165, v165
v_mfma_f32_16x16x32_bf16 v[64:67], a[200:203], a[144:147], v[64:67]
ds_read_b128 a[212:215], v1 offset:5120
buffer_load_dwordx4 v3, s[40:43], s71 offen offset:0 lds
v_mfma_f32_16x16x32_bf16 v[64:67], a[204:207], a[152:155], v[64:67]
v_exp_f32 v166, v166
v_and_b32 v220, 0x1ffff, v19
v_mfma_f32_16x16x32_bf16 v[72:75], a[192:195], a[132:135], 0
ds_read_b128 a[216:219], v1 offset:6144
v_cvt_pk_bf16_f32 v133, v162, v163
v_mfma_f32_16x16x32_bf16 v[72:75], a[196:199], a[140:143], v[72:75]
buffer_load_dwordx4 v3, s[40:43], s71 offen offset:1024 lds
v_cvt_pk_bf16_f32 v134, v164, v165
v_mfma_f32_16x16x32_bf16 v[72:75], a[200:203], a[148:151], v[72:75]
)"
R"(ds_read_b128 a[220:223], v1 offset:7168
v_exp_f32 v167, v167
v_mfma_f32_16x16x32_bf16 v[72:75], a[204:207], a[156:159], v[72:75]
v_exp_f32 v168, v168
v_xor_b32 v223, 16, v220
v_mfma_f32_16x16x32_bf16 v[80:83], a[192:195], a[160:163], v[200:203]
v_exp_f32 v169, v169
v_mfma_f32_16x16x32_bf16 v[80:83], a[196:199], a[168:171], v[80:83]
buffer_load_dwordx4 v3, s[40:43], s71 offen offset:2048 lds
v_exp_f32 v170, v170
v_mfma_f32_16x16x32_bf16 v[80:83], a[200:203], a[176:179], v[80:83]
v_exp_f32 v171, v171
v_add_u32 v221, 3072, v223
v_mfma_f32_16x16x32_bf16 v[80:83], a[204:207], a[184:187], v[80:83]
buffer_load_dwordx4 v3, s[40:43], s71 offen offset:3072 lds
v_cvt_pk_bf16_f32 v135, v166, v167
v_mfma_f32_16x16x32_bf16 v[88:91], a[192:195], a[164:167], 0
s_add_u32 m0, s75, 102400
v_exp_f32 v172, v172
v_mfma_f32_16x16x32_bf16 v[88:91], a[196:199], a[172:175], v[88:91]
v_add_u32 v222, 6144, v220
v_cvt_pk_bf16_f32 v140, v168, v169
v_mfma_f32_16x16x32_bf16 v[88:91], a[200:203], a[180:183], v[88:91]
buffer_load_dwordx4 v4, s[40:43], s71 offen offset:0 lds
v_exp_f32 v173, v173
v_mfma_f32_16x16x32_bf16 v[88:91], a[204:207], a[188:191], v[88:91]
v_add_u32 v223, 9216, v223
v_cvt_pk_bf16_f32 v141, v170, v171
s_waitcnt lgkmcnt(0)
v_mfma_f32_16x16x32_bf16 v[68:71], a[208:211], a[128:131], v[196:199]
ds_read_b128 a[192:195], v1 offset:8192
buffer_load_dwordx4 v4, s[40:43], s71 offen offset:1024 lds
v_mfma_f32_16x16x32_bf16 v[68:71], a[212:215], a[136:139], v[68:71]
v_cvt_pk_bf16_f32 v142, v172, v173
v_exp_f32 v174, v174
v_mfma_f32_16x16x32_bf16 v[68:71], a[216:219], a[144:147], v[68:71]
ds_read_b128 a[196:199], v1 offset:9216
v_exp_f32 v175, v175
v_mfma_f32_16x16x32_bf16 v[68:71], a[220:223], a[152:155], v[68:71]
v_exp_f32 v176, v176
v_mfma_f32_16x16x32_bf16 v[76:79], a[208:211], a[132:135], 0
ds_read_b128 a[200:203], v1 offset:10240
buffer_load_dwordx4 v4, s[40:43], s71 offen offset:2048 lds
v_mfma_f32_16x16x32_bf16 v[76:79], a[212:215], a[140:143], v[76:79]
v_exp_f32 v177, v177
v_mfma_f32_16x16x32_bf16 v[76:79], a[216:219], a[148:151], v[76:79]
ds_read_b128 a[204:207], v1 offset:11264
v_exp_f32 v178, v178
v_mfma_f32_16x16x32_bf16 v[76:79], a[220:223], a[156:159], v[76:79]
buffer_load_dwordx4 v4, s[40:43], s71 offen offset:3072 lds
v_cvt_pk_bf16_f32 v143, v174, v175
v_mfma_f32_16x16x32_bf16 v[84:87], a[208:211], a[160:163], v[204:207]
s_add_u32 m0, s75, 65536
v_exp_f32 v179, v179
v_mfma_f32_16x16x32_bf16 v[84:87], a[212:215], a[168:171], v[84:87]
v_cvt_pk_bf16_f32 v148, v176, v177
s_lshl_b32 s70, s46, 17
v_mfma_f32_16x16x32_bf16 v[84:87], a[216:219], a[176:179], v[84:87]
buffer_load_dwordx4 v220, s[20:23], s70 offen offset:0 lds
v_exp_f32 v180, v180
v_mfma_f32_16x16x32_bf16 v[84:87], a[220:223], a[184:187], v[84:87]
v_cvt_pk_bf16_f32 v149, v178, v179
buffer_load_dwordx4 v221, s[20:23], s70 offen offset:1024 lds
v_mfma_f32_16x16x32_bf16 v[92:95], a[208:211], a[164:167], 0
v_exp_f32 v181, v181
v_mfma_f32_16x16x32_bf16 v[92:95], a[212:215], a[172:175], v[92:95]
v_exp_f32 v182, v182
v_mfma_f32_16x16x32_bf16 v[92:95], a[216:219], a[180:183], v[92:95]
v_exp_f32 v183, v183
v_mfma_f32_16x16x32_bf16 v[92:95], a[220:223], a[188:191], v[92:95]
buffer_load_dwordx4 v222, s[20:23], s70 offen offset:2048 lds
s_waitcnt lgkmcnt(0)
v_mfma_f32_16x16x32_bf16 v[104:107], a[192:195], a[132:135], v[192:195]
ds_read_b128 a[208:211], v1 offset:12288
v_exp_f32 v184, v184
v_mfma_f32_16x16x32_bf16 v[104:107], a[196:199], a[140:143], v[104:107]
ds_read_b128 a[212:215], v1 offset:13312
v_exp_f32 v185, v185
v_mfma_f32_16x16x32_bf16 v[104:107], a[200:203], a[148:151], v[104:107]
ds_read_b128 a[216:219], v1 offset:14336
buffer_load_dwordx4 v223, s[20:23], s70 offen offset:3072 lds
v_mfma_f32_16x16x32_bf16 v[104:107], a[204:207], a[156:159], v[104:107]
ds_read_b128 a[220:223], v1 offset:15360
v_cvt_pk_bf16_f32 v150, v180, v181
s_add_u32 m0, s75, 69632
v_mfma_f32_16x16x32_bf16 v[120:123], a[192:195], a[164:167], v[200:203]
v_exp_f32 v186, v186
v_cvt_pk_bf16_f32 v151, v182, v183
v_mfma_f32_16x16x32_bf16 v[120:123], a[196:199], a[172:175], v[120:123]
s_add_u32 s70, s70, 0x4000
buffer_load_dwordx4 v220, s[20:23], s70 offen offset:0 lds
v_mfma_f32_16x16x32_bf16 v[120:123], a[200:203], a[180:183], v[120:123]
v_bfe_u32 v4, v22, 2, 7
v_exp_f32 v187, v187
v_mfma_f32_16x16x32_bf16 v[120:123], a[204:207], a[188:191], v[120:123]
v_cvt_pk_bf16_f32 v156, v184, v185
s_waitcnt lgkmcnt(0)
v_mfma_f32_16x16x32_bf16 v[108:111], a[208:211], a[132:135], v[196:199]
ds_read_b128 a[192:195], v1 offset:32768
v_add_u32 v4, s59, v4
v_mfma_f32_16x16x32_bf16 v[108:111], a[212:215], a[140:143], v[108:111]
ds_read_b128 a[196:199], v1 offset:40960
buffer_load_dwordx4 v221, s[20:23], s70 offen offset:1024 lds
v_mfma_f32_16x16x32_bf16 v[108:111], a[216:219], a[148:151], v[108:111]
ds_read_b128 a[200:203], v1 offset:33792
v_cvt_pk_bf16_f32 v157, v186, v187
v_mfma_f32_16x16x32_bf16 v[108:111], a[220:223], a[156:159], v[108:111]
ds_read_b128 a[204:207], v1 offset:41984
v_exp_f32 v188, v188
v_mfma_f32_16x16x32_bf16 v[124:127], a[208:211], a[164:167], v[204:207]
v_exp_f32 v189, v189
v_mfma_f32_16x16x32_bf16 v[124:127], a[212:215], a[172:175], v[124:127]
v_exp_f32 v190, v190
buffer_load_dwordx4 v222, s[20:23], s70 offen offset:2048 lds
v_mfma_f32_16x16x32_bf16 v[124:127], a[216:219], a[180:183], v[124:127]
v_exp_f32 v191, v191
v_cvt_pk_bf16_f32 v158, v188, v189
v_mfma_f32_16x16x32_bf16 v[124:127], a[220:223], a[188:191], v[124:127]
v_cvt_pk_bf16_f32 v159, v190, v191
s_waitcnt lgkmcnt(0)
v_mfma_f32_16x16x32_bf16 a[0:3], a[192:195], v[128:131], a[0:3]
ds_read_b128 a[208:211], v1 offset:34816
v_mfma_f32_16x16x32_bf16 a[0:3], a[196:199], v[132:135], a[0:3]
v_smfmac_f32_16x16x64_bf16 a[224:227], a[240:243], v[128:135], v14
ds_read_b128 a[212:215], v1 offset:43008
v_smfmac_f32_16x16x64_bf16 a[228:231], a[240:243], v[136:143], v14
v_mfma_f32_16x16x32_bf16 a[4:7], a[200:203], v[128:131], a[4:7]
ds_read_b128 a[216:219], v1 offset:35840
v_mfma_f32_16x16x32_bf16 a[4:7], a[204:207], v[132:135], a[4:7]
v_smfmac_f32_16x16x64_bf16 a[232:235], a[240:243], v[144:151], v14
ds_read_b128 a[220:223], v1 offset:44032
v_smfmac_f32_16x16x64_bf16 a[236:239], a[240:243], v[152:159], v14
v_mfma_f32_16x16x32_bf16 a[32:35], a[192:195], v[136:139], a[32:35]
v_mfma_f32_16x16x32_bf16 a[32:35], a[196:199], v[140:143], a[32:35]
RARER4_%=:
v_mfma_f32_16x16x32_bf16 a[36:39], a[200:203], v[136:139], a[36:39]
v_exp_f32 v64, v64
v_mfma_f32_16x16x32_bf16 a[36:39], a[204:207], v[140:143], a[36:39]
v_exp_f32 v65, v65
v_mfma_f32_16x16x32_bf16 a[64:67], a[192:195], v[144:147], a[64:67]
v_exp_f32 v66, v66
v_mfma_f32_16x16x32_bf16 a[64:67], a[196:199], v[148:151], a[64:67]
v_exp_f32 v67, v67
v_mfma_f32_16x16x32_bf16 a[68:71], a[200:203], v[144:147], a[68:71]
v_exp_f32 v68, v68
v_mfma_f32_16x16x32_bf16 a[68:71], a[204:207], v[148:151], a[68:71]
v_exp_f32 v69, v69
v_mfma_f32_16x16x32_bf16 a[96:99], a[192:195], v[152:155], a[96:99]
v_exp_f32 v70, v70
v_mfma_f32_16x16x32_bf16 a[96:99], a[196:199], v[156:159], a[96:99]
v_exp_f32 v71, v71
v_mfma_f32_16x16x32_bf16 a[100:103], a[200:203], v[152:155], a[100:103]
v_cvt_pk_bf16_f32 v64, v64, v65
v_cvt_pk_bf16_f32 v65, v66, v67
v_mfma_f32_16x16x32_bf16 a[100:103], a[204:207], v[156:159], a[100:103]
v_cvt_pk_bf16_f32 v66, v68, v69
s_waitcnt lgkmcnt(0)
v_mfma_f32_16x16x32_bf16 a[8:11], a[208:211], v[128:131], a[8:11]
ds_read_b128 a[192:195], v1 offset:36864
v_cvt_pk_bf16_f32 v67, v70, v71
v_mfma_f32_16x16x32_bf16 a[8:11], a[212:215], v[132:135], a[8:11]
v_exp_f32 v72, v72
v_mfma_f32_16x16x32_bf16 a[12:15], a[216:219], v[128:131], a[12:15]
ds_read_b128 a[196:199], v1 offset:45056
v_mfma_f32_16x16x32_bf16 a[12:15], a[220:223], v[132:135], a[12:15]
v_exp_f32 v73, v73
v_mfma_f32_16x16x32_bf16 a[40:43], a[208:211], v[136:139], a[40:43]
ds_read_b128 a[200:203], v1 offset:37888
v_cvt_pk_bf16_f32 v72, v72, v73
v_mfma_f32_16x16x32_bf16 a[40:43], a[212:215], v[140:143], a[40:43]
v_exp_f32 v74, v74
v_mfma_f32_16x16x32_bf16 a[44:47], a[216:219], v[136:139], a[44:47]
ds_read_b128 a[204:207], v1 offset:46080
v_mfma_f32_16x16x32_bf16 a[44:47], a[220:223], v[140:143], a[44:47]
v_exp_f32 v75, v75
v_mfma_f32_16x16x32_bf16 a[72:75], a[208:211], v[144:147], a[72:75]
v_exp_f32 v76, v76
v_mfma_f32_16x16x32_bf16 a[72:75], a[212:215], v[148:151], a[72:75]
v_exp_f32 v77, v77
v_mfma_f32_16x16x32_bf16 a[76:79], a[216:219], v[144:147], a[76:79]
v_exp_f32 v78, v78
v_mfma_f32_16x16x32_bf16 a[76:79], a[220:223], v[148:151], a[76:79]
v_exp_f32 v79, v79
v_mfma_f32_16x16x32_bf16 a[104:107], a[208:211], v[152:155], a[104:107]
v_exp_f32 v80, v80
v_mfma_f32_16x16x32_bf16 a[104:107], a[212:215], v[156:159], a[104:107]
v_exp_f32 v81, v81
v_mfma_f32_16x16x32_bf16 a[108:111], a[216:219], v[152:155], a[108:111]
v_cvt_pk_bf16_f32 v73, v74, v75
v_cvt_pk_bf16_f32 v74, v76, v77
v_mfma_f32_16x16x32_bf16 a[108:111], a[220:223], v[156:159], a[108:111]
v_cvt_pk_bf16_f32 v75, v78, v79
s_waitcnt lgkmcnt(0)
v_mfma_f32_16x16x32_bf16 a[16:19], a[192:195], v[128:131], a[16:19]
ds_read_b128 a[208:211], v1 offset:38912
v_lshrrev_b32 v196, 17, v19
v_mfma_f32_16x16x32_bf16 a[16:19], a[196:199], v[132:135], a[16:19]
v_exp_f32 v82, v82
v_mfma_f32_16x16x32_bf16 a[20:23], a[200:203], v[128:131], a[20:23]
ds_read_b128 a[212:215], v1 offset:47104
v_xor_b32 v197, 16, v196
v_mfma_f32_16x16x32_bf16 a[20:23], a[204:207], v[132:135], a[20:23]
v_exp_f32 v83, v83
v_mfma_f32_16x16x32_bf16 a[48:51], a[192:195], v[136:139], a[48:51]
ds_read_b128 a[216:219], v1 offset:39936
v_add_u32 v198, 0x10000, v196
v_mfma_f32_16x16x32_bf16 a[48:51], a[196:199], v[140:143], a[48:51]
v_exp_f32 v84, v84
v_mfma_f32_16x16x32_bf16 a[52:55], a[200:203], v[136:139], a[52:55]
ds_read_b128 a[220:223], v1 offset:48128
v_add_u32 v199, 0x10000, v197
v_mfma_f32_16x16x32_bf16 a[52:55], a[204:207], v[140:143], a[52:55]
v_exp_f32 v85, v85
v_mfma_f32_16x16x32_bf16 a[80:83], a[192:195], v[144:147], a[80:83]
v_exp_f32 v86, v86
v_mfma_f32_16x16x32_bf16 a[80:83], a[196:199], v[148:151], a[80:83]
v_exp_f32 v87, v87
v_mfma_f32_16x16x32_bf16 a[84:87], a[200:203], v[144:147], a[84:87]
v_cvt_pk_bf16_f32 v80, v80, v81
v_cvt_pk_bf16_f32 v81, v82, v83
v_mfma_f32_16x16x32_bf16 a[84:87], a[204:207], v[148:151], a[84:87]
v_exp_f32 v88, v88
v_mfma_f32_16x16x32_bf16 a[112:115], a[192:195], v[152:155], a[112:115]
v_exp_f32 v89, v89
v_mfma_f32_16x16x32_bf16 a[112:115], a[196:199], v[156:159], a[112:115]
v_exp_f32 v90, v90
s_lshl_b32 s70, s47, 17
v_mfma_f32_16x16x32_bf16 a[116:119], a[200:203], v[152:155], a[116:119]
v_exp_f32 v91, v91
s_add_u32 s70, s70, 0x4000
v_mfma_f32_16x16x32_bf16 a[116:119], a[204:207], v[156:159], a[116:119]
s_add_u32 m0, s75, 4096
s_waitcnt vmcnt(7) lgkmcnt(0)
s_barrier
s_cmp_eq_u32 s55, 14
s_cbranch_scc1 SIG4_%=
SIGR4_%=:
v_mfma_f32_16x16x32_bf16 a[24:27], a[208:211], v[128:131], a[24:27]
ds_read_b128 a[192:195], v2 offset:32768
buffer_load_dwordx4 v220, s[20:23], s70 offen offset:0 lds
v_cvt_pk_bf16_f32 v82, v84, v85
v_mfma_f32_16x16x32_bf16 a[24:27], a[212:215], v[132:135], a[24:27]
v_exp_f32 v92, v92
v_mfma_f32_16x16x32_bf16 a[28:31], a[216:219], v[128:131], a[28:31]
ds_read_b128 a[196:199], v2 offset:40960
buffer_load_dwordx4 v221, s[20:23], s70 offen offset:1024 lds
v_cvt_pk_bf16_f32 v83, v86, v87
v_mfma_f32_16x16x32_bf16 a[28:31], a[220:223], v[132:135], a[28:31]
v_exp_f32 v93, v93
v_mfma_f32_16x16x32_bf16 a[56:59], a[208:211], v[136:139], a[56:59]
ds_read_b128 a[200:203], v2 offset:33792
v_cvt_pk_bf16_f32 v88, v88, v89
v_mfma_f32_16x16x32_bf16 a[56:59], a[212:215], v[140:143], a[56:59]
buffer_load_dwordx4 v222, s[20:23], s70 offen offset:2048 lds
v_exp_f32 v94, v94
v_mfma_f32_16x16x32_bf16 a[60:63], a[216:219], v[136:139], a[60:63]
ds_read_b128 a[204:207], v2 offset:41984
v_cvt_pk_bf16_f32 v89, v90, v91
v_mfma_f32_16x16x32_bf16 a[60:63], a[220:223], v[140:143], a[60:63]
buffer_load_dwordx4 v223, s[20:23], s70 offen offset:3072 lds
v_exp_f32 v95, v95
v_mfma_f32_16x16x32_bf16 a[88:91], a[208:211], v[144:147], a[88:91]
v_exp_f32 v104, v104
s_lshl_b32 s70, s47, 17
v_mfma_f32_16x16x32_bf16 a[88:91], a[212:215], v[148:151], a[88:91]
v_exp_f32 v105, v105
s_add_u32 m0, s75, 0
v_mfma_f32_16x16x32_bf16 a[92:95], a[216:219], v[144:147], a[92:95]
buffer_load_dwordx4 v221, s[20:23], s70 offen offset:1024 lds
v_exp_f32 v106, v106
v_mfma_f32_16x16x32_bf16 a[92:95], a[220:223], v[148:151], a[92:95]
v_exp_f32 v107, v107
v_mfma_f32_16x16x32_bf16 a[120:123], a[208:211], v[152:155], a[120:123]
buffer_load_dwordx4 v222, s[20:23], s70 offen offset:2048 lds
v_cvt_pk_bf16_f32 v90, v92, v93
v_cvt_pk_bf16_f32 v91, v94, v95
v_mfma_f32_16x16x32_bf16 a[120:123], a[212:215], v[156:159], a[120:123]
v_exp_f32 v108, v108
v_mfma_f32_16x16x32_bf16 a[124:127], a[216:219], v[152:155], a[124:127]
buffer_load_dwordx4 v223, s[20:23], s70 offen offset:3072 lds
v_cvt_pk_bf16_f32 v76, v104, v105
s_bitcmp1_b32 s57, s54
v_mfma_f32_16x16x32_bf16 a[124:127], a[220:223], v[156:159], a[124:127]
s_cbranch_scc1 END4_%=
ENDR4_%=:
DRNS_%=:
s_add_u32 m0, s75, 0
s_lshl_b32 s82, s47, 13
buffer_load_dwordx4 v220, s[20:23], s70 offen offset:0 lds
v_exp_f32 v109, v109
v_cvt_pk_bf16_f32 v77, v106, v107
v_exp_f32 v110, v110
v_cvt_pk_bf16_f32 v78, v108, v109
v_exp_f32 v111, v111
s_add_u32 s47, s47, s29
s_cmp_ge_u32 s47, s100
s_cselect_b32 s47, s101, s47
s_add_u32 m0, s75, 32768
s_lshl_b32 s70, s47, 17
buffer_load_dwordx4 v220, s[20:23], s70 offen offset:0 lds
v_exp_f32 v120, v120
v_exp_f32 v121, v121
v_exp_f32 v122, v122
v_exp_f32 v123, v123
buffer_load_dwordx4 v221, s[20:23], s70 offen offset:1024 lds
v_cvt_pk_bf16_f32 v79, v110, v111
v_cvt_pk_bf16_f32 v92, v120, v121
v_exp_f32 v124, v124
v_cvt_pk_bf16_f32 v93, v122, v123
v_exp_f32 v125, v125
buffer_load_dwordx4 v222, s[20:23], s70 offen offset:2048 lds
v_cvt_pk_bf16_f32 v94, v124, v125
v_exp_f32 v126, v126
v_exp_f32 v127, v127
s_nop 0
v_cvt_pk_bf16_f32 v95, v126, v127
s_waitcnt lgkmcnt(3)
v_mfma_f32_16x16x32_bf16 a[0:3], a[192:195], v[64:67], a[0:3]
ds_read_b128 a[208:211], v2 offset:34816
s_waitcnt lgkmcnt(2)
v_mfma_f32_16x16x32_bf16 a[4:7], a[200:203], v[64:67], a[4:7]
ds_read_b128 a[212:215], v2 offset:43008
buffer_load_dwordx4 v223, s[20:23], s70 offen offset:3072 lds
v_mfma_f32_16x16x32_bf16 a[32:35], a[192:195], v[72:75], a[32:35]
ds_read_b128 a[216:219], v2 offset:35840
v_add_u32 v30, 0x20000, v3
PAD13_%=:
s_cbranch_execz PAD22_%=
v_mfma_f32_16x16x32_bf16 a[32:35], a[196:199], v[76:79], a[32:35]
s_add_u32 m0, s75, 36864
s_add_u32 s70, s70, 0x4000
buffer_load_dwordx4 v220, s[20:23], s70 offen offset:0 lds
v_mfma_f32_16x16x32_bf16 a[36:39], a[200:203], v[72:75], a[36:39]
ds_read_b128 a[220:223], v2 offset:44032
v_add_u32 v31, 0x18000, v3
s_waitcnt lgkmcnt(4)
v_mfma_f32_16x16x32_bf16 a[36:39], a[204:207], v[76:79], a[36:39]
buffer_load_dwordx4 v221, s[20:23], s70 offen offset:1024 lds
v_xor_b32 v28, 0x2000, v30
v_mfma_f32_16x16x32_bf16 a[64:67], a[192:195], v[80:83], a[64:67]
v_xor_b32 v29, 0x2000, v31
v_add_u32 v26, 0x10000, v3
v_mfma_f32_16x16x32_bf16 a[68:71], a[200:203], v[80:83], a[68:71]
buffer_load_dwordx4 v222, s[20:23], s70 offen offset:2048 lds
v_and_b32 v26, 0x1fcff, v26
v_mfma_f32_16x16x32_bf16 a[96:99], a[192:195], v[88:91], a[96:99]
ds_write_b32 v26, v10 offset:7424
v_xor_b32 v27, 0x2000, v26
v_mfma_f32_16x16x32_bf16 a[96:99], a[196:199], v[92:95], a[96:99]
buffer_load_dwordx4 v223, s[20:23], s70 offen offset:3072 lds
v_mov_b32 v212, v10
v_mfma_f32_16x16x32_bf16 a[100:103], a[200:203], v[88:91], a[100:103]
global_load_dword v193, v4, s[92:93] sc1
v_mov_b32 v214, v10
v_mfma_f32_16x16x32_bf16 a[100:103], a[204:207], v[92:95], a[100:103]
v_mov_b32 v216, v10
s_waitcnt lgkmcnt(1)
v_mfma_f32_16x16x32_bf16 a[8:11], a[208:211], v[64:67], a[8:11]
ds_read_b128 a[192:195], v2 offset:36864
v_mov_b32 v218, v10
v_mfma_f32_16x16x32_bf16 a[12:15], a[216:219], v[64:67], a[12:15]
ds_read_b128 a[196:199], v2 offset:45056
v_accvgpr_read_b32 v32, a0
v_mfma_f32_16x16x32_bf16 a[40:43], a[208:211], v[72:75], a[40:43]
ds_read_b128 a[200:203], v2 offset:37888
v_accvgpr_read_b32 v33, a1
v_mfma_f32_16x16x32_bf16 a[40:43], a[212:215], v[76:79], a[40:43]
v_accvgpr_read_b32 v34, a2
v_accvgpr_read_b32 v35, a3
v_mfma_f32_16x16x32_bf16 a[44:47], a[216:219], v[72:75], a[44:47]
ds_read_b128 a[204:207], v2 offset:46080
v_accvgpr_read_b32 v36, a4
v_mfma_f32_16x16x32_bf16 a[44:47], a[220:223], v[76:79], a[44:47]
ds_write_b128 v30, a[64:67] offset:0
v_accvgpr_read_b32 v37, a5
v_mfma_f32_16x16x32_bf16 a[72:75], a[208:211], v[80:83], a[72:75]
v_accvgpr_read_b32 v38, a6
v_accvgpr_read_b32 v39, a7
v_mfma_f32_16x16x32_bf16 a[76:79], a[216:219], v[80:83], a[76:79]
ds_write_b128 v30, a[68:71] offset:1024
v_accvgpr_read_b32 v224, a32
v_mfma_f32_16x16x32_bf16 a[104:107], a[208:211], v[88:91], a[104:107]
ds_write_b128 v30, a[96:99] offset:4096
v_accvgpr_read_b32 v225, a33
v_mfma_f32_16x16x32_bf16 a[104:107], a[212:215], v[92:95], a[104:107]
v_accvgpr_read_b32 v226, a34
v_accvgpr_read_b32 v227, a35
v_mfma_f32_16x16x32_bf16 a[108:111], a[216:219], v[88:91], a[108:111]
ds_write_b128 v30, a[100:103] offset:5120
v_accvgpr_read_b32 v228, a36
v_mfma_f32_16x16x32_bf16 a[108:111], a[220:223], v[92:95], a[108:111]
v_accvgpr_read_b32 v229, a37
v_accvgpr_read_b32 v230, a38
v_smfmac_f32_16x16x32_bf16 a[232:235], a[240:241], v[80:83], v14
v_accvgpr_read_b32 v231, a39
v_smfmac_f32_16x16x64_bf16 a[236:239], a[240:243], v[88:95], v14
v_accvgpr_read_b32 v40, a8
v_smfmac_f32_16x16x32_bf16 a[224:227], a[240:241], v[64:67], v14
ds_write_b128 v30, a[72:75] offset:2048
v_smfmac_f32_16x16x64_bf16 a[228:231], a[240:243], v[72:79], v14
)"
R"(s_waitcnt lgkmcnt(5)
v_mfma_f32_16x16x32_bf16 a[16:19], a[192:195], v[64:67], a[16:19]
ds_read_b128 a[208:211], v2 offset:38912
ds_write_b128 v30, a[76:79] offset:3072
v_mfma_f32_16x16x32_bf16 a[20:23], a[200:203], v[64:67], a[20:23]
ds_read_b128 a[212:215], v2 offset:47104
ds_write_b128 v30, a[104:107] offset:6144
v_mfma_f32_16x16x32_bf16 a[48:51], a[192:195], v[72:75], a[48:51]
ds_read_b128 a[216:219], v2 offset:39936
v_accvgpr_read_b32 v217, a232
v_mfma_f32_16x16x32_bf16 a[48:51], a[196:199], v[76:79], a[48:51]
v_accvgpr_read_b32 v210, a233
v_accvgpr_read_b32 v219, a236
v_mfma_f32_16x16x32_bf16 a[52:55], a[200:203], v[72:75], a[52:55]
ds_read_b128 a[220:223], v2 offset:48128
v_accvgpr_read_b32 v211, a237
v_mfma_f32_16x16x32_bf16 a[52:55], a[204:207], v[76:79], a[52:55]
ds_write_b128 v30, a[108:111] offset:7168
v_add_f32 v217, v217, v210
v_mfma_f32_16x16x32_bf16 a[80:83], a[192:195], v[80:83], a[80:83]
v_add_f32 v219, v219, v211
v_accvgpr_read_b32 v213, a224
v_mfma_f32_16x16x32_bf16 a[84:87], a[200:203], v[80:83], a[84:87]
ds_write_b128 v27, v[216:219] offset:7168
v_accvgpr_read_b32 v208, a225
v_mfma_f32_16x16x32_bf16 a[112:115], a[192:195], v[88:91], a[112:115]
v_accvgpr_read_b32 v215, a228
v_accvgpr_read_b32 v209, a229
v_mfma_f32_16x16x32_bf16 a[112:115], a[196:199], v[92:95], a[112:115]
s_cmp_eq_u32 s55, 15
s_cbranch_scc1 EPX4_%=
EPXR4_%=:
v_mfma_f32_16x16x32_bf16 a[116:119], a[200:203], v[88:91], a[116:119]
s_waitcnt vmcnt(0)
v_cmp_eq_u32 vcc, s98, v193
s_andn2_b32 s84, s89, vcc_lo
s_cselect_b32 s14, 0, s14
v_mfma_f32_16x16x32_bf16 a[116:119], a[204:207], v[92:95], a[116:119]
s_waitcnt lgkmcnt(0)
s_barrier
v_mfma_f32_16x16x32_bf16 a[88:91], a[208:211], v[80:83], a[88:91]
buffer_load_dwordx4 a[132:135], v22, s[12:15], s82 offen offset:1024 sc1
v_add_f32 v213, v213, v208
v_mfma_f32_16x16x32_bf16 a[92:95], a[216:219], v[80:83], a[92:95]
buffer_load_dwordx4 a[136:139], v22, s[12:15], s82 offen offset:2048 sc1
v_add_f32 v215, v215, v209
v_mfma_f32_16x16x32_bf16 a[120:123], a[208:211], v[88:91], a[120:123]
buffer_load_dwordx4 a[140:143], v22, s[12:15], s82 offen offset:3072 sc1
ds_read_b128 v[6:9], v26 offset:7168
v_mfma_f32_16x16x32_bf16 a[120:123], a[212:215], v[92:95], a[120:123]
ds_write_b128 v31, a[80:83] offset:0
ds_write_b128 v31, a[84:87] offset:1024
v_mfma_f32_16x16x32_bf16 a[124:127], a[216:219], v[88:91], a[124:127]
buffer_load_dwordx4 a[128:131], v22, s[12:15], s82 offen offset:0 sc1
ds_write_b128 v31, a[112:115] offset:4096
v_mfma_f32_16x16x32_bf16 a[124:127], a[220:223], v[92:95], a[124:127]
ds_write_b128 v31, a[116:119] offset:5120
ds_write_b128 v31, a[88:91] offset:2048
v_mfma_f32_16x16x32_bf16 a[24:27], a[208:211], v[64:67], a[24:27]
s_add_u32 s71, s82, 0x1000
buffer_load_dwordx4 a[148:151], v22, s[12:15], s71 offen offset:1024 sc1
v_mfma_f32_16x16x32_bf16 a[28:31], a[216:219], v[64:67], a[28:31]
buffer_load_dwordx4 a[152:155], v22, s[12:15], s71 offen offset:2048 sc1
ds_write_b128 v31, a[92:95] offset:3072
v_mfma_f32_16x16x32_bf16 a[56:59], a[208:211], v[72:75], a[56:59]
buffer_load_dwordx4 a[156:159], v22, s[12:15], s71 offen offset:3072 sc1
ds_write_b128 v31, a[120:123] offset:6144
v_mfma_f32_16x16x32_bf16 a[56:59], a[212:215], v[76:79], a[56:59]
ds_write_b128 v31, a[124:127] offset:7168
s_waitcnt lgkmcnt(8)
v_max_f32 v24, v6, v212
v_mfma_f32_16x16x32_bf16 a[60:63], a[216:219], v[72:75], a[60:63]
buffer_load_dwordx4 a[144:147], v22, s[12:15], s71 offen offset:0 sc1
v_max_f32 v25, v8, v214
v_mfma_f32_16x16x32_bf16 a[60:63], a[220:223], v[76:79], a[60:63]
v_sub_f32 v208, v212, v24
v_sub_f32 v209, v6, v24
v_sub_f32 v210, v214, v25
v_sub_f32 v211, v8, v25
v_exp_f32 v208, v208
v_exp_f32 v209, v209
v_exp_f32 v210, v210
v_exp_f32 v211, v211
v_mul_f32 v24, v208, v213
v_mul_f32 v25, v210, v215
v_fmac_f32 v24, v209, v7
v_fmac_f32 v25, v211, v9
v_rcp_f32 v24, v24
v_rcp_f32 v25, v25
ds_read_b128 v[128:131], v28 offset:0
ds_read_b128 v[132:135], v28 offset:1024
ds_read_b128 v[136:139], v28 offset:2048
ds_read_b128 v[140:143], v28 offset:3072
ds_read_b128 v[160:163], v28 offset:4096
ds_read_b128 v[164:167], v28 offset:5120
ds_read_b128 v[168:171], v28 offset:6144
ds_read_b128 v[172:175], v28 offset:7168
v_accvgpr_read_b32 v41, a9
v_accvgpr_read_b32 v42, a10
v_accvgpr_read_b32 v43, a11
v_accvgpr_read_b32 v44, a12
v_accvgpr_read_b32 v45, a13
v_accvgpr_read_b32 v46, a14
v_accvgpr_read_b32 v47, a15
v_accvgpr_read_b32 v232, a40
v_accvgpr_read_b32 v233, a41
v_accvgpr_read_b32 v234, a42
v_accvgpr_read_b32 v235, a43
v_accvgpr_read_b32 v236, a44
v_accvgpr_read_b32 v237, a45
v_accvgpr_read_b32 v238, a46
v_accvgpr_read_b32 v239, a47
v_accvgpr_read_b32 v48, a16
v_accvgpr_read_b32 v49, a17
v_accvgpr_read_b32 v50, a18
v_accvgpr_read_b32 v51, a19
v_accvgpr_read_b32 v52, a20
v_accvgpr_read_b32 v53, a21
v_accvgpr_read_b32 v54, a22
v_accvgpr_read_b32 v55, a23
v_accvgpr_read_b32 v240, a48
v_accvgpr_read_b32 v241, a49
v_accvgpr_read_b32 v242, a50
v_accvgpr_read_b32 v243, a51
v_accvgpr_read_b32 v244, a52
v_accvgpr_read_b32 v245, a53
v_accvgpr_read_b32 v246, a54
v_accvgpr_read_b32 v247, a55
v_mul_f32 v208, v208, v24
v_mul_f32 v209, v209, v24
v_mul_f32 v210, v210, v25
v_mul_f32 v211, v211, v25
v_max3_f32 v5, v213, v215, v7
v_max3_f32 v5, v5, v9, v24
v_max_f32 v5, v5, v25
v_cmp_lt_f32 vcc, s76, v5
s_cbranch_vccnz LFD_%=
LFDR_%=:
v_pk_mul_f32 v[32:33], v[32:33], v[208:209] op_sel_hi:[1,0]
v_pk_mul_f32 v[34:35], v[34:35], v[208:209] op_sel_hi:[1,0]
v_pk_mul_f32 v[36:37], v[36:37], v[208:209] op_sel_hi:[1,0]
v_pk_mul_f32 v[38:39], v[38:39], v[208:209] op_sel_hi:[1,0]
s_lshl_b32 s83, s47, 13
buffer_load_dwordx4 a[164:167], v22, s[12:15], s83 offen offset:1024 sc1
s_waitcnt lgkmcnt(7)
v_pk_fma_f32 v[32:33], v[128:129], v[208:209], v[32:33] op_sel:[0,1,0] op_sel_hi:[1,1,1]
v_pk_fma_f32 v[34:35], v[130:131], v[208:209], v[34:35] op_sel:[0,1,0] op_sel_hi:[1,1,1]
s_waitcnt lgkmcnt(6)
v_pk_fma_f32 v[36:37], v[132:133], v[208:209], v[36:37] op_sel:[0,1,0] op_sel_hi:[1,1,1]
v_pk_fma_f32 v[38:39], v[134:135], v[208:209], v[38:39] op_sel:[0,1,0] op_sel_hi:[1,1,1]
v_cvt_pk_bf16_f32 v32, v32, v33
v_cvt_pk_bf16_f32 v33, v34, v35
v_cvt_pk_bf16_f32 v34, v36, v37
v_cvt_pk_bf16_f32 v35, v38, v39
global_store_dwordx4 v22, v[32:35], s[52:53] offset:-4096 sc1
v_pk_mul_f32 v[40:41], v[40:41], v[208:209] op_sel_hi:[1,0]
v_pk_mul_f32 v[42:43], v[42:43], v[208:209] op_sel_hi:[1,0]
v_pk_mul_f32 v[44:45], v[44:45], v[208:209] op_sel_hi:[1,0]
v_pk_mul_f32 v[46:47], v[46:47], v[208:209] op_sel_hi:[1,0]
buffer_load_dwordx4 a[168:171], v22, s[12:15], s83 offen offset:2048 sc1
s_waitcnt lgkmcnt(5)
v_pk_fma_f32 v[40:41], v[136:137], v[208:209], v[40:41] op_sel:[0,1,0] op_sel_hi:[1,1,1]
v_pk_fma_f32 v[42:43], v[138:139], v[208:209], v[42:43] op_sel:[0,1,0] op_sel_hi:[1,1,1]
s_waitcnt lgkmcnt(4)
v_pk_fma_f32 v[44:45], v[140:141], v[208:209], v[44:45] op_sel:[0,1,0] op_sel_hi:[1,1,1]
v_pk_fma_f32 v[46:47], v[142:143], v[208:209], v[46:47] op_sel:[0,1,0] op_sel_hi:[1,1,1]
v_cvt_pk_bf16_f32 v40, v40, v41
v_cvt_pk_bf16_f32 v41, v42, v43
v_cvt_pk_bf16_f32 v42, v44, v45
v_cvt_pk_bf16_f32 v43, v46, v47
global_store_dwordx4 v22, v[40:43], s[52:53] offset:-2048 sc1
s_waitcnt lgkmcnt(0)
s_barrier
ds_read_b32 v5, v26 offset:7424
ds_read_b128 v[144:147], v29 offset:0
ds_read_b128 v[148:151], v29 offset:1024
ds_read_b128 v[176:179], v29 offset:4096
ds_read_b128 v[180:183], v29 offset:5120
ds_read_b128 v[152:155], v29 offset:2048
ds_read_b128 v[156:159], v29 offset:3072
ds_read_b128 v[184:187], v29 offset:6144
ds_read_b128 v[188:191], v29 offset:7168
v_pk_mul_f32 v[224:225], v[224:225], v[210:211] op_sel_hi:[1,0]
v_pk_mul_f32 v[226:227], v[226:227], v[210:211] op_sel_hi:[1,0]
v_pk_mul_f32 v[228:229], v[228:229], v[210:211] op_sel_hi:[1,0]
v_pk_mul_f32 v[230:231], v[230:231], v[210:211] op_sel_hi:[1,0]
buffer_load_dwordx4 a[172:175], v22, s[12:15], s83 offen offset:3072 sc1
v_pk_fma_f32 v[224:225], v[160:161], v[210:211], v[224:225] op_sel:[0,1,0] op_sel_hi:[1,1,1]
v_pk_fma_f32 v[226:227], v[162:163], v[210:211], v[226:227] op_sel:[0,1,0] op_sel_hi:[1,1,1]
v_pk_fma_f32 v[228:229], v[164:165], v[210:211], v[228:229] op_sel:[0,1,0] op_sel_hi:[1,1,1]
v_pk_fma_f32 v[230:231], v[166:167], v[210:211], v[230:231] op_sel:[0,1,0] op_sel_hi:[1,1,1]
v_cvt_pk_bf16_f32 v224, v224, v225
v_cvt_pk_bf16_f32 v225, v226, v227
v_cvt_pk_bf16_f32 v226, v228, v229
v_cvt_pk_bf16_f32 v227, v230, v231
global_load_dword v193, v4, s[92:93] sc1
global_store_dwordx4 v22, v[224:227], s[52:53] offset:-3072 sc1
v_pk_mul_f32 v[232:233], v[232:233], v[210:211] op_sel_hi:[1,0]
v_pk_mul_f32 v[234:235], v[234:235], v[210:211] op_sel_hi:[1,0]
v_pk_mul_f32 v[236:237], v[236:237], v[210:211] op_sel_hi:[1,0]
v_pk_mul_f32 v[238:239], v[238:239], v[210:211] op_sel_hi:[1,0]
buffer_load_dwordx4 a[160:163], v22, s[12:15], s83 offen offset:0 sc1
v_pk_fma_f32 v[232:233], v[168:169], v[210:211], v[232:233] op_sel:[0,1,0] op_sel_hi:[1,1,1]
v_pk_fma_f32 v[234:235], v[170:171], v[210:211], v[234:235] op_sel:[0,1,0] op_sel_hi:[1,1,1]
v_pk_fma_f32 v[236:237], v[172:173], v[210:211], v[236:237] op_sel:[0,1,0] op_sel_hi:[1,1,1]
v_pk_fma_f32 v[238:239], v[174:175], v[210:211], v[238:239] op_sel:[0,1,0] op_sel_hi:[1,1,1]
v_cvt_pk_bf16_f32 v232, v232, v233
v_cvt_pk_bf16_f32 v233, v234, v235
v_cvt_pk_bf16_f32 v234, v236, v237
v_cvt_pk_bf16_f32 v235, v238, v239
global_store_dwordx4 v22, v[232:235], s[52:53] offset:-1024 sc1
s_waitcnt lgkmcnt(8)
v_cmp_ne_u32 vcc, 0, v5
s_cbranch_vccnz REDO_%=
s_add_u32 m0, s75, 69632
s_lshl_b32 s70, s46, 17
s_add_u32 s70, s70, 0x4000
buffer_load_dwordx4 v223, s[20:23], s70 offen offset:3072 lds
v_pk_mul_f32 v[48:49], v[48:49], v[208:209] op_sel_hi:[1,0]
v_pk_mul_f32 v[50:51], v[50:51], v[208:209] op_sel_hi:[1,0]
v_pk_mul_f32 v[52:53], v[52:53], v[208:209] op_sel_hi:[1,0]
v_pk_mul_f32 v[54:55], v[54:55], v[208:209] op_sel_hi:[1,0]
s_add_u32 s71, s83, 0x1000
buffer_load_dwordx4 a[180:183], v22, s[12:15], s71 offen offset:1024 sc1
s_waitcnt lgkmcnt(7)
v_pk_fma_f32 v[48:49], v[144:145], v[208:209], v[48:49] op_sel:[0,1,0] op_sel_hi:[1,1,1]
v_pk_fma_f32 v[50:51], v[146:147], v[208:209], v[50:51] op_sel:[0,1,0] op_sel_hi:[1,1,1]
s_waitcnt lgkmcnt(6)
v_pk_fma_f32 v[52:53], v[148:149], v[208:209], v[52:53] op_sel:[0,1,0] op_sel_hi:[1,1,1]
v_pk_fma_f32 v[54:55], v[150:151], v[208:209], v[54:55] op_sel:[0,1,0] op_sel_hi:[1,1,1]
v_cvt_pk_bf16_f32 v48, v48, v49
v_cvt_pk_bf16_f32 v49, v50, v51
v_cvt_pk_bf16_f32 v50, v52, v53
v_cvt_pk_bf16_f32 v51, v54, v55
global_store_dwordx4 v22, v[48:51], s[52:53] offset:0 sc1
v_pk_mul_f32 v[240:241], v[240:241], v[210:211] op_sel_hi:[1,0]
v_pk_mul_f32 v[242:243], v[242:243], v[210:211] op_sel_hi:[1,0]
v_pk_mul_f32 v[244:245], v[244:245], v[210:211] op_sel_hi:[1,0]
v_pk_mul_f32 v[246:247], v[246:247], v[210:211] op_sel_hi:[1,0]
buffer_load_dwordx4 a[184:187], v22, s[12:15], s71 offen offset:2048 sc1
s_waitcnt lgkmcnt(5)
v_pk_fma_f32 v[240:241], v[176:177], v[210:211], v[240:241] op_sel:[0,1,0] op_sel_hi:[1,1,1]
v_pk_fma_f32 v[242:243], v[178:179], v[210:211], v[242:243] op_sel:[0,1,0] op_sel_hi:[1,1,1]
s_waitcnt lgkmcnt(4)
v_pk_fma_f32 v[244:245], v[180:181], v[210:211], v[244:245] op_sel:[0,1,0] op_sel_hi:[1,1,1]
v_pk_fma_f32 v[246:247], v[182:183], v[210:211], v[246:247] op_sel:[0,1,0] op_sel_hi:[1,1,1]
v_cvt_pk_bf16_f32 v240, v240, v241
v_cvt_pk_bf16_f32 v241, v242, v243
v_cvt_pk_bf16_f32 v242, v244, v245
v_cvt_pk_bf16_f32 v243, v246, v247
global_store_dwordx4 v22, v[240:243], s[52:53] offset:1024 sc1
v_accvgpr_read_b32 v56, a24
v_accvgpr_read_b32 v57, a25
v_accvgpr_read_b32 v58, a26
v_accvgpr_read_b32 v59, a27
v_accvgpr_read_b32 v60, a28
v_accvgpr_read_b32 v61, a29
v_accvgpr_read_b32 v62, a30
v_accvgpr_read_b32 v63, a31
v_pk_mul_f32 v[56:57], v[56:57], v[208:209] op_sel_hi:[1,0]
v_pk_mul_f32 v[58:59], v[58:59], v[208:209] op_sel_hi:[1,0]
v_pk_mul_f32 v[60:61], v[60:61], v[208:209] op_sel_hi:[1,0]
v_pk_mul_f32 v[62:63], v[62:63], v[208:209] op_sel_hi:[1,0]
buffer_load_dwordx4 a[188:191], v22, s[12:15], s71 offen offset:3072 sc1
s_waitcnt lgkmcnt(3)
v_pk_fma_f32 v[56:57], v[152:153], v[208:209], v[56:57] op_sel:[0,1,0] op_sel_hi:[1,1,1]
v_pk_fma_f32 v[58:59], v[154:155], v[208:209], v[58:59] op_sel:[0,1,0] op_sel_hi:[1,1,1]
s_waitcnt lgkmcnt(2)
v_pk_fma_f32 v[60:61], v[156:157], v[208:209], v[60:61] op_sel:[0,1,0] op_sel_hi:[1,1,1]
v_pk_fma_f32 v[62:63], v[158:159], v[208:209], v[62:63] op_sel:[0,1,0] op_sel_hi:[1,1,1]
v_cvt_pk_bf16_f32 v56, v56, v57
v_cvt_pk_bf16_f32 v57, v58, v59
v_cvt_pk_bf16_f32 v58, v60, v61
v_cvt_pk_bf16_f32 v59, v62, v63
global_store_dwordx4 v22, v[56:59], s[52:53] offset:2048 sc1
v_accvgpr_read_b32 v248, a56
v_accvgpr_read_b32 v249, a57
v_accvgpr_read_b32 v250, a58
v_accvgpr_read_b32 v251, a59
v_accvgpr_read_b32 v252, a60
v_accvgpr_read_b32 v253, a61
v_accvgpr_read_b32 v254, a62
v_accvgpr_read_b32 v255, a63
v_pk_mul_f32 v[248:249], v[248:249], v[210:211] op_sel_hi:[1,0]
v_pk_mul_f32 v[250:251], v[250:251], v[210:211] op_sel_hi:[1,0]
v_pk_mul_f32 v[252:253], v[252:253], v[210:211] op_sel_hi:[1,0]
v_pk_mul_f32 v[254:255], v[254:255], v[210:211] op_sel_hi:[1,0]
buffer_load_dwordx4 a[176:179], v22, s[12:15], s71 offen offset:0 sc1
s_mov_b32 s47, s46
s_add_u32 s46, s46, s29
s_cmp_ge_u32 s46, s100
s_cselect_b32 s46, s101, s46
s_waitcnt lgkmcnt(1)
v_pk_fma_f32 v[248:249], v[184:185], v[210:211], v[248:249] op_sel:[0,1,0] op_sel_hi:[1,1,1]
v_pk_fma_f32 v[250:251], v[186:187], v[210:211], v[250:251] op_sel:[0,1,0] op_sel_hi:[1,1,1]
s_waitcnt lgkmcnt(0)
v_pk_fma_f32 v[252:253], v[188:189], v[210:211], v[252:253] op_sel:[0,1,0] op_sel_hi:[1,1,1]
v_pk_fma_f32 v[254:255], v[190:191], v[210:211], v[254:255] op_sel:[0,1,0] op_sel_hi:[1,1,1]
v_cvt_pk_bf16_f32 v248, v248, v249
v_cvt_pk_bf16_f32 v249, v250, v251
v_cvt_pk_bf16_f32 v250, v252, v253
v_cvt_pk_bf16_f32 v251, v254, v255
global_store_dwordx4 v22, v[248:251], s[52:53] offset:3072 sc1
JOIN_%=:
s_cmp_lg_u32 s84, 0
s_cbranch_scc1 ERL_%=
ERLR_%=:
s_nop 1
s_mov_b32 s89, s99
s_waitcnt lgkmcnt(0)
s_barrier
ds_read_b64_tr_b16 v[64:65], v196 offset:256
ds_read_b64_tr_b16 v[66:67], v196 offset:2304
ds_read_b64_tr_b16 v[68:69], v196 offset:8448
ds_read_b64_tr_b16 v[70:71], v196 offset:10496
ds_read_b64_tr_b16 v[72:73], v197 offset:256
ds_read_b64_tr_b16 v[74:75], v197 offset:2304
ds_read_b64_tr_b16 v[76:77], v197 offset:8448
ds_read_b64_tr_b16 v[78:79], v197 offset:10496
ds_read_b64_tr_b16 v[80:81], v196 offset:512
ds_read_b64_tr_b16 v[82:83], v196 offset:2560
ds_read_b64_tr_b16 v[84:85], v196 offset:8704
ds_read_b64_tr_b16 v[86:87], v196 offset:10752
ds_read_b64_tr_b16 v[88:89], v197 offset:512
ds_read_b64_tr_b16 v[90:91], v197 offset:2560
ds_read_b64_tr_b16 v[92:93], v197 offset:8704
ds_read_b64_tr_b16 v[94:95], v197 offset:10752
s_cmp_lg_u32 s84, 0
s_cbranch_scc1 ERM_%=
ERMR_%=:
s_waitcnt vmcnt(22) lgkmcnt(8)
v_mfma_f32_16x16x32_bf16 v[144:147], v[64:67], a[128:131], 0
ds_read_b64_tr_b16 v[96:97], v196 offset:768
v_mov_b32 v24, 1
v_mfma_f32_16x16x32_bf16 v[144:147], v[68:71], a[136:139], v[144:147]
ds_read_b64_tr_b16 v[98:99], v196 offset:2816
s_sub_u32 s44, s44, 1
v_mfma_f32_16x16x32_bf16 v[152:155], v[64:67], a[132:135], 0
ds_read_b64_tr_b16 v[100:101], v196 offset:8960
s_lshl_b32 s77, s31, 6
v_mfma_f32_16x16x32_bf16 v[152:155], v[68:71], a[140:143], v[152:155]
ds_read_b64_tr_b16 v[102:103], v196 offset:11008
v_mfma_f32_16x16x32_bf16 v[148:151], v[72:75], a[128:131], 0
ds_read_b64_tr_b16 v[104:105], v197 offset:768
s_add_u32 s77, s77, s26
v_mfma_f32_16x16x32_bf16 v[148:151], v[76:79], a[136:139], v[148:151]
ds_read_b64_tr_b16 v[106:107], v197 offset:2816
s_lshl_b32 s77, s77, 7
v_mfma_f32_16x16x32_bf16 v[156:159], v[72:75], a[132:135], 0
ds_read_b64_tr_b16 v[108:109], v197 offset:8960
s_add_u32 s77, s77, 12320
v_mfma_f32_16x16x32_bf16 v[156:159], v[76:79], a[140:143], v[156:159]
ds_read_b64_tr_b16 v[110:111], v197 offset:11008
s_waitcnt lgkmcnt(8)
v_mfma_f32_16x16x32_bf16 v[160:163], v[80:83], a[128:131], 0
ds_read_b64_tr_b16 v[112:113], v196 offset:0
v_mfma_f32_16x16x32_bf16 v[160:163], v[84:87], a[136:139], v[160:163]
ds_read_b64_tr_b16 v[114:115], v196 offset:2048
v_bfe_u32 v27, v22, 4, 2
v_mfma_f32_16x16x32_bf16 v[168:171], v[80:83], a[132:135], 0
ds_read_b64_tr_b16 v[116:117], v196 offset:8192
v_lshl_add_u32 v27, v27, 2, s77
v_mfma_f32_16x16x32_bf16 v[168:171], v[84:87], a[140:143], v[168:171]
ds_read_b64_tr_b16 v[118:119], v196 offset:10240
v_accvgpr_read_b32 v208, a244
v_mfma_f32_16x16x32_bf16 v[164:167], v[88:91], a[128:131], 0
ds_read_b64_tr_b16 v[120:121], v197 offset:0
v_mfma_f32_16x16x32_bf16 v[164:167], v[92:95], a[136:139], v[164:167]
ds_read_b64_tr_b16 v[122:123], v197 offset:2048
v_accvgpr_read_b32 v209, a245
v_mfma_f32_16x16x32_bf16 v[172:175], v[88:91], a[132:135], 0
ds_read_b64_tr_b16 v[124:125], v197 offset:8192
v_accvgpr_read_b32 v210, a246
v_mfma_f32_16x16x32_bf16 v[172:175], v[92:95], a[140:143], v[172:175]
ds_read_b64_tr_b16 v[126:127], v197 offset:10240
s_waitcnt lgkmcnt(8)
v_mfma_f32_16x16x32_bf16 v[176:179], v[96:99], a[128:131], 0
ds_read_b64_tr_b16 v[64:65], v196 offset:16640
v_accvgpr_read_b32 v211, a247
v_mfma_f32_16x16x32_bf16 v[176:179], v[100:103], a[136:139], v[176:179]
ds_read_b64_tr_b16 v[66:67], v196 offset:18688
v_mfma_f32_16x16x32_bf16 v[184:187], v[96:99], a[132:135], 0
ds_read_b64_tr_b16 v[68:69], v196 offset:24832
v_accvgpr_read_b32 v212, a248
v_mfma_f32_16x16x32_bf16 v[184:187], v[100:103], a[140:143], v[184:187]
ds_read_b64_tr_b16 v[70:71], v196 offset:26880
v_accvgpr_read_b32 v213, a249
v_mfma_f32_16x16x32_bf16 v[180:183], v[104:107], a[128:131], 0
ds_read_b64_tr_b16 v[72:73], v197 offset:16640
v_accvgpr_read_b32 v214, a250
v_mfma_f32_16x16x32_bf16 v[180:183], v[108:111], a[136:139], v[180:183]
ds_read_b64_tr_b16 v[74:75], v197 offset:18688
v_mfma_f32_16x16x32_bf16 v[188:191], v[104:107], a[132:135], 0
ds_read_b64_tr_b16 v[76:77], v197 offset:24832
v_accvgpr_read_b32 v215, a251
v_mfma_f32_16x16x32_bf16 v[188:191], v[108:111], a[140:143], v[188:191]
ds_read_b64_tr_b16 v[78:79], v197 offset:26880
s_waitcnt lgkmcnt(8)
v_mfma_f32_16x16x32_bf16 v[128:131], v[112:115], a[128:131], 0
ds_read_b64_tr_b16 v[80:81], v196 offset:16896
)"
R"(s_lshl_b32 s77, s26, 4
v_mfma_f32_16x16x32_bf16 v[128:131], v[116:119], a[136:139], v[128:131]
ds_read_b64_tr_b16 v[82:83], v196 offset:18944
s_add_u32 s77, s77, %2
v_mfma_f32_16x16x32_bf16 v[136:139], v[112:115], a[132:135], 0
ds_read_b64_tr_b16 v[84:85], v196 offset:25088
v_mfma_f32_16x16x32_bf16 v[136:139], v[116:119], a[140:143], v[136:139]
ds_read_b64_tr_b16 v[86:87], v196 offset:27136
s_lshl_b32 s77, s77, 13
v_mfma_f32_16x16x32_bf16 v[132:135], v[120:123], a[128:131], 0
ds_read_b64_tr_b16 v[88:89], v197 offset:16896
s_add_u32 s77, s77, 0x5001000
v_mfma_f32_16x16x32_bf16 v[132:135], v[124:127], a[136:139], v[132:135]
ds_read_b64_tr_b16 v[90:91], v197 offset:18944
s_mov_b64 s[36:37], s[10:11]
v_mfma_f32_16x16x32_bf16 v[140:143], v[120:123], a[132:135], 0
ds_read_b64_tr_b16 v[92:93], v197 offset:25088
v_mfma_f32_16x16x32_bf16 v[140:143], v[124:127], a[140:143], v[140:143]
ds_read_b64_tr_b16 v[94:95], v197 offset:27136
s_waitcnt vmcnt(18) lgkmcnt(8)
v_mfma_f32_16x16x32_bf16 v[144:147], v[64:67], a[144:147], v[144:147]
ds_read_b64_tr_b16 v[96:97], v196 offset:17152
s_add_u32 s36, s36, s77
s_addc_u32 s37, s37, 0
v_mfma_f32_16x16x32_bf16 v[144:147], v[68:71], a[152:155], v[144:147]
ds_read_b64_tr_b16 v[98:99], v196 offset:19200
v_permlane16_swap_b32 v208, v210
v_mfma_f32_16x16x32_bf16 v[152:155], v[64:67], a[148:151], v[152:155]
ds_read_b64_tr_b16 v[100:101], v196 offset:25344
v_permlane16_swap_b32 v209, v211
v_mfma_f32_16x16x32_bf16 v[152:155], v[68:71], a[156:159], v[152:155]
ds_read_b64_tr_b16 v[102:103], v196 offset:27392
v_mfma_f32_16x16x32_bf16 v[148:151], v[72:75], a[144:147], v[148:151]
ds_read_b64_tr_b16 v[104:105], v197 offset:17152
v_permlane16_swap_b32 v212, v214
v_mfma_f32_16x16x32_bf16 v[148:151], v[76:79], a[152:155], v[148:151]
ds_read_b64_tr_b16 v[106:107], v197 offset:19200
v_permlane16_swap_b32 v213, v215
v_mfma_f32_16x16x32_bf16 v[156:159], v[72:75], a[148:151], v[156:159]
ds_read_b64_tr_b16 v[108:109], v197 offset:25344
v_mfma_f32_16x16x32_bf16 v[156:159], v[76:79], a[156:159], v[156:159]
ds_read_b64_tr_b16 v[110:111], v197 offset:27392
s_waitcnt lgkmcnt(8)
v_mfma_f32_16x16x32_bf16 v[160:163], v[80:83], a[144:147], v[160:163]
ds_read_b64_tr_b16 v[112:113], v196 offset:16384
s_waitcnt vmcnt(0)
s_cmp_eq_u32 s84, 0
s_cselect_b64 exec, 1, 0
global_atomic_add v10, v24, s[16:17] sc1
s_mov_b64 exec, -1
v_mfma_f32_16x16x32_bf16 v[160:163], v[84:87], a[152:155], v[160:163]
ds_read_b64_tr_b16 v[114:115], v196 offset:18432
s_add_u32 m0, s75, 98304
s_lshl_b32 s70, s46, 17
buffer_load_dwordx4 v220, s[20:23], s70 offen offset:0 lds
s_lshl_b32 s71, s47, 13
buffer_load_dwordx4 a[4:7], v22, s[12:15], s71 offen offset:1024 sc1
v_mfma_f32_16x16x32_bf16 v[168:171], v[80:83], a[148:151], v[168:171]
ds_read_b64_tr_b16 v[116:117], v196 offset:24576
buffer_load_dwordx4 v221, s[20:23], s70 offen offset:1024 lds
v_mfma_f32_16x16x32_bf16 v[168:171], v[84:87], a[156:159], v[168:171]
ds_read_b64_tr_b16 v[118:119], v196 offset:26624
buffer_load_dwordx4 a[8:11], v22, s[12:15], s71 offen offset:2048 sc1
v_mfma_f32_16x16x32_bf16 v[164:167], v[88:91], a[144:147], v[164:167]
ds_read_b64_tr_b16 v[120:121], v197 offset:16384
buffer_load_dwordx4 v222, s[20:23], s70 offen offset:2048 lds
v_mfma_f32_16x16x32_bf16 v[164:167], v[92:95], a[152:155], v[164:167]
ds_read_b64_tr_b16 v[122:123], v197 offset:18432
buffer_load_dwordx4 a[12:15], v22, s[12:15], s71 offen offset:3072 sc1
v_mfma_f32_16x16x32_bf16 v[172:175], v[88:91], a[148:151], v[172:175]
ds_read_b64_tr_b16 v[124:125], v197 offset:24576
buffer_load_dwordx4 v223, s[20:23], s70 offen offset:3072 lds
v_mfma_f32_16x16x32_bf16 v[172:175], v[92:95], a[156:159], v[172:175]
ds_read_b64_tr_b16 v[126:127], v197 offset:26624
s_waitcnt lgkmcnt(8)
v_mfma_f32_16x16x32_bf16 v[176:179], v[96:99], a[144:147], v[176:179]
buffer_load_dwordx4 a[0:3], v22, s[12:15], s71 offen offset:0 sc1
v_mfma_f32_16x16x32_bf16 v[176:179], v[100:103], a[152:155], v[176:179]
s_add_u32 m0, s75, 102400
s_add_u32 s70, s70, 0x4000
buffer_load_dwordx4 v220, s[20:23], s70 offen offset:0 lds
s_add_u32 s71, s71, 0x1000
buffer_load_dwordx4 a[20:23], v22, s[12:15], s71 offen offset:1024 sc1
v_mfma_f32_16x16x32_bf16 v[184:187], v[96:99], a[148:151], v[184:187]
buffer_load_dwordx4 v221, s[20:23], s70 offen offset:1024 lds
v_mfma_f32_16x16x32_bf16 v[184:187], v[100:103], a[156:159], v[184:187]
s_waitcnt lgkmcnt(0)
s_barrier
v_mfma_f32_16x16x32_bf16 v[180:183], v[104:107], a[144:147], v[180:183]
ds_read_b64_tr_b16 v[64:65], v196 offset:33024
ds_read_b64_tr_b16 v[66:67], v196 offset:35072
v_mfma_f32_16x16x32_bf16 v[180:183], v[108:111], a[152:155], v[180:183]
ds_read_b64_tr_b16 v[68:69], v196 offset:41216
ds_read_b64_tr_b16 v[70:71], v196 offset:43264
v_mfma_f32_16x16x32_bf16 v[188:191], v[104:107], a[148:151], v[188:191]
ds_read_b64_tr_b16 v[72:73], v197 offset:33024
ds_read_b64_tr_b16 v[74:75], v197 offset:35072
PAD14_%=:
s_cbranch_execz PAD23_%=
v_mfma_f32_16x16x32_bf16 v[188:191], v[108:111], a[156:159], v[188:191]
ds_read_b64_tr_b16 v[76:77], v197 offset:41216
v_mfma_f32_16x16x32_bf16 v[128:131], v[112:115], a[144:147], v[128:131]
ds_read_b64_tr_b16 v[78:79], v197 offset:43264
ds_read_b64_tr_b16 v[80:81], v196 offset:33280
v_mfma_f32_16x16x32_bf16 v[128:131], v[116:119], a[152:155], v[128:131]
ds_read_b64_tr_b16 v[82:83], v196 offset:35328
buffer_load_dwordx4 a[24:27], v22, s[12:15], s71 offen offset:2048 sc1
v_mfma_f32_16x16x32_bf16 v[136:139], v[112:115], a[148:151], v[136:139]
ds_read_b64_tr_b16 v[84:85], v196 offset:41472
buffer_load_dwordx4 v222, s[20:23], s70 offen offset:2048 lds
v_mfma_f32_16x16x32_bf16 v[136:139], v[116:119], a[156:159], v[136:139]
ds_read_b64_tr_b16 v[86:87], v196 offset:43520
buffer_load_dwordx4 a[28:31], v22, s[12:15], s71 offen offset:3072 sc1
v_mfma_f32_16x16x32_bf16 v[132:135], v[120:123], a[144:147], v[132:135]
ds_read_b64_tr_b16 v[88:89], v197 offset:33280
buffer_load_dwordx4 v223, s[20:23], s70 offen offset:3072 lds
v_mfma_f32_16x16x32_bf16 v[132:135], v[124:127], a[152:155], v[132:135]
ds_read_b64_tr_b16 v[90:91], v197 offset:35328
buffer_load_dwordx4 a[16:19], v22, s[12:15], s71 offen offset:0 sc1
v_mfma_f32_16x16x32_bf16 v[140:143], v[120:123], a[148:151], v[140:143]
ds_read_b64_tr_b16 v[92:93], v197 offset:41472
global_load_dword v28, v27, s[92:93] sc1
v_mfma_f32_16x16x32_bf16 v[140:143], v[124:127], a[156:159], v[140:143]
ds_read_b64_tr_b16 v[94:95], v197 offset:43520
s_mov_b32 s47, s46
s_add_u32 s46, s46, s29
s_cmp_ge_u32 s46, s100
s_cselect_b32 s46, s101, s46
L3_%=:
s_waitcnt vmcnt(23) lgkmcnt(8)
v_mfma_f32_16x16x32_bf16 v[144:147], v[64:67], a[160:163], v[144:147]
ds_read_b64_tr_b16 v[96:97], v196 offset:33536
s_add_u32 m0, s75, 0
v_mfma_f32_16x16x32_bf16 v[144:147], v[68:71], a[168:171], v[144:147]
ds_read_b64_tr_b16 v[98:99], v196 offset:35584
s_lshl_b32 s70, s46, 17
v_mfma_f32_16x16x32_bf16 v[152:155], v[64:67], a[164:167], v[152:155]
ds_read_b64_tr_b16 v[100:101], v196 offset:41728
buffer_load_dwordx4 v220, s[20:23], s70 offen offset:0 lds
v_mfma_f32_16x16x32_bf16 v[152:155], v[68:71], a[172:175], v[152:155]
ds_read_b64_tr_b16 v[102:103], v196 offset:43776
v_mfma_f32_16x16x32_bf16 v[148:151], v[72:75], a[160:163], v[148:151]
ds_read_b64_tr_b16 v[104:105], v197 offset:33536
v_mfma_f32_16x16x32_bf16 v[148:151], v[76:79], a[168:171], v[148:151]
ds_read_b64_tr_b16 v[106:107], v197 offset:35584
s_lshl_b32 s71, s47, 13
v_mfma_f32_16x16x32_bf16 v[156:159], v[72:75], a[164:167], v[156:159]
ds_read_b64_tr_b16 v[108:109], v197 offset:41728
buffer_load_dwordx4 a[36:39], v22, s[12:15], s71 offen offset:1024 sc1
v_mfma_f32_16x16x32_bf16 v[156:159], v[76:79], a[172:175], v[156:159]
ds_read_b64_tr_b16 v[110:111], v197 offset:43776
s_waitcnt lgkmcnt(8)
v_mfma_f32_16x16x32_bf16 v[160:163], v[80:83], a[160:163], v[160:163]
ds_read_b64_tr_b16 v[112:113], v196 offset:32768
v_mfma_f32_16x16x32_bf16 v[160:163], v[84:87], a[168:171], v[160:163]
ds_read_b64_tr_b16 v[114:115], v196 offset:34816
v_mfma_f32_16x16x32_bf16 v[168:171], v[80:83], a[164:167], v[168:171]
ds_read_b64_tr_b16 v[116:117], v196 offset:40960
buffer_load_dwordx4 v221, s[20:23], s70 offen offset:1024 lds
v_mfma_f32_16x16x32_bf16 v[168:171], v[84:87], a[172:175], v[168:171]
ds_read_b64_tr_b16 v[118:119], v196 offset:43008
v_mfma_f32_16x16x32_bf16 v[164:167], v[88:91], a[160:163], v[164:167]
ds_read_b64_tr_b16 v[120:121], v197 offset:32768
v_mfma_f32_16x16x32_bf16 v[164:167], v[92:95], a[168:171], v[164:167]
ds_read_b64_tr_b16 v[122:123], v197 offset:34816
v_mfma_f32_16x16x32_bf16 v[172:175], v[88:91], a[164:167], v[172:175]
ds_read_b64_tr_b16 v[124:125], v197 offset:40960
buffer_load_dwordx4 a[40:43], v22, s[12:15], s71 offen offset:2048 sc1
v_mfma_f32_16x16x32_bf16 v[172:175], v[92:95], a[172:175], v[172:175]
ds_read_b64_tr_b16 v[126:127], v197 offset:43008
s_waitcnt lgkmcnt(8)
v_mfma_f32_16x16x32_bf16 v[176:179], v[96:99], a[160:163], v[176:179]
ds_read_b64_tr_b16 v[64:65], v196 offset:49408
v_mfma_f32_16x16x32_bf16 v[176:179], v[100:103], a[168:171], v[176:179]
ds_read_b64_tr_b16 v[66:67], v196 offset:51456
v_mfma_f32_16x16x32_bf16 v[184:187], v[96:99], a[164:167], v[184:187]
ds_read_b64_tr_b16 v[68:69], v196 offset:57600
buffer_load_dwordx4 v222, s[20:23], s70 offen offset:2048 lds
v_mfma_f32_16x16x32_bf16 v[184:187], v[100:103], a[172:175], v[184:187]
ds_read_b64_tr_b16 v[70:71], v196 offset:59648
v_mfma_f32_16x16x32_bf16 v[180:183], v[104:107], a[160:163], v[180:183]
ds_read_b64_tr_b16 v[72:73], v197 offset:49408
v_mfma_f32_16x16x32_bf16 v[180:183], v[108:111], a[168:171], v[180:183]
ds_read_b64_tr_b16 v[74:75], v197 offset:51456
v_mfma_f32_16x16x32_bf16 v[188:191], v[104:107], a[164:167], v[188:191]
ds_read_b64_tr_b16 v[76:77], v197 offset:57600
buffer_load_dwordx4 v223, s[20:23], s70 offen offset:3072 lds
v_mfma_f32_16x16x32_bf16 v[188:191], v[108:111], a[172:175], v[188:191]
ds_read_b64_tr_b16 v[78:79], v197 offset:59648
s_waitcnt lgkmcnt(8)
v_mfma_f32_16x16x32_bf16 v[128:131], v[112:115], a[160:163], v[128:131]
ds_read_b64_tr_b16 v[80:81], v196 offset:49664
v_mfma_f32_16x16x32_bf16 v[128:131], v[116:119], a[168:171], v[128:131]
ds_read_b64_tr_b16 v[82:83], v196 offset:51712
v_mfma_f32_16x16x32_bf16 v[136:139], v[112:115], a[164:167], v[136:139]
ds_read_b64_tr_b16 v[84:85], v196 offset:57856
buffer_load_dwordx4 a[44:47], v22, s[12:15], s71 offen offset:3072 sc1
v_mfma_f32_16x16x32_bf16 v[136:139], v[116:119], a[172:175], v[136:139]
ds_read_b64_tr_b16 v[86:87], v196 offset:59904
v_mfma_f32_16x16x32_bf16 v[132:135], v[120:123], a[160:163], v[132:135]
ds_read_b64_tr_b16 v[88:89], v197 offset:49664
s_add_u32 m0, s75, 4096
v_mfma_f32_16x16x32_bf16 v[132:135], v[124:127], a[168:171], v[132:135]
ds_read_b64_tr_b16 v[90:91], v197 offset:51712
s_add_u32 s70, s70, 0x4000
v_mfma_f32_16x16x32_bf16 v[140:143], v[120:123], a[164:167], v[140:143]
ds_read_b64_tr_b16 v[92:93], v197 offset:57856
buffer_load_dwordx4 v220, s[20:23], s70 offen offset:0 lds
v_mfma_f32_16x16x32_bf16 v[140:143], v[124:127], a[172:175], v[140:143]
ds_read_b64_tr_b16 v[94:95], v197 offset:59904
s_waitcnt vmcnt(24) lgkmcnt(8)
v_mfma_f32_16x16x32_bf16 v[144:147], v[64:67], a[176:179], v[144:147]
ds_read_b64_tr_b16 v[96:97], v196 offset:49920
s_mov_b32 s47, s46
v_mfma_f32_16x16x32_bf16 v[144:147], v[68:71], a[184:187], v[144:147]
ds_read_b64_tr_b16 v[98:99], v196 offset:51968
s_add_u32 s46, s46, s29
v_mfma_f32_16x16x32_bf16 v[152:155], v[64:67], a[180:183], v[152:155]
ds_read_b64_tr_b16 v[100:101], v196 offset:58112
buffer_load_dwordx4 a[32:35], v22, s[12:15], s71 offen offset:0 sc1
v_mfma_f32_16x16x32_bf16 v[152:155], v[68:71], a[188:191], v[152:155]
ds_read_b64_tr_b16 v[102:103], v196 offset:60160
s_cmp_ge_u32 s46, s100
v_mfma_f32_16x16x32_bf16 v[148:151], v[72:75], a[176:179], v[148:151]
ds_read_b64_tr_b16 v[104:105], v197 offset:49920
s_cselect_b32 s46, s101, s46
v_mfma_f32_16x16x32_bf16 v[148:151], v[76:79], a[184:187], v[148:151]
ds_read_b64_tr_b16 v[106:107], v197 offset:51968
s_cmp_eq_u32 s44, s90
v_mfma_f32_16x16x32_bf16 v[156:159], v[72:75], a[180:183], v[156:159]
ds_read_b64_tr_b16 v[108:109], v197 offset:58112
buffer_load_dwordx4 v221, s[20:23], s70 offen offset:1024 lds
v_mfma_f32_16x16x32_bf16 v[156:159], v[76:79], a[188:191], v[156:159]
ds_read_b64_tr_b16 v[110:111], v197 offset:60160
s_waitcnt lgkmcnt(8)
v_mfma_f32_16x16x32_bf16 v[160:163], v[80:83], a[176:179], v[160:163]
ds_read_b64_tr_b16 v[112:113], v196 offset:49152
s_cselect_b32 s46, s65, s46
v_mfma_f32_16x16x32_bf16 v[160:163], v[84:87], a[184:187], v[160:163]
ds_read_b64_tr_b16 v[114:115], v196 offset:51200
s_cselect_b32 s101, s48, s101
v_mfma_f32_16x16x32_bf16 v[168:171], v[80:83], a[180:183], v[168:171]
ds_read_b64_tr_b16 v[116:117], v196 offset:57344
buffer_load_dwordx4 v222, s[20:23], s70 offen offset:2048 lds
v_mfma_f32_16x16x32_bf16 v[168:171], v[84:87], a[188:191], v[168:171]
ds_read_b64_tr_b16 v[118:119], v196 offset:59392
v_mfma_f32_16x16x32_bf16 v[164:167], v[88:91], a[176:179], v[164:167]
ds_read_b64_tr_b16 v[120:121], v197 offset:49152
v_mfma_f32_16x16x32_bf16 v[164:167], v[92:95], a[184:187], v[164:167]
ds_read_b64_tr_b16 v[122:123], v197 offset:51200
v_mfma_f32_16x16x32_bf16 v[172:175], v[88:91], a[180:183], v[172:175]
ds_read_b64_tr_b16 v[124:125], v197 offset:57344
buffer_load_dwordx4 v223, s[20:23], s70 offen offset:3072 lds
v_mfma_f32_16x16x32_bf16 v[172:175], v[92:95], a[188:191], v[172:175]
ds_read_b64_tr_b16 v[126:127], v197 offset:59392
s_waitcnt lgkmcnt(8)
v_mfma_f32_16x16x32_bf16 v[176:179], v[96:99], a[176:179], v[176:179]
v_mfma_f32_16x16x32_bf16 v[176:179], v[100:103], a[184:187], v[176:179]
s_add_u32 s71, s71, 0x1000
v_mfma_f32_16x16x32_bf16 v[184:187], v[96:99], a[180:183], v[184:187]
buffer_load_dwordx4 a[52:55], v22, s[12:15], s71 offen offset:1024 sc1
s_sub_u32 s44, s44, 1
v_mfma_f32_16x16x32_bf16 v[184:187], v[100:103], a[188:191], v[184:187]
s_waitcnt lgkmcnt(0)
s_barrier
v_mfma_f32_16x16x32_bf16 v[180:183], v[104:107], a[176:179], v[180:183]
ds_read_b64_tr_b16 v[64:65], v198 offset:256
ds_read_b64_tr_b16 v[66:67], v198 offset:2304
v_mfma_f32_16x16x32_bf16 v[180:183], v[108:111], a[184:187], v[180:183]
ds_read_b64_tr_b16 v[68:69], v198 offset:8448
ds_read_b64_tr_b16 v[70:71], v198 offset:10496
v_mfma_f32_16x16x32_bf16 v[188:191], v[104:107], a[180:183], v[188:191]
ds_read_b64_tr_b16 v[72:73], v199 offset:256
ds_read_b64_tr_b16 v[74:75], v199 offset:2304
v_mfma_f32_16x16x32_bf16 v[188:191], v[108:111], a[188:191], v[188:191]
ds_read_b64_tr_b16 v[76:77], v199 offset:8448
v_mfma_f32_16x16x32_bf16 v[128:131], v[112:115], a[176:179], v[128:131]
ds_read_b64_tr_b16 v[78:79], v199 offset:10496
ds_read_b64_tr_b16 v[80:81], v198 offset:512
v_mfma_f32_16x16x32_bf16 v[128:131], v[116:119], a[184:187], v[128:131]
ds_read_b64_tr_b16 v[82:83], v198 offset:2560
buffer_load_dwordx4 a[56:59], v22, s[12:15], s71 offen offset:2048 sc1
v_mfma_f32_16x16x32_bf16 v[136:139], v[112:115], a[180:183], v[136:139]
ds_read_b64_tr_b16 v[84:85], v198 offset:8704
v_mfma_f32_16x16x32_bf16 v[136:139], v[116:119], a[188:191], v[136:139]
ds_read_b64_tr_b16 v[86:87], v198 offset:10752
v_mfma_f32_16x16x32_bf16 v[132:135], v[120:123], a[176:179], v[132:135]
ds_read_b64_tr_b16 v[88:89], v199 offset:512
buffer_load_dwordx4 a[60:63], v22, s[12:15], s71 offen offset:3072 sc1
v_mfma_f32_16x16x32_bf16 v[132:135], v[124:127], a[184:187], v[132:135]
ds_read_b64_tr_b16 v[90:91], v199 offset:2560
v_mfma_f32_16x16x32_bf16 v[140:143], v[120:123], a[180:183], v[140:143]
ds_read_b64_tr_b16 v[92:93], v199 offset:8704
buffer_load_dwordx4 a[48:51], v22, s[12:15], s71 offen offset:0 sc1
v_mfma_f32_16x16x32_bf16 v[140:143], v[124:127], a[188:191], v[140:143]
ds_read_b64_tr_b16 v[94:95], v199 offset:10752
s_waitcnt vmcnt(23) lgkmcnt(8)
v_mfma_f32_16x16x32_bf16 v[144:147], v[64:67], a[0:3], v[144:147]
ds_read_b64_tr_b16 v[96:97], v198 offset:768
s_add_u32 m0, s75, 32768
v_mfma_f32_16x16x32_bf16 v[144:147], v[68:71], a[8:11], v[144:147]
ds_read_b64_tr_b16 v[98:99], v198 offset:2816
s_lshl_b32 s70, s46, 17
v_mfma_f32_16x16x32_bf16 v[152:155], v[64:67], a[4:7], v[152:155]
ds_read_b64_tr_b16 v[100:101], v198 offset:8960
buffer_load_dwordx4 v220, s[20:23], s70 offen offset:0 lds
v_mfma_f32_16x16x32_bf16 v[152:155], v[68:71], a[12:15], v[152:155]
ds_read_b64_tr_b16 v[102:103], v198 offset:11008
v_mfma_f32_16x16x32_bf16 v[148:151], v[72:75], a[0:3], v[148:151]
ds_read_b64_tr_b16 v[104:105], v199 offset:768
v_mfma_f32_16x16x32_bf16 v[148:151], v[76:79], a[8:11], v[148:151]
ds_read_b64_tr_b16 v[106:107], v199 offset:2816
s_lshl_b32 s71, s47, 13
v_mfma_f32_16x16x32_bf16 v[156:159], v[72:75], a[4:7], v[156:159]
ds_read_b64_tr_b16 v[108:109], v199 offset:8960
buffer_load_dwordx4 a[132:135], v22, s[12:15], s71 offen offset:1024 sc1
v_mfma_f32_16x16x32_bf16 v[156:159], v[76:79], a[12:15], v[156:159]
ds_read_b64_tr_b16 v[110:111], v199 offset:11008
s_waitcnt lgkmcnt(8)
v_mfma_f32_16x16x32_bf16 v[160:163], v[80:83], a[0:3], v[160:163]
ds_read_b64_tr_b16 v[112:113], v198 offset:0
global_load_dword v193, v4, s[92:93] sc1
v_mfma_f32_16x16x32_bf16 v[160:163], v[84:87], a[8:11], v[160:163]
ds_read_b64_tr_b16 v[114:115], v198 offset:2048
v_mfma_f32_16x16x32_bf16 v[168:171], v[80:83], a[4:7], v[168:171]
ds_read_b64_tr_b16 v[116:117], v198 offset:8192
buffer_load_dwordx4 v221, s[20:23], s70 offen offset:1024 lds
v_mfma_f32_16x16x32_bf16 v[168:171], v[84:87], a[12:15], v[168:171]
ds_read_b64_tr_b16 v[118:119], v198 offset:10240
v_mfma_f32_16x16x32_bf16 v[164:167], v[88:91], a[0:3], v[164:167]
ds_read_b64_tr_b16 v[120:121], v199 offset:0
v_mfma_f32_16x16x32_bf16 v[164:167], v[92:95], a[8:11], v[164:167]
ds_read_b64_tr_b16 v[122:123], v199 offset:2048
v_mfma_f32_16x16x32_bf16 v[172:175], v[88:91], a[4:7], v[172:175]
ds_read_b64_tr_b16 v[124:125], v199 offset:8192
buffer_load_dwordx4 a[136:139], v22, s[12:15], s71 offen offset:2048 sc1
v_mfma_f32_16x16x32_bf16 v[172:175], v[92:95], a[12:15], v[172:175]
ds_read_b64_tr_b16 v[126:127], v199 offset:10240
s_waitcnt lgkmcnt(8)
v_mfma_f32_16x16x32_bf16 v[176:179], v[96:99], a[0:3], v[176:179]
ds_read_b64_tr_b16 v[64:65], v198 offset:16640
v_mfma_f32_16x16x32_bf16 v[176:179], v[100:103], a[8:11], v[176:179]
ds_read_b64_tr_b16 v[66:67], v198 offset:18688
v_mfma_f32_16x16x32_bf16 v[184:187], v[96:99], a[4:7], v[184:187]
ds_read_b64_tr_b16 v[68:69], v198 offset:24832
buffer_load_dwordx4 v222, s[20:23], s70 offen offset:2048 lds
v_mfma_f32_16x16x32_bf16 v[184:187], v[100:103], a[12:15], v[184:187]
ds_read_b64_tr_b16 v[70:71], v198 offset:26880
v_mfma_f32_16x16x32_bf16 v[180:183], v[104:107], a[0:3], v[180:183]
ds_read_b64_tr_b16 v[72:73], v199 offset:16640
v_mfma_f32_16x16x32_bf16 v[180:183], v[108:111], a[8:11], v[180:183]
ds_read_b64_tr_b16 v[74:75], v199 offset:18688
v_mfma_f32_16x16x32_bf16 v[188:191], v[104:107], a[4:7], v[188:191]
ds_read_b64_tr_b16 v[76:77], v199 offset:24832
buffer_load_dwordx4 v223, s[20:23], s70 offen offset:3072 lds
v_mfma_f32_16x16x32_bf16 v[188:191], v[108:111], a[12:15], v[188:191]
ds_read_b64_tr_b16 v[78:79], v199 offset:26880
s_waitcnt lgkmcnt(8)
v_mfma_f32_16x16x32_bf16 v[128:131], v[112:115], a[0:3], v[128:131]
ds_read_b64_tr_b16 v[80:81], v198 offset:16896
v_mfma_f32_16x16x32_bf16 v[128:131], v[116:119], a[8:11], v[128:131]
ds_read_b64_tr_b16 v[82:83], v198 offset:18944
v_mfma_f32_16x16x32_bf16 v[136:139], v[112:115], a[4:7], v[136:139]
ds_read_b64_tr_b16 v[84:85], v198 offset:25088
buffer_load_dwordx4 a[140:143], v22, s[12:15], s71 offen offset:3072 sc1
v_mfma_f32_16x16x32_bf16 v[136:139], v[116:119], a[12:15], v[136:139]
ds_read_b64_tr_b16 v[86:87], v198 offset:27136
v_mfma_f32_16x16x32_bf16 v[132:135], v[120:123], a[0:3], v[132:135]
ds_read_b64_tr_b16 v[88:89], v199 offset:16896
s_add_u32 m0, s75, 36864
v_mfma_f32_16x16x32_bf16 v[132:135], v[124:127], a[8:11], v[132:135]
ds_read_b64_tr_b16 v[90:91], v199 offset:18944
s_add_u32 s70, s70, 0x4000
v_mfma_f32_16x16x32_bf16 v[140:143], v[120:123], a[4:7], v[140:143]
ds_read_b64_tr_b16 v[92:93], v199 offset:25088
buffer_load_dwordx4 v220, s[20:23], s70 offen offset:0 lds
v_mfma_f32_16x16x32_bf16 v[140:143], v[124:127], a[12:15], v[140:143]
ds_read_b64_tr_b16 v[94:95], v199 offset:27136
s_waitcnt vmcnt(24) lgkmcnt(8)
v_mfma_f32_16x16x32_bf16 v[144:147], v[64:67], a[16:19], v[144:147]
ds_read_b64_tr_b16 v[96:97], v198 offset:17152
s_mov_b32 s47, s46
v_mfma_f32_16x16x32_bf16 v[144:147], v[68:71], a[24:27], v[144:147]
ds_read_b64_tr_b16 v[98:99], v198 offset:19200
s_add_u32 s46, s46, s29
v_mfma_f32_16x16x32_bf16 v[152:155], v[64:67], a[20:23], v[152:155]
ds_read_b64_tr_b16 v[100:101], v198 offset:25344
buffer_load_dwordx4 a[128:131], v22, s[12:15], s71 offen offset:0 sc1
v_mfma_f32_16x16x32_bf16 v[152:155], v[68:71], a[28:31], v[152:155]
ds_read_b64_tr_b16 v[102:103], v198 offset:27392
s_cmp_ge_u32 s46, s100
v_mfma_f32_16x16x32_bf16 v[148:151], v[72:75], a[16:19], v[148:151]
ds_read_b64_tr_b16 v[104:105], v199 offset:17152
s_cselect_b32 s46, s101, s46
v_mfma_f32_16x16x32_bf16 v[148:151], v[76:79], a[24:27], v[148:151]
ds_read_b64_tr_b16 v[106:107], v199 offset:19200
s_cmp_eq_u32 s44, s90
v_mfma_f32_16x16x32_bf16 v[156:159], v[72:75], a[20:23], v[156:159]
ds_read_b64_tr_b16 v[108:109], v199 offset:25344
buffer_load_dwordx4 v221, s[20:23], s70 offen offset:1024 lds
)"
R"(v_mfma_f32_16x16x32_bf16 v[156:159], v[76:79], a[28:31], v[156:159]
ds_read_b64_tr_b16 v[110:111], v199 offset:27392
s_waitcnt lgkmcnt(8)
v_mfma_f32_16x16x32_bf16 v[160:163], v[80:83], a[16:19], v[160:163]
ds_read_b64_tr_b16 v[112:113], v198 offset:16384
s_cselect_b32 s46, s65, s46
v_mfma_f32_16x16x32_bf16 v[160:163], v[84:87], a[24:27], v[160:163]
ds_read_b64_tr_b16 v[114:115], v198 offset:18432
s_cselect_b32 s101, s48, s101
v_mfma_f32_16x16x32_bf16 v[168:171], v[80:83], a[20:23], v[168:171]
ds_read_b64_tr_b16 v[116:117], v198 offset:24576
buffer_load_dwordx4 v222, s[20:23], s70 offen offset:2048 lds
v_mfma_f32_16x16x32_bf16 v[168:171], v[84:87], a[28:31], v[168:171]
ds_read_b64_tr_b16 v[118:119], v198 offset:26624
v_mfma_f32_16x16x32_bf16 v[164:167], v[88:91], a[16:19], v[164:167]
ds_read_b64_tr_b16 v[120:121], v199 offset:16384
v_mfma_f32_16x16x32_bf16 v[164:167], v[92:95], a[24:27], v[164:167]
ds_read_b64_tr_b16 v[122:123], v199 offset:18432
v_mfma_f32_16x16x32_bf16 v[172:175], v[88:91], a[20:23], v[172:175]
ds_read_b64_tr_b16 v[124:125], v199 offset:24576
buffer_load_dwordx4 v223, s[20:23], s70 offen offset:3072 lds
v_mfma_f32_16x16x32_bf16 v[172:175], v[92:95], a[28:31], v[172:175]
ds_read_b64_tr_b16 v[126:127], v199 offset:26624
s_waitcnt lgkmcnt(8)
v_mfma_f32_16x16x32_bf16 v[176:179], v[96:99], a[16:19], v[176:179]
s_add_u32 s71, s71, 0x1000
v_mfma_f32_16x16x32_bf16 v[176:179], v[100:103], a[24:27], v[176:179]
s_cmp_eq_u32 s44, s45
s_cbranch_scc1 CHK2_%=
CHKR2_%=:
v_mfma_f32_16x16x32_bf16 v[184:187], v[96:99], a[20:23], v[184:187]
buffer_load_dwordx4 a[148:151], v22, s[12:15], s71 offen offset:1024 sc1
s_sub_u32 s44, s44, 1
v_mfma_f32_16x16x32_bf16 v[184:187], v[100:103], a[28:31], v[184:187]
s_waitcnt lgkmcnt(0)
s_barrier
v_mfma_f32_16x16x32_bf16 v[180:183], v[104:107], a[16:19], v[180:183]
ds_read_b64_tr_b16 v[64:65], v198 offset:33024
ds_read_b64_tr_b16 v[66:67], v198 offset:35072
v_mfma_f32_16x16x32_bf16 v[180:183], v[108:111], a[24:27], v[180:183]
ds_read_b64_tr_b16 v[68:69], v198 offset:41216
ds_read_b64_tr_b16 v[70:71], v198 offset:43264
v_mfma_f32_16x16x32_bf16 v[188:191], v[104:107], a[20:23], v[188:191]
ds_read_b64_tr_b16 v[72:73], v199 offset:33024
ds_read_b64_tr_b16 v[74:75], v199 offset:35072
v_mfma_f32_16x16x32_bf16 v[188:191], v[108:111], a[28:31], v[188:191]
ds_read_b64_tr_b16 v[76:77], v199 offset:41216
v_mfma_f32_16x16x32_bf16 v[128:131], v[112:115], a[16:19], v[128:131]
ds_read_b64_tr_b16 v[78:79], v199 offset:43264
ds_read_b64_tr_b16 v[80:81], v198 offset:33280
v_mfma_f32_16x16x32_bf16 v[128:131], v[116:119], a[24:27], v[128:131]
ds_read_b64_tr_b16 v[82:83], v198 offset:35328
buffer_load_dwordx4 a[152:155], v22, s[12:15], s71 offen offset:2048 sc1
v_mfma_f32_16x16x32_bf16 v[136:139], v[112:115], a[20:23], v[136:139]
ds_read_b64_tr_b16 v[84:85], v198 offset:41472
s_cmp_eq_u32 s44, 3
v_mfma_f32_16x16x32_bf16 v[136:139], v[116:119], a[28:31], v[136:139]
ds_read_b64_tr_b16 v[86:87], v198 offset:43520
v_mfma_f32_16x16x32_bf16 v[132:135], v[120:123], a[16:19], v[132:135]
ds_read_b64_tr_b16 v[88:89], v199 offset:33280
buffer_load_dwordx4 a[156:159], v22, s[12:15], s71 offen offset:3072 sc1
v_mfma_f32_16x16x32_bf16 v[132:135], v[124:127], a[24:27], v[132:135]
ds_read_b64_tr_b16 v[90:91], v199 offset:35328
v_mfma_f32_16x16x32_bf16 v[140:143], v[120:123], a[20:23], v[140:143]
ds_read_b64_tr_b16 v[92:93], v199 offset:41472
buffer_load_dwordx4 a[144:147], v22, s[12:15], s71 offen offset:0 sc1
v_mfma_f32_16x16x32_bf16 v[140:143], v[124:127], a[28:31], v[140:143]
ds_read_b64_tr_b16 v[94:95], v199 offset:43520
s_cbranch_scc1 P3L3_%=
s_waitcnt vmcnt(23) lgkmcnt(8)
v_mfma_f32_16x16x32_bf16 v[144:147], v[64:67], a[32:35], v[144:147]
ds_read_b64_tr_b16 v[96:97], v198 offset:33536
s_add_u32 m0, s75, 65536
v_mfma_f32_16x16x32_bf16 v[144:147], v[68:71], a[40:43], v[144:147]
ds_read_b64_tr_b16 v[98:99], v198 offset:35584
s_lshl_b32 s70, s46, 17
v_mfma_f32_16x16x32_bf16 v[152:155], v[64:67], a[36:39], v[152:155]
ds_read_b64_tr_b16 v[100:101], v198 offset:41728
buffer_load_dwordx4 v220, s[20:23], s70 offen offset:0 lds
v_mfma_f32_16x16x32_bf16 v[152:155], v[68:71], a[44:47], v[152:155]
ds_read_b64_tr_b16 v[102:103], v198 offset:43776
v_mfma_f32_16x16x32_bf16 v[148:151], v[72:75], a[32:35], v[148:151]
ds_read_b64_tr_b16 v[104:105], v199 offset:33536
v_mfma_f32_16x16x32_bf16 v[148:151], v[76:79], a[40:43], v[148:151]
ds_read_b64_tr_b16 v[106:107], v199 offset:35584
s_lshl_b32 s71, s47, 13
v_mfma_f32_16x16x32_bf16 v[156:159], v[72:75], a[36:39], v[156:159]
ds_read_b64_tr_b16 v[108:109], v199 offset:41728
buffer_load_dwordx4 a[164:167], v22, s[12:15], s71 offen offset:1024 sc1
v_mfma_f32_16x16x32_bf16 v[156:159], v[76:79], a[44:47], v[156:159]
ds_read_b64_tr_b16 v[110:111], v199 offset:43776
s_waitcnt lgkmcnt(8)
v_mfma_f32_16x16x32_bf16 v[160:163], v[80:83], a[32:35], v[160:163]
ds_read_b64_tr_b16 v[112:113], v198 offset:32768
global_load_dword v193, v4, s[92:93] sc1
v_mfma_f32_16x16x32_bf16 v[160:163], v[84:87], a[40:43], v[160:163]
ds_read_b64_tr_b16 v[114:115], v198 offset:34816
v_mfma_f32_16x16x32_bf16 v[168:171], v[80:83], a[36:39], v[168:171]
ds_read_b64_tr_b16 v[116:117], v198 offset:40960
buffer_load_dwordx4 v221, s[20:23], s70 offen offset:1024 lds
v_mfma_f32_16x16x32_bf16 v[168:171], v[84:87], a[44:47], v[168:171]
ds_read_b64_tr_b16 v[118:119], v198 offset:43008
v_mfma_f32_16x16x32_bf16 v[164:167], v[88:91], a[32:35], v[164:167]
ds_read_b64_tr_b16 v[120:121], v199 offset:32768
v_mfma_f32_16x16x32_bf16 v[164:167], v[92:95], a[40:43], v[164:167]
ds_read_b64_tr_b16 v[122:123], v199 offset:34816
v_mfma_f32_16x16x32_bf16 v[172:175], v[88:91], a[36:39], v[172:175]
ds_read_b64_tr_b16 v[124:125], v199 offset:40960
buffer_load_dwordx4 a[168:171], v22, s[12:15], s71 offen offset:2048 sc1
v_mfma_f32_16x16x32_bf16 v[172:175], v[92:95], a[44:47], v[172:175]
ds_read_b64_tr_b16 v[126:127], v199 offset:43008
s_waitcnt lgkmcnt(8)
v_mfma_f32_16x16x32_bf16 v[176:179], v[96:99], a[32:35], v[176:179]
ds_read_b64_tr_b16 v[64:65], v198 offset:49408
v_mfma_f32_16x16x32_bf16 v[176:179], v[100:103], a[40:43], v[176:179]
ds_read_b64_tr_b16 v[66:67], v198 offset:51456
v_mfma_f32_16x16x32_bf16 v[184:187], v[96:99], a[36:39], v[184:187]
ds_read_b64_tr_b16 v[68:69], v198 offset:57600
buffer_load_dwordx4 v222, s[20:23], s70 offen offset:2048 lds
v_mfma_f32_16x16x32_bf16 v[184:187], v[100:103], a[44:47], v[184:187]
ds_read_b64_tr_b16 v[70:71], v198 offset:59648
v_mfma_f32_16x16x32_bf16 v[180:183], v[104:107], a[32:35], v[180:183]
ds_read_b64_tr_b16 v[72:73], v199 offset:49408
v_mfma_f32_16x16x32_bf16 v[180:183], v[108:111], a[40:43], v[180:183]
ds_read_b64_tr_b16 v[74:75], v199 offset:51456
v_mfma_f32_16x16x32_bf16 v[188:191], v[104:107], a[36:39], v[188:191]
ds_read_b64_tr_b16 v[76:77], v199 offset:57600
buffer_load_dwordx4 v223, s[20:23], s70 offen offset:3072 lds
v_mfma_f32_16x16x32_bf16 v[188:191], v[108:111], a[44:47], v[188:191]
ds_read_b64_tr_b16 v[78:79], v199 offset:59648
s_waitcnt lgkmcnt(8)
v_mfma_f32_16x16x32_bf16 v[128:131], v[112:115], a[32:35], v[128:131]
ds_read_b64_tr_b16 v[80:81], v198 offset:49664
v_mfma_f32_16x16x32_bf16 v[128:131], v[116:119], a[40:43], v[128:131]
ds_read_b64_tr_b16 v[82:83], v198 offset:51712
v_mfma_f32_16x16x32_bf16 v[136:139], v[112:115], a[36:39], v[136:139]
ds_read_b64_tr_b16 v[84:85], v198 offset:57856
buffer_load_dwordx4 a[172:175], v22, s[12:15], s71 offen offset:3072 sc1
v_mfma_f32_16x16x32_bf16 v[136:139], v[116:119], a[44:47], v[136:139]
ds_read_b64_tr_b16 v[86:87], v198 offset:59904
v_mfma_f32_16x16x32_bf16 v[132:135], v[120:123], a[32:35], v[132:135]
ds_read_b64_tr_b16 v[88:89], v199 offset:49664
s_add_u32 m0, s75, 69632
v_mfma_f32_16x16x32_bf16 v[132:135], v[124:127], a[40:43], v[132:135]
ds_read_b64_tr_b16 v[90:91], v199 offset:51712
s_add_u32 s70, s70, 0x4000
v_mfma_f32_16x16x32_bf16 v[140:143], v[120:123], a[36:39], v[140:143]
ds_read_b64_tr_b16 v[92:93], v199 offset:57856
buffer_load_dwordx4 v220, s[20:23], s70 offen offset:0 lds
v_mfma_f32_16x16x32_bf16 v[140:143], v[124:127], a[44:47], v[140:143]
ds_read_b64_tr_b16 v[94:95], v199 offset:59904
s_waitcnt vmcnt(24) lgkmcnt(8)
v_mfma_f32_16x16x32_bf16 v[144:147], v[64:67], a[48:51], v[144:147]
ds_read_b64_tr_b16 v[96:97], v198 offset:49920
s_mov_b32 s47, s46
v_mfma_f32_16x16x32_bf16 v[144:147], v[68:71], a[56:59], v[144:147]
ds_read_b64_tr_b16 v[98:99], v198 offset:51968
s_add_u32 s46, s46, s29
v_mfma_f32_16x16x32_bf16 v[152:155], v[64:67], a[52:55], v[152:155]
ds_read_b64_tr_b16 v[100:101], v198 offset:58112
buffer_load_dwordx4 a[160:163], v22, s[12:15], s71 offen offset:0 sc1
v_mfma_f32_16x16x32_bf16 v[152:155], v[68:71], a[60:63], v[152:155]
ds_read_b64_tr_b16 v[102:103], v198 offset:60160
s_cmp_ge_u32 s46, s100
v_mfma_f32_16x16x32_bf16 v[148:151], v[72:75], a[48:51], v[148:151]
ds_read_b64_tr_b16 v[104:105], v199 offset:49920
s_cselect_b32 s46, s101, s46
v_mfma_f32_16x16x32_bf16 v[148:151], v[76:79], a[56:59], v[148:151]
ds_read_b64_tr_b16 v[106:107], v199 offset:51968
v_mfma_f32_16x16x32_bf16 v[156:159], v[72:75], a[52:55], v[156:159]
ds_read_b64_tr_b16 v[108:109], v199 offset:58112
buffer_load_dwordx4 v221, s[20:23], s70 offen offset:1024 lds
v_mfma_f32_16x16x32_bf16 v[156:159], v[76:79], a[60:63], v[156:159]
ds_read_b64_tr_b16 v[110:111], v199 offset:60160
s_waitcnt lgkmcnt(8)
v_mfma_f32_16x16x32_bf16 v[160:163], v[80:83], a[48:51], v[160:163]
ds_read_b64_tr_b16 v[112:113], v198 offset:49152
v_mfma_f32_16x16x32_bf16 v[160:163], v[84:87], a[56:59], v[160:163]
ds_read_b64_tr_b16 v[114:115], v198 offset:51200
v_mfma_f32_16x16x32_bf16 v[168:171], v[80:83], a[52:55], v[168:171]
ds_read_b64_tr_b16 v[116:117], v198 offset:57344
buffer_load_dwordx4 v222, s[20:23], s70 offen offset:2048 lds
v_mfma_f32_16x16x32_bf16 v[168:171], v[84:87], a[60:63], v[168:171]
ds_read_b64_tr_b16 v[118:119], v198 offset:59392
v_mfma_f32_16x16x32_bf16 v[164:167], v[88:91], a[48:51], v[164:167]
ds_read_b64_tr_b16 v[120:121], v199 offset:49152
v_mfma_f32_16x16x32_bf16 v[164:167], v[92:95], a[56:59], v[164:167]
ds_read_b64_tr_b16 v[122:123], v199 offset:51200
v_mfma_f32_16x16x32_bf16 v[172:175], v[88:91], a[52:55], v[172:175]
ds_read_b64_tr_b16 v[124:125], v199 offset:57344
buffer_load_dwordx4 v223, s[20:23], s70 offen offset:3072 lds
v_mfma_f32_16x16x32_bf16 v[172:175], v[92:95], a[60:63], v[172:175]
ds_read_b64_tr_b16 v[126:127], v199 offset:59392
s_waitcnt lgkmcnt(8)
v_mfma_f32_16x16x32_bf16 v[176:179], v[96:99], a[48:51], v[176:179]
s_add_u32 s71, s71, 0x1000
v_mfma_f32_16x16x32_bf16 v[176:179], v[100:103], a[56:59], v[176:179]
s_cmp_eq_u32 s44, s45
s_cbranch_scc1 CHK3_%=
CHKR3_%=:
v_mfma_f32_16x16x32_bf16 v[184:187], v[96:99], a[52:55], v[184:187]
buffer_load_dwordx4 a[180:183], v22, s[12:15], s71 offen offset:1024 sc1
s_sub_u32 s44, s44, 1
v_mfma_f32_16x16x32_bf16 v[184:187], v[100:103], a[60:63], v[184:187]
s_waitcnt lgkmcnt(0)
s_barrier
v_mfma_f32_16x16x32_bf16 v[180:183], v[104:107], a[48:51], v[180:183]
ds_read_b64_tr_b16 v[64:65], v196 offset:256
ds_read_b64_tr_b16 v[66:67], v196 offset:2304
v_mfma_f32_16x16x32_bf16 v[180:183], v[108:111], a[56:59], v[180:183]
ds_read_b64_tr_b16 v[68:69], v196 offset:8448
ds_read_b64_tr_b16 v[70:71], v196 offset:10496
v_mfma_f32_16x16x32_bf16 v[188:191], v[104:107], a[52:55], v[188:191]
ds_read_b64_tr_b16 v[72:73], v197 offset:256
ds_read_b64_tr_b16 v[74:75], v197 offset:2304
v_mfma_f32_16x16x32_bf16 v[188:191], v[108:111], a[60:63], v[188:191]
ds_read_b64_tr_b16 v[76:77], v197 offset:8448
v_mfma_f32_16x16x32_bf16 v[128:131], v[112:115], a[48:51], v[128:131]
ds_read_b64_tr_b16 v[78:79], v197 offset:10496
ds_read_b64_tr_b16 v[80:81], v196 offset:512
v_mfma_f32_16x16x32_bf16 v[128:131], v[116:119], a[56:59], v[128:131]
ds_read_b64_tr_b16 v[82:83], v196 offset:2560
buffer_load_dwordx4 a[184:187], v22, s[12:15], s71 offen offset:2048 sc1
v_mfma_f32_16x16x32_bf16 v[136:139], v[112:115], a[52:55], v[136:139]
ds_read_b64_tr_b16 v[84:85], v196 offset:8704
v_mfma_f32_16x16x32_bf16 v[136:139], v[116:119], a[60:63], v[136:139]
ds_read_b64_tr_b16 v[86:87], v196 offset:10752
v_mfma_f32_16x16x32_bf16 v[132:135], v[120:123], a[48:51], v[132:135]
ds_read_b64_tr_b16 v[88:89], v197 offset:512
buffer_load_dwordx4 a[188:191], v22, s[12:15], s71 offen offset:3072 sc1
v_mfma_f32_16x16x32_bf16 v[132:135], v[124:127], a[56:59], v[132:135]
ds_read_b64_tr_b16 v[90:91], v197 offset:2560
v_mfma_f32_16x16x32_bf16 v[140:143], v[120:123], a[52:55], v[140:143]
ds_read_b64_tr_b16 v[92:93], v197 offset:8704
buffer_load_dwordx4 a[176:179], v22, s[12:15], s71 offen offset:0 sc1
v_mfma_f32_16x16x32_bf16 v[140:143], v[124:127], a[60:63], v[140:143]
ds_read_b64_tr_b16 v[94:95], v197 offset:10752
s_waitcnt vmcnt(23) lgkmcnt(8)
v_mfma_f32_16x16x32_bf16 v[144:147], v[64:67], a[128:131], v[144:147]
ds_read_b64_tr_b16 v[96:97], v196 offset:768
s_add_u32 m0, s75, 98304
v_mfma_f32_16x16x32_bf16 v[144:147], v[68:71], a[136:139], v[144:147]
ds_read_b64_tr_b16 v[98:99], v196 offset:2816
s_lshl_b32 s70, s46, 17
v_mfma_f32_16x16x32_bf16 v[152:155], v[64:67], a[132:135], v[152:155]
ds_read_b64_tr_b16 v[100:101], v196 offset:8960
buffer_load_dwordx4 v220, s[20:23], s70 offen offset:0 lds
PAD15_%=:
s_cbranch_execz PAD24_%=
v_mfma_f32_16x16x32_bf16 v[152:155], v[68:71], a[140:143], v[152:155]
ds_read_b64_tr_b16 v[102:103], v196 offset:11008
v_mfma_f32_16x16x32_bf16 v[148:151], v[72:75], a[128:131], v[148:151]
ds_read_b64_tr_b16 v[104:105], v197 offset:768
v_mfma_f32_16x16x32_bf16 v[148:151], v[76:79], a[136:139], v[148:151]
ds_read_b64_tr_b16 v[106:107], v197 offset:2816
s_lshl_b32 s71, s47, 13
v_mfma_f32_16x16x32_bf16 v[156:159], v[72:75], a[132:135], v[156:159]
ds_read_b64_tr_b16 v[108:109], v197 offset:8960
buffer_load_dwordx4 a[4:7], v22, s[12:15], s71 offen offset:1024 sc1
v_mfma_f32_16x16x32_bf16 v[156:159], v[76:79], a[140:143], v[156:159]
ds_read_b64_tr_b16 v[110:111], v197 offset:11008
s_waitcnt lgkmcnt(8)
v_mfma_f32_16x16x32_bf16 v[160:163], v[80:83], a[128:131], v[160:163]
ds_read_b64_tr_b16 v[112:113], v196 offset:0
v_mfma_f32_16x16x32_bf16 v[160:163], v[84:87], a[136:139], v[160:163]
ds_read_b64_tr_b16 v[114:115], v196 offset:2048
v_mfma_f32_16x16x32_bf16 v[168:171], v[80:83], a[132:135], v[168:171]
ds_read_b64_tr_b16 v[116:117], v196 offset:8192
buffer_load_dwordx4 v221, s[20:23], s70 offen offset:1024 lds
v_mfma_f32_16x16x32_bf16 v[168:171], v[84:87], a[140:143], v[168:171]
ds_read_b64_tr_b16 v[118:119], v196 offset:10240
v_mfma_f32_16x16x32_bf16 v[164:167], v[88:91], a[128:131], v[164:167]
ds_read_b64_tr_b16 v[120:121], v197 offset:0
v_mfma_f32_16x16x32_bf16 v[164:167], v[92:95], a[136:139], v[164:167]
ds_read_b64_tr_b16 v[122:123], v197 offset:2048
v_mfma_f32_16x16x32_bf16 v[172:175], v[88:91], a[132:135], v[172:175]
ds_read_b64_tr_b16 v[124:125], v197 offset:8192
buffer_load_dwordx4 a[8:11], v22, s[12:15], s71 offen offset:2048 sc1
v_mfma_f32_16x16x32_bf16 v[172:175], v[92:95], a[140:143], v[172:175]
ds_read_b64_tr_b16 v[126:127], v197 offset:10240
s_waitcnt lgkmcnt(8)
v_mfma_f32_16x16x32_bf16 v[176:179], v[96:99], a[128:131], v[176:179]
ds_read_b64_tr_b16 v[64:65], v196 offset:16640
v_mfma_f32_16x16x32_bf16 v[176:179], v[100:103], a[136:139], v[176:179]
ds_read_b64_tr_b16 v[66:67], v196 offset:18688
v_mfma_f32_16x16x32_bf16 v[184:187], v[96:99], a[132:135], v[184:187]
ds_read_b64_tr_b16 v[68:69], v196 offset:24832
buffer_load_dwordx4 v222, s[20:23], s70 offen offset:2048 lds
v_mfma_f32_16x16x32_bf16 v[184:187], v[100:103], a[140:143], v[184:187]
ds_read_b64_tr_b16 v[70:71], v196 offset:26880
v_mfma_f32_16x16x32_bf16 v[180:183], v[104:107], a[128:131], v[180:183]
ds_read_b64_tr_b16 v[72:73], v197 offset:16640
v_mfma_f32_16x16x32_bf16 v[180:183], v[108:111], a[136:139], v[180:183]
ds_read_b64_tr_b16 v[74:75], v197 offset:18688
v_mfma_f32_16x16x32_bf16 v[188:191], v[104:107], a[132:135], v[188:191]
ds_read_b64_tr_b16 v[76:77], v197 offset:24832
buffer_load_dwordx4 v223, s[20:23], s70 offen offset:3072 lds
v_mfma_f32_16x16x32_bf16 v[188:191], v[108:111], a[140:143], v[188:191]
ds_read_b64_tr_b16 v[78:79], v197 offset:26880
s_waitcnt lgkmcnt(8)
v_mfma_f32_16x16x32_bf16 v[128:131], v[112:115], a[128:131], v[128:131]
ds_read_b64_tr_b16 v[80:81], v196 offset:16896
v_mfma_f32_16x16x32_bf16 v[128:131], v[116:119], a[136:139], v[128:131]
ds_read_b64_tr_b16 v[82:83], v196 offset:18944
v_mfma_f32_16x16x32_bf16 v[136:139], v[112:115], a[132:135], v[136:139]
ds_read_b64_tr_b16 v[84:85], v196 offset:25088
buffer_load_dwordx4 a[12:15], v22, s[12:15], s71 offen offset:3072 sc1
v_mfma_f32_16x16x32_bf16 v[136:139], v[116:119], a[140:143], v[136:139]
ds_read_b64_tr_b16 v[86:87], v196 offset:27136
v_mfma_f32_16x16x32_bf16 v[132:135], v[120:123], a[128:131], v[132:135]
ds_read_b64_tr_b16 v[88:89], v197 offset:16896
s_add_u32 m0, s75, 102400
v_mfma_f32_16x16x32_bf16 v[132:135], v[124:127], a[136:139], v[132:135]
ds_read_b64_tr_b16 v[90:91], v197 offset:18944
s_add_u32 s70, s70, 0x4000
v_mfma_f32_16x16x32_bf16 v[140:143], v[120:123], a[132:135], v[140:143]
ds_read_b64_tr_b16 v[92:93], v197 offset:25088
buffer_load_dwordx4 v220, s[20:23], s70 offen offset:0 lds
v_mfma_f32_16x16x32_bf16 v[140:143], v[124:127], a[140:143], v[140:143]
ds_read_b64_tr_b16 v[94:95], v197 offset:27136
s_waitcnt vmcnt(24) lgkmcnt(8)
v_mfma_f32_16x16x32_bf16 v[144:147], v[64:67], a[144:147], v[144:147]
ds_read_b64_tr_b16 v[96:97], v196 offset:17152
s_mov_b32 s47, s46
v_mfma_f32_16x16x32_bf16 v[144:147], v[68:71], a[152:155], v[144:147]
ds_read_b64_tr_b16 v[98:99], v196 offset:19200
s_add_u32 s46, s46, s29
v_mfma_f32_16x16x32_bf16 v[152:155], v[64:67], a[148:151], v[152:155]
ds_read_b64_tr_b16 v[100:101], v196 offset:25344
buffer_load_dwordx4 a[0:3], v22, s[12:15], s71 offen offset:0 sc1
v_mfma_f32_16x16x32_bf16 v[152:155], v[68:71], a[156:159], v[152:155]
ds_read_b64_tr_b16 v[102:103], v196 offset:27392
s_cmp_ge_u32 s46, s100
v_mfma_f32_16x16x32_bf16 v[148:151], v[72:75], a[144:147], v[148:151]
ds_read_b64_tr_b16 v[104:105], v197 offset:17152
s_cselect_b32 s46, s101, s46
v_mfma_f32_16x16x32_bf16 v[148:151], v[76:79], a[152:155], v[148:151]
ds_read_b64_tr_b16 v[106:107], v197 offset:19200
v_mfma_f32_16x16x32_bf16 v[156:159], v[72:75], a[148:151], v[156:159]
ds_read_b64_tr_b16 v[108:109], v197 offset:25344
buffer_load_dwordx4 v221, s[20:23], s70 offen offset:1024 lds
v_mfma_f32_16x16x32_bf16 v[156:159], v[76:79], a[156:159], v[156:159]
ds_read_b64_tr_b16 v[110:111], v197 offset:27392
s_waitcnt lgkmcnt(8)
v_mfma_f32_16x16x32_bf16 v[160:163], v[80:83], a[144:147], v[160:163]
ds_read_b64_tr_b16 v[112:113], v196 offset:16384
v_mfma_f32_16x16x32_bf16 v[160:163], v[84:87], a[152:155], v[160:163]
ds_read_b64_tr_b16 v[114:115], v196 offset:18432
v_mfma_f32_16x16x32_bf16 v[168:171], v[80:83], a[148:151], v[168:171]
ds_read_b64_tr_b16 v[116:117], v196 offset:24576
buffer_load_dwordx4 v222, s[20:23], s70 offen offset:2048 lds
v_mfma_f32_16x16x32_bf16 v[168:171], v[84:87], a[156:159], v[168:171]
ds_read_b64_tr_b16 v[118:119], v196 offset:26624
v_mfma_f32_16x16x32_bf16 v[164:167], v[88:91], a[144:147], v[164:167]
ds_read_b64_tr_b16 v[120:121], v197 offset:16384
v_mfma_f32_16x16x32_bf16 v[164:167], v[92:95], a[152:155], v[164:167]
ds_read_b64_tr_b16 v[122:123], v197 offset:18432
v_mfma_f32_16x16x32_bf16 v[172:175], v[88:91], a[148:151], v[172:175]
ds_read_b64_tr_b16 v[124:125], v197 offset:24576
buffer_load_dwordx4 v223, s[20:23], s70 offen offset:3072 lds
v_mfma_f32_16x16x32_bf16 v[172:175], v[92:95], a[156:159], v[172:175]
ds_read_b64_tr_b16 v[126:127], v197 offset:26624
s_waitcnt lgkmcnt(8)
v_mfma_f32_16x16x32_bf16 v[176:179], v[96:99], a[144:147], v[176:179]
v_mfma_f32_16x16x32_bf16 v[176:179], v[100:103], a[152:155], v[176:179]
s_add_u32 s71, s71, 0x1000
v_mfma_f32_16x16x32_bf16 v[184:187], v[96:99], a[148:151], v[184:187]
buffer_load_dwordx4 a[20:23], v22, s[12:15], s71 offen offset:1024 sc1
s_sub_u32 s44, s44, 1
v_mfma_f32_16x16x32_bf16 v[184:187], v[100:103], a[156:159], v[184:187]
s_waitcnt lgkmcnt(0)
s_barrier
v_mfma_f32_16x16x32_bf16 v[180:183], v[104:107], a[144:147], v[180:183]
ds_read_b64_tr_b16 v[64:65], v196 offset:33024
ds_read_b64_tr_b16 v[66:67], v196 offset:35072
v_mfma_f32_16x16x32_bf16 v[180:183], v[108:111], a[152:155], v[180:183]
ds_read_b64_tr_b16 v[68:69], v196 offset:41216
ds_read_b64_tr_b16 v[70:71], v196 offset:43264
v_mfma_f32_16x16x32_bf16 v[188:191], v[104:107], a[148:151], v[188:191]
ds_read_b64_tr_b16 v[72:73], v197 offset:33024
ds_read_b64_tr_b16 v[74:75], v197 offset:35072
v_mfma_f32_16x16x32_bf16 v[188:191], v[108:111], a[156:159], v[188:191]
ds_read_b64_tr_b16 v[76:77], v197 offset:41216
v_mfma_f32_16x16x32_bf16 v[128:131], v[112:115], a[144:147], v[128:131]
ds_read_b64_tr_b16 v[78:79], v197 offset:43264
ds_read_b64_tr_b16 v[80:81], v196 offset:33280
v_mfma_f32_16x16x32_bf16 v[128:131], v[116:119], a[152:155], v[128:131]
ds_read_b64_tr_b16 v[82:83], v196 offset:35328
buffer_load_dwordx4 a[24:27], v22, s[12:15], s71 offen offset:2048 sc1
v_mfma_f32_16x16x32_bf16 v[136:139], v[112:115], a[148:151], v[136:139]
ds_read_b64_tr_b16 v[84:85], v196 offset:41472
s_cmp_eq_u32 s44, 3
v_mfma_f32_16x16x32_bf16 v[136:139], v[116:119], a[156:159], v[136:139]
ds_read_b64_tr_b16 v[86:87], v196 offset:43520
v_mfma_f32_16x16x32_bf16 v[132:135], v[120:123], a[144:147], v[132:135]
ds_read_b64_tr_b16 v[88:89], v197 offset:33280
buffer_load_dwordx4 a[28:31], v22, s[12:15], s71 offen offset:3072 sc1
v_mfma_f32_16x16x32_bf16 v[132:135], v[124:127], a[152:155], v[132:135]
ds_read_b64_tr_b16 v[90:91], v197 offset:35328
v_mfma_f32_16x16x32_bf16 v[140:143], v[120:123], a[148:151], v[140:143]
)"
R"(ds_read_b64_tr_b16 v[92:93], v197 offset:41472
buffer_load_dwordx4 a[16:19], v22, s[12:15], s71 offen offset:0 sc1
v_mfma_f32_16x16x32_bf16 v[140:143], v[124:127], a[156:159], v[140:143]
ds_read_b64_tr_b16 v[94:95], v197 offset:43520
s_cbranch_scc1 P3L1_%=
s_branch L3_%=
P3L1_%=:
s_waitcnt vmcnt(23) lgkmcnt(8)
v_mfma_f32_16x16x32_bf16 v[144:147], v[64:67], a[160:163], v[144:147]
ds_read_b64_tr_b16 v[96:97], v196 offset:33536
s_lshl_b32 s71, s47, 13
v_mfma_f32_16x16x32_bf16 v[144:147], v[68:71], a[168:171], v[144:147]
ds_read_b64_tr_b16 v[98:99], v196 offset:35584
buffer_load_dwordx4 a[36:39], v22, s[12:15], s71 offen offset:1024 sc1
v_mfma_f32_16x16x32_bf16 v[152:155], v[64:67], a[164:167], v[152:155]
ds_read_b64_tr_b16 v[100:101], v196 offset:41728
v_mfma_f32_16x16x32_bf16 v[152:155], v[68:71], a[172:175], v[152:155]
ds_read_b64_tr_b16 v[102:103], v196 offset:43776
v_mfma_f32_16x16x32_bf16 v[148:151], v[72:75], a[160:163], v[148:151]
ds_read_b64_tr_b16 v[104:105], v197 offset:33536
v_mfma_f32_16x16x32_bf16 v[148:151], v[76:79], a[168:171], v[148:151]
ds_read_b64_tr_b16 v[106:107], v197 offset:35584
buffer_load_dwordx4 a[40:43], v22, s[12:15], s71 offen offset:2048 sc1
v_mfma_f32_16x16x32_bf16 v[156:159], v[72:75], a[164:167], v[156:159]
ds_read_b64_tr_b16 v[108:109], v197 offset:41728
v_mfma_f32_16x16x32_bf16 v[156:159], v[76:79], a[172:175], v[156:159]
ds_read_b64_tr_b16 v[110:111], v197 offset:43776
s_waitcnt lgkmcnt(8)
v_mfma_f32_16x16x32_bf16 v[160:163], v[80:83], a[160:163], v[160:163]
ds_read_b64_tr_b16 v[112:113], v196 offset:49408
v_mfma_f32_16x16x32_bf16 v[160:163], v[84:87], a[168:171], v[160:163]
ds_read_b64_tr_b16 v[114:115], v196 offset:51456
buffer_load_dwordx4 a[44:47], v22, s[12:15], s71 offen offset:3072 sc1
v_mfma_f32_16x16x32_bf16 v[168:171], v[80:83], a[164:167], v[168:171]
ds_read_b64_tr_b16 v[116:117], v196 offset:57600
v_mfma_f32_16x16x32_bf16 v[168:171], v[84:87], a[172:175], v[168:171]
ds_read_b64_tr_b16 v[118:119], v196 offset:59648
v_mfma_f32_16x16x32_bf16 v[164:167], v[88:91], a[160:163], v[164:167]
ds_read_b64_tr_b16 v[120:121], v197 offset:49408
v_mfma_f32_16x16x32_bf16 v[164:167], v[92:95], a[168:171], v[164:167]
ds_read_b64_tr_b16 v[122:123], v197 offset:51456
buffer_load_dwordx4 a[32:35], v22, s[12:15], s71 offen offset:0 sc1
v_mfma_f32_16x16x32_bf16 v[172:175], v[88:91], a[164:167], v[172:175]
ds_read_b64_tr_b16 v[124:125], v197 offset:57600
s_add_u32 s71, s71, 0x1000
v_mfma_f32_16x16x32_bf16 v[172:175], v[92:95], a[172:175], v[172:175]
ds_read_b64_tr_b16 v[126:127], v197 offset:59648
s_waitcnt lgkmcnt(8)
v_mfma_f32_16x16x32_bf16 v[176:179], v[96:99], a[160:163], v[176:179]
ds_read_b64_tr_b16 v[64:65], v196 offset:49664
v_mfma_f32_16x16x32_bf16 v[176:179], v[100:103], a[168:171], v[176:179]
ds_read_b64_tr_b16 v[66:67], v196 offset:51712
buffer_load_dwordx4 a[52:55], v22, s[12:15], s71 offen offset:1024 sc1
v_mfma_f32_16x16x32_bf16 v[184:187], v[96:99], a[164:167], v[184:187]
ds_read_b64_tr_b16 v[68:69], v196 offset:57856
v_mfma_f32_16x16x32_bf16 v[184:187], v[100:103], a[172:175], v[184:187]
ds_read_b64_tr_b16 v[70:71], v196 offset:59904
v_mfma_f32_16x16x32_bf16 v[180:183], v[104:107], a[160:163], v[180:183]
ds_read_b64_tr_b16 v[72:73], v197 offset:49664
v_mfma_f32_16x16x32_bf16 v[180:183], v[108:111], a[168:171], v[180:183]
ds_read_b64_tr_b16 v[74:75], v197 offset:51712
buffer_load_dwordx4 a[56:59], v22, s[12:15], s71 offen offset:2048 sc1
v_mfma_f32_16x16x32_bf16 v[188:191], v[104:107], a[164:167], v[188:191]
ds_read_b64_tr_b16 v[76:77], v197 offset:57856
v_mfma_f32_16x16x32_bf16 v[188:191], v[108:111], a[172:175], v[188:191]
ds_read_b64_tr_b16 v[78:79], v197 offset:59904
s_waitcnt vmcnt(22) lgkmcnt(8)
v_mfma_f32_16x16x32_bf16 v[144:147], v[112:115], a[176:179], v[144:147]
ds_read_b64_tr_b16 v[80:81], v196 offset:49920
v_mfma_f32_16x16x32_bf16 v[144:147], v[116:119], a[184:187], v[144:147]
ds_read_b64_tr_b16 v[82:83], v196 offset:51968
buffer_load_dwordx4 a[60:63], v22, s[12:15], s71 offen offset:3072 sc1
v_mfma_f32_16x16x32_bf16 v[152:155], v[112:115], a[180:183], v[152:155]
ds_read_b64_tr_b16 v[84:85], v196 offset:58112
v_mfma_f32_16x16x32_bf16 v[152:155], v[116:119], a[188:191], v[152:155]
ds_read_b64_tr_b16 v[86:87], v196 offset:60160
v_mfma_f32_16x16x32_bf16 v[148:151], v[120:123], a[176:179], v[148:151]
ds_read_b64_tr_b16 v[88:89], v197 offset:49920
v_mfma_f32_16x16x32_bf16 v[148:151], v[124:127], a[184:187], v[148:151]
ds_read_b64_tr_b16 v[90:91], v197 offset:51968
buffer_load_dwordx4 a[48:51], v22, s[12:15], s71 offen offset:0 sc1
v_mfma_f32_16x16x32_bf16 v[156:159], v[120:123], a[180:183], v[156:159]
ds_read_b64_tr_b16 v[92:93], v197 offset:58112
s_lshl_b32 s77, s31, 15
v_mfma_f32_16x16x32_bf16 v[156:159], v[124:127], a[188:191], v[156:159]
ds_read_b64_tr_b16 v[94:95], v197 offset:60160
s_waitcnt vmcnt(12) lgkmcnt(0)
s_barrier
v_mfma_f32_16x16x32_bf16 v[160:163], v[64:67], a[176:179], v[160:163]
ds_read_b64_tr_b16 v[96:97], v198 offset:256
v_add_u32 v200, s77, v22
v_mfma_f32_16x16x32_bf16 v[160:163], v[68:71], a[184:187], v[160:163]
ds_read_b64_tr_b16 v[98:99], v198 offset:2304
s_mov_b32 s98, 1
v_mfma_f32_16x16x32_bf16 v[168:171], v[64:67], a[180:183], v[168:171]
ds_read_b64_tr_b16 v[100:101], v198 offset:8448
s_mov_b32 s89, 15
v_mfma_f32_16x16x32_bf16 v[168:171], v[68:71], a[188:191], v[168:171]
ds_read_b64_tr_b16 v[102:103], v198 offset:10496
v_cmp_eq_u32 vcc, s98, v28
v_mfma_f32_16x16x32_bf16 v[164:167], v[72:75], a[176:179], v[164:167]
ds_read_b64_tr_b16 v[104:105], v199 offset:256
s_and_b32 s77, vcc_lo, s89
v_mfma_f32_16x16x32_bf16 v[164:167], v[76:79], a[184:187], v[164:167]
ds_read_b64_tr_b16 v[106:107], v199 offset:2304
s_cmp_lg_u32 s77, s89
v_mfma_f32_16x16x32_bf16 v[172:175], v[72:75], a[180:183], v[172:175]
ds_read_b64_tr_b16 v[108:109], v199 offset:8448
s_cbranch_scc1 XF1SP_%=
XF1OK_%=:
v_mfma_f32_16x16x32_bf16 v[172:175], v[76:79], a[188:191], v[172:175]
ds_read_b64_tr_b16 v[110:111], v199 offset:10496
v_mfma_f32_16x16x32_bf16 v[176:179], v[80:83], a[176:179], v[176:179]
ds_read_b64_tr_b16 v[112:113], v198 offset:512
s_bfe_u32 s77, s30, 0x6000c
v_mfma_f32_16x16x32_bf16 v[176:179], v[84:87], a[184:187], v[176:179]
ds_read_b64_tr_b16 v[114:115], v198 offset:2560
v_lshl_add_u32 v201, s77, 11, v22
v_mfma_f32_16x16x32_bf16 v[184:187], v[80:83], a[180:183], v[184:187]
ds_read_b64_tr_b16 v[116:117], v198 offset:8704
s_bfe_u32 s77, s30, 0x60012
v_mfma_f32_16x16x32_bf16 v[184:187], v[84:87], a[188:191], v[184:187]
ds_read_b64_tr_b16 v[118:119], v198 offset:10752
v_lshl_add_u32 v202, s77, 11, v22
v_mfma_f32_16x16x32_bf16 v[180:183], v[88:91], a[176:179], v[180:183]
ds_read_b64_tr_b16 v[120:121], v199 offset:512
s_bfe_u32 s77, s30, 0x60018
v_mfma_f32_16x16x32_bf16 v[180:183], v[92:95], a[184:187], v[180:183]
ds_read_b64_tr_b16 v[122:123], v199 offset:2560
v_lshl_add_u32 v203, s77, 11, v22
v_mfma_f32_16x16x32_bf16 v[188:191], v[88:91], a[180:183], v[188:191]
ds_read_b64_tr_b16 v[124:125], v199 offset:8704
v_mov_b32 v207, 0xbfb8aa3b
v_mfma_f32_16x16x32_bf16 v[188:191], v[92:95], a[188:191], v[188:191]
ds_read_b64_tr_b16 v[126:127], v199 offset:10752
s_waitcnt lgkmcnt(8)
v_mfma_f32_16x16x32_bf16 v[144:147], v[96:99], a[0:3], v[144:147]
ds_read_b64_tr_b16 v[64:65], v198 offset:16640
v_lshlrev_b32 v32, 16, v208
v_mfma_f32_16x16x32_bf16 v[144:147], v[100:103], a[8:11], v[144:147]
ds_read_b64_tr_b16 v[66:67], v198 offset:18688
v_and_b32 v33, 0xffff0000, v208
v_mfma_f32_16x16x32_bf16 v[152:155], v[96:99], a[4:7], v[152:155]
ds_read_b64_tr_b16 v[68:69], v198 offset:24832
v_lshlrev_b32 v34, 16, v209
v_mfma_f32_16x16x32_bf16 v[152:155], v[100:103], a[12:15], v[152:155]
ds_read_b64_tr_b16 v[70:71], v198 offset:26880
v_and_b32 v35, 0xffff0000, v209
v_mfma_f32_16x16x32_bf16 v[148:151], v[104:107], a[0:3], v[148:151]
ds_read_b64_tr_b16 v[72:73], v199 offset:16640
v_lshlrev_b32 v36, 16, v210
v_mfma_f32_16x16x32_bf16 v[148:151], v[108:111], a[8:11], v[148:151]
ds_read_b64_tr_b16 v[74:75], v199 offset:18688
v_and_b32 v37, 0xffff0000, v210
v_mfma_f32_16x16x32_bf16 v[156:159], v[104:107], a[4:7], v[156:159]
ds_read_b64_tr_b16 v[76:77], v199 offset:24832
v_lshlrev_b32 v38, 16, v211
v_mfma_f32_16x16x32_bf16 v[156:159], v[108:111], a[12:15], v[156:159]
ds_read_b64_tr_b16 v[78:79], v199 offset:26880
s_waitcnt lgkmcnt(8)
v_mfma_f32_16x16x32_bf16 v[160:163], v[112:115], a[0:3], v[160:163]
ds_read_b64_tr_b16 v[80:81], v198 offset:16896
v_and_b32 v39, 0xffff0000, v211
v_mfma_f32_16x16x32_bf16 v[160:163], v[116:119], a[8:11], v[160:163]
ds_read_b64_tr_b16 v[82:83], v198 offset:18944
v_lshlrev_b32 v40, 16, v212
v_mfma_f32_16x16x32_bf16 v[168:171], v[112:115], a[4:7], v[168:171]
ds_read_b64_tr_b16 v[84:85], v198 offset:25088
v_and_b32 v41, 0xffff0000, v212
v_mfma_f32_16x16x32_bf16 v[168:171], v[116:119], a[12:15], v[168:171]
ds_read_b64_tr_b16 v[86:87], v198 offset:27136
v_lshlrev_b32 v42, 16, v213
v_mfma_f32_16x16x32_bf16 v[164:167], v[120:123], a[0:3], v[164:167]
ds_read_b64_tr_b16 v[88:89], v199 offset:16896
v_and_b32 v43, 0xffff0000, v213
v_mfma_f32_16x16x32_bf16 v[164:167], v[124:127], a[8:11], v[164:167]
ds_read_b64_tr_b16 v[90:91], v199 offset:18944
v_lshlrev_b32 v44, 16, v214
v_mfma_f32_16x16x32_bf16 v[172:175], v[120:123], a[4:7], v[172:175]
ds_read_b64_tr_b16 v[92:93], v199 offset:25088
v_and_b32 v45, 0xffff0000, v214
v_mfma_f32_16x16x32_bf16 v[172:175], v[124:127], a[12:15], v[172:175]
ds_read_b64_tr_b16 v[94:95], v199 offset:27136
s_waitcnt vmcnt(8) lgkmcnt(8)
v_mfma_f32_16x16x32_bf16 v[144:147], v[64:67], a[16:19], v[144:147]
ds_read_b64_tr_b16 v[96:97], v198 offset:33024
v_lshlrev_b32 v46, 16, v215
v_mfma_f32_16x16x32_bf16 v[144:147], v[68:71], a[24:27], v[144:147]
ds_read_b64_tr_b16 v[98:99], v198 offset:35072
v_and_b32 v47, 0xffff0000, v215
v_mfma_f32_16x16x32_bf16 v[152:155], v[64:67], a[20:23], v[152:155]
ds_read_b64_tr_b16 v[100:101], v198 offset:41216
v_mfma_f32_16x16x32_bf16 v[152:155], v[68:71], a[28:31], v[152:155]
ds_read_b64_tr_b16 v[102:103], v198 offset:43264
v_mfma_f32_16x16x32_bf16 v[148:151], v[72:75], a[16:19], v[148:151]
ds_read_b64_tr_b16 v[104:105], v199 offset:33024
v_mfma_f32_16x16x32_bf16 v[148:151], v[76:79], a[24:27], v[148:151]
ds_read_b64_tr_b16 v[106:107], v199 offset:35072
v_mfma_f32_16x16x32_bf16 v[156:159], v[72:75], a[20:23], v[156:159]
ds_read_b64_tr_b16 v[108:109], v199 offset:41216
v_mfma_f32_16x16x32_bf16 v[156:159], v[76:79], a[28:31], v[156:159]
ds_read_b64_tr_b16 v[110:111], v199 offset:43264
s_waitcnt lgkmcnt(8)
v_mfma_f32_16x16x32_bf16 v[160:163], v[80:83], a[16:19], v[160:163]
ds_read_b64_tr_b16 v[112:113], v198 offset:49408
v_mfma_f32_16x16x32_bf16 v[160:163], v[84:87], a[24:27], v[160:163]
ds_read_b64_tr_b16 v[114:115], v198 offset:51456
v_mfma_f32_16x16x32_bf16 v[168:171], v[80:83], a[20:23], v[168:171]
ds_read_b64_tr_b16 v[116:117], v198 offset:57600
v_mfma_f32_16x16x32_bf16 v[168:171], v[84:87], a[28:31], v[168:171]
ds_read_b64_tr_b16 v[118:119], v198 offset:59648
v_mfma_f32_16x16x32_bf16 v[164:167], v[88:91], a[16:19], v[164:167]
ds_read_b64_tr_b16 v[120:121], v199 offset:49408
v_mfma_f32_16x16x32_bf16 v[164:167], v[92:95], a[24:27], v[164:167]
ds_read_b64_tr_b16 v[122:123], v199 offset:51456
v_mfma_f32_16x16x32_bf16 v[172:175], v[88:91], a[20:23], v[172:175]
ds_read_b64_tr_b16 v[124:125], v199 offset:57600
v_mfma_f32_16x16x32_bf16 v[172:175], v[92:95], a[28:31], v[172:175]
ds_read_b64_tr_b16 v[126:127], v199 offset:59648
s_waitcnt vmcnt(4) lgkmcnt(8)
v_mfma_f32_16x16x32_bf16 v[144:147], v[96:99], a[32:35], v[144:147]
ds_read_b64_tr_b16 v[64:65], v198 offset:33280
v_mfma_f32_16x16x32_bf16 v[144:147], v[100:103], a[40:43], v[144:147]
ds_read_b64_tr_b16 v[66:67], v198 offset:35328
v_mfma_f32_16x16x32_bf16 v[152:155], v[96:99], a[36:39], v[152:155]
ds_read_b64_tr_b16 v[68:69], v198 offset:41472
v_mfma_f32_16x16x32_bf16 v[152:155], v[100:103], a[44:47], v[152:155]
ds_read_b64_tr_b16 v[70:71], v198 offset:43520
v_mfma_f32_16x16x32_bf16 v[148:151], v[104:107], a[32:35], v[148:151]
ds_read_b64_tr_b16 v[72:73], v199 offset:33280
v_mfma_f32_16x16x32_bf16 v[148:151], v[108:111], a[40:43], v[148:151]
ds_read_b64_tr_b16 v[74:75], v199 offset:35328
v_mfma_f32_16x16x32_bf16 v[156:159], v[104:107], a[36:39], v[156:159]
ds_read_b64_tr_b16 v[76:77], v199 offset:41472
v_mfma_f32_16x16x32_bf16 v[156:159], v[108:111], a[44:47], v[156:159]
ds_read_b64_tr_b16 v[78:79], v199 offset:43520
s_waitcnt vmcnt(0) lgkmcnt(8)
v_mfma_f32_16x16x32_bf16 v[144:147], v[112:115], a[48:51], v[144:147]
ds_read_b64_tr_b16 v[80:81], v198 offset:49664
v_mfma_f32_16x16x32_bf16 v[144:147], v[116:119], a[56:59], v[144:147]
ds_read_b64_tr_b16 v[82:83], v198 offset:51712
v_mfma_f32_16x16x32_bf16 v[152:155], v[112:115], a[52:55], v[152:155]
ds_read_b64_tr_b16 v[84:85], v198 offset:57856
v_mfma_f32_16x16x32_bf16 v[152:155], v[116:119], a[60:63], v[152:155]
ds_read_b64_tr_b16 v[86:87], v198 offset:59904
v_mfma_f32_16x16x32_bf16 v[148:151], v[120:123], a[48:51], v[148:151]
ds_read_b64_tr_b16 v[88:89], v199 offset:49664
v_mfma_f32_16x16x32_bf16 v[148:151], v[124:127], a[56:59], v[148:151]
ds_read_b64_tr_b16 v[90:91], v199 offset:51712
v_cvt_pk_bf16_f32 v144, v144, v145
v_mfma_f32_16x16x32_bf16 v[156:159], v[120:123], a[52:55], v[156:159]
ds_read_b64_tr_b16 v[92:93], v199 offset:57856
v_cvt_pk_bf16_f32 v145, v146, v147
v_mfma_f32_16x16x32_bf16 v[156:159], v[124:127], a[60:63], v[156:159]
ds_read_b64_tr_b16 v[94:95], v199 offset:59904
s_waitcnt lgkmcnt(8)
v_mfma_f32_16x16x32_bf16 v[160:163], v[64:67], a[32:35], v[160:163]
ds_read_b64_tr_b16 v[96:97], v198 offset:768
v_mfma_f32_16x16x32_bf16 v[160:163], v[68:71], a[40:43], v[160:163]
ds_read_b64_tr_b16 v[98:99], v198 offset:2816
v_cvt_pk_bf16_f32 v146, v148, v149
v_mfma_f32_16x16x32_bf16 v[168:171], v[64:67], a[36:39], v[168:171]
ds_read_b64_tr_b16 v[100:101], v198 offset:8960
v_cvt_pk_bf16_f32 v147, v150, v151
v_mfma_f32_16x16x32_bf16 v[168:171], v[68:71], a[44:47], v[168:171]
ds_read_b64_tr_b16 v[102:103], v198 offset:11008
global_store_dwordx4 v201, v[144:147], s[36:37] offset:-4096 sc1
v_mfma_f32_16x16x32_bf16 v[164:167], v[72:75], a[32:35], v[164:167]
ds_read_b64_tr_b16 v[104:105], v199 offset:768
v_cvt_pk_bf16_f32 v148, v152, v153
v_mfma_f32_16x16x32_bf16 v[164:167], v[76:79], a[40:43], v[164:167]
ds_read_b64_tr_b16 v[106:107], v199 offset:2816
v_cvt_pk_bf16_f32 v149, v154, v155
v_mfma_f32_16x16x32_bf16 v[172:175], v[72:75], a[36:39], v[172:175]
ds_read_b64_tr_b16 v[108:109], v199 offset:8960
v_cvt_pk_bf16_f32 v150, v156, v157
v_mfma_f32_16x16x32_bf16 v[172:175], v[76:79], a[44:47], v[172:175]
ds_read_b64_tr_b16 v[110:111], v199 offset:11008
s_waitcnt lgkmcnt(8)
v_mfma_f32_16x16x32_bf16 v[160:163], v[80:83], a[48:51], v[160:163]
ds_read_b64_tr_b16 v[112:113], v198 offset:17152
v_cvt_pk_bf16_f32 v151, v158, v159
v_mfma_f32_16x16x32_bf16 v[160:163], v[84:87], a[56:59], v[160:163]
ds_read_b64_tr_b16 v[114:115], v198 offset:19200
global_store_dwordx4 v201, v[148:151], s[36:37] offset:-3072 sc1
v_mfma_f32_16x16x32_bf16 v[168:171], v[80:83], a[52:55], v[168:171]
ds_read_b64_tr_b16 v[116:117], v198 offset:25344
v_mfma_f32_16x16x32_bf16 v[168:171], v[84:87], a[60:63], v[168:171]
ds_read_b64_tr_b16 v[118:119], v198 offset:27392
v_mfma_f32_16x16x32_bf16 v[164:167], v[88:91], a[48:51], v[164:167]
ds_read_b64_tr_b16 v[120:121], v199 offset:17152
v_mfma_f32_16x16x32_bf16 v[164:167], v[92:95], a[56:59], v[164:167]
ds_read_b64_tr_b16 v[122:123], v199 offset:19200
v_cvt_pk_bf16_f32 v160, v160, v161
v_mfma_f32_16x16x32_bf16 v[172:175], v[88:91], a[52:55], v[172:175]
ds_read_b64_tr_b16 v[124:125], v199 offset:25344
v_cvt_pk_bf16_f32 v161, v162, v163
v_mfma_f32_16x16x32_bf16 v[172:175], v[92:95], a[60:63], v[172:175]
ds_read_b64_tr_b16 v[126:127], v199 offset:27392
s_waitcnt lgkmcnt(8)
v_mfma_f32_16x16x32_bf16 v[176:179], v[96:99], a[0:3], v[176:179]
ds_read_b64_tr_b16 v[64:65], v198 offset:33536
v_mfma_f32_16x16x32_bf16 v[176:179], v[100:103], a[8:11], v[176:179]
ds_read_b64_tr_b16 v[66:67], v198 offset:35584
v_cvt_pk_bf16_f32 v162, v164, v165
v_mfma_f32_16x16x32_bf16 v[184:187], v[96:99], a[4:7], v[184:187]
ds_read_b64_tr_b16 v[68:69], v198 offset:41728
v_cvt_pk_bf16_f32 v163, v166, v167
v_mfma_f32_16x16x32_bf16 v[184:187], v[100:103], a[12:15], v[184:187]
ds_read_b64_tr_b16 v[70:71], v198 offset:43776
global_store_dwordx4 v202, v[160:163], s[36:37] offset:-4096 sc1
v_mfma_f32_16x16x32_bf16 v[180:183], v[104:107], a[0:3], v[180:183]
ds_read_b64_tr_b16 v[72:73], v199 offset:33536
v_cvt_pk_bf16_f32 v164, v168, v169
v_mfma_f32_16x16x32_bf16 v[180:183], v[108:111], a[8:11], v[180:183]
ds_read_b64_tr_b16 v[74:75], v199 offset:35584
v_cvt_pk_bf16_f32 v165, v170, v171
v_mfma_f32_16x16x32_bf16 v[188:191], v[104:107], a[4:7], v[188:191]
ds_read_b64_tr_b16 v[76:77], v199 offset:41728
v_cvt_pk_bf16_f32 v166, v172, v173
v_mfma_f32_16x16x32_bf16 v[188:191], v[108:111], a[12:15], v[188:191]
ds_read_b64_tr_b16 v[78:79], v199 offset:43776
s_waitcnt lgkmcnt(8)
v_mfma_f32_16x16x32_bf16 v[176:179], v[112:115], a[16:19], v[176:179]
ds_read_b64_tr_b16 v[80:81], v198 offset:49920
v_cvt_pk_bf16_f32 v167, v174, v175
v_mfma_f32_16x16x32_bf16 v[176:179], v[116:119], a[24:27], v[176:179]
ds_read_b64_tr_b16 v[82:83], v198 offset:51968
global_store_dwordx4 v202, v[164:167], s[36:37] offset:-3072 sc1
v_mfma_f32_16x16x32_bf16 v[184:187], v[112:115], a[20:23], v[184:187]
ds_read_b64_tr_b16 v[84:85], v198 offset:58112
v_mfma_f32_16x16x32_bf16 v[184:187], v[116:119], a[28:31], v[184:187]
ds_read_b64_tr_b16 v[86:87], v198 offset:60160
v_mfma_f32_16x16x32_bf16 v[180:183], v[120:123], a[16:19], v[180:183]
ds_read_b64_tr_b16 v[88:89], v199 offset:49920
v_mfma_f32_16x16x32_bf16 v[180:183], v[124:127], a[24:27], v[180:183]
ds_read_b64_tr_b16 v[90:91], v199 offset:51968
v_mfma_f32_16x16x32_bf16 v[188:191], v[120:123], a[20:23], v[188:191]
ds_read_b64_tr_b16 v[92:93], v199 offset:58112
v_mfma_f32_16x16x32_bf16 v[188:191], v[124:127], a[28:31], v[188:191]
ds_read_b64_tr_b16 v[94:95], v199 offset:60160
s_waitcnt lgkmcnt(8)
v_mfma_f32_16x16x32_bf16 v[176:179], v[64:67], a[32:35], v[176:179]
ds_read_b64_tr_b16 v[96:97], v196 offset:32768
v_mfma_f32_16x16x32_bf16 v[176:179], v[68:71], a[40:43], v[176:179]
ds_read_b64_tr_b16 v[98:99], v196 offset:34816
v_mfma_f32_16x16x32_bf16 v[184:187], v[64:67], a[36:39], v[184:187]
ds_read_b64_tr_b16 v[100:101], v196 offset:40960
v_mfma_f32_16x16x32_bf16 v[184:187], v[68:71], a[44:47], v[184:187]
ds_read_b64_tr_b16 v[102:103], v196 offset:43008
v_mfma_f32_16x16x32_bf16 v[180:183], v[72:75], a[32:35], v[180:183]
ds_read_b64_tr_b16 v[104:105], v197 offset:32768
v_mfma_f32_16x16x32_bf16 v[180:183], v[76:79], a[40:43], v[180:183]
ds_read_b64_tr_b16 v[106:107], v197 offset:34816
v_mfma_f32_16x16x32_bf16 v[188:191], v[72:75], a[36:39], v[188:191]
ds_read_b64_tr_b16 v[108:109], v197 offset:40960
v_mfma_f32_16x16x32_bf16 v[188:191], v[76:79], a[44:47], v[188:191]
ds_read_b64_tr_b16 v[110:111], v197 offset:43008
s_waitcnt lgkmcnt(8)
v_mfma_f32_16x16x32_bf16 v[176:179], v[80:83], a[48:51], v[176:179]
ds_read_b64_tr_b16 v[112:113], v196 offset:49152
v_mfma_f32_16x16x32_bf16 v[176:179], v[84:87], a[56:59], v[176:179]
ds_read_b64_tr_b16 v[114:115], v196 offset:51200
v_mfma_f32_16x16x32_bf16 v[184:187], v[80:83], a[52:55], v[184:187]
ds_read_b64_tr_b16 v[116:117], v196 offset:57344
v_mfma_f32_16x16x32_bf16 v[184:187], v[84:87], a[60:63], v[184:187]
ds_read_b64_tr_b16 v[118:119], v196 offset:59392
v_mfma_f32_16x16x32_bf16 v[180:183], v[88:91], a[48:51], v[180:183]
ds_read_b64_tr_b16 v[120:121], v197 offset:49152
v_mfma_f32_16x16x32_bf16 v[180:183], v[92:95], a[56:59], v[180:183]
ds_read_b64_tr_b16 v[122:123], v197 offset:51200
v_cvt_pk_bf16_f32 v176, v176, v177
v_cvt_pk_bf16_f32 v177, v178, v179
v_mfma_f32_16x16x32_bf16 v[188:191], v[88:91], a[52:55], v[188:191]
ds_read_b64_tr_b16 v[124:125], v197 offset:57344
v_mfma_f32_16x16x32_bf16 v[188:191], v[92:95], a[60:63], v[188:191]
ds_read_b64_tr_b16 v[126:127], v197 offset:59392
s_waitcnt lgkmcnt(8)
v_mfma_f32_16x16x32_bf16 v[128:131], v[96:99], a[160:163], v[128:131]
ds_read_b64_tr_b16 v[64:65], v198 offset:0
v_mfma_f32_16x16x32_bf16 v[128:131], v[100:103], a[168:171], v[128:131]
ds_read_b64_tr_b16 v[66:67], v198 offset:2048
v_cvt_pk_bf16_f32 v178, v180, v181
v_cvt_pk_bf16_f32 v179, v182, v183
v_mfma_f32_16x16x32_bf16 v[136:139], v[96:99], a[164:167], v[136:139]
ds_read_b64_tr_b16 v[68:69], v198 offset:8192
global_store_dwordx4 v203, v[176:179], s[36:37] offset:-4096 sc1
PAD16_%=:
s_cbranch_execz PAD25_%=
v_mfma_f32_16x16x32_bf16 v[136:139], v[100:103], a[172:175], v[136:139]
ds_read_b64_tr_b16 v[70:71], v198 offset:10240
v_cvt_pk_bf16_f32 v180, v184, v185
v_cvt_pk_bf16_f32 v181, v186, v187
v_mfma_f32_16x16x32_bf16 v[132:135], v[104:107], a[160:163], v[132:135]
ds_read_b64_tr_b16 v[72:73], v199 offset:0
v_cvt_pk_bf16_f32 v182, v188, v189
v_cvt_pk_bf16_f32 v183, v190, v191
v_mfma_f32_16x16x32_bf16 v[132:135], v[108:111], a[168:171], v[132:135]
ds_read_b64_tr_b16 v[74:75], v199 offset:2048
global_store_dwordx4 v203, v[180:183], s[36:37] offset:-3072 sc1
)"
R"(v_mfma_f32_16x16x32_bf16 v[140:143], v[104:107], a[164:167], v[140:143]
ds_read_b64_tr_b16 v[76:77], v199 offset:8192
v_mfma_f32_16x16x32_bf16 v[140:143], v[108:111], a[172:175], v[140:143]
ds_read_b64_tr_b16 v[78:79], v199 offset:10240
s_waitcnt lgkmcnt(8)
v_mfma_f32_16x16x32_bf16 v[128:131], v[112:115], a[176:179], v[128:131]
ds_read_b64_tr_b16 v[80:81], v198 offset:16384
global_load_dwordx4 v[240:243], v200, s[36:37] offset:0 sc1
v_mfma_f32_16x16x32_bf16 v[128:131], v[116:119], a[184:187], v[128:131]
ds_read_b64_tr_b16 v[82:83], v198 offset:18432
v_mfma_f32_16x16x32_bf16 v[136:139], v[112:115], a[180:183], v[136:139]
ds_read_b64_tr_b16 v[84:85], v198 offset:24576
global_load_dwordx4 v[244:247], v200, s[36:37] offset:1024 sc1
v_mfma_f32_16x16x32_bf16 v[136:139], v[116:119], a[188:191], v[136:139]
ds_read_b64_tr_b16 v[86:87], v198 offset:26624
v_mfma_f32_16x16x32_bf16 v[132:135], v[120:123], a[176:179], v[132:135]
ds_read_b64_tr_b16 v[88:89], v199 offset:16384
global_load_dwordx4 v[224:227], v200, s[36:37] offset:-4096 sc1
v_mfma_f32_16x16x32_bf16 v[132:135], v[124:127], a[184:187], v[132:135]
ds_read_b64_tr_b16 v[90:91], v199 offset:18432
v_mfma_f32_16x16x32_bf16 v[140:143], v[120:123], a[180:183], v[140:143]
ds_read_b64_tr_b16 v[92:93], v199 offset:24576
global_load_dwordx4 v[228:231], v200, s[36:37] offset:-3072 sc1
v_mfma_f32_16x16x32_bf16 v[140:143], v[124:127], a[188:191], v[140:143]
ds_read_b64_tr_b16 v[94:95], v199 offset:26624
s_waitcnt lgkmcnt(8)
v_mfma_f32_16x16x32_bf16 v[128:131], v[64:67], a[0:3], v[128:131]
ds_read_b64_tr_b16 v[96:97], v198 offset:32768
global_load_dwordx4 v[232:235], v200, s[36:37] offset:-2048 sc1
v_mfma_f32_16x16x32_bf16 v[128:131], v[68:71], a[8:11], v[128:131]
ds_read_b64_tr_b16 v[98:99], v198 offset:34816
v_mfma_f32_16x16x32_bf16 v[136:139], v[64:67], a[4:7], v[136:139]
ds_read_b64_tr_b16 v[100:101], v198 offset:40960
global_load_dwordx4 v[236:239], v200, s[36:37] offset:-1024 sc1
v_mfma_f32_16x16x32_bf16 v[136:139], v[68:71], a[12:15], v[136:139]
ds_read_b64_tr_b16 v[102:103], v198 offset:43008
v_mfma_f32_16x16x32_bf16 v[132:135], v[72:75], a[0:3], v[132:135]
ds_read_b64_tr_b16 v[104:105], v199 offset:32768
v_mfma_f32_16x16x32_bf16 v[132:135], v[76:79], a[8:11], v[132:135]
ds_read_b64_tr_b16 v[106:107], v199 offset:34816
v_mfma_f32_16x16x32_bf16 v[140:143], v[72:75], a[4:7], v[140:143]
ds_read_b64_tr_b16 v[108:109], v199 offset:40960
v_mfma_f32_16x16x32_bf16 v[140:143], v[76:79], a[12:15], v[140:143]
ds_read_b64_tr_b16 v[110:111], v199 offset:43008
s_waitcnt lgkmcnt(8)
v_mfma_f32_16x16x32_bf16 v[128:131], v[80:83], a[16:19], v[128:131]
ds_read_b64_tr_b16 v[112:113], v198 offset:49152
v_mfma_f32_16x16x32_bf16 v[128:131], v[84:87], a[24:27], v[128:131]
ds_read_b64_tr_b16 v[114:115], v198 offset:51200
v_mfma_f32_16x16x32_bf16 v[136:139], v[80:83], a[20:23], v[136:139]
ds_read_b64_tr_b16 v[116:117], v198 offset:57344
v_mfma_f32_16x16x32_bf16 v[136:139], v[84:87], a[28:31], v[136:139]
ds_read_b64_tr_b16 v[118:119], v198 offset:59392
v_mfma_f32_16x16x32_bf16 v[132:135], v[88:91], a[16:19], v[132:135]
ds_read_b64_tr_b16 v[120:121], v199 offset:49152
v_mfma_f32_16x16x32_bf16 v[132:135], v[92:95], a[24:27], v[132:135]
ds_read_b64_tr_b16 v[122:123], v199 offset:51200
v_mfma_f32_16x16x32_bf16 v[140:143], v[88:91], a[20:23], v[140:143]
ds_read_b64_tr_b16 v[124:125], v199 offset:57344
v_mfma_f32_16x16x32_bf16 v[140:143], v[92:95], a[28:31], v[140:143]
ds_read_b64_tr_b16 v[126:127], v199 offset:59392
s_waitcnt lgkmcnt(8)
v_mfma_f32_16x16x32_bf16 v[128:131], v[96:99], a[32:35], v[128:131]
v_mfma_f32_16x16x32_bf16 v[128:131], v[100:103], a[40:43], v[128:131]
v_mfma_f32_16x16x32_bf16 v[136:139], v[96:99], a[36:39], v[136:139]
v_mfma_f32_16x16x32_bf16 v[136:139], v[100:103], a[44:47], v[136:139]
v_mfma_f32_16x16x32_bf16 v[132:135], v[104:107], a[32:35], v[132:135]
v_mfma_f32_16x16x32_bf16 v[132:135], v[108:111], a[40:43], v[132:135]
v_mfma_f32_16x16x32_bf16 v[140:143], v[104:107], a[36:39], v[140:143]
v_mfma_f32_16x16x32_bf16 v[140:143], v[108:111], a[44:47], v[140:143]
s_waitcnt lgkmcnt(0)
v_mfma_f32_16x16x32_bf16 v[128:131], v[112:115], a[48:51], v[128:131]
v_mfma_f32_16x16x32_bf16 v[128:131], v[116:119], a[56:59], v[128:131]
v_mfma_f32_16x16x32_bf16 v[136:139], v[112:115], a[52:55], v[136:139]
v_mfma_f32_16x16x32_bf16 v[136:139], v[116:119], a[60:63], v[136:139]
v_mfma_f32_16x16x32_bf16 v[132:135], v[120:123], a[48:51], v[132:135]
v_mfma_f32_16x16x32_bf16 v[132:135], v[124:127], a[56:59], v[132:135]
v_mfma_f32_16x16x32_bf16 v[140:143], v[120:123], a[52:55], v[140:143]
v_mfma_f32_16x16x32_bf16 v[140:143], v[124:127], a[60:63], v[140:143]
s_branch P3FIN_%=
PAD17_%=:
s_cbranch_execz PAD26_%=
P3L3_%=:
s_waitcnt vmcnt(23) lgkmcnt(8)
v_mfma_f32_16x16x32_bf16 v[144:147], v[64:67], a[32:35], v[144:147]
ds_read_b64_tr_b16 v[96:97], v198 offset:33536
s_lshl_b32 s71, s47, 13
v_mfma_f32_16x16x32_bf16 v[144:147], v[68:71], a[40:43], v[144:147]
ds_read_b64_tr_b16 v[98:99], v198 offset:35584
buffer_load_dwordx4 a[164:167], v22, s[12:15], s71 offen offset:1024 sc1
v_mfma_f32_16x16x32_bf16 v[152:155], v[64:67], a[36:39], v[152:155]
ds_read_b64_tr_b16 v[100:101], v198 offset:41728
v_mfma_f32_16x16x32_bf16 v[152:155], v[68:71], a[44:47], v[152:155]
ds_read_b64_tr_b16 v[102:103], v198 offset:43776
v_mfma_f32_16x16x32_bf16 v[148:151], v[72:75], a[32:35], v[148:151]
ds_read_b64_tr_b16 v[104:105], v199 offset:33536
v_mfma_f32_16x16x32_bf16 v[148:151], v[76:79], a[40:43], v[148:151]
ds_read_b64_tr_b16 v[106:107], v199 offset:35584
buffer_load_dwordx4 a[168:171], v22, s[12:15], s71 offen offset:2048 sc1
v_mfma_f32_16x16x32_bf16 v[156:159], v[72:75], a[36:39], v[156:159]
ds_read_b64_tr_b16 v[108:109], v199 offset:41728
v_mfma_f32_16x16x32_bf16 v[156:159], v[76:79], a[44:47], v[156:159]
ds_read_b64_tr_b16 v[110:111], v199 offset:43776
s_waitcnt lgkmcnt(8)
v_mfma_f32_16x16x32_bf16 v[160:163], v[80:83], a[32:35], v[160:163]
ds_read_b64_tr_b16 v[112:113], v198 offset:49408
v_mfma_f32_16x16x32_bf16 v[160:163], v[84:87], a[40:43], v[160:163]
ds_read_b64_tr_b16 v[114:115], v198 offset:51456
buffer_load_dwordx4 a[172:175], v22, s[12:15], s71 offen offset:3072 sc1
v_mfma_f32_16x16x32_bf16 v[168:171], v[80:83], a[36:39], v[168:171]
ds_read_b64_tr_b16 v[116:117], v198 offset:57600
v_mfma_f32_16x16x32_bf16 v[168:171], v[84:87], a[44:47], v[168:171]
ds_read_b64_tr_b16 v[118:119], v198 offset:59648
v_mfma_f32_16x16x32_bf16 v[164:167], v[88:91], a[32:35], v[164:167]
ds_read_b64_tr_b16 v[120:121], v199 offset:49408
v_mfma_f32_16x16x32_bf16 v[164:167], v[92:95], a[40:43], v[164:167]
ds_read_b64_tr_b16 v[122:123], v199 offset:51456
buffer_load_dwordx4 a[160:163], v22, s[12:15], s71 offen offset:0 sc1
v_mfma_f32_16x16x32_bf16 v[172:175], v[88:91], a[36:39], v[172:175]
ds_read_b64_tr_b16 v[124:125], v199 offset:57600
s_add_u32 s71, s71, 0x1000
v_mfma_f32_16x16x32_bf16 v[172:175], v[92:95], a[44:47], v[172:175]
ds_read_b64_tr_b16 v[126:127], v199 offset:59648
s_waitcnt lgkmcnt(8)
v_mfma_f32_16x16x32_bf16 v[176:179], v[96:99], a[32:35], v[176:179]
ds_read_b64_tr_b16 v[64:65], v198 offset:49664
v_mfma_f32_16x16x32_bf16 v[176:179], v[100:103], a[40:43], v[176:179]
ds_read_b64_tr_b16 v[66:67], v198 offset:51712
buffer_load_dwordx4 a[180:183], v22, s[12:15], s71 offen offset:1024 sc1
v_mfma_f32_16x16x32_bf16 v[184:187], v[96:99], a[36:39], v[184:187]
ds_read_b64_tr_b16 v[68:69], v198 offset:57856
v_mfma_f32_16x16x32_bf16 v[184:187], v[100:103], a[44:47], v[184:187]
ds_read_b64_tr_b16 v[70:71], v198 offset:59904
v_mfma_f32_16x16x32_bf16 v[180:183], v[104:107], a[32:35], v[180:183]
ds_read_b64_tr_b16 v[72:73], v199 offset:49664
v_mfma_f32_16x16x32_bf16 v[180:183], v[108:111], a[40:43], v[180:183]
ds_read_b64_tr_b16 v[74:75], v199 offset:51712
buffer_load_dwordx4 a[184:187], v22, s[12:15], s71 offen offset:2048 sc1
v_mfma_f32_16x16x32_bf16 v[188:191], v[104:107], a[36:39], v[188:191]
ds_read_b64_tr_b16 v[76:77], v199 offset:57856
v_mfma_f32_16x16x32_bf16 v[188:191], v[108:111], a[44:47], v[188:191]
ds_read_b64_tr_b16 v[78:79], v199 offset:59904
s_waitcnt vmcnt(22) lgkmcnt(8)
v_mfma_f32_16x16x32_bf16 v[144:147], v[112:115], a[48:51], v[144:147]
ds_read_b64_tr_b16 v[80:81], v198 offset:49920
v_mfma_f32_16x16x32_bf16 v[144:147], v[116:119], a[56:59], v[144:147]
ds_read_b64_tr_b16 v[82:83], v198 offset:51968
buffer_load_dwordx4 a[188:191], v22, s[12:15], s71 offen offset:3072 sc1
v_mfma_f32_16x16x32_bf16 v[152:155], v[112:115], a[52:55], v[152:155]
ds_read_b64_tr_b16 v[84:85], v198 offset:58112
v_mfma_f32_16x16x32_bf16 v[152:155], v[116:119], a[60:63], v[152:155]
ds_read_b64_tr_b16 v[86:87], v198 offset:60160
v_mfma_f32_16x16x32_bf16 v[148:151], v[120:123], a[48:51], v[148:151]
ds_read_b64_tr_b16 v[88:89], v199 offset:49920
v_mfma_f32_16x16x32_bf16 v[148:151], v[124:127], a[56:59], v[148:151]
ds_read_b64_tr_b16 v[90:91], v199 offset:51968
buffer_load_dwordx4 a[176:179], v22, s[12:15], s71 offen offset:0 sc1
v_mfma_f32_16x16x32_bf16 v[156:159], v[120:123], a[52:55], v[156:159]
ds_read_b64_tr_b16 v[92:93], v199 offset:58112
s_lshl_b32 s77, s31, 15
v_mfma_f32_16x16x32_bf16 v[156:159], v[124:127], a[60:63], v[156:159]
ds_read_b64_tr_b16 v[94:95], v199 offset:60160
s_waitcnt vmcnt(12) lgkmcnt(0)
s_barrier
v_mfma_f32_16x16x32_bf16 v[160:163], v[64:67], a[48:51], v[160:163]
ds_read_b64_tr_b16 v[96:97], v196 offset:256
v_add_u32 v200, s77, v22
v_mfma_f32_16x16x32_bf16 v[160:163], v[68:71], a[56:59], v[160:163]
ds_read_b64_tr_b16 v[98:99], v196 offset:2304
s_mov_b32 s98, 1
v_mfma_f32_16x16x32_bf16 v[168:171], v[64:67], a[52:55], v[168:171]
ds_read_b64_tr_b16 v[100:101], v196 offset:8448
s_mov_b32 s89, 15
v_mfma_f32_16x16x32_bf16 v[168:171], v[68:71], a[60:63], v[168:171]
ds_read_b64_tr_b16 v[102:103], v196 offset:10496
v_cmp_eq_u32 vcc, s98, v28
v_mfma_f32_16x16x32_bf16 v[164:167], v[72:75], a[48:51], v[164:167]
ds_read_b64_tr_b16 v[104:105], v197 offset:256
s_and_b32 s77, vcc_lo, s89
v_mfma_f32_16x16x32_bf16 v[164:167], v[76:79], a[56:59], v[164:167]
ds_read_b64_tr_b16 v[106:107], v197 offset:2304
s_cmp_lg_u32 s77, s89
v_mfma_f32_16x16x32_bf16 v[172:175], v[72:75], a[52:55], v[172:175]
ds_read_b64_tr_b16 v[108:109], v197 offset:8448
s_cbranch_scc1 XF3SP_%=
XF3OK_%=:
v_mfma_f32_16x16x32_bf16 v[172:175], v[76:79], a[60:63], v[172:175]
ds_read_b64_tr_b16 v[110:111], v197 offset:10496
v_mfma_f32_16x16x32_bf16 v[176:179], v[80:83], a[48:51], v[176:179]
ds_read_b64_tr_b16 v[112:113], v196 offset:512
s_bfe_u32 s77, s30, 0x6000c
v_mfma_f32_16x16x32_bf16 v[176:179], v[84:87], a[56:59], v[176:179]
ds_read_b64_tr_b16 v[114:115], v196 offset:2560
v_lshl_add_u32 v201, s77, 11, v22
v_mfma_f32_16x16x32_bf16 v[184:187], v[80:83], a[52:55], v[184:187]
ds_read_b64_tr_b16 v[116:117], v196 offset:8704
s_bfe_u32 s77, s30, 0x60012
v_mfma_f32_16x16x32_bf16 v[184:187], v[84:87], a[60:63], v[184:187]
ds_read_b64_tr_b16 v[118:119], v196 offset:10752
v_lshl_add_u32 v202, s77, 11, v22
v_mfma_f32_16x16x32_bf16 v[180:183], v[88:91], a[48:51], v[180:183]
ds_read_b64_tr_b16 v[120:121], v197 offset:512
s_bfe_u32 s77, s30, 0x60018
v_mfma_f32_16x16x32_bf16 v[180:183], v[92:95], a[56:59], v[180:183]
ds_read_b64_tr_b16 v[122:123], v197 offset:2560
v_lshl_add_u32 v203, s77, 11, v22
v_mfma_f32_16x16x32_bf16 v[188:191], v[88:91], a[52:55], v[188:191]
ds_read_b64_tr_b16 v[124:125], v197 offset:8704
v_mov_b32 v207, 0xbfb8aa3b
v_mfma_f32_16x16x32_bf16 v[188:191], v[92:95], a[60:63], v[188:191]
ds_read_b64_tr_b16 v[126:127], v197 offset:10752
s_waitcnt lgkmcnt(8)
v_mfma_f32_16x16x32_bf16 v[144:147], v[96:99], a[128:131], v[144:147]
ds_read_b64_tr_b16 v[64:65], v196 offset:16640
v_lshlrev_b32 v32, 16, v208
v_mfma_f32_16x16x32_bf16 v[144:147], v[100:103], a[136:139], v[144:147]
ds_read_b64_tr_b16 v[66:67], v196 offset:18688
v_and_b32 v33, 0xffff0000, v208
v_mfma_f32_16x16x32_bf16 v[152:155], v[96:99], a[132:135], v[152:155]
ds_read_b64_tr_b16 v[68:69], v196 offset:24832
v_lshlrev_b32 v34, 16, v209
v_mfma_f32_16x16x32_bf16 v[152:155], v[100:103], a[140:143], v[152:155]
ds_read_b64_tr_b16 v[70:71], v196 offset:26880
v_and_b32 v35, 0xffff0000, v209
v_mfma_f32_16x16x32_bf16 v[148:151], v[104:107], a[128:131], v[148:151]
ds_read_b64_tr_b16 v[72:73], v197 offset:16640
v_lshlrev_b32 v36, 16, v210
v_mfma_f32_16x16x32_bf16 v[148:151], v[108:111], a[136:139], v[148:151]
ds_read_b64_tr_b16 v[74:75], v197 offset:18688
v_and_b32 v37, 0xffff0000, v210
v_mfma_f32_16x16x32_bf16 v[156:159], v[104:107], a[132:135], v[156:159]
ds_read_b64_tr_b16 v[76:77], v197 offset:24832
v_lshlrev_b32 v38, 16, v211
v_mfma_f32_16x16x32_bf16 v[156:159], v[108:111], a[140:143], v[156:159]
ds_read_b64_tr_b16 v[78:79], v197 offset:26880
s_waitcnt lgkmcnt(8)
v_mfma_f32_16x16x32_bf16 v[160:163], v[112:115], a[128:131], v[160:163]
ds_read_b64_tr_b16 v[80:81], v196 offset:16896
v_and_b32 v39, 0xffff0000, v211
v_mfma_f32_16x16x32_bf16 v[160:163], v[116:119], a[136:139], v[160:163]
ds_read_b64_tr_b16 v[82:83], v196 offset:18944
v_lshlrev_b32 v40, 16, v212
v_mfma_f32_16x16x32_bf16 v[168:171], v[112:115], a[132:135], v[168:171]
ds_read_b64_tr_b16 v[84:85], v196 offset:25088
v_and_b32 v41, 0xffff0000, v212
v_mfma_f32_16x16x32_bf16 v[168:171], v[116:119], a[140:143], v[168:171]
ds_read_b64_tr_b16 v[86:87], v196 offset:27136
v_lshlrev_b32 v42, 16, v213
v_mfma_f32_16x16x32_bf16 v[164:167], v[120:123], a[128:131], v[164:167]
ds_read_b64_tr_b16 v[88:89], v197 offset:16896
v_and_b32 v43, 0xffff0000, v213
v_mfma_f32_16x16x32_bf16 v[164:167], v[124:127], a[136:139], v[164:167]
ds_read_b64_tr_b16 v[90:91], v197 offset:18944
v_lshlrev_b32 v44, 16, v214
v_mfma_f32_16x16x32_bf16 v[172:175], v[120:123], a[132:135], v[172:175]
ds_read_b64_tr_b16 v[92:93], v197 offset:25088
v_and_b32 v45, 0xffff0000, v214
v_mfma_f32_16x16x32_bf16 v[172:175], v[124:127], a[140:143], v[172:175]
ds_read_b64_tr_b16 v[94:95], v197 offset:27136
s_waitcnt vmcnt(8) lgkmcnt(8)
v_mfma_f32_16x16x32_bf16 v[144:147], v[64:67], a[144:147], v[144:147]
ds_read_b64_tr_b16 v[96:97], v196 offset:33024
v_lshlrev_b32 v46, 16, v215
v_mfma_f32_16x16x32_bf16 v[144:147], v[68:71], a[152:155], v[144:147]
ds_read_b64_tr_b16 v[98:99], v196 offset:35072
v_and_b32 v47, 0xffff0000, v215
v_mfma_f32_16x16x32_bf16 v[152:155], v[64:67], a[148:151], v[152:155]
ds_read_b64_tr_b16 v[100:101], v196 offset:41216
v_mfma_f32_16x16x32_bf16 v[152:155], v[68:71], a[156:159], v[152:155]
ds_read_b64_tr_b16 v[102:103], v196 offset:43264
v_mfma_f32_16x16x32_bf16 v[148:151], v[72:75], a[144:147], v[148:151]
ds_read_b64_tr_b16 v[104:105], v197 offset:33024
v_mfma_f32_16x16x32_bf16 v[148:151], v[76:79], a[152:155], v[148:151]
ds_read_b64_tr_b16 v[106:107], v197 offset:35072
v_mfma_f32_16x16x32_bf16 v[156:159], v[72:75], a[148:151], v[156:159]
ds_read_b64_tr_b16 v[108:109], v197 offset:41216
v_mfma_f32_16x16x32_bf16 v[156:159], v[76:79], a[156:159], v[156:159]
ds_read_b64_tr_b16 v[110:111], v197 offset:43264
s_waitcnt lgkmcnt(8)
v_mfma_f32_16x16x32_bf16 v[160:163], v[80:83], a[144:147], v[160:163]
ds_read_b64_tr_b16 v[112:113], v196 offset:49408
v_mfma_f32_16x16x32_bf16 v[160:163], v[84:87], a[152:155], v[160:163]
ds_read_b64_tr_b16 v[114:115], v196 offset:51456
v_mfma_f32_16x16x32_bf16 v[168:171], v[80:83], a[148:151], v[168:171]
ds_read_b64_tr_b16 v[116:117], v196 offset:57600
v_mfma_f32_16x16x32_bf16 v[168:171], v[84:87], a[156:159], v[168:171]
ds_read_b64_tr_b16 v[118:119], v196 offset:59648
v_mfma_f32_16x16x32_bf16 v[164:167], v[88:91], a[144:147], v[164:167]
ds_read_b64_tr_b16 v[120:121], v197 offset:49408
v_mfma_f32_16x16x32_bf16 v[164:167], v[92:95], a[152:155], v[164:167]
ds_read_b64_tr_b16 v[122:123], v197 offset:51456
v_mfma_f32_16x16x32_bf16 v[172:175], v[88:91], a[148:151], v[172:175]
ds_read_b64_tr_b16 v[124:125], v197 offset:57600
v_mfma_f32_16x16x32_bf16 v[172:175], v[92:95], a[156:159], v[172:175]
ds_read_b64_tr_b16 v[126:127], v197 offset:59648
s_waitcnt vmcnt(4) lgkmcnt(8)
v_mfma_f32_16x16x32_bf16 v[144:147], v[96:99], a[160:163], v[144:147]
ds_read_b64_tr_b16 v[64:65], v196 offset:33280
v_mfma_f32_16x16x32_bf16 v[144:147], v[100:103], a[168:171], v[144:147]
ds_read_b64_tr_b16 v[66:67], v196 offset:35328
v_mfma_f32_16x16x32_bf16 v[152:155], v[96:99], a[164:167], v[152:155]
ds_read_b64_tr_b16 v[68:69], v196 offset:41472
v_mfma_f32_16x16x32_bf16 v[152:155], v[100:103], a[172:175], v[152:155]
ds_read_b64_tr_b16 v[70:71], v196 offset:43520
v_mfma_f32_16x16x32_bf16 v[148:151], v[104:107], a[160:163], v[148:151]
ds_read_b64_tr_b16 v[72:73], v197 offset:33280
v_mfma_f32_16x16x32_bf16 v[148:151], v[108:111], a[168:171], v[148:151]
ds_read_b64_tr_b16 v[74:75], v197 offset:35328
v_mfma_f32_16x16x32_bf16 v[156:159], v[104:107], a[164:167], v[156:159]
ds_read_b64_tr_b16 v[76:77], v197 offset:41472
v_mfma_f32_16x16x32_bf16 v[156:159], v[108:111], a[172:175], v[156:159]
ds_read_b64_tr_b16 v[78:79], v197 offset:43520
s_waitcnt vmcnt(0) lgkmcnt(8)
v_mfma_f32_16x16x32_bf16 v[144:147], v[112:115], a[176:179], v[144:147]
ds_read_b64_tr_b16 v[80:81], v196 offset:49664
v_mfma_f32_16x16x32_bf16 v[144:147], v[116:119], a[184:187], v[144:147]
ds_read_b64_tr_b16 v[82:83], v196 offset:51712
v_mfma_f32_16x16x32_bf16 v[152:155], v[112:115], a[180:183], v[152:155]
ds_read_b64_tr_b16 v[84:85], v196 offset:57856
v_mfma_f32_16x16x32_bf16 v[152:155], v[116:119], a[188:191], v[152:155]
ds_read_b64_tr_b16 v[86:87], v196 offset:59904
v_mfma_f32_16x16x32_bf16 v[148:151], v[120:123], a[176:179], v[148:151]
ds_read_b64_tr_b16 v[88:89], v197 offset:49664
v_mfma_f32_16x16x32_bf16 v[148:151], v[124:127], a[184:187], v[148:151]
ds_read_b64_tr_b16 v[90:91], v197 offset:51712
v_cvt_pk_bf16_f32 v144, v144, v145
v_mfma_f32_16x16x32_bf16 v[156:159], v[120:123], a[180:183], v[156:159]
ds_read_b64_tr_b16 v[92:93], v197 offset:57856
v_cvt_pk_bf16_f32 v145, v146, v147
v_mfma_f32_16x16x32_bf16 v[156:159], v[124:127], a[188:191], v[156:159]
ds_read_b64_tr_b16 v[94:95], v197 offset:59904
s_waitcnt lgkmcnt(8)
v_mfma_f32_16x16x32_bf16 v[160:163], v[64:67], a[160:163], v[160:163]
ds_read_b64_tr_b16 v[96:97], v196 offset:768
v_mfma_f32_16x16x32_bf16 v[160:163], v[68:71], a[168:171], v[160:163]
ds_read_b64_tr_b16 v[98:99], v196 offset:2816
v_cvt_pk_bf16_f32 v146, v148, v149
v_mfma_f32_16x16x32_bf16 v[168:171], v[64:67], a[164:167], v[168:171]
ds_read_b64_tr_b16 v[100:101], v196 offset:8960
v_cvt_pk_bf16_f32 v147, v150, v151
v_mfma_f32_16x16x32_bf16 v[168:171], v[68:71], a[172:175], v[168:171]
ds_read_b64_tr_b16 v[102:103], v196 offset:11008
global_store_dwordx4 v201, v[144:147], s[36:37] offset:-4096 sc1
v_mfma_f32_16x16x32_bf16 v[164:167], v[72:75], a[160:163], v[164:167]
ds_read_b64_tr_b16 v[104:105], v197 offset:768
v_cvt_pk_bf16_f32 v148, v152, v153
v_mfma_f32_16x16x32_bf16 v[164:167], v[76:79], a[168:171], v[164:167]
ds_read_b64_tr_b16 v[106:107], v197 offset:2816
v_cvt_pk_bf16_f32 v149, v154, v155
v_mfma_f32_16x16x32_bf16 v[172:175], v[72:75], a[164:167], v[172:175]
ds_read_b64_tr_b16 v[108:109], v197 offset:8960
v_cvt_pk_bf16_f32 v150, v156, v157
v_mfma_f32_16x16x32_bf16 v[172:175], v[76:79], a[172:175], v[172:175]
ds_read_b64_tr_b16 v[110:111], v197 offset:11008
s_waitcnt lgkmcnt(8)
v_mfma_f32_16x16x32_bf16 v[160:163], v[80:83], a[176:179], v[160:163]
ds_read_b64_tr_b16 v[112:113], v196 offset:17152
v_cvt_pk_bf16_f32 v151, v158, v159
v_mfma_f32_16x16x32_bf16 v[160:163], v[84:87], a[184:187], v[160:163]
ds_read_b64_tr_b16 v[114:115], v196 offset:19200
global_store_dwordx4 v201, v[148:151], s[36:37] offset:-3072 sc1
v_mfma_f32_16x16x32_bf16 v[168:171], v[80:83], a[180:183], v[168:171]
ds_read_b64_tr_b16 v[116:117], v196 offset:25344
v_mfma_f32_16x16x32_bf16 v[168:171], v[84:87], a[188:191], v[168:171]
ds_read_b64_tr_b16 v[118:119], v196 offset:27392
v_mfma_f32_16x16x32_bf16 v[164:167], v[88:91], a[176:179], v[164:167]
ds_read_b64_tr_b16 v[120:121], v197 offset:17152
v_mfma_f32_16x16x32_bf16 v[164:167], v[92:95], a[184:187], v[164:167]
ds_read_b64_tr_b16 v[122:123], v197 offset:19200
v_cvt_pk_bf16_f32 v160, v160, v161
v_mfma_f32_16x16x32_bf16 v[172:175], v[88:91], a[180:183], v[172:175]
ds_read_b64_tr_b16 v[124:125], v197 offset:25344
v_cvt_pk_bf16_f32 v161, v162, v163
v_mfma_f32_16x16x32_bf16 v[172:175], v[92:95], a[188:191], v[172:175]
ds_read_b64_tr_b16 v[126:127], v197 offset:27392
s_waitcnt lgkmcnt(8)
v_mfma_f32_16x16x32_bf16 v[176:179], v[96:99], a[128:131], v[176:179]
ds_read_b64_tr_b16 v[64:65], v196 offset:33536
v_mfma_f32_16x16x32_bf16 v[176:179], v[100:103], a[136:139], v[176:179]
ds_read_b64_tr_b16 v[66:67], v196 offset:35584
v_cvt_pk_bf16_f32 v162, v164, v165
v_mfma_f32_16x16x32_bf16 v[184:187], v[96:99], a[132:135], v[184:187]
ds_read_b64_tr_b16 v[68:69], v196 offset:41728
v_cvt_pk_bf16_f32 v163, v166, v167
v_mfma_f32_16x16x32_bf16 v[184:187], v[100:103], a[140:143], v[184:187]
ds_read_b64_tr_b16 v[70:71], v196 offset:43776
global_store_dwordx4 v202, v[160:163], s[36:37] offset:-4096 sc1
v_mfma_f32_16x16x32_bf16 v[180:183], v[104:107], a[128:131], v[180:183]
ds_read_b64_tr_b16 v[72:73], v197 offset:33536
v_cvt_pk_bf16_f32 v164, v168, v169
v_mfma_f32_16x16x32_bf16 v[180:183], v[108:111], a[136:139], v[180:183]
ds_read_b64_tr_b16 v[74:75], v197 offset:35584
v_cvt_pk_bf16_f32 v165, v170, v171
v_mfma_f32_16x16x32_bf16 v[188:191], v[104:107], a[132:135], v[188:191]
ds_read_b64_tr_b16 v[76:77], v197 offset:41728
v_cvt_pk_bf16_f32 v166, v172, v173
v_mfma_f32_16x16x32_bf16 v[188:191], v[108:111], a[140:143], v[188:191]
ds_read_b64_tr_b16 v[78:79], v197 offset:43776
s_waitcnt lgkmcnt(8)
v_mfma_f32_16x16x32_bf16 v[176:179], v[112:115], a[144:147], v[176:179]
)"
R"(ds_read_b64_tr_b16 v[80:81], v196 offset:49920
v_cvt_pk_bf16_f32 v167, v174, v175
v_mfma_f32_16x16x32_bf16 v[176:179], v[116:119], a[152:155], v[176:179]
ds_read_b64_tr_b16 v[82:83], v196 offset:51968
global_store_dwordx4 v202, v[164:167], s[36:37] offset:-3072 sc1
v_mfma_f32_16x16x32_bf16 v[184:187], v[112:115], a[148:151], v[184:187]
ds_read_b64_tr_b16 v[84:85], v196 offset:58112
v_mfma_f32_16x16x32_bf16 v[184:187], v[116:119], a[156:159], v[184:187]
ds_read_b64_tr_b16 v[86:87], v196 offset:60160
v_mfma_f32_16x16x32_bf16 v[180:183], v[120:123], a[144:147], v[180:183]
ds_read_b64_tr_b16 v[88:89], v197 offset:49920
v_mfma_f32_16x16x32_bf16 v[180:183], v[124:127], a[152:155], v[180:183]
ds_read_b64_tr_b16 v[90:91], v197 offset:51968
v_mfma_f32_16x16x32_bf16 v[188:191], v[120:123], a[148:151], v[188:191]
ds_read_b64_tr_b16 v[92:93], v197 offset:58112
v_mfma_f32_16x16x32_bf16 v[188:191], v[124:127], a[156:159], v[188:191]
ds_read_b64_tr_b16 v[94:95], v197 offset:60160
s_waitcnt lgkmcnt(8)
v_mfma_f32_16x16x32_bf16 v[176:179], v[64:67], a[160:163], v[176:179]
ds_read_b64_tr_b16 v[96:97], v198 offset:32768
v_mfma_f32_16x16x32_bf16 v[176:179], v[68:71], a[168:171], v[176:179]
ds_read_b64_tr_b16 v[98:99], v198 offset:34816
v_mfma_f32_16x16x32_bf16 v[184:187], v[64:67], a[164:167], v[184:187]
ds_read_b64_tr_b16 v[100:101], v198 offset:40960
v_mfma_f32_16x16x32_bf16 v[184:187], v[68:71], a[172:175], v[184:187]
ds_read_b64_tr_b16 v[102:103], v198 offset:43008
v_mfma_f32_16x16x32_bf16 v[180:183], v[72:75], a[160:163], v[180:183]
ds_read_b64_tr_b16 v[104:105], v199 offset:32768
v_mfma_f32_16x16x32_bf16 v[180:183], v[76:79], a[168:171], v[180:183]
ds_read_b64_tr_b16 v[106:107], v199 offset:34816
v_mfma_f32_16x16x32_bf16 v[188:191], v[72:75], a[164:167], v[188:191]
ds_read_b64_tr_b16 v[108:109], v199 offset:40960
v_mfma_f32_16x16x32_bf16 v[188:191], v[76:79], a[172:175], v[188:191]
ds_read_b64_tr_b16 v[110:111], v199 offset:43008
s_waitcnt lgkmcnt(8)
v_mfma_f32_16x16x32_bf16 v[176:179], v[80:83], a[176:179], v[176:179]
ds_read_b64_tr_b16 v[112:113], v198 offset:49152
v_mfma_f32_16x16x32_bf16 v[176:179], v[84:87], a[184:187], v[176:179]
ds_read_b64_tr_b16 v[114:115], v198 offset:51200
v_mfma_f32_16x16x32_bf16 v[184:187], v[80:83], a[180:183], v[184:187]
ds_read_b64_tr_b16 v[116:117], v198 offset:57344
v_mfma_f32_16x16x32_bf16 v[184:187], v[84:87], a[188:191], v[184:187]
ds_read_b64_tr_b16 v[118:119], v198 offset:59392
v_mfma_f32_16x16x32_bf16 v[180:183], v[88:91], a[176:179], v[180:183]
ds_read_b64_tr_b16 v[120:121], v199 offset:49152
v_mfma_f32_16x16x32_bf16 v[180:183], v[92:95], a[184:187], v[180:183]
ds_read_b64_tr_b16 v[122:123], v199 offset:51200
v_cvt_pk_bf16_f32 v176, v176, v177
v_cvt_pk_bf16_f32 v177, v178, v179
v_mfma_f32_16x16x32_bf16 v[188:191], v[88:91], a[180:183], v[188:191]
ds_read_b64_tr_b16 v[124:125], v199 offset:57344
v_mfma_f32_16x16x32_bf16 v[188:191], v[92:95], a[188:191], v[188:191]
ds_read_b64_tr_b16 v[126:127], v199 offset:59392
s_waitcnt lgkmcnt(8)
v_mfma_f32_16x16x32_bf16 v[128:131], v[96:99], a[32:35], v[128:131]
ds_read_b64_tr_b16 v[64:65], v196 offset:0
v_mfma_f32_16x16x32_bf16 v[128:131], v[100:103], a[40:43], v[128:131]
ds_read_b64_tr_b16 v[66:67], v196 offset:2048
v_cvt_pk_bf16_f32 v178, v180, v181
v_cvt_pk_bf16_f32 v179, v182, v183
v_mfma_f32_16x16x32_bf16 v[136:139], v[96:99], a[36:39], v[136:139]
ds_read_b64_tr_b16 v[68:69], v196 offset:8192
global_store_dwordx4 v203, v[176:179], s[36:37] offset:-4096 sc1
v_mfma_f32_16x16x32_bf16 v[136:139], v[100:103], a[44:47], v[136:139]
ds_read_b64_tr_b16 v[70:71], v196 offset:10240
v_cvt_pk_bf16_f32 v180, v184, v185
v_cvt_pk_bf16_f32 v181, v186, v187
v_mfma_f32_16x16x32_bf16 v[132:135], v[104:107], a[32:35], v[132:135]
ds_read_b64_tr_b16 v[72:73], v197 offset:0
v_cvt_pk_bf16_f32 v182, v188, v189
v_cvt_pk_bf16_f32 v183, v190, v191
v_mfma_f32_16x16x32_bf16 v[132:135], v[108:111], a[40:43], v[132:135]
ds_read_b64_tr_b16 v[74:75], v197 offset:2048
global_store_dwordx4 v203, v[180:183], s[36:37] offset:-3072 sc1
v_mfma_f32_16x16x32_bf16 v[140:143], v[104:107], a[36:39], v[140:143]
ds_read_b64_tr_b16 v[76:77], v197 offset:8192
v_mfma_f32_16x16x32_bf16 v[140:143], v[108:111], a[44:47], v[140:143]
ds_read_b64_tr_b16 v[78:79], v197 offset:10240
s_waitcnt lgkmcnt(8)
v_mfma_f32_16x16x32_bf16 v[128:131], v[112:115], a[48:51], v[128:131]
ds_read_b64_tr_b16 v[80:81], v196 offset:16384
global_load_dwordx4 v[240:243], v200, s[36:37] offset:0 sc1
v_mfma_f32_16x16x32_bf16 v[128:131], v[116:119], a[56:59], v[128:131]
ds_read_b64_tr_b16 v[82:83], v196 offset:18432
v_mfma_f32_16x16x32_bf16 v[136:139], v[112:115], a[52:55], v[136:139]
ds_read_b64_tr_b16 v[84:85], v196 offset:24576
global_load_dwordx4 v[244:247], v200, s[36:37] offset:1024 sc1
v_mfma_f32_16x16x32_bf16 v[136:139], v[116:119], a[60:63], v[136:139]
ds_read_b64_tr_b16 v[86:87], v196 offset:26624
v_mfma_f32_16x16x32_bf16 v[132:135], v[120:123], a[48:51], v[132:135]
ds_read_b64_tr_b16 v[88:89], v197 offset:16384
global_load_dwordx4 v[224:227], v200, s[36:37] offset:-4096 sc1
v_mfma_f32_16x16x32_bf16 v[132:135], v[124:127], a[56:59], v[132:135]
ds_read_b64_tr_b16 v[90:91], v197 offset:18432
v_mfma_f32_16x16x32_bf16 v[140:143], v[120:123], a[52:55], v[140:143]
ds_read_b64_tr_b16 v[92:93], v197 offset:24576
global_load_dwordx4 v[228:231], v200, s[36:37] offset:-3072 sc1
v_mfma_f32_16x16x32_bf16 v[140:143], v[124:127], a[60:63], v[140:143]
ds_read_b64_tr_b16 v[94:95], v197 offset:26624
s_waitcnt lgkmcnt(8)
v_mfma_f32_16x16x32_bf16 v[128:131], v[64:67], a[128:131], v[128:131]
ds_read_b64_tr_b16 v[96:97], v196 offset:32768
global_load_dwordx4 v[232:235], v200, s[36:37] offset:-2048 sc1
v_mfma_f32_16x16x32_bf16 v[128:131], v[68:71], a[136:139], v[128:131]
ds_read_b64_tr_b16 v[98:99], v196 offset:34816
v_mfma_f32_16x16x32_bf16 v[136:139], v[64:67], a[132:135], v[136:139]
ds_read_b64_tr_b16 v[100:101], v196 offset:40960
global_load_dwordx4 v[236:239], v200, s[36:37] offset:-1024 sc1
v_mfma_f32_16x16x32_bf16 v[136:139], v[68:71], a[140:143], v[136:139]
ds_read_b64_tr_b16 v[102:103], v196 offset:43008
v_mfma_f32_16x16x32_bf16 v[132:135], v[72:75], a[128:131], v[132:135]
ds_read_b64_tr_b16 v[104:105], v197 offset:32768
v_mfma_f32_16x16x32_bf16 v[132:135], v[76:79], a[136:139], v[132:135]
ds_read_b64_tr_b16 v[106:107], v197 offset:34816
v_mfma_f32_16x16x32_bf16 v[140:143], v[72:75], a[132:135], v[140:143]
ds_read_b64_tr_b16 v[108:109], v197 offset:40960
v_mfma_f32_16x16x32_bf16 v[140:143], v[76:79], a[140:143], v[140:143]
ds_read_b64_tr_b16 v[110:111], v197 offset:43008
s_waitcnt lgkmcnt(8)
v_mfma_f32_16x16x32_bf16 v[128:131], v[80:83], a[144:147], v[128:131]
ds_read_b64_tr_b16 v[112:113], v196 offset:49152
v_mfma_f32_16x16x32_bf16 v[128:131], v[84:87], a[152:155], v[128:131]
ds_read_b64_tr_b16 v[114:115], v196 offset:51200
v_mfma_f32_16x16x32_bf16 v[136:139], v[80:83], a[148:151], v[136:139]
ds_read_b64_tr_b16 v[116:117], v196 offset:57344
v_mfma_f32_16x16x32_bf16 v[136:139], v[84:87], a[156:159], v[136:139]
ds_read_b64_tr_b16 v[118:119], v196 offset:59392
PAD18_%=:
s_cbranch_execz WRET_%=
v_mfma_f32_16x16x32_bf16 v[132:135], v[88:91], a[144:147], v[132:135]
ds_read_b64_tr_b16 v[120:121], v197 offset:49152
v_mfma_f32_16x16x32_bf16 v[132:135], v[92:95], a[152:155], v[132:135]
ds_read_b64_tr_b16 v[122:123], v197 offset:51200
v_mfma_f32_16x16x32_bf16 v[140:143], v[88:91], a[148:151], v[140:143]
ds_read_b64_tr_b16 v[124:125], v197 offset:57344
v_mfma_f32_16x16x32_bf16 v[140:143], v[92:95], a[156:159], v[140:143]
ds_read_b64_tr_b16 v[126:127], v197 offset:59392
s_waitcnt lgkmcnt(8)
v_mfma_f32_16x16x32_bf16 v[128:131], v[96:99], a[160:163], v[128:131]
v_mfma_f32_16x16x32_bf16 v[128:131], v[100:103], a[168:171], v[128:131]
v_mfma_f32_16x16x32_bf16 v[136:139], v[96:99], a[164:167], v[136:139]
v_mfma_f32_16x16x32_bf16 v[136:139], v[100:103], a[172:175], v[136:139]
v_mfma_f32_16x16x32_bf16 v[132:135], v[104:107], a[160:163], v[132:135]
v_mfma_f32_16x16x32_bf16 v[132:135], v[108:111], a[168:171], v[132:135]
v_mfma_f32_16x16x32_bf16 v[140:143], v[104:107], a[164:167], v[140:143]
v_mfma_f32_16x16x32_bf16 v[140:143], v[108:111], a[172:175], v[140:143]
s_waitcnt lgkmcnt(0)
v_mfma_f32_16x16x32_bf16 v[128:131], v[112:115], a[176:179], v[128:131]
v_mfma_f32_16x16x32_bf16 v[128:131], v[116:119], a[184:187], v[128:131]
v_mfma_f32_16x16x32_bf16 v[136:139], v[112:115], a[180:183], v[136:139]
v_mfma_f32_16x16x32_bf16 v[136:139], v[116:119], a[188:191], v[136:139]
v_mfma_f32_16x16x32_bf16 v[132:135], v[120:123], a[176:179], v[132:135]
v_mfma_f32_16x16x32_bf16 v[132:135], v[124:127], a[184:187], v[132:135]
v_mfma_f32_16x16x32_bf16 v[140:143], v[120:123], a[180:183], v[140:143]
v_mfma_f32_16x16x32_bf16 v[140:143], v[124:127], a[188:191], v[140:143]
PAD19_%=:
s_nop 0
P3FIN_%=:
s_waitcnt vmcnt(0)
s_nop 3
s_cmp_eq_u32 s31, 3
s_cbranch_scc1 XD3_%=
v_max3_u32 v220, v240, v241, v242
v_max3_u32 v221, v243, v244, v245
v_max3_u32 v220, v220, v246, v247
v_max_u32 v220, v220, v221
v_cmp_eq_u32 vcc, -1, v220
s_cbranch_vccnz ADSP_%=
ADOK_%=:
v_lshlrev_b32 v48, 16, v240
v_and_b32 v49, 0xffff0000, v240
v_lshlrev_b32 v50, 16, v241
v_and_b32 v51, 0xffff0000, v241
v_lshlrev_b32 v52, 16, v242
v_and_b32 v53, 0xffff0000, v242
v_lshlrev_b32 v54, 16, v243
v_and_b32 v55, 0xffff0000, v243
v_lshlrev_b32 v56, 16, v244
v_and_b32 v57, 0xffff0000, v244
v_lshlrev_b32 v58, 16, v245
v_and_b32 v59, 0xffff0000, v245
v_lshlrev_b32 v60, 16, v246
v_and_b32 v61, 0xffff0000, v246
v_lshlrev_b32 v62, 16, v247
v_and_b32 v63, 0xffff0000, v247
XD3R_%=:
v_mul_f32 v64, v207, v48
v_mul_f32 v65, v207, v49
v_mul_f32 v66, v207, v50
v_mul_f32 v67, v207, v51
v_mul_f32 v68, v207, v52
v_mul_f32 v69, v207, v53
v_mul_f32 v70, v207, v54
v_mul_f32 v71, v207, v55
v_mul_f32 v72, v207, v56
v_mul_f32 v73, v207, v57
v_mul_f32 v74, v207, v58
v_mul_f32 v75, v207, v59
v_mul_f32 v76, v207, v60
v_mul_f32 v77, v207, v61
v_mul_f32 v78, v207, v62
v_mul_f32 v79, v207, v63
v_exp_f32 v64, v64
v_exp_f32 v65, v65
v_exp_f32 v66, v66
v_exp_f32 v67, v67
v_exp_f32 v68, v68
v_exp_f32 v69, v69
v_exp_f32 v70, v70
v_exp_f32 v71, v71
v_exp_f32 v72, v72
v_exp_f32 v73, v73
v_exp_f32 v74, v74
v_exp_f32 v75, v75
v_exp_f32 v76, v76
v_exp_f32 v77, v77
v_exp_f32 v78, v78
v_exp_f32 v79, v79
v_add_f32 v64, 1.0, v64
v_add_f32 v65, 1.0, v65
v_add_f32 v66, 1.0, v66
v_add_f32 v67, 1.0, v67
v_add_f32 v68, 1.0, v68
v_add_f32 v69, 1.0, v69
v_add_f32 v70, 1.0, v70
v_add_f32 v71, 1.0, v71
v_add_f32 v72, 1.0, v72
v_add_f32 v73, 1.0, v73
v_add_f32 v74, 1.0, v74
v_add_f32 v75, 1.0, v75
v_add_f32 v76, 1.0, v76
v_add_f32 v77, 1.0, v77
v_add_f32 v78, 1.0, v78
v_add_f32 v79, 1.0, v79
v_rcp_f32 v64, v64
v_rcp_f32 v65, v65
v_rcp_f32 v66, v66
v_rcp_f32 v67, v67
v_rcp_f32 v68, v68
v_rcp_f32 v69, v69
v_rcp_f32 v70, v70
v_rcp_f32 v71, v71
v_rcp_f32 v72, v72
v_rcp_f32 v73, v73
v_rcp_f32 v74, v74
v_rcp_f32 v75, v75
v_rcp_f32 v76, v76
v_rcp_f32 v77, v77
v_rcp_f32 v78, v78
v_rcp_f32 v79, v79
v_pk_mul_f32 v[48:49], v[48:49], v[64:65]
v_pk_mul_f32 v[50:51], v[50:51], v[66:67]
v_pk_mul_f32 v[52:53], v[52:53], v[68:69]
v_pk_mul_f32 v[54:55], v[54:55], v[70:71]
v_pk_mul_f32 v[56:57], v[56:57], v[72:73]
v_pk_mul_f32 v[58:59], v[58:59], v[74:75]
v_pk_mul_f32 v[60:61], v[60:61], v[76:77]
v_pk_mul_f32 v[62:63], v[62:63], v[78:79]
s_cmp_eq_u32 s31, 3
s_cbranch_scc1 XS3_%=
XS3R_%=:
v_max3_u32 v220, v224, v225, v226
v_max3_u32 v221, v227, v228, v229
v_max3_u32 v220, v220, v230, v231
v_max_u32 v220, v220, v221
v_cmp_eq_u32 vcc, -1, v220
s_cbranch_vccnz A0SP_%=
A0OK_%=:
v_lshlrev_b32 v64, 16, v224
v_and_b32 v65, 0xffff0000, v224
v_lshlrev_b32 v66, 16, v225
v_and_b32 v67, 0xffff0000, v225
v_lshlrev_b32 v68, 16, v226
v_and_b32 v69, 0xffff0000, v226
v_lshlrev_b32 v70, 16, v227
v_and_b32 v71, 0xffff0000, v227
v_lshlrev_b32 v72, 16, v228
v_and_b32 v73, 0xffff0000, v228
v_lshlrev_b32 v74, 16, v229
v_and_b32 v75, 0xffff0000, v229
v_lshlrev_b32 v76, 16, v230
v_and_b32 v77, 0xffff0000, v230
v_lshlrev_b32 v78, 16, v231
v_and_b32 v79, 0xffff0000, v231
v_pk_add_f32 v[128:129], v[128:129], v[64:65]
v_pk_add_f32 v[130:131], v[130:131], v[66:67]
v_pk_add_f32 v[132:133], v[132:133], v[68:69]
v_pk_add_f32 v[134:135], v[134:135], v[70:71]
v_pk_add_f32 v[136:137], v[136:137], v[72:73]
v_pk_add_f32 v[138:139], v[138:139], v[74:75]
v_pk_add_f32 v[140:141], v[140:141], v[76:77]
v_pk_add_f32 v[142:143], v[142:143], v[78:79]
v_max3_u32 v220, v232, v233, v234
v_max3_u32 v221, v235, v236, v237
v_max3_u32 v220, v220, v238, v239
v_max_u32 v220, v220, v221
v_cmp_eq_u32 vcc, -1, v220
s_cbranch_vccnz A1SP_%=
A1OK_%=:
v_lshlrev_b32 v64, 16, v232
v_and_b32 v65, 0xffff0000, v232
v_lshlrev_b32 v66, 16, v233
v_and_b32 v67, 0xffff0000, v233
v_lshlrev_b32 v68, 16, v234
v_and_b32 v69, 0xffff0000, v234
v_lshlrev_b32 v70, 16, v235
v_and_b32 v71, 0xffff0000, v235
v_lshlrev_b32 v72, 16, v236
v_and_b32 v73, 0xffff0000, v236
v_lshlrev_b32 v74, 16, v237
v_and_b32 v75, 0xffff0000, v237
v_lshlrev_b32 v76, 16, v238
v_and_b32 v77, 0xffff0000, v238
v_lshlrev_b32 v78, 16, v239
v_and_b32 v79, 0xffff0000, v239
v_pk_add_f32 v[128:129], v[128:129], v[64:65]
v_pk_add_f32 v[130:131], v[130:131], v[66:67]
v_pk_add_f32 v[132:133], v[132:133], v[68:69]
v_pk_add_f32 v[134:135], v[134:135], v[70:71]
v_pk_add_f32 v[136:137], v[136:137], v[72:73]
v_pk_add_f32 v[138:139], v[138:139], v[74:75]
v_pk_add_f32 v[140:141], v[140:141], v[76:77]
v_pk_add_f32 v[142:143], v[142:143], v[78:79]
v_pk_fma_f32 v[32:33], v[128:129], v[48:49], v[32:33]
v_pk_fma_f32 v[34:35], v[130:131], v[50:51], v[34:35]
v_pk_fma_f32 v[36:37], v[132:133], v[52:53], v[36:37]
v_pk_fma_f32 v[38:39], v[134:135], v[54:55], v[38:39]
v_pk_fma_f32 v[40:41], v[136:137], v[56:57], v[40:41]
v_pk_fma_f32 v[42:43], v[138:139], v[58:59], v[42:43]
v_pk_fma_f32 v[44:45], v[140:141], v[60:61], v[44:45]
v_pk_fma_f32 v[46:47], v[142:143], v[62:63], v[46:47]
v_cvt_pk_bf16_f32 v208, v32, v33
v_cvt_pk_bf16_f32 v209, v34, v35
v_cvt_pk_bf16_f32 v210, v36, v37
v_cvt_pk_bf16_f32 v211, v38, v39
v_cvt_pk_bf16_f32 v212, v40, v41
v_cvt_pk_bf16_f32 v213, v42, v43
v_cvt_pk_bf16_f32 v214, v44, v45
v_cvt_pk_bf16_f32 v215, v46, v47
v_permlane16_swap_b32 v208, v210
v_permlane16_swap_b32 v209, v211
v_permlane16_swap_b32 v212, v214
v_permlane16_swap_b32 v213, v215
global_store_dwordx4 v21, v[208:211], s[24:25]
global_store_dwordx4 v21, v[212:215], s[8:9]
s_endpgm
PAD20_%=:
s_cbranch_execz WRET_%=
XF1SP_%=:
s_mov_b32 s91, 0x20000
XF1L_%=:
s_sleep 1
global_load_dword v28, v27, s[92:93] sc1
s_waitcnt vmcnt(0)
v_cmp_eq_u32 vcc, s98, v28
s_and_b32 s77, vcc_lo, s89
s_cmp_eq_u32 s77, s89
s_cbranch_scc1 XF1OK_%=
s_sub_u32 s91, s91, 1
s_cmp_lg_u32 s91, 0
s_cbranch_scc1 XF1L_%=
s_branch XF1OK_%=
XF3SP_%=:
s_mov_b32 s91, 0x20000
XF3L_%=:
s_sleep 1
global_load_dword v28, v27, s[92:93] sc1
s_waitcnt vmcnt(0)
v_cmp_eq_u32 vcc, s98, v28
s_and_b32 s77, vcc_lo, s89
s_cmp_eq_u32 s77, s89
s_cbranch_scc1 XF3OK_%=
s_sub_u32 s91, s91, 1
s_cmp_lg_u32 s91, 0
s_cbranch_scc1 XF3L_%=
s_branch XF3OK_%=
ADSP_%=:
s_mov_b32 s91, 0x20000
ADL_%=:
global_load_dwordx4 v[240:243], v200, s[36:37] offset:0 sc1
global_load_dwordx4 v[244:247], v200, s[36:37] offset:1024 sc1
global_load_dwordx4 v[224:227], v200, s[36:37] offset:-4096 sc1
global_load_dwordx4 v[228:231], v200, s[36:37] offset:-3072 sc1
global_load_dwordx4 v[232:235], v200, s[36:37] offset:-2048 sc1
global_load_dwordx4 v[236:239], v200, s[36:37] offset:-1024 sc1
s_waitcnt vmcnt(0)
v_max3_u32 v220, v240, v241, v242
v_max3_u32 v221, v243, v244, v245
v_max3_u32 v220, v220, v246, v247
v_max_u32 v220, v220, v221
v_cmp_eq_u32 vcc, -1, v220
s_cbranch_vccz ADOK_%=
s_sub_u32 s91, s91, 1
s_cmp_lg_u32 s91, 0
s_cbranch_scc1 ADL_%=
s_branch ADOK_%=
XD3_%=:
v_mov_b32 v48, v128
v_mov_b32 v49, v129
v_mov_b32 v50, v130
)"
R"(v_mov_b32 v51, v131
v_mov_b32 v52, v132
v_mov_b32 v53, v133
v_mov_b32 v54, v134
v_mov_b32 v55, v135
v_mov_b32 v56, v136
v_mov_b32 v57, v137
v_mov_b32 v58, v138
v_mov_b32 v59, v139
v_mov_b32 v60, v140
v_mov_b32 v61, v141
v_mov_b32 v62, v142
v_mov_b32 v63, v143
s_branch XD3R_%=
ASSP_%=:
s_mov_b32 s91, 0x20000
ASL_%=:
global_load_dwordx4 v[240:243], v200, s[36:37] offset:0 sc1
global_load_dwordx4 v[244:247], v200, s[36:37] offset:1024 sc1
global_load_dwordx4 v[224:227], v200, s[36:37] offset:-4096 sc1
global_load_dwordx4 v[228:231], v200, s[36:37] offset:-3072 sc1
global_load_dwordx4 v[232:235], v200, s[36:37] offset:-2048 sc1
global_load_dwordx4 v[236:239], v200, s[36:37] offset:-1024 sc1
s_waitcnt vmcnt(0)
v_max3_u32 v220, v240, v241, v242
v_max3_u32 v221, v243, v244, v245
v_max3_u32 v220, v220, v246, v247
v_max_u32 v220, v220, v221
v_cmp_eq_u32 vcc, -1, v220
s_cbranch_vccz ASOK_%=
s_sub_u32 s91, s91, 1
s_cmp_lg_u32 s91, 0
s_cbranch_scc1 ASL_%=
s_branch ASOK_%=
XS3_%=:
v_max3_u32 v220, v240, v241, v242
v_max3_u32 v221, v243, v244, v245
v_max3_u32 v220, v220, v246, v247
v_max_u32 v220, v220, v221
v_cmp_eq_u32 vcc, -1, v220
s_cbranch_vccnz ASSP_%=
ASOK_%=:
v_lshlrev_b32 v128, 16, v240
v_and_b32 v129, 0xffff0000, v240
v_lshlrev_b32 v130, 16, v241
v_and_b32 v131, 0xffff0000, v241
v_lshlrev_b32 v132, 16, v242
v_and_b32 v133, 0xffff0000, v242
v_lshlrev_b32 v134, 16, v243
v_and_b32 v135, 0xffff0000, v243
v_lshlrev_b32 v136, 16, v244
v_and_b32 v137, 0xffff0000, v244
v_lshlrev_b32 v138, 16, v245
v_and_b32 v139, 0xffff0000, v245
v_lshlrev_b32 v140, 16, v246
v_and_b32 v141, 0xffff0000, v246
v_lshlrev_b32 v142, 16, v247
v_and_b32 v143, 0xffff0000, v247
s_branch XS3R_%=
A0SP_%=:
s_mov_b32 s91, 0x20000
A0L_%=:
global_load_dwordx4 v[224:227], v200, s[36:37] offset:-4096 sc1
global_load_dwordx4 v[228:231], v200, s[36:37] offset:-3072 sc1
global_load_dwordx4 v[232:235], v200, s[36:37] offset:-2048 sc1
global_load_dwordx4 v[236:239], v200, s[36:37] offset:-1024 sc1
s_waitcnt vmcnt(0)
v_max3_u32 v220, v224, v225, v226
v_max3_u32 v221, v227, v228, v229
v_max3_u32 v220, v220, v230, v231
v_max_u32 v220, v220, v221
v_cmp_eq_u32 vcc, -1, v220
s_cbranch_vccz A0OK_%=
s_sub_u32 s91, s91, 1
s_cmp_lg_u32 s91, 0
s_cbranch_scc1 A0L_%=
s_branch A0OK_%=
A1SP_%=:
s_mov_b32 s91, 0x20000
A1L_%=:
global_load_dwordx4 v[232:235], v200, s[36:37] offset:-2048 sc1
global_load_dwordx4 v[236:239], v200, s[36:37] offset:-1024 sc1
s_waitcnt vmcnt(0)
v_max3_u32 v220, v232, v233, v234
v_max3_u32 v221, v235, v236, v237
v_max3_u32 v220, v220, v238, v239
v_max_u32 v220, v220, v221
v_cmp_eq_u32 vcc, -1, v220
s_cbranch_vccz A1OK_%=
s_sub_u32 s91, s91, 1
s_cmp_lg_u32 s91, 0
s_cbranch_scc1 A1L_%=
s_branch A1OK_%=
CHK2_%=:
s_mov_b32 s76, 2
s_branch CHKH_%=
CHK3_%=:
s_mov_b32 s76, 3
CHKH_%=:
s_waitcnt vmcnt(10)
v_cmp_eq_u32 vcc, s98, v193
s_and_b32 s77, vcc_lo, s89
s_cmp_lg_u32 s77, s89
s_cbranch_scc1 LTSP_%=
LTOK_%=:
s_cmp_eq_u32 s76, 2
s_cbranch_scc1 CHKR2_%=
s_branch CHKR3_%=
LTSP_%=:
s_mov_b32 s91, 0x20000
LTL_%=:
s_sleep 1
global_load_dword v193, v4, s[92:93] sc1
s_waitcnt vmcnt(0)
v_cmp_eq_u32 vcc, s98, v193
s_and_b32 s77, vcc_lo, s89
s_cmp_eq_u32 s77, s89
s_cbranch_scc1 LTOK_%=
s_sub_u32 s91, s91, 1
s_cmp_lg_u32 s91, 0
s_cbranch_scc1 LTL_%=
s_branch LTOK_%=
PAD21_%=:
s_cbranch_execz WRET_%=
ERL_%=:
s_mov_b32 s84, 1
s_waitcnt vmcnt(12)
v_cmp_eq_u32 vcc, s98, v193
s_andn2_b32 s77, s89, vcc_lo
s_cbranch_scc0 ERLR_%=
global_load_dword v193, v4, s[92:93] sc1
v_mov_b32 v24, 1
s_waitcnt vmcnt(0)
s_mov_b64 exec, 1
global_atomic_add v10, v24, s[16:17] sc1
s_mov_b64 exec, -1
s_mov_b32 s84, 2
s_mov_b32 s91, 0x20000
ERX_%=:
v_cmp_eq_u32 vcc, s98, v193
s_andn2_b32 s77, s89, vcc_lo
s_cbranch_scc0 ERLR_%=
s_sleep 1
global_load_dword v193, v4, s[92:93] sc1
s_waitcnt vmcnt(0)
s_sub_u32 s91, s91, 1
s_cmp_lg_u32 s91, 0
s_cbranch_scc1 ERX_%=
s_branch ERLR_%=
ERM_%=:
s_mov_b32 s14, 0x40000
buffer_load_dwordx4 a[132:135], v22, s[12:15], s82 offen offset:1024 sc1
buffer_load_dwordx4 a[136:139], v22, s[12:15], s82 offen offset:2048 sc1
buffer_load_dwordx4 a[140:143], v22, s[12:15], s82 offen offset:3072 sc1
buffer_load_dwordx4 a[128:131], v22, s[12:15], s82 offen offset:0 sc1
s_add_u32 s71, s82, 0x1000
buffer_load_dwordx4 a[148:151], v22, s[12:15], s71 offen offset:1024 sc1
buffer_load_dwordx4 a[152:155], v22, s[12:15], s71 offen offset:2048 sc1
buffer_load_dwordx4 a[156:159], v22, s[12:15], s71 offen offset:3072 sc1
buffer_load_dwordx4 a[144:147], v22, s[12:15], s71 offen offset:0 sc1
v_mov_b32 v24, 1
s_waitcnt vmcnt(0)
s_cmp_eq_u32 s84, 1
s_cselect_b64 exec, 1, 0
global_atomic_add v10, v24, s[16:17] sc1
s_mov_b64 exec, -1
buffer_load_dwordx4 a[164:167], v22, s[12:15], s83 offen offset:1024 sc1
buffer_load_dwordx4 a[168:171], v22, s[12:15], s83 offen offset:2048 sc1
buffer_load_dwordx4 a[172:175], v22, s[12:15], s83 offen offset:3072 sc1
buffer_load_dwordx4 a[160:163], v22, s[12:15], s83 offen offset:0 sc1
s_add_u32 s71, s83, 0x1000
buffer_load_dwordx4 a[180:183], v22, s[12:15], s71 offen offset:1024 sc1
buffer_load_dwordx4 a[184:187], v22, s[12:15], s71 offen offset:2048 sc1
buffer_load_dwordx4 a[188:191], v22, s[12:15], s71 offen offset:3072 sc1
buffer_load_dwordx4 a[176:179], v22, s[12:15], s71 offen offset:0 sc1
s_branch ERMR_%=
PAD22_%=:
s_cbranch_execz WRET_%=
END1_%=:
s_cmp_eq_u32 s54, s74
s_cbranch_scc1 EPI1_%=
ENE1_%=:
v_mov_b32 v24, 1
s_waitcnt vmcnt(0)
s_cmp_eq_u32 s54, s73
s_cselect_b64 exec, 1, 0
global_atomic_add v10, v24, s[66:67] sc1
s_cmp_eq_u32 s54, s79
s_cselect_b64 exec, 1, 0
global_atomic_add v10, v24, s[18:19] sc1
s_mov_b64 exec, -1
s_cmp_lt_u32 s54, 15
s_cbranch_scc1 LOOPE_%=
s_branch ENDR1_%=
END2_%=:
s_cmp_eq_u32 s54, s74
s_cbranch_scc1 EPI2_%=
ENE2_%=:
v_mov_b32 v24, 1
s_waitcnt vmcnt(0)
s_cmp_eq_u32 s54, s73
s_cselect_b64 exec, 1, 0
global_atomic_add v10, v24, s[66:67] sc1
s_cmp_eq_u32 s54, s79
s_cselect_b64 exec, 1, 0
global_atomic_add v10, v24, s[18:19] sc1
s_mov_b64 exec, -1
s_branch ENDR2_%=
END4_%=:
s_branch EPI2_%=
SIG4_%=:
v_mov_b32 v24, 1
s_waitcnt vmcnt(0)
s_mov_b64 exec, 1
global_atomic_add v10, v24, s[66:67] sc1
s_mov_b64 exec, -1
s_branch SIGR4_%=
EPX4_%=:
v_mov_b32 v5, 1
s_waitcnt vmcnt(0)
s_mov_b64 exec, 1
global_atomic_add v10, v5, s[66:67] sc1
s_mov_b64 exec, -1
s_branch EPXR4_%=
PAD23_%=:
s_cbranch_execz WRET_%=
MASK5_%=:
v_add_u32 v24, 0x20000, v3
v_xor_b32 v25, 0x2000, v24
ds_read_b128 a[128:131], v24 offset:0
ds_read_b128 a[160:163], v25 offset:0
ds_read_b128 a[132:135], v24 offset:1024
ds_read_b128 a[164:167], v25 offset:1024
ds_read_b128 a[136:139], v24 offset:2048
ds_read_b128 a[168:171], v25 offset:2048
ds_read_b128 a[140:143], v24 offset:3072
ds_read_b128 a[172:175], v25 offset:3072
ds_read_b128 a[144:147], v24 offset:4096
ds_read_b128 a[176:179], v25 offset:4096
ds_read_b128 a[148:151], v24 offset:5120
ds_read_b128 a[180:183], v25 offset:5120
ds_read_b128 a[152:155], v24 offset:6144
ds_read_b128 a[184:187], v25 offset:6144
ds_read_b128 a[156:159], v24 offset:7168
ds_read_b128 a[188:191], v25 offset:7168
s_branch MASKR5_%=
MASK2_%=:
v_add_u32 v24, 0x20000, v3
v_xor_b32 v25, 0x2000, v24
ds_read_b128 a[128:131], v24 offset:0
ds_read_b128 a[160:163], v25 offset:0
ds_read_b128 a[132:135], v24 offset:1024
ds_read_b128 a[164:167], v25 offset:1024
ds_read_b128 a[136:139], v24 offset:2048
ds_read_b128 a[168:171], v25 offset:2048
ds_read_b128 a[140:143], v24 offset:3072
ds_read_b128 a[172:175], v25 offset:3072
ds_read_b128 a[144:147], v24 offset:4096
ds_read_b128 a[176:179], v25 offset:4096
ds_read_b128 a[148:151], v24 offset:5120
ds_read_b128 a[180:183], v25 offset:5120
ds_read_b128 a[152:155], v24 offset:6144
ds_read_b128 a[184:187], v25 offset:6144
ds_read_b128 a[156:159], v24 offset:7168
ds_read_b128 a[188:191], v25 offset:7168
s_branch MASKR2_%=
MASK1_%=:
v_add_u32 v24, 0x20000, v3
v_xor_b32 v25, 0x2000, v24
ds_read_b128 a[128:131], v24 offset:0
ds_read_b128 a[160:163], v25 offset:0
ds_read_b128 a[132:135], v24 offset:1024
ds_read_b128 a[164:167], v25 offset:1024
ds_read_b128 a[136:139], v24 offset:2048
ds_read_b128 a[168:171], v25 offset:2048
ds_read_b128 a[140:143], v24 offset:3072
ds_read_b128 a[172:175], v25 offset:3072
ds_read_b128 a[144:147], v24 offset:4096
ds_read_b128 a[176:179], v25 offset:4096
ds_read_b128 a[148:151], v24 offset:5120
ds_read_b128 a[180:183], v25 offset:5120
ds_read_b128 a[152:155], v24 offset:6144
ds_read_b128 a[184:187], v25 offset:6144
ds_read_b128 a[156:159], v24 offset:7168
ds_read_b128 a[188:191], v25 offset:7168
s_branch MASKR1_%=
MASK0_%=:
v_add_u32 v24, 0x20000, v3
v_xor_b32 v25, 0x2000, v24
ds_read_b128 a[128:131], v24 offset:0
ds_read_b128 a[160:163], v25 offset:0
ds_read_b128 a[132:135], v24 offset:1024
ds_read_b128 a[164:167], v25 offset:1024
ds_read_b128 a[136:139], v24 offset:2048
ds_read_b128 a[168:171], v25 offset:2048
ds_read_b128 a[140:143], v24 offset:3072
ds_read_b128 a[172:175], v25 offset:3072
ds_read_b128 a[144:147], v24 offset:4096
ds_read_b128 a[176:179], v25 offset:4096
ds_read_b128 a[148:151], v24 offset:5120
ds_read_b128 a[180:183], v25 offset:5120
ds_read_b128 a[152:155], v24 offset:6144
ds_read_b128 a[184:187], v25 offset:6144
ds_read_b128 a[156:159], v24 offset:7168
ds_read_b128 a[188:191], v25 offset:7168
s_branch MASKR0_%=
PAD24_%=:
s_cbranch_execz WRET_%=
EPI1_%=:
s_cmp_gt_u32 s55, 11
s_cbranch_scc1 ENF1_%=
v_lshlrev_b32 v223, 3, v22
global_load_dword v223, v223, s[34:35]
ENF1_%=:
v_add_u32 v28, 0x10000, v3
v_xor_b32 v30, 0x2000, v28
v_add_u32 v29, 0x20000, v3
v_mov_b32 v212, 0
v_mov_b32 v214, 0
v_mov_b32 v216, 0
v_mov_b32 v218, 0
ds_write_b128 v30, a[64:67] offset:32768
v_accvgpr_read_b32 v32, a0
v_accvgpr_read_b32 v33, a1
v_accvgpr_read_b32 v34, a2
v_accvgpr_read_b32 v35, a3
ds_write_b128 v30, a[68:71] offset:33792
v_accvgpr_read_b32 v217, a232
v_accvgpr_read_b32 v210, a233
v_accvgpr_read_b32 v219, a236
v_accvgpr_read_b32 v211, a237
v_add_f32 v217, v217, v210
v_add_f32 v219, v219, v211
v_accvgpr_read_b32 v213, a224
v_accvgpr_read_b32 v208, a225
v_accvgpr_read_b32 v215, a228
v_accvgpr_read_b32 v209, a229
v_add_f32 v213, v213, v208
v_add_f32 v215, v215, v209
ds_write_b128 v30, v[216:219]
v_accvgpr_read_b32 v36, a4
v_accvgpr_read_b32 v37, a5
v_accvgpr_read_b32 v38, a6
v_accvgpr_read_b32 v39, a7
ds_write_b128 v30, a[72:75] offset:34816
v_accvgpr_read_b32 v40, a8
v_accvgpr_read_b32 v41, a9
v_accvgpr_read_b32 v42, a10
v_accvgpr_read_b32 v43, a11
ds_write_b128 v30, a[76:79] offset:35840
v_accvgpr_read_b32 v44, a12
v_accvgpr_read_b32 v45, a13
v_accvgpr_read_b32 v46, a14
v_accvgpr_read_b32 v47, a15
v_mfma_f32_32x32x16_bf16 a[0:15], v[10:13], v[10:13], 0
ds_write_b128 v30, a[80:83] offset:36864
v_accvgpr_read_b32 v48, a16
v_accvgpr_read_b32 v49, a17
v_accvgpr_read_b32 v50, a18
v_accvgpr_read_b32 v51, a19
ds_write_b128 v30, a[84:87] offset:37888
v_accvgpr_read_b32 v52, a20
v_accvgpr_read_b32 v53, a21
v_accvgpr_read_b32 v54, a22
v_accvgpr_read_b32 v55, a23
ds_write_b128 v30, a[88:91] offset:38912
v_accvgpr_read_b32 v56, a24
v_accvgpr_read_b32 v57, a25
v_accvgpr_read_b32 v58, a26
v_accvgpr_read_b32 v59, a27
ds_write_b128 v30, a[92:95] offset:39936
v_accvgpr_read_b32 v60, a28
v_accvgpr_read_b32 v61, a29
v_accvgpr_read_b32 v62, a30
v_accvgpr_read_b32 v63, a31
v_mfma_f32_32x32x16_bf16 a[16:31], v[10:13], v[10:13], 0
ds_write_b128 v23, a[96:99] offset:0
v_accvgpr_read_b32 v224, a32
v_accvgpr_read_b32 v225, a33
v_accvgpr_read_b32 v226, a34
v_accvgpr_read_b32 v227, a35
ds_write_b128 v23, a[100:103] offset:1024
v_accvgpr_read_b32 v228, a36
v_accvgpr_read_b32 v229, a37
v_accvgpr_read_b32 v230, a38
v_accvgpr_read_b32 v231, a39
ds_write_b128 v23, a[104:107] offset:2048
v_accvgpr_read_b32 v232, a40
v_accvgpr_read_b32 v233, a41
v_accvgpr_read_b32 v234, a42
v_accvgpr_read_b32 v235, a43
ds_write_b128 v23, a[108:111] offset:3072
v_accvgpr_read_b32 v236, a44
v_accvgpr_read_b32 v237, a45
v_accvgpr_read_b32 v238, a46
v_accvgpr_read_b32 v239, a47
v_mfma_f32_32x32x16_bf16 a[32:47], v[10:13], v[10:13], 0
ds_write_b128 v23, a[112:115] offset:4096
v_accvgpr_read_b32 v240, a48
v_accvgpr_read_b32 v241, a49
)"
R"(v_accvgpr_read_b32 v242, a50
v_accvgpr_read_b32 v243, a51
ds_write_b128 v23, a[116:119] offset:5120
v_accvgpr_read_b32 v244, a52
v_accvgpr_read_b32 v245, a53
v_accvgpr_read_b32 v246, a54
v_accvgpr_read_b32 v247, a55
ds_write_b128 v23, a[120:123] offset:6144
v_accvgpr_read_b32 v248, a56
v_accvgpr_read_b32 v249, a57
v_accvgpr_read_b32 v250, a58
v_accvgpr_read_b32 v251, a59
ds_write_b128 v23, a[124:127] offset:7168
v_accvgpr_read_b32 v252, a60
v_accvgpr_read_b32 v253, a61
v_accvgpr_read_b32 v254, a62
v_accvgpr_read_b32 v255, a63
s_waitcnt lgkmcnt(0)
s_barrier
ds_read_b128 v[6:9], v28
ds_read_b128 v[64:67], v28 offset:32768
ds_read_b128 v[68:71], v28 offset:33792
v_mfma_f32_32x32x16_bf16 a[48:63], v[10:13], v[10:13], 0
ds_read_b128 v[72:75], v28 offset:34816
ds_read_b128 v[76:79], v28 offset:35840
ds_read_b128 v[80:83], v28 offset:36864
ds_read_b128 v[84:87], v28 offset:37888
v_mfma_f32_32x32x16_bf16 a[224:239], v[10:13], v[10:13], 0
ds_read_b128 v[88:91], v28 offset:38912
ds_read_b128 v[92:95], v28 offset:39936
ds_read_b128 v[96:99], v29 offset:0
ds_read_b128 v[100:103], v29 offset:1024
v_mfma_f32_32x32x16_bf16 a[64:79], v[10:13], v[10:13], 0
ds_read_b128 v[104:107], v29 offset:2048
ds_read_b128 v[108:111], v29 offset:3072
ds_read_b128 v[112:115], v29 offset:4096
ds_read_b128 v[116:119], v29 offset:5120
PAD25_%=:
s_cbranch_execz WRET_%=
v_mfma_f32_32x32x16_bf16 a[80:95], v[10:13], v[10:13], 0
ds_read_b128 v[120:123], v29 offset:6144
ds_read_b128 v[124:127], v29 offset:7168
s_waitcnt lgkmcnt(14)
v_max_f32 v24, v6, v212
v_max_f32 v25, v8, v214
v_sub_f32 v208, v212, v24
v_sub_f32 v209, v6, v24
v_sub_f32 v210, v214, v25
v_sub_f32 v211, v8, v25
v_mfma_f32_32x32x16_bf16 a[96:111], v[10:13], v[10:13], 0
v_exp_f32 v208, v208
v_exp_f32 v209, v209
v_exp_f32 v210, v210
v_exp_f32 v211, v211
v_mul_f32 v24, v208, v213
v_mul_f32 v25, v210, v215
v_fmac_f32 v24, v209, v7
v_fmac_f32 v25, v211, v9
v_mfma_f32_32x32x16_bf16 a[112:127], v[10:13], v[10:13], 0
v_rcp_f32 v24, v24
v_rcp_f32 v25, v25
v_mul_f32 v208, v208, v24
v_mul_f32 v209, v209, v24
v_mul_f32 v210, v210, v25
v_mul_f32 v211, v211, v25
v_max3_f32 v5, v213, v215, v7
v_max3_f32 v5, v5, v9, v24
v_max_f32 v5, v5, v25
v_cmp_lt_f32 vcc, s76, v5
s_cbranch_vccnz LF1_%=
LF1R_%=:
v_pk_mul_f32 v[32:33], v[32:33], v[208:209] op_sel_hi:[1,0]
v_pk_mul_f32 v[34:35], v[34:35], v[208:209] op_sel_hi:[1,0]
v_pk_mul_f32 v[36:37], v[36:37], v[208:209] op_sel_hi:[1,0]
v_pk_mul_f32 v[38:39], v[38:39], v[208:209] op_sel_hi:[1,0]
v_pk_fma_f32 v[32:33], v[64:65], v[208:209], v[32:33] op_sel:[0,1,0] op_sel_hi:[1,1,1]
v_pk_fma_f32 v[34:35], v[66:67], v[208:209], v[34:35] op_sel:[0,1,0] op_sel_hi:[1,1,1]
v_pk_fma_f32 v[36:37], v[68:69], v[208:209], v[36:37] op_sel:[0,1,0] op_sel_hi:[1,1,1]
v_pk_fma_f32 v[38:39], v[70:71], v[208:209], v[38:39] op_sel:[0,1,0] op_sel_hi:[1,1,1]
v_cvt_pk_bf16_f32 v32, v32, v33
v_cvt_pk_bf16_f32 v33, v34, v35
v_cvt_pk_bf16_f32 v34, v36, v37
v_cvt_pk_bf16_f32 v35, v38, v39
global_store_dwordx4 v22, v[32:35], s[50:51] offset:-4096 sc1
v_pk_mul_f32 v[40:41], v[40:41], v[208:209] op_sel_hi:[1,0]
v_pk_mul_f32 v[42:43], v[42:43], v[208:209] op_sel_hi:[1,0]
v_pk_mul_f32 v[44:45], v[44:45], v[208:209] op_sel_hi:[1,0]
v_pk_mul_f32 v[46:47], v[46:47], v[208:209] op_sel_hi:[1,0]
s_waitcnt lgkmcnt(13)
v_pk_fma_f32 v[40:41], v[72:73], v[208:209], v[40:41] op_sel:[0,1,0] op_sel_hi:[1,1,1]
v_pk_fma_f32 v[42:43], v[74:75], v[208:209], v[42:43] op_sel:[0,1,0] op_sel_hi:[1,1,1]
s_waitcnt lgkmcnt(12)
v_pk_fma_f32 v[44:45], v[76:77], v[208:209], v[44:45] op_sel:[0,1,0] op_sel_hi:[1,1,1]
v_pk_fma_f32 v[46:47], v[78:79], v[208:209], v[46:47] op_sel:[0,1,0] op_sel_hi:[1,1,1]
v_cvt_pk_bf16_f32 v40, v40, v41
v_cvt_pk_bf16_f32 v41, v42, v43
v_cvt_pk_bf16_f32 v42, v44, v45
v_cvt_pk_bf16_f32 v43, v46, v47
global_store_dwordx4 v22, v[40:43], s[50:51] offset:-2048 sc1
v_pk_mul_f32 v[48:49], v[48:49], v[208:209] op_sel_hi:[1,0]
v_pk_mul_f32 v[50:51], v[50:51], v[208:209] op_sel_hi:[1,0]
v_pk_mul_f32 v[52:53], v[52:53], v[208:209] op_sel_hi:[1,0]
v_pk_mul_f32 v[54:55], v[54:55], v[208:209] op_sel_hi:[1,0]
s_waitcnt lgkmcnt(11)
v_pk_fma_f32 v[48:49], v[80:81], v[208:209], v[48:49] op_sel:[0,1,0] op_sel_hi:[1,1,1]
v_pk_fma_f32 v[50:51], v[82:83], v[208:209], v[50:51] op_sel:[0,1,0] op_sel_hi:[1,1,1]
s_waitcnt lgkmcnt(10)
v_pk_fma_f32 v[52:53], v[84:85], v[208:209], v[52:53] op_sel:[0,1,0] op_sel_hi:[1,1,1]
v_pk_fma_f32 v[54:55], v[86:87], v[208:209], v[54:55] op_sel:[0,1,0] op_sel_hi:[1,1,1]
v_cvt_pk_bf16_f32 v48, v48, v49
v_cvt_pk_bf16_f32 v49, v50, v51
v_cvt_pk_bf16_f32 v50, v52, v53
v_cvt_pk_bf16_f32 v51, v54, v55
global_store_dwordx4 v22, v[48:51], s[50:51] offset:0 sc1
v_pk_mul_f32 v[56:57], v[56:57], v[208:209] op_sel_hi:[1,0]
v_pk_mul_f32 v[58:59], v[58:59], v[208:209] op_sel_hi:[1,0]
v_pk_mul_f32 v[60:61], v[60:61], v[208:209] op_sel_hi:[1,0]
v_pk_mul_f32 v[62:63], v[62:63], v[208:209] op_sel_hi:[1,0]
s_waitcnt lgkmcnt(9)
v_pk_fma_f32 v[56:57], v[88:89], v[208:209], v[56:57] op_sel:[0,1,0] op_sel_hi:[1,1,1]
v_pk_fma_f32 v[58:59], v[90:91], v[208:209], v[58:59] op_sel:[0,1,0] op_sel_hi:[1,1,1]
s_waitcnt lgkmcnt(8)
v_pk_fma_f32 v[60:61], v[92:93], v[208:209], v[60:61] op_sel:[0,1,0] op_sel_hi:[1,1,1]
v_pk_fma_f32 v[62:63], v[94:95], v[208:209], v[62:63] op_sel:[0,1,0] op_sel_hi:[1,1,1]
v_cvt_pk_bf16_f32 v56, v56, v57
v_cvt_pk_bf16_f32 v57, v58, v59
v_cvt_pk_bf16_f32 v58, v60, v61
v_cvt_pk_bf16_f32 v59, v62, v63
global_store_dwordx4 v22, v[56:59], s[50:51] offset:2048 sc1
v_pk_mul_f32 v[224:225], v[224:225], v[210:211] op_sel_hi:[1,0]
v_pk_mul_f32 v[226:227], v[226:227], v[210:211] op_sel_hi:[1,0]
v_pk_mul_f32 v[228:229], v[228:229], v[210:211] op_sel_hi:[1,0]
v_pk_mul_f32 v[230:231], v[230:231], v[210:211] op_sel_hi:[1,0]
s_waitcnt lgkmcnt(7)
v_pk_fma_f32 v[224:225], v[96:97], v[210:211], v[224:225] op_sel:[0,1,0] op_sel_hi:[1,1,1]
v_pk_fma_f32 v[226:227], v[98:99], v[210:211], v[226:227] op_sel:[0,1,0] op_sel_hi:[1,1,1]
s_waitcnt lgkmcnt(6)
v_pk_fma_f32 v[228:229], v[100:101], v[210:211], v[228:229] op_sel:[0,1,0] op_sel_hi:[1,1,1]
v_pk_fma_f32 v[230:231], v[102:103], v[210:211], v[230:231] op_sel:[0,1,0] op_sel_hi:[1,1,1]
v_cvt_pk_bf16_f32 v224, v224, v225
v_cvt_pk_bf16_f32 v225, v226, v227
v_cvt_pk_bf16_f32 v226, v228, v229
v_cvt_pk_bf16_f32 v227, v230, v231
global_store_dwordx4 v22, v[224:227], s[50:51] offset:-3072 sc1
v_pk_mul_f32 v[232:233], v[232:233], v[210:211] op_sel_hi:[1,0]
v_pk_mul_f32 v[234:235], v[234:235], v[210:211] op_sel_hi:[1,0]
v_pk_mul_f32 v[236:237], v[236:237], v[210:211] op_sel_hi:[1,0]
v_pk_mul_f32 v[238:239], v[238:239], v[210:211] op_sel_hi:[1,0]
s_waitcnt lgkmcnt(5)
v_pk_fma_f32 v[232:233], v[104:105], v[210:211], v[232:233] op_sel:[0,1,0] op_sel_hi:[1,1,1]
v_pk_fma_f32 v[234:235], v[106:107], v[210:211], v[234:235] op_sel:[0,1,0] op_sel_hi:[1,1,1]
s_waitcnt lgkmcnt(4)
v_pk_fma_f32 v[236:237], v[108:109], v[210:211], v[236:237] op_sel:[0,1,0] op_sel_hi:[1,1,1]
v_pk_fma_f32 v[238:239], v[110:111], v[210:211], v[238:239] op_sel:[0,1,0] op_sel_hi:[1,1,1]
v_cvt_pk_bf16_f32 v232, v232, v233
v_cvt_pk_bf16_f32 v233, v234, v235
v_cvt_pk_bf16_f32 v234, v236, v237
v_cvt_pk_bf16_f32 v235, v238, v239
global_store_dwordx4 v22, v[232:235], s[50:51] offset:-1024 sc1
v_pk_mul_f32 v[240:241], v[240:241], v[210:211] op_sel_hi:[1,0]
v_pk_mul_f32 v[242:243], v[242:243], v[210:211] op_sel_hi:[1,0]
v_pk_mul_f32 v[244:245], v[244:245], v[210:211] op_sel_hi:[1,0]
v_pk_mul_f32 v[246:247], v[246:247], v[210:211] op_sel_hi:[1,0]
s_waitcnt lgkmcnt(3)
v_pk_fma_f32 v[240:241], v[112:113], v[210:211], v[240:241] op_sel:[0,1,0] op_sel_hi:[1,1,1]
v_pk_fma_f32 v[242:243], v[114:115], v[210:211], v[242:243] op_sel:[0,1,0] op_sel_hi:[1,1,1]
s_waitcnt lgkmcnt(2)
v_pk_fma_f32 v[244:245], v[116:117], v[210:211], v[244:245] op_sel:[0,1,0] op_sel_hi:[1,1,1]
v_pk_fma_f32 v[246:247], v[118:119], v[210:211], v[246:247] op_sel:[0,1,0] op_sel_hi:[1,1,1]
v_cvt_pk_bf16_f32 v240, v240, v241
v_cvt_pk_bf16_f32 v241, v242, v243
v_cvt_pk_bf16_f32 v242, v244, v245
v_cvt_pk_bf16_f32 v243, v246, v247
global_store_dwordx4 v22, v[240:243], s[50:51] offset:1024 sc1
v_pk_mul_f32 v[248:249], v[248:249], v[210:211] op_sel_hi:[1,0]
v_pk_mul_f32 v[250:251], v[250:251], v[210:211] op_sel_hi:[1,0]
v_pk_mul_f32 v[252:253], v[252:253], v[210:211] op_sel_hi:[1,0]
v_pk_mul_f32 v[254:255], v[254:255], v[210:211] op_sel_hi:[1,0]
s_waitcnt lgkmcnt(1)
v_pk_fma_f32 v[248:249], v[120:121], v[210:211], v[248:249] op_sel:[0,1,0] op_sel_hi:[1,1,1]
v_pk_fma_f32 v[250:251], v[122:123], v[210:211], v[250:251] op_sel:[0,1,0] op_sel_hi:[1,1,1]
s_waitcnt lgkmcnt(0)
v_pk_fma_f32 v[252:253], v[124:125], v[210:211], v[252:253] op_sel:[0,1,0] op_sel_hi:[1,1,1]
v_pk_fma_f32 v[254:255], v[126:127], v[210:211], v[254:255] op_sel:[0,1,0] op_sel_hi:[1,1,1]
v_cvt_pk_bf16_f32 v248, v248, v249
v_cvt_pk_bf16_f32 v249, v250, v251
v_cvt_pk_bf16_f32 v250, v252, v253
v_cvt_pk_bf16_f32 v251, v254, v255
global_store_dwordx4 v22, v[248:251], s[50:51] offset:3072 sc1
s_cmp_lt_u32 s54, 15
s_cbranch_scc1 LOOPE_%=
s_branch ENDR1_%=
PAD26_%=:
s_cbranch_execz WRET_%=
EPI2_%=:
s_cmp_gt_u32 s55, 11
s_cbranch_scc1 ENF2_%=
v_lshlrev_b32 v223, 3, v22
global_load_dword v223, v223, s[34:35]
ENF2_%=:
v_add_u32 v28, 0x0, v3
v_xor_b32 v30, 0x2000, v28
v_add_u32 v29, 0x20000, v3
v_mov_b32 v212, 0
v_mov_b32 v214, 0
v_mov_b32 v216, 0
v_mov_b32 v218, 0
ds_write_b128 v30, a[64:67] offset:32768
v_accvgpr_read_b32 v32, a0
v_accvgpr_read_b32 v33, a1
v_accvgpr_read_b32 v34, a2
v_accvgpr_read_b32 v35, a3
ds_write_b128 v30, a[68:71] offset:33792
v_accvgpr_read_b32 v217, a232
v_accvgpr_read_b32 v210, a233
v_accvgpr_read_b32 v219, a236
v_accvgpr_read_b32 v211, a237
v_add_f32 v217, v217, v210
v_add_f32 v219, v219, v211
v_accvgpr_read_b32 v213, a224
v_accvgpr_read_b32 v208, a225
v_accvgpr_read_b32 v215, a228
v_accvgpr_read_b32 v209, a229
v_add_f32 v213, v213, v208
v_add_f32 v215, v215, v209
ds_write_b128 v30, v[216:219]
v_accvgpr_read_b32 v36, a4
v_accvgpr_read_b32 v37, a5
v_accvgpr_read_b32 v38, a6
v_accvgpr_read_b32 v39, a7
ds_write_b128 v30, a[72:75] offset:34816
v_accvgpr_read_b32 v40, a8
v_accvgpr_read_b32 v41, a9
v_accvgpr_read_b32 v42, a10
v_accvgpr_read_b32 v43, a11
ds_write_b128 v30, a[76:79] offset:35840
v_accvgpr_read_b32 v44, a12
v_accvgpr_read_b32 v45, a13
v_accvgpr_read_b32 v46, a14
v_accvgpr_read_b32 v47, a15
v_mfma_f32_32x32x16_bf16 a[0:15], v[10:13], v[10:13], 0
ds_write_b128 v30, a[80:83] offset:36864
v_accvgpr_read_b32 v48, a16
v_accvgpr_read_b32 v49, a17
v_accvgpr_read_b32 v50, a18
v_accvgpr_read_b32 v51, a19
ds_write_b128 v30, a[84:87] offset:37888
v_accvgpr_read_b32 v52, a20
v_accvgpr_read_b32 v53, a21
v_accvgpr_read_b32 v54, a22
v_accvgpr_read_b32 v55, a23
ds_write_b128 v30, a[88:91] offset:38912
v_accvgpr_read_b32 v56, a24
v_accvgpr_read_b32 v57, a25
v_accvgpr_read_b32 v58, a26
v_accvgpr_read_b32 v59, a27
ds_write_b128 v30, a[92:95] offset:39936
v_accvgpr_read_b32 v60, a28
v_accvgpr_read_b32 v61, a29
v_accvgpr_read_b32 v62, a30
v_accvgpr_read_b32 v63, a31
v_mfma_f32_32x32x16_bf16 a[16:31], v[10:13], v[10:13], 0
ds_write_b128 v23, a[96:99] offset:0
v_accvgpr_read_b32 v224, a32
v_accvgpr_read_b32 v225, a33
v_accvgpr_read_b32 v226, a34
v_accvgpr_read_b32 v227, a35
ds_write_b128 v23, a[100:103] offset:1024
v_accvgpr_read_b32 v228, a36
v_accvgpr_read_b32 v229, a37
v_accvgpr_read_b32 v230, a38
v_accvgpr_read_b32 v231, a39
ds_write_b128 v23, a[104:107] offset:2048
v_accvgpr_read_b32 v232, a40
v_accvgpr_read_b32 v233, a41
v_accvgpr_read_b32 v234, a42
v_accvgpr_read_b32 v235, a43
ds_write_b128 v23, a[108:111] offset:3072
v_accvgpr_read_b32 v236, a44
v_accvgpr_read_b32 v237, a45
v_accvgpr_read_b32 v238, a46
v_accvgpr_read_b32 v239, a47
v_mfma_f32_32x32x16_bf16 a[32:47], v[10:13], v[10:13], 0
ds_write_b128 v23, a[112:115] offset:4096
v_accvgpr_read_b32 v240, a48
v_accvgpr_read_b32 v241, a49
v_accvgpr_read_b32 v242, a50
v_accvgpr_read_b32 v243, a51
ds_write_b128 v23, a[116:119] offset:5120
v_accvgpr_read_b32 v244, a52
v_accvgpr_read_b32 v245, a53
v_accvgpr_read_b32 v246, a54
v_accvgpr_read_b32 v247, a55
ds_write_b128 v23, a[120:123] offset:6144
v_accvgpr_read_b32 v248, a56
v_accvgpr_read_b32 v249, a57
v_accvgpr_read_b32 v250, a58
v_accvgpr_read_b32 v251, a59
ds_write_b128 v23, a[124:127] offset:7168
v_accvgpr_read_b32 v252, a60
v_accvgpr_read_b32 v253, a61
v_accvgpr_read_b32 v254, a62
v_accvgpr_read_b32 v255, a63
s_waitcnt lgkmcnt(0)
s_barrier
ds_read_b128 v[6:9], v28
ds_read_b128 v[128:131], v28 offset:32768
ds_read_b128 v[132:135], v28 offset:33792
v_mfma_f32_32x32x16_bf16 a[48:63], v[10:13], v[10:13], 0
ds_read_b128 v[136:139], v28 offset:34816
ds_read_b128 v[140:143], v28 offset:35840
ds_read_b128 v[144:147], v28 offset:36864
ds_read_b128 v[148:151], v28 offset:37888
v_mfma_f32_32x32x16_bf16 a[224:239], v[10:13], v[10:13], 0
ds_read_b128 v[152:155], v28 offset:38912
ds_read_b128 v[156:159], v28 offset:39936
ds_read_b128 v[160:163], v29 offset:0
ds_read_b128 v[164:167], v29 offset:1024
v_mfma_f32_32x32x16_bf16 a[64:79], v[10:13], v[10:13], 0
ds_read_b128 v[168:171], v29 offset:2048
ds_read_b128 v[172:175], v29 offset:3072
ds_read_b128 v[176:179], v29 offset:4096
ds_read_b128 v[180:183], v29 offset:5120
v_mfma_f32_32x32x16_bf16 a[80:95], v[10:13], v[10:13], 0
ds_read_b128 v[184:187], v29 offset:6144
ds_read_b128 v[188:191], v29 offset:7168
s_waitcnt lgkmcnt(14)
v_max_f32 v24, v6, v212
v_max_f32 v25, v8, v214
v_sub_f32 v208, v212, v24
v_sub_f32 v209, v6, v24
v_sub_f32 v210, v214, v25
v_sub_f32 v211, v8, v25
v_mfma_f32_32x32x16_bf16 a[96:111], v[10:13], v[10:13], 0
v_exp_f32 v208, v208
v_exp_f32 v209, v209
v_exp_f32 v210, v210
v_exp_f32 v211, v211
v_mul_f32 v24, v208, v213
v_mul_f32 v25, v210, v215
v_fmac_f32 v24, v209, v7
v_fmac_f32 v25, v211, v9
v_mfma_f32_32x32x16_bf16 a[112:127], v[10:13], v[10:13], 0
v_rcp_f32 v24, v24
v_rcp_f32 v25, v25
v_mul_f32 v208, v208, v24
v_mul_f32 v209, v209, v24
v_mul_f32 v210, v210, v25
v_mul_f32 v211, v211, v25
v_max3_f32 v5, v213, v215, v7
v_max3_f32 v5, v5, v9, v24
v_max_f32 v5, v5, v25
v_cmp_lt_f32 vcc, s76, v5
s_cbranch_vccnz LF0_%=
LF0R_%=:
v_pk_mul_f32 v[32:33], v[32:33], v[208:209] op_sel_hi:[1,0]
v_pk_mul_f32 v[34:35], v[34:35], v[208:209] op_sel_hi:[1,0]
v_pk_mul_f32 v[36:37], v[36:37], v[208:209] op_sel_hi:[1,0]
v_pk_mul_f32 v[38:39], v[38:39], v[208:209] op_sel_hi:[1,0]
v_pk_fma_f32 v[32:33], v[128:129], v[208:209], v[32:33] op_sel:[0,1,0] op_sel_hi:[1,1,1]
v_pk_fma_f32 v[34:35], v[130:131], v[208:209], v[34:35] op_sel:[0,1,0] op_sel_hi:[1,1,1]
v_pk_fma_f32 v[36:37], v[132:133], v[208:209], v[36:37] op_sel:[0,1,0] op_sel_hi:[1,1,1]
v_pk_fma_f32 v[38:39], v[134:135], v[208:209], v[38:39] op_sel:[0,1,0] op_sel_hi:[1,1,1]
v_cvt_pk_bf16_f32 v32, v32, v33
v_cvt_pk_bf16_f32 v33, v34, v35
v_cvt_pk_bf16_f32 v34, v36, v37
v_cvt_pk_bf16_f32 v35, v38, v39
global_store_dwordx4 v22, v[32:35], s[50:51] offset:-4096 sc1
v_pk_mul_f32 v[40:41], v[40:41], v[208:209] op_sel_hi:[1,0]
v_pk_mul_f32 v[42:43], v[42:43], v[208:209] op_sel_hi:[1,0]
v_pk_mul_f32 v[44:45], v[44:45], v[208:209] op_sel_hi:[1,0]
v_pk_mul_f32 v[46:47], v[46:47], v[208:209] op_sel_hi:[1,0]
s_waitcnt lgkmcnt(13)
v_pk_fma_f32 v[40:41], v[136:137], v[208:209], v[40:41] op_sel:[0,1,0] op_sel_hi:[1,1,1]
v_pk_fma_f32 v[42:43], v[138:139], v[208:209], v[42:43] op_sel:[0,1,0] op_sel_hi:[1,1,1]
s_waitcnt lgkmcnt(12)
v_pk_fma_f32 v[44:45], v[140:141], v[208:209], v[44:45] op_sel:[0,1,0] op_sel_hi:[1,1,1]
v_pk_fma_f32 v[46:47], v[142:143], v[208:209], v[46:47] op_sel:[0,1,0] op_sel_hi:[1,1,1]
v_cvt_pk_bf16_f32 v40, v40, v41
v_cvt_pk_bf16_f32 v41, v42, v43
v_cvt_pk_bf16_f32 v42, v44, v45
v_cvt_pk_bf16_f32 v43, v46, v47
global_store_dwordx4 v22, v[40:43], s[50:51] offset:-2048 sc1
v_pk_mul_f32 v[48:49], v[48:49], v[208:209] op_sel_hi:[1,0]
v_pk_mul_f32 v[50:51], v[50:51], v[208:209] op_sel_hi:[1,0]
v_pk_mul_f32 v[52:53], v[52:53], v[208:209] op_sel_hi:[1,0]
v_pk_mul_f32 v[54:55], v[54:55], v[208:209] op_sel_hi:[1,0]
s_waitcnt lgkmcnt(11)
v_pk_fma_f32 v[48:49], v[144:145], v[208:209], v[48:49] op_sel:[0,1,0] op_sel_hi:[1,1,1]
v_pk_fma_f32 v[50:51], v[146:147], v[208:209], v[50:51] op_sel:[0,1,0] op_sel_hi:[1,1,1]
s_waitcnt lgkmcnt(10)
v_pk_fma_f32 v[52:53], v[148:149], v[208:209], v[52:53] op_sel:[0,1,0] op_sel_hi:[1,1,1]
v_pk_fma_f32 v[54:55], v[150:151], v[208:209], v[54:55] op_sel:[0,1,0] op_sel_hi:[1,1,1]
v_cvt_pk_bf16_f32 v48, v48, v49
v_cvt_pk_bf16_f32 v49, v50, v51
v_cvt_pk_bf16_f32 v50, v52, v53
v_cvt_pk_bf16_f32 v51, v54, v55
global_store_dwordx4 v22, v[48:51], s[50:51] offset:0 sc1
v_pk_mul_f32 v[56:57], v[56:57], v[208:209] op_sel_hi:[1,0]
v_pk_mul_f32 v[58:59], v[58:59], v[208:209] op_sel_hi:[1,0]
)"
R"(v_pk_mul_f32 v[60:61], v[60:61], v[208:209] op_sel_hi:[1,0]
v_pk_mul_f32 v[62:63], v[62:63], v[208:209] op_sel_hi:[1,0]
s_waitcnt lgkmcnt(9)
v_pk_fma_f32 v[56:57], v[152:153], v[208:209], v[56:57] op_sel:[0,1,0] op_sel_hi:[1,1,1]
v_pk_fma_f32 v[58:59], v[154:155], v[208:209], v[58:59] op_sel:[0,1,0] op_sel_hi:[1,1,1]
s_waitcnt lgkmcnt(8)
v_pk_fma_f32 v[60:61], v[156:157], v[208:209], v[60:61] op_sel:[0,1,0] op_sel_hi:[1,1,1]
v_pk_fma_f32 v[62:63], v[158:159], v[208:209], v[62:63] op_sel:[0,1,0] op_sel_hi:[1,1,1]
v_cvt_pk_bf16_f32 v56, v56, v57
v_cvt_pk_bf16_f32 v57, v58, v59
v_cvt_pk_bf16_f32 v58, v60, v61
v_cvt_pk_bf16_f32 v59, v62, v63
global_store_dwordx4 v22, v[56:59], s[50:51] offset:2048 sc1
v_pk_mul_f32 v[224:225], v[224:225], v[210:211] op_sel_hi:[1,0]
v_pk_mul_f32 v[226:227], v[226:227], v[210:211] op_sel_hi:[1,0]
v_pk_mul_f32 v[228:229], v[228:229], v[210:211] op_sel_hi:[1,0]
v_pk_mul_f32 v[230:231], v[230:231], v[210:211] op_sel_hi:[1,0]
s_waitcnt lgkmcnt(7)
v_pk_fma_f32 v[224:225], v[160:161], v[210:211], v[224:225] op_sel:[0,1,0] op_sel_hi:[1,1,1]
v_pk_fma_f32 v[226:227], v[162:163], v[210:211], v[226:227] op_sel:[0,1,0] op_sel_hi:[1,1,1]
s_waitcnt lgkmcnt(6)
v_pk_fma_f32 v[228:229], v[164:165], v[210:211], v[228:229] op_sel:[0,1,0] op_sel_hi:[1,1,1]
v_pk_fma_f32 v[230:231], v[166:167], v[210:211], v[230:231] op_sel:[0,1,0] op_sel_hi:[1,1,1]
v_cvt_pk_bf16_f32 v224, v224, v225
v_cvt_pk_bf16_f32 v225, v226, v227
v_cvt_pk_bf16_f32 v226, v228, v229
v_cvt_pk_bf16_f32 v227, v230, v231
global_store_dwordx4 v22, v[224:227], s[50:51] offset:-3072 sc1
v_pk_mul_f32 v[232:233], v[232:233], v[210:211] op_sel_hi:[1,0]
v_pk_mul_f32 v[234:235], v[234:235], v[210:211] op_sel_hi:[1,0]
v_pk_mul_f32 v[236:237], v[236:237], v[210:211] op_sel_hi:[1,0]
v_pk_mul_f32 v[238:239], v[238:239], v[210:211] op_sel_hi:[1,0]
s_waitcnt lgkmcnt(5)
v_pk_fma_f32 v[232:233], v[168:169], v[210:211], v[232:233] op_sel:[0,1,0] op_sel_hi:[1,1,1]
v_pk_fma_f32 v[234:235], v[170:171], v[210:211], v[234:235] op_sel:[0,1,0] op_sel_hi:[1,1,1]
s_waitcnt lgkmcnt(4)
v_pk_fma_f32 v[236:237], v[172:173], v[210:211], v[236:237] op_sel:[0,1,0] op_sel_hi:[1,1,1]
v_pk_fma_f32 v[238:239], v[174:175], v[210:211], v[238:239] op_sel:[0,1,0] op_sel_hi:[1,1,1]
v_cvt_pk_bf16_f32 v232, v232, v233
v_cvt_pk_bf16_f32 v233, v234, v235
v_cvt_pk_bf16_f32 v234, v236, v237
v_cvt_pk_bf16_f32 v235, v238, v239
global_store_dwordx4 v22, v[232:235], s[50:51] offset:-1024 sc1
v_pk_mul_f32 v[240:241], v[240:241], v[210:211] op_sel_hi:[1,0]
v_pk_mul_f32 v[242:243], v[242:243], v[210:211] op_sel_hi:[1,0]
v_pk_mul_f32 v[244:245], v[244:245], v[210:211] op_sel_hi:[1,0]
v_pk_mul_f32 v[246:247], v[246:247], v[210:211] op_sel_hi:[1,0]
s_waitcnt lgkmcnt(3)
v_pk_fma_f32 v[240:241], v[176:177], v[210:211], v[240:241] op_sel:[0,1,0] op_sel_hi:[1,1,1]
v_pk_fma_f32 v[242:243], v[178:179], v[210:211], v[242:243] op_sel:[0,1,0] op_sel_hi:[1,1,1]
s_waitcnt lgkmcnt(2)
v_pk_fma_f32 v[244:245], v[180:181], v[210:211], v[244:245] op_sel:[0,1,0] op_sel_hi:[1,1,1]
v_pk_fma_f32 v[246:247], v[182:183], v[210:211], v[246:247] op_sel:[0,1,0] op_sel_hi:[1,1,1]
v_cvt_pk_bf16_f32 v240, v240, v241
v_cvt_pk_bf16_f32 v241, v242, v243
v_cvt_pk_bf16_f32 v242, v244, v245
v_cvt_pk_bf16_f32 v243, v246, v247
global_store_dwordx4 v22, v[240:243], s[50:51] offset:1024 sc1
v_pk_mul_f32 v[248:249], v[248:249], v[210:211] op_sel_hi:[1,0]
v_pk_mul_f32 v[250:251], v[250:251], v[210:211] op_sel_hi:[1,0]
v_pk_mul_f32 v[252:253], v[252:253], v[210:211] op_sel_hi:[1,0]
v_pk_mul_f32 v[254:255], v[254:255], v[210:211] op_sel_hi:[1,0]
s_waitcnt lgkmcnt(1)
v_pk_fma_f32 v[248:249], v[184:185], v[210:211], v[248:249] op_sel:[0,1,0] op_sel_hi:[1,1,1]
v_pk_fma_f32 v[250:251], v[186:187], v[210:211], v[250:251] op_sel:[0,1,0] op_sel_hi:[1,1,1]
s_waitcnt lgkmcnt(0)
v_pk_fma_f32 v[252:253], v[188:189], v[210:211], v[252:253] op_sel:[0,1,0] op_sel_hi:[1,1,1]
v_pk_fma_f32 v[254:255], v[190:191], v[210:211], v[254:255] op_sel:[0,1,0] op_sel_hi:[1,1,1]
v_cvt_pk_bf16_f32 v248, v248, v249
v_cvt_pk_bf16_f32 v249, v250, v251
v_cvt_pk_bf16_f32 v250, v252, v253
v_cvt_pk_bf16_f32 v251, v254, v255
global_store_dwordx4 v22, v[248:251], s[50:51] offset:3072 sc1
s_cmp_eq_u32 s54, 16
s_cbranch_scc1 ENDR4_%=
s_branch ENDR2_%=
COLD_%=:
REDO_%=:
s_waitcnt vmcnt(0) lgkmcnt(0)
s_barrier
s_cmp_lt_i32 s76, 0
s_cselect_b32 %3, 4, 20
s_branch START_%=
LF1_%=:
s_mov_b32 s76, -1.0
s_add_u32 s66, s66, 0x10000
s_addc_u32 s67, s67, 0
s_branch LF1R_%=
LF0_%=:
s_mov_b32 s76, -1.0
s_add_u32 s66, s66, 0x10000
s_addc_u32 s67, s67, 0
s_branch LF0R_%=
LFD_%=:
v_lshlrev_b32 v5, 9, v22
v_add_u32 v5, 0x10000, v5
s_mov_b64 exec, 15
ds_write_b32 v5, v5 offset:7424
s_mov_b64 exec, -1
s_branch LFDR_%=
HSP0N_%=:
s_mov_b32 s77, 0x20000
HSL0N_%=:
s_mov_b64 exec, 1
global_load_dword v24, v101, s[92:93] offset:0 sc1
s_and_b32 s91, s26, 7
s_and_b32 exec_hi, s28, 7
s_cmp_eq_u32 s91, exec_hi
s_cselect_b32 exec_hi, 0xff, 0
s_lshl_b32 s91, s91, 2
s_cmp_eq_u32 s91, s28
s_cselect_b32 exec_lo, -1, 0
global_load_dword v102, v25, s[92:93] offset:16 sc1
s_waitcnt vmcnt(0)
global_store_dwordx2 v25, v[102:103], s[92:93] sc1
s_mov_b64 exec, -1
v_cmp_ne_u32 vcc, s54, v24
s_and_b32 s91, vcc_lo, 1
s_cbranch_scc0 HOK0N_%=
s_sleep 4
s_sub_u32 s77, s77, 1
s_cmp_lg_u32 s77, 0
s_cbranch_scc1 HSL0N_%=
s_branch HOK0N_%=
HSP1N_%=:
s_mov_b32 s77, 0x20000
HSL1N_%=:
s_mov_b64 exec, 2
global_load_dword v24, v101, s[92:93] offset:0 sc1
s_and_b32 s91, s26, 7
s_and_b32 exec_hi, s28, 7
s_cmp_eq_u32 s91, exec_hi
s_cselect_b32 exec_hi, 0xff, 0
s_lshl_b32 s91, s91, 2
s_cmp_eq_u32 s91, s28
s_cselect_b32 exec_lo, -1, 0
global_load_dword v102, v25, s[92:93] offset:16 sc1
s_waitcnt vmcnt(0)
global_store_dwordx2 v25, v[102:103], s[92:93] sc1
s_mov_b64 exec, -1
v_cmp_ne_u32 vcc, s90, v24
s_and_b32 s91, vcc_lo, 2
s_cbranch_scc0 HOK1N_%=
s_sleep 4
s_sub_u32 s77, s77, 1
s_cmp_lg_u32 s77, 0
s_cbranch_scc1 HSL1N_%=
s_branch HOK1N_%=
HSP2N_%=:
s_mov_b32 s77, 0x20000
HSL2N_%=:
s_mov_b64 exec, 15
global_load_dword v24, v100, s[92:93] offset:0 sc1
s_and_b32 s91, s26, 7
s_and_b32 exec_hi, s28, 7
s_cmp_eq_u32 s91, exec_hi
s_cselect_b32 exec_hi, 0xff, 0
s_lshl_b32 s91, s91, 2
s_cmp_eq_u32 s91, s28
s_cselect_b32 exec_lo, -1, 0
global_load_dword v102, v25, s[92:93] offset:16 sc1
s_waitcnt vmcnt(0)
global_store_dwordx2 v25, v[102:103], s[92:93] sc1
s_mov_b64 exec, -1
v_cmp_ne_u32 vcc, s55, v24
s_and_b32 s91, vcc_lo, 5
s_cbranch_scc0 HOK2N_%=
s_sleep 4
s_sub_u32 s77, s77, 1
s_cmp_lg_u32 s77, 0
s_cbranch_scc1 HSL2N_%=
s_branch HOK2N_%=
HSP0S_%=:
s_mov_b32 s77, 0x20000
HSL0S_%=:
s_mov_b64 exec, 1
global_load_dword v24, v101, s[92:93] offset:0 sc1
s_and_b32 s91, s26, 7
s_and_b32 exec_hi, s28, 7
s_cmp_eq_u32 s91, exec_hi
s_cselect_b32 exec_hi, 0xff, 0
s_lshl_b32 s91, s91, 2
s_cmp_eq_u32 s91, s28
s_cselect_b32 exec_lo, -1, 0
global_load_dword v102, v25, s[92:93] offset:16 sc1
s_waitcnt vmcnt(0)
global_store_dwordx2 v25, v[102:103], s[92:93] sc1
s_mov_b64 exec, -1
v_cmp_ne_u32 vcc, s54, v24
s_and_b32 s91, vcc_lo, 1
s_cbranch_scc0 HOK0S_%=
s_sleep 4
s_sub_u32 s77, s77, 1
s_cmp_lg_u32 s77, 0
s_cbranch_scc1 HSL0S_%=
s_branch HOK0S_%=
HSP1S_%=:
s_mov_b32 s77, 0x20000
HSL1S_%=:
s_mov_b64 exec, 2
global_load_dword v24, v101, s[92:93] offset:0 sc1
s_and_b32 s91, s26, 7
s_and_b32 exec_hi, s28, 7
s_cmp_eq_u32 s91, exec_hi
s_cselect_b32 exec_hi, 0xff, 0
s_lshl_b32 s91, s91, 2
s_cmp_eq_u32 s91, s28
s_cselect_b32 exec_lo, -1, 0
global_load_dword v102, v25, s[92:93] offset:16 sc1
s_waitcnt vmcnt(0)
global_store_dwordx2 v25, v[102:103], s[92:93] sc1
s_mov_b64 exec, -1
v_cmp_ne_u32 vcc, s90, v24
s_and_b32 s91, vcc_lo, 2
s_cbranch_scc0 HOK1S_%=
s_sleep 4
s_sub_u32 s77, s77, 1
s_cmp_lg_u32 s77, 0
s_cbranch_scc1 HSL1S_%=
s_branch HOK1S_%=
HSP2S_%=:
s_mov_b32 s77, 0x20000
HSL2S_%=:
s_mov_b64 exec, 15
global_load_dword v24, v100, s[92:93] offset:0 sc1
s_and_b32 s91, s26, 7
s_and_b32 exec_hi, s28, 7
s_cmp_eq_u32 s91, exec_hi
s_cselect_b32 exec_hi, 0xff, 0
s_lshl_b32 s91, s91, 2
s_cmp_eq_u32 s91, s28
s_cselect_b32 exec_lo, -1, 0
global_load_dword v102, v25, s[92:93] offset:16 sc1
s_waitcnt vmcnt(0)
global_store_dwordx2 v25, v[102:103], s[92:93] sc1
s_mov_b64 exec, -1
v_cmp_ne_u32 vcc, s55, v24
s_and_b32 s91, vcc_lo, 5
s_cbranch_scc0 HOK2S_%=
s_sleep 4
s_sub_u32 s77, s77, 1
s_cmp_lg_u32 s77, 0
s_cbranch_scc1 HSL2S_%=
s_branch HOK2S_%=
PAD27_%=:
s_cbranch_execz PAD35_%=
P1S_%=:
ds_read_b64_tr_b16 v[64:65], v96 offset:32768
ds_read_b64_tr_b16 v[66:67], v96 offset:33024
ds_read_b64_tr_b16 v[68:69], v96 offset:40960
ds_read_b64_tr_b16 v[70:71], v96 offset:41216
ds_read_b64_tr_b16 v[72:73], v97 offset:32768
ds_read_b64_tr_b16 v[74:75], v97 offset:33024
ds_read_b64_tr_b16 v[76:77], v97 offset:40960
ds_read_b64_tr_b16 v[78:79], v97 offset:41216
ds_read_b64_tr_b16 v[80:81], v96 offset:33280
ds_read_b64_tr_b16 v[82:83], v96 offset:33536
ds_read_b64_tr_b16 v[84:85], v96 offset:41472
ds_read_b64_tr_b16 v[86:87], v96 offset:41728
s_waitcnt lgkmcnt(10)
v_mfma_f32_16x16x32_bf16 v[192:195], v[64:67], a[0:3], 0
ds_read_b64_tr_b16 v[88:89], v97 offset:33280
s_add_u32 m0, s75, 65536
s_waitcnt lgkmcnt(9)
v_mfma_f32_16x16x32_bf16 v[192:195], v[68:71], a[16:19], v[192:195]
ds_read_b64_tr_b16 v[90:91], v97 offset:33536
s_xor_b32 s48, s44, 256
v_mfma_f32_16x16x32_bf16 v[208:211], v[64:67], a[4:7], 0
buffer_load_dwordx4 v112, s[84:87], s48 offen offset:0 lds
v_mfma_f32_16x16x32_bf16 v[208:211], v[68:71], a[20:23], v[208:211]
v_mfma_f32_16x16x32_bf16 v[224:227], v[64:67], a[8:11], 0
ds_read_b64_tr_b16 v[92:93], v97 offset:41472
v_mfma_f32_16x16x32_bf16 v[224:227], v[68:71], a[24:27], v[224:227]
ds_read_b64_tr_b16 v[94:95], v97 offset:41728
v_mfma_f32_16x16x32_bf16 v[240:243], v[64:67], a[12:15], 0
buffer_load_dwordx4 v113, s[84:87], s48 offen offset:1024 lds
v_mfma_f32_16x16x32_bf16 v[240:243], v[68:71], a[28:31], v[240:243]
s_waitcnt lgkmcnt(8)
v_mfma_f32_16x16x32_bf16 v[196:199], v[72:75], a[0:3], 0
ds_read_b64_tr_b16 v[64:65], v96 offset:36864
v_mfma_f32_16x16x32_bf16 v[196:199], v[76:79], a[16:19], v[196:199]
ds_read_b64_tr_b16 v[66:67], v96 offset:37120
PAD28_%=:
s_cbranch_execz PAD10_%=
v_mfma_f32_16x16x32_bf16 v[212:215], v[72:75], a[4:7], 0
buffer_load_dwordx4 v114, s[84:87], s48 offen offset:2048 lds
v_mfma_f32_16x16x32_bf16 v[212:215], v[76:79], a[20:23], v[212:215]
v_mfma_f32_16x16x32_bf16 v[228:231], v[72:75], a[8:11], 0
ds_read_b64_tr_b16 v[68:69], v96 offset:45056
v_mfma_f32_16x16x32_bf16 v[228:231], v[76:79], a[24:27], v[228:231]
ds_read_b64_tr_b16 v[70:71], v96 offset:45312
v_mfma_f32_16x16x32_bf16 v[244:247], v[72:75], a[12:15], 0
buffer_load_dwordx4 v115, s[84:87], s48 offen offset:3072 lds
s_add_u32 m0, s75, 69632
v_mfma_f32_16x16x32_bf16 v[244:247], v[76:79], a[28:31], v[244:247]
s_xor_b32 s48, s46, 256
s_waitcnt lgkmcnt(8)
v_mfma_f32_16x16x32_bf16 v[200:203], v[80:83], a[0:3], 0
ds_read_b64_tr_b16 v[72:73], v97 offset:36864
v_mfma_f32_16x16x32_bf16 v[200:203], v[84:87], a[16:19], v[200:203]
ds_read_b64_tr_b16 v[74:75], v97 offset:37120
v_mfma_f32_16x16x32_bf16 v[216:219], v[80:83], a[4:7], 0
buffer_load_dwordx4 v112, s[84:87], s48 offen offset:0 lds
v_mfma_f32_16x16x32_bf16 v[216:219], v[84:87], a[20:23], v[216:219]
v_mfma_f32_16x16x32_bf16 v[232:235], v[80:83], a[8:11], 0
ds_read_b64_tr_b16 v[76:77], v97 offset:45056
v_mfma_f32_16x16x32_bf16 v[232:235], v[84:87], a[24:27], v[232:235]
ds_read_b64_tr_b16 v[78:79], v97 offset:45312
v_mfma_f32_16x16x32_bf16 v[248:251], v[80:83], a[12:15], 0
buffer_load_dwordx4 v113, s[84:87], s48 offen offset:1024 lds
v_mfma_f32_16x16x32_bf16 v[248:251], v[84:87], a[28:31], v[248:251]
buffer_load_dwordx4 v114, s[84:87], s48 offen offset:2048 lds
s_waitcnt lgkmcnt(8)
v_mfma_f32_16x16x32_bf16 v[204:207], v[88:91], a[0:3], 0
ds_read_b64_tr_b16 v[80:81], v96 offset:37376
v_mfma_f32_16x16x32_bf16 v[204:207], v[92:95], a[16:19], v[204:207]
ds_read_b64_tr_b16 v[82:83], v96 offset:37632
v_mfma_f32_16x16x32_bf16 v[220:223], v[88:91], a[4:7], 0
v_mfma_f32_16x16x32_bf16 v[220:223], v[92:95], a[20:23], v[220:223]
buffer_load_dwordx4 v115, s[84:87], s48 offen offset:3072 lds
s_add_u32 m0, s75, 0
v_mfma_f32_16x16x32_bf16 v[236:239], v[88:91], a[8:11], 0
ds_read_b64_tr_b16 v[84:85], v96 offset:45568
s_xor_b32 s48, s66, 1048576
v_mfma_f32_16x16x32_bf16 v[236:239], v[92:95], a[24:27], v[236:239]
ds_read_b64_tr_b16 v[86:87], v96 offset:45824
v_mfma_f32_16x16x32_bf16 v[252:255], v[88:91], a[12:15], 0
buffer_load_dwordx4 v104, s[80:83], s48 offen offset:0 lds
s_waitcnt vmcnt(9)
v_mfma_f32_16x16x32_bf16 v[252:255], v[92:95], a[28:31], v[252:255]
s_waitcnt lgkmcnt(8)
v_mfma_f32_16x16x32_bf16 v[128:131], v[64:67], a[0:3], 0
ds_read_b64_tr_b16 v[88:89], v97 offset:37376
ds_read_b128 a[32:35], v40 offset:0
v_mfma_f32_16x16x32_bf16 v[128:131], v[68:71], a[16:19], v[128:131]
ds_read_b64_tr_b16 v[90:91], v97 offset:37632
ds_read_b128 a[36:39], v40 offset:2048
v_mfma_f32_16x16x32_bf16 v[144:147], v[64:67], a[4:7], 0
buffer_load_dwordx4 v105, s[80:83], s48 offen offset:1024 lds
v_mfma_f32_16x16x32_bf16 v[144:147], v[68:71], a[20:23], v[144:147]
v_mfma_f32_16x16x32_bf16 v[160:163], v[64:67], a[8:11], 0
ds_read_b64_tr_b16 v[92:93], v97 offset:45568
ds_read_b128 a[40:43], v40 offset:4096
v_mfma_f32_16x16x32_bf16 v[160:163], v[68:71], a[24:27], v[160:163]
ds_read_b64_tr_b16 v[94:95], v97 offset:45824
ds_read_b128 a[44:47], v40 offset:6144
v_mfma_f32_16x16x32_bf16 v[176:179], v[64:67], a[12:15], 0
buffer_load_dwordx4 v106, s[80:83], s48 offen offset:2048 lds
v_mfma_f32_16x16x32_bf16 v[176:179], v[68:71], a[28:31], v[176:179]
s_waitcnt lgkmcnt(12)
v_mfma_f32_16x16x32_bf16 v[132:135], v[72:75], a[0:3], 0
ds_read_b64_tr_b16 v[64:65], v96 offset:49152
ds_read_b128 a[48:51], v41 offset:0
v_mfma_f32_16x16x32_bf16 v[132:135], v[76:79], a[16:19], v[132:135]
ds_read_b64_tr_b16 v[66:67], v96 offset:49408
ds_read_b128 a[52:55], v41 offset:2048
v_mfma_f32_16x16x32_bf16 v[148:151], v[72:75], a[4:7], 0
buffer_load_dwordx4 v107, s[80:83], s48 offen offset:3072 lds
s_add_u32 m0, s75, 4096
v_mfma_f32_16x16x32_bf16 v[148:151], v[76:79], a[20:23], v[148:151]
s_xor_b32 s48, s66, 1048704
v_mfma_f32_16x16x32_bf16 v[164:167], v[72:75], a[8:11], 0
ds_read_b64_tr_b16 v[68:69], v96 offset:57344
ds_read_b128 a[56:59], v41 offset:4096
v_mfma_f32_16x16x32_bf16 v[164:167], v[76:79], a[24:27], v[164:167]
ds_read_b64_tr_b16 v[70:71], v96 offset:57600
ds_read_b128 a[60:63], v41 offset:6144
v_mfma_f32_16x16x32_bf16 v[180:183], v[72:75], a[12:15], 0
buffer_load_dwordx4 v104, s[80:83], s48 offen offset:0 lds
v_mfma_f32_16x16x32_bf16 v[180:183], v[76:79], a[28:31], v[180:183]
s_waitcnt lgkmcnt(14)
v_mfma_f32_16x16x32_bf16 v[136:139], v[80:83], a[0:3], 0
ds_read_b64_tr_b16 v[72:73], v97 offset:49152
v_mfma_f32_16x16x32_bf16 v[136:139], v[84:87], a[16:19], v[136:139]
ds_read_b64_tr_b16 v[74:75], v97 offset:49408
v_mfma_f32_16x16x32_bf16 v[152:155], v[80:83], a[4:7], 0
buffer_load_dwordx4 v105, s[80:83], s48 offen offset:1024 lds
v_mfma_f32_16x16x32_bf16 v[152:155], v[84:87], a[20:23], v[152:155]
buffer_load_dwordx4 v106, s[80:83], s48 offen offset:2048 lds
v_mfma_f32_16x16x32_bf16 v[168:171], v[80:83], a[8:11], 0
ds_read_b64_tr_b16 v[76:77], v97 offset:57344
v_mfma_f32_16x16x32_bf16 v[168:171], v[84:87], a[24:27], v[168:171]
ds_read_b64_tr_b16 v[78:79], v97 offset:57600
v_mfma_f32_16x16x32_bf16 v[184:187], v[80:83], a[12:15], 0
v_mfma_f32_16x16x32_bf16 v[184:187], v[84:87], a[28:31], v[184:187]
buffer_load_dwordx4 v107, s[80:83], s48 offen offset:3072 lds
s_waitcnt lgkmcnt(13)
v_mfma_f32_16x16x32_bf16 v[140:143], v[88:91], a[0:3], 0
ds_read_b64_tr_b16 v[80:81], v96 offset:49664
s_add_u32 m0, s75, 98304
v_mfma_f32_16x16x32_bf16 v[140:143], v[92:95], a[16:19], v[140:143]
ds_read_b64_tr_b16 v[82:83], v96 offset:49920
s_xor_b32 s48, s44, 384
v_mfma_f32_16x16x32_bf16 v[156:159], v[88:91], a[4:7], 0
buffer_load_dwordx4 v112, s[84:87], s48 offen offset:0 lds
v_mfma_f32_16x16x32_bf16 v[156:159], v[92:95], a[20:23], v[156:159]
v_mfma_f32_16x16x32_bf16 v[172:175], v[88:91], a[8:11], 0
ds_read_b64_tr_b16 v[84:85], v96 offset:57856
v_mfma_f32_16x16x32_bf16 v[172:175], v[92:95], a[24:27], v[172:175]
ds_read_b64_tr_b16 v[86:87], v96 offset:58112
)"
R"(v_mfma_f32_16x16x32_bf16 v[188:191], v[88:91], a[12:15], 0
buffer_load_dwordx4 v113, s[84:87], s48 offen offset:1024 lds
v_mfma_f32_16x16x32_bf16 v[188:191], v[92:95], a[28:31], v[188:191]
s_waitcnt lgkmcnt(8)
v_mfma_f32_16x16x32_bf16 v[192:195], v[64:67], a[32:35], v[192:195]
ds_read_b64_tr_b16 v[88:89], v97 offset:49664
v_mfma_f32_16x16x32_bf16 v[192:195], v[68:71], a[48:51], v[192:195]
ds_read_b64_tr_b16 v[90:91], v97 offset:49920
v_mfma_f32_16x16x32_bf16 v[208:211], v[64:67], a[36:39], v[208:211]
buffer_load_dwordx4 v114, s[84:87], s48 offen offset:2048 lds
v_mfma_f32_16x16x32_bf16 v[208:211], v[68:71], a[52:55], v[208:211]
v_mfma_f32_16x16x32_bf16 v[224:227], v[64:67], a[40:43], v[224:227]
ds_read_b64_tr_b16 v[92:93], v97 offset:57856
v_mfma_f32_16x16x32_bf16 v[224:227], v[68:71], a[56:59], v[224:227]
ds_read_b64_tr_b16 v[94:95], v97 offset:58112
v_mfma_f32_16x16x32_bf16 v[240:243], v[64:67], a[44:47], v[240:243]
buffer_load_dwordx4 v115, s[84:87], s48 offen offset:3072 lds
s_add_u32 m0, s75, 102400
v_mfma_f32_16x16x32_bf16 v[240:243], v[68:71], a[60:63], v[240:243]
s_xor_b32 s48, s46, 384
s_waitcnt lgkmcnt(8)
v_mfma_f32_16x16x32_bf16 v[196:199], v[72:75], a[32:35], v[196:199]
ds_read_b64_tr_b16 v[64:65], v96 offset:53248
v_mfma_f32_16x16x32_bf16 v[196:199], v[76:79], a[48:51], v[196:199]
ds_read_b64_tr_b16 v[66:67], v96 offset:53504
v_mfma_f32_16x16x32_bf16 v[212:215], v[72:75], a[36:39], v[212:215]
buffer_load_dwordx4 v112, s[84:87], s48 offen offset:0 lds
v_mfma_f32_16x16x32_bf16 v[212:215], v[76:79], a[52:55], v[212:215]
v_mfma_f32_16x16x32_bf16 v[228:231], v[72:75], a[40:43], v[228:231]
ds_read_b64_tr_b16 v[68:69], v96 offset:61440
v_mfma_f32_16x16x32_bf16 v[228:231], v[76:79], a[56:59], v[228:231]
ds_read_b64_tr_b16 v[70:71], v96 offset:61696
v_mfma_f32_16x16x32_bf16 v[244:247], v[72:75], a[44:47], v[244:247]
buffer_load_dwordx4 v113, s[84:87], s48 offen offset:1024 lds
v_mfma_f32_16x16x32_bf16 v[244:247], v[76:79], a[60:63], v[244:247]
buffer_load_dwordx4 v114, s[84:87], s48 offen offset:2048 lds
s_waitcnt lgkmcnt(8)
v_mfma_f32_16x16x32_bf16 v[200:203], v[80:83], a[32:35], v[200:203]
ds_read_b64_tr_b16 v[72:73], v97 offset:53248
v_mfma_f32_16x16x32_bf16 v[200:203], v[84:87], a[48:51], v[200:203]
ds_read_b64_tr_b16 v[74:75], v97 offset:53504
v_mfma_f32_16x16x32_bf16 v[216:219], v[80:83], a[36:39], v[216:219]
v_mfma_f32_16x16x32_bf16 v[216:219], v[84:87], a[52:55], v[216:219]
buffer_load_dwordx4 v115, s[84:87], s48 offen offset:3072 lds
s_add_u32 m0, s75, 131072
v_mfma_f32_16x16x32_bf16 v[232:235], v[80:83], a[40:43], v[232:235]
ds_read_b64_tr_b16 v[76:77], v97 offset:61440
s_xor_b32 s48, s44, 512
v_mfma_f32_16x16x32_bf16 v[232:235], v[84:87], a[56:59], v[232:235]
ds_read_b64_tr_b16 v[78:79], v97 offset:61696
v_mfma_f32_16x16x32_bf16 v[248:251], v[80:83], a[44:47], v[248:251]
buffer_load_dwordx4 v112, s[84:87], s48 offen offset:0 lds
v_mfma_f32_16x16x32_bf16 v[248:251], v[84:87], a[60:63], v[248:251]
s_waitcnt lgkmcnt(8)
v_mfma_f32_16x16x32_bf16 v[204:207], v[88:91], a[32:35], v[204:207]
ds_read_b64_tr_b16 v[80:81], v96 offset:53760
v_mfma_f32_16x16x32_bf16 v[204:207], v[92:95], a[48:51], v[204:207]
ds_read_b64_tr_b16 v[82:83], v96 offset:54016
v_mfma_f32_16x16x32_bf16 v[220:223], v[88:91], a[36:39], v[220:223]
buffer_load_dwordx4 v113, s[84:87], s48 offen offset:1024 lds
s_waitcnt vmcnt(18)
v_mfma_f32_16x16x32_bf16 v[220:223], v[92:95], a[52:55], v[220:223]
v_mfma_f32_16x16x32_bf16 v[236:239], v[88:91], a[40:43], v[236:239]
ds_read_b64_tr_b16 v[84:85], v96 offset:61952
ds_read_b128 a[64:67], v38 offset:0
v_mfma_f32_16x16x32_bf16 v[236:239], v[92:95], a[56:59], v[236:239]
ds_read_b64_tr_b16 v[86:87], v96 offset:62208
ds_read_b128 a[68:71], v38 offset:2048
v_mfma_f32_16x16x32_bf16 v[252:255], v[88:91], a[44:47], v[252:255]
buffer_load_dwordx4 v114, s[84:87], s48 offen offset:2048 lds
v_mfma_f32_16x16x32_bf16 v[252:255], v[92:95], a[60:63], v[252:255]
s_waitcnt lgkmcnt(10)
v_mfma_f32_16x16x32_bf16 v[128:131], v[64:67], a[32:35], v[128:131]
ds_read_b64_tr_b16 v[88:89], v97 offset:53760
ds_read_b128 a[72:75], v38 offset:4096
v_mfma_f32_16x16x32_bf16 v[128:131], v[68:71], a[48:51], v[128:131]
ds_read_b64_tr_b16 v[90:91], v97 offset:54016
ds_read_b128 a[76:79], v38 offset:6144
v_mfma_f32_16x16x32_bf16 v[144:147], v[64:67], a[36:39], v[144:147]
buffer_load_dwordx4 v115, s[84:87], s48 offen offset:3072 lds
s_add_u32 m0, s75, 135168
v_mfma_f32_16x16x32_bf16 v[144:147], v[68:71], a[52:55], v[144:147]
s_xor_b32 s48, s46, 512
v_mfma_f32_16x16x32_bf16 v[160:163], v[64:67], a[40:43], v[160:163]
ds_read_b64_tr_b16 v[92:93], v97 offset:61952
ds_read_b128 a[80:83], v39 offset:0
v_mfma_f32_16x16x32_bf16 v[160:163], v[68:71], a[56:59], v[160:163]
ds_read_b64_tr_b16 v[94:95], v97 offset:62208
ds_read_b128 a[84:87], v39 offset:2048
v_mfma_f32_16x16x32_bf16 v[176:179], v[64:67], a[44:47], v[176:179]
buffer_load_dwordx4 v112, s[84:87], s48 offen offset:0 lds
v_mfma_f32_16x16x32_bf16 v[176:179], v[68:71], a[60:63], v[176:179]
s_waitcnt lgkmcnt(14)
v_mfma_f32_16x16x32_bf16 v[132:135], v[72:75], a[32:35], v[132:135]
buffer_load_dwordx4 v113, s[84:87], s48 offen offset:1024 lds
v_mfma_f32_16x16x32_bf16 v[132:135], v[76:79], a[48:51], v[132:135]
v_mfma_f32_16x16x32_bf16 v[148:151], v[72:75], a[36:39], v[148:151]
v_mfma_f32_16x16x32_bf16 v[148:151], v[76:79], a[52:55], v[148:151]
buffer_load_dwordx4 v114, s[84:87], s48 offen offset:2048 lds
v_mfma_f32_16x16x32_bf16 v[164:167], v[72:75], a[40:43], v[164:167]
v_mfma_f32_16x16x32_bf16 v[164:167], v[76:79], a[56:59], v[164:167]
s_waitcnt vmcnt(15) lgkmcnt(0)
s_barrier
v_mfma_f32_16x16x32_bf16 v[180:183], v[72:75], a[44:47], v[180:183]
ds_read_b64_tr_b16 v[64:65], v96 offset:0
ds_read_b128 a[88:91], v39 offset:4096
v_mfma_f32_16x16x32_bf16 v[180:183], v[76:79], a[60:63], v[180:183]
ds_read_b64_tr_b16 v[66:67], v96 offset:256
v_mfma_f32_16x16x32_bf16 v[136:139], v[80:83], a[32:35], v[136:139]
ds_read_b64_tr_b16 v[68:69], v96 offset:8192
ds_read_b128 a[92:95], v39 offset:6144
v_mfma_f32_16x16x32_bf16 v[136:139], v[84:87], a[48:51], v[136:139]
ds_read_b64_tr_b16 v[70:71], v96 offset:8448
v_mfma_f32_16x16x32_bf16 v[152:155], v[80:83], a[36:39], v[152:155]
ds_read_b64_tr_b16 v[72:73], v97 offset:0
v_mfma_f32_16x16x32_bf16 v[152:155], v[84:87], a[52:55], v[152:155]
ds_read_b64_tr_b16 v[74:75], v97 offset:256
v_mfma_f32_16x16x32_bf16 v[168:171], v[80:83], a[40:43], v[168:171]
ds_read_b64_tr_b16 v[76:77], v97 offset:8192
v_mfma_f32_16x16x32_bf16 v[168:171], v[84:87], a[56:59], v[168:171]
ds_read_b64_tr_b16 v[78:79], v97 offset:8448
v_mfma_f32_16x16x32_bf16 v[184:187], v[80:83], a[44:47], v[184:187]
buffer_load_dwordx4 v115, s[84:87], s48 offen offset:3072 lds
s_add_u32 m0, s75, 32768
v_mfma_f32_16x16x32_bf16 v[184:187], v[84:87], a[60:63], v[184:187]
s_xor_b32 s48, s66, 2097152
v_mfma_f32_16x16x32_bf16 v[140:143], v[88:91], a[32:35], v[140:143]
ds_read_b64_tr_b16 v[80:81], v96 offset:512
v_mfma_f32_16x16x32_bf16 v[140:143], v[92:95], a[48:51], v[140:143]
ds_read_b64_tr_b16 v[82:83], v96 offset:768
v_mfma_f32_16x16x32_bf16 v[156:159], v[88:91], a[36:39], v[156:159]
buffer_load_dwordx4 v104, s[80:83], s48 offen offset:0 lds
v_mfma_f32_16x16x32_bf16 v[156:159], v[92:95], a[52:55], v[156:159]
buffer_load_dwordx4 v105, s[80:83], s48 offen offset:1024 lds
v_mfma_f32_16x16x32_bf16 v[172:175], v[88:91], a[40:43], v[172:175]
ds_read_b64_tr_b16 v[84:85], v96 offset:8704
v_mfma_f32_16x16x32_bf16 v[172:175], v[92:95], a[56:59], v[172:175]
ds_read_b64_tr_b16 v[86:87], v96 offset:8960
v_mfma_f32_16x16x32_bf16 v[188:191], v[88:91], a[44:47], v[188:191]
v_mfma_f32_16x16x32_bf16 v[188:191], v[92:95], a[60:63], v[188:191]
buffer_load_dwordx4 v106, s[80:83], s48 offen offset:2048 lds
s_waitcnt lgkmcnt(8)
v_mfma_f32_16x16x32_bf16 v[192:195], v[64:67], a[64:67], v[192:195]
ds_read_b64_tr_b16 v[88:89], v97 offset:512
v_mfma_f32_16x16x32_bf16 v[192:195], v[68:71], a[80:83], v[192:195]
ds_read_b64_tr_b16 v[90:91], v97 offset:768
v_mfma_f32_16x16x32_bf16 v[208:211], v[64:67], a[68:71], v[208:211]
s_cmp_lg_u32 %3, 0
s_cbranch_scc1 W0S79_%=
s_mov_b64 exec, s[56:57]
global_load_dword v102, v25, s[92:93] offset:16 sc1
s_mov_b64 exec, -1
W0S79_%=:
v_mfma_f32_16x16x32_bf16 v[208:211], v[68:71], a[84:87], v[208:211]
buffer_load_dwordx4 v107, s[80:83], s48 offen offset:3072 lds
s_add_u32 m0, s75, 36864
v_mfma_f32_16x16x32_bf16 v[224:227], v[64:67], a[72:75], v[224:227]
ds_read_b64_tr_b16 v[92:93], v97 offset:8704
s_xor_b32 s48, s66, 2097280
v_mfma_f32_16x16x32_bf16 v[224:227], v[68:71], a[88:91], v[224:227]
ds_read_b64_tr_b16 v[94:95], v97 offset:8960
v_mfma_f32_16x16x32_bf16 v[240:243], v[64:67], a[76:79], v[240:243]
buffer_load_dwordx4 v104, s[80:83], s48 offen offset:0 lds
v_mfma_f32_16x16x32_bf16 v[240:243], v[68:71], a[92:95], v[240:243]
s_waitcnt lgkmcnt(8)
v_mfma_f32_16x16x32_bf16 v[196:199], v[72:75], a[64:67], v[196:199]
ds_read_b64_tr_b16 v[64:65], v96 offset:4096
v_mfma_f32_16x16x32_bf16 v[196:199], v[76:79], a[80:83], v[196:199]
ds_read_b64_tr_b16 v[66:67], v96 offset:4352
v_mfma_f32_16x16x32_bf16 v[212:215], v[72:75], a[68:71], v[212:215]
buffer_load_dwordx4 v105, s[80:83], s48 offen offset:1024 lds
v_mfma_f32_16x16x32_bf16 v[212:215], v[76:79], a[84:87], v[212:215]
v_mfma_f32_16x16x32_bf16 v[228:231], v[72:75], a[72:75], v[228:231]
ds_read_b64_tr_b16 v[68:69], v96 offset:12288
s_waitcnt vmcnt(14)
v_mfma_f32_16x16x32_bf16 v[228:231], v[76:79], a[88:91], v[228:231]
ds_read_b64_tr_b16 v[70:71], v96 offset:12544
ds_read_b128 a[96:99], v38 offset:32768
v_mfma_f32_16x16x32_bf16 v[244:247], v[72:75], a[76:79], v[244:247]
buffer_load_dwordx4 v106, s[80:83], s48 offen offset:2048 lds
v_mfma_f32_16x16x32_bf16 v[244:247], v[76:79], a[92:95], v[244:247]
s_waitcnt lgkmcnt(9)
v_mfma_f32_16x16x32_bf16 v[200:203], v[80:83], a[64:67], v[200:203]
ds_read_b64_tr_b16 v[72:73], v97 offset:4096
ds_read_b128 a[100:103], v38 offset:34816
v_mfma_f32_16x16x32_bf16 v[200:203], v[84:87], a[80:83], v[200:203]
ds_read_b64_tr_b16 v[74:75], v97 offset:4352
ds_read_b128 a[104:107], v38 offset:36864
v_mfma_f32_16x16x32_bf16 v[216:219], v[80:83], a[68:71], v[216:219]
buffer_load_dwordx4 v107, s[80:83], s48 offen offset:3072 lds
s_add_u32 m0, s75, 65536
v_mfma_f32_16x16x32_bf16 v[216:219], v[84:87], a[84:87], v[216:219]
s_xor_b32 s48, s44, 640
v_mfma_f32_16x16x32_bf16 v[232:235], v[80:83], a[72:75], v[232:235]
ds_read_b64_tr_b16 v[76:77], v97 offset:12288
ds_read_b128 a[108:111], v38 offset:38912
v_mfma_f32_16x16x32_bf16 v[232:235], v[84:87], a[88:91], v[232:235]
ds_read_b64_tr_b16 v[78:79], v97 offset:12544
ds_read_b128 a[112:115], v39 offset:32768
v_mfma_f32_16x16x32_bf16 v[248:251], v[80:83], a[76:79], v[248:251]
buffer_load_dwordx4 v112, s[84:87], s48 offen offset:0 lds
v_mfma_f32_16x16x32_bf16 v[248:251], v[84:87], a[92:95], v[248:251]
buffer_load_dwordx4 v113, s[84:87], s48 offen offset:1024 lds
s_waitcnt lgkmcnt(13)
v_mfma_f32_16x16x32_bf16 v[204:207], v[88:91], a[64:67], v[204:207]
ds_read_b64_tr_b16 v[80:81], v96 offset:4608
ds_read_b128 a[116:119], v39 offset:34816
v_mfma_f32_16x16x32_bf16 v[204:207], v[92:95], a[80:83], v[204:207]
ds_read_b64_tr_b16 v[82:83], v96 offset:4864
ds_read_b128 a[120:123], v39 offset:36864
v_mfma_f32_16x16x32_bf16 v[220:223], v[88:91], a[68:71], v[220:223]
v_mfma_f32_16x16x32_bf16 v[220:223], v[92:95], a[84:87], v[220:223]
buffer_load_dwordx4 v114, s[84:87], s48 offen offset:2048 lds
v_mfma_f32_16x16x32_bf16 v[236:239], v[88:91], a[72:75], v[236:239]
ds_read_b64_tr_b16 v[84:85], v96 offset:12800
ds_read_b128 a[124:127], v39 offset:38912
v_mfma_f32_16x16x32_bf16 v[236:239], v[92:95], a[88:91], v[236:239]
ds_read_b64_tr_b16 v[86:87], v96 offset:13056
v_mfma_f32_16x16x32_bf16 v[252:255], v[88:91], a[76:79], v[252:255]
buffer_load_dwordx4 v115, s[84:87], s48 offen offset:3072 lds
v_mfma_f32_16x16x32_bf16 v[252:255], v[92:95], a[92:95], v[252:255]
s_add_u32 m0, s75, 69632
s_waitcnt lgkmcnt(14)
v_mfma_f32_16x16x32_bf16 v[128:131], v[64:67], a[64:67], v[128:131]
ds_read_b64_tr_b16 v[88:89], v97 offset:4608
s_xor_b32 s48, s46, 640
v_mfma_f32_16x16x32_bf16 v[128:131], v[68:71], a[80:83], v[128:131]
ds_read_b64_tr_b16 v[90:91], v97 offset:4864
v_mfma_f32_16x16x32_bf16 v[144:147], v[64:67], a[68:71], v[144:147]
buffer_load_dwordx4 v112, s[84:87], s48 offen offset:0 lds
v_mfma_f32_16x16x32_bf16 v[144:147], v[68:71], a[84:87], v[144:147]
v_mfma_f32_16x16x32_bf16 v[160:163], v[64:67], a[72:75], v[160:163]
ds_read_b64_tr_b16 v[92:93], v97 offset:12800
v_mfma_f32_16x16x32_bf16 v[160:163], v[68:71], a[88:91], v[160:163]
ds_read_b64_tr_b16 v[94:95], v97 offset:13056
v_mfma_f32_16x16x32_bf16 v[176:179], v[64:67], a[76:79], v[176:179]
buffer_load_dwordx4 v113, s[84:87], s48 offen offset:1024 lds
v_mfma_f32_16x16x32_bf16 v[176:179], v[68:71], a[92:95], v[176:179]
s_waitcnt lgkmcnt(12)
v_mfma_f32_16x16x32_bf16 v[132:135], v[72:75], a[64:67], v[132:135]
ds_read_b64_tr_b16 v[64:65], v96 offset:16384
v_mfma_f32_16x16x32_bf16 v[132:135], v[76:79], a[80:83], v[132:135]
ds_read_b64_tr_b16 v[66:67], v96 offset:16640
v_mfma_f32_16x16x32_bf16 v[148:151], v[72:75], a[68:71], v[148:151]
buffer_load_dwordx4 v114, s[84:87], s48 offen offset:2048 lds
v_mfma_f32_16x16x32_bf16 v[148:151], v[76:79], a[84:87], v[148:151]
v_mfma_f32_16x16x32_bf16 v[164:167], v[72:75], a[72:75], v[164:167]
ds_read_b64_tr_b16 v[68:69], v96 offset:24576
v_mfma_f32_16x16x32_bf16 v[164:167], v[76:79], a[88:91], v[164:167]
ds_read_b64_tr_b16 v[70:71], v96 offset:24832
v_mfma_f32_16x16x32_bf16 v[180:183], v[72:75], a[76:79], v[180:183]
buffer_load_dwordx4 v115, s[84:87], s48 offen offset:3072 lds
v_mfma_f32_16x16x32_bf16 v[180:183], v[76:79], a[92:95], v[180:183]
s_waitcnt lgkmcnt(8)
v_mfma_f32_16x16x32_bf16 v[136:139], v[80:83], a[64:67], v[136:139]
ds_read_b64_tr_b16 v[72:73], v97 offset:16384
v_mfma_f32_16x16x32_bf16 v[136:139], v[84:87], a[80:83], v[136:139]
ds_read_b64_tr_b16 v[74:75], v97 offset:16640
v_mfma_f32_16x16x32_bf16 v[152:155], v[80:83], a[68:71], v[152:155]
global_store_dwordx4 v63, v[2:5], s[10:11] offset:0 sc1
v_mfma_f32_16x16x32_bf16 v[152:155], v[84:87], a[84:87], v[152:155]
s_waitcnt vmcnt(17)
v_mfma_f32_16x16x32_bf16 v[168:171], v[80:83], a[72:75], v[168:171]
ds_read_b64_tr_b16 v[76:77], v97 offset:24576
ds_read_b128 a[128:131], v40 offset:0
v_mfma_f32_16x16x32_bf16 v[168:171], v[84:87], a[88:91], v[168:171]
ds_read_b64_tr_b16 v[78:79], v97 offset:24832
ds_read_b128 a[132:135], v40 offset:2048
v_mfma_f32_16x16x32_bf16 v[184:187], v[80:83], a[76:79], v[184:187]
global_store_dwordx4 v63, v[6:9], s[10:11] offset:2048 sc1
v_mfma_f32_16x16x32_bf16 v[184:187], v[84:87], a[92:95], v[184:187]
s_add_u32 m0, s75, 98304
s_waitcnt lgkmcnt(10)
v_mfma_f32_16x16x32_bf16 v[140:143], v[88:91], a[64:67], v[140:143]
ds_read_b64_tr_b16 v[80:81], v96 offset:16896
ds_read_b128 a[136:139], v40 offset:4096
v_mfma_f32_16x16x32_bf16 v[140:143], v[92:95], a[80:83], v[140:143]
ds_read_b64_tr_b16 v[82:83], v96 offset:17152
ds_read_b128 a[140:143], v40 offset:6144
v_mfma_f32_16x16x32_bf16 v[156:159], v[88:91], a[68:71], v[156:159]
s_xor_b32 s48, s44, 768
v_mfma_f32_16x16x32_bf16 v[156:159], v[92:95], a[84:87], v[156:159]
buffer_load_dwordx4 v112, s[84:87], s48 offen offset:0 lds
v_mfma_f32_16x16x32_bf16 v[172:175], v[88:91], a[72:75], v[172:175]
ds_read_b64_tr_b16 v[84:85], v96 offset:25088
ds_read_b128 a[144:147], v41 offset:0
v_mfma_f32_16x16x32_bf16 v[172:175], v[92:95], a[88:91], v[172:175]
ds_read_b64_tr_b16 v[86:87], v96 offset:25344
ds_read_b128 a[148:151], v41 offset:2048
v_mfma_f32_16x16x32_bf16 v[188:191], v[88:91], a[76:79], v[188:191]
s_cmp_lg_u32 %3, 0
s_cbranch_scc1 W0S80_%=
s_mov_b64 exec, s[56:57]
s_waitcnt vmcnt(16)
global_store_dwordx2 v25, v[102:103], s[92:93] sc1
s_mov_b64 exec, -1
W0S80_%=:
v_mfma_f32_16x16x32_bf16 v[188:191], v[92:95], a[92:95], v[188:191]
buffer_load_dwordx4 v113, s[84:87], s48 offen offset:1024 lds
s_waitcnt lgkmcnt(14)
v_mfma_f32_16x16x32_bf16 v[192:195], v[64:67], a[96:99], v[192:195]
ds_read_b64_tr_b16 v[88:89], v97 offset:16896
ds_read_b128 a[152:155], v41 offset:4096
v_mfma_f32_16x16x32_bf16 v[192:195], v[68:71], a[112:115], v[192:195]
ds_read_b64_tr_b16 v[90:91], v97 offset:17152
ds_read_b128 a[156:159], v41 offset:6144
v_mfma_f32_16x16x32_bf16 v[208:211], v[64:67], a[100:103], v[208:211]
buffer_load_dwordx4 v114, s[84:87], s48 offen offset:2048 lds
v_mfma_f32_16x16x32_bf16 v[208:211], v[68:71], a[116:119], v[208:211]
v_mfma_f32_16x16x32_bf16 v[224:227], v[64:67], a[104:107], v[224:227]
ds_read_b64_tr_b16 v[92:93], v97 offset:25088
v_mfma_f32_16x16x32_bf16 v[224:227], v[68:71], a[120:123], v[224:227]
ds_read_b64_tr_b16 v[94:95], v97 offset:25344
v_mfma_f32_16x16x32_bf16 v[240:243], v[64:67], a[108:111], v[240:243]
buffer_load_dwordx4 v115, s[84:87], s48 offen offset:3072 lds
s_add_u32 m0, s75, 102400
v_mfma_f32_16x16x32_bf16 v[240:243], v[68:71], a[124:127], v[240:243]
s_xor_b32 s48, s46, 768
s_waitcnt lgkmcnt(14)
v_mfma_f32_16x16x32_bf16 v[196:199], v[72:75], a[96:99], v[196:199]
ds_read_b64_tr_b16 v[64:65], v96 offset:20480
v_mfma_f32_16x16x32_bf16 v[196:199], v[76:79], a[112:115], v[196:199]
ds_read_b64_tr_b16 v[66:67], v96 offset:20736
v_mfma_f32_16x16x32_bf16 v[212:215], v[72:75], a[100:103], v[212:215]
buffer_load_dwordx4 v112, s[84:87], s48 offen offset:0 lds
v_mfma_f32_16x16x32_bf16 v[212:215], v[76:79], a[116:119], v[212:215]
v_mfma_f32_16x16x32_bf16 v[228:231], v[72:75], a[104:107], v[228:231]
ds_read_b64_tr_b16 v[68:69], v96 offset:28672
v_mfma_f32_16x16x32_bf16 v[228:231], v[76:79], a[120:123], v[228:231]
ds_read_b64_tr_b16 v[70:71], v96 offset:28928
v_mfma_f32_16x16x32_bf16 v[244:247], v[72:75], a[108:111], v[244:247]
buffer_load_dwordx4 v113, s[84:87], s48 offen offset:1024 lds
v_mfma_f32_16x16x32_bf16 v[244:247], v[76:79], a[124:127], v[244:247]
buffer_load_dwordx4 v114, s[84:87], s48 offen offset:2048 lds
s_waitcnt lgkmcnt(11)
v_mfma_f32_16x16x32_bf16 v[200:203], v[80:83], a[96:99], v[200:203]
ds_read_b64_tr_b16 v[72:73], v97 offset:20480
v_mfma_f32_16x16x32_bf16 v[200:203], v[84:87], a[112:115], v[200:203]
ds_read_b64_tr_b16 v[74:75], v97 offset:20736
v_mfma_f32_16x16x32_bf16 v[216:219], v[80:83], a[100:103], v[216:219]
v_mfma_f32_16x16x32_bf16 v[216:219], v[84:87], a[116:119], v[216:219]
buffer_load_dwordx4 v115, s[84:87], s48 offen offset:3072 lds
s_waitcnt lgkmcnt(8)
s_add_u32 m0, s75, 131072
v_mfma_f32_16x16x32_bf16 v[232:235], v[80:83], a[104:107], v[232:235]
ds_read_b64_tr_b16 v[76:77], v97 offset:28672
s_xor_b32 s48, s44, 896
v_mfma_f32_16x16x32_bf16 v[232:235], v[84:87], a[120:123], v[232:235]
ds_read_b64_tr_b16 v[78:79], v97 offset:28928
v_mfma_f32_16x16x32_bf16 v[248:251], v[80:83], a[108:111], v[248:251]
buffer_load_dwordx4 v112, s[84:87], s48 offen offset:0 lds
v_mfma_f32_16x16x32_bf16 v[248:251], v[84:87], a[124:127], v[248:251]
s_waitcnt lgkmcnt(8)
v_mfma_f32_16x16x32_bf16 v[204:207], v[88:91], a[96:99], v[204:207]
ds_read_b64_tr_b16 v[80:81], v96 offset:20992
v_mfma_f32_16x16x32_bf16 v[204:207], v[92:95], a[112:115], v[204:207]
ds_read_b64_tr_b16 v[82:83], v96 offset:21248
v_mfma_f32_16x16x32_bf16 v[220:223], v[88:91], a[100:103], v[220:223]
buffer_load_dwordx4 v113, s[84:87], s48 offen offset:1024 lds
v_mfma_f32_16x16x32_bf16 v[220:223], v[92:95], a[116:119], v[220:223]
v_mfma_f32_16x16x32_bf16 v[236:239], v[88:91], a[104:107], v[236:239]
ds_read_b64_tr_b16 v[84:85], v96 offset:29184
v_mfma_f32_16x16x32_bf16 v[236:239], v[92:95], a[120:123], v[236:239]
ds_read_b64_tr_b16 v[86:87], v96 offset:29440
v_mfma_f32_16x16x32_bf16 v[252:255], v[88:91], a[108:111], v[252:255]
buffer_load_dwordx4 v114, s[84:87], s48 offen offset:2048 lds
v_mfma_f32_16x16x32_bf16 v[252:255], v[92:95], a[124:127], v[252:255]
s_waitcnt lgkmcnt(8)
v_mfma_f32_16x16x32_bf16 v[128:131], v[64:67], a[96:99], v[128:131]
ds_read_b64_tr_b16 v[88:89], v97 offset:20992
v_mfma_f32_16x16x32_bf16 v[128:131], v[68:71], a[112:115], v[128:131]
ds_read_b64_tr_b16 v[90:91], v97 offset:21248
v_mfma_f32_16x16x32_bf16 v[144:147], v[64:67], a[100:103], v[144:147]
buffer_load_dwordx4 v115, s[84:87], s48 offen offset:3072 lds
s_add_u32 m0, s75, 135168
v_mfma_f32_16x16x32_bf16 v[144:147], v[68:71], a[116:119], v[144:147]
s_xor_b32 s48, s46, 896
v_mfma_f32_16x16x32_bf16 v[160:163], v[64:67], a[104:107], v[160:163]
ds_read_b64_tr_b16 v[92:93], v97 offset:29184
v_mfma_f32_16x16x32_bf16 v[160:163], v[68:71], a[120:123], v[160:163]
ds_read_b64_tr_b16 v[94:95], v97 offset:29440
v_mfma_f32_16x16x32_bf16 v[176:179], v[64:67], a[108:111], v[176:179]
buffer_load_dwordx4 v112, s[84:87], s48 offen offset:0 lds
v_mfma_f32_16x16x32_bf16 v[176:179], v[68:71], a[124:127], v[176:179]
s_waitcnt lgkmcnt(8)
v_mfma_f32_16x16x32_bf16 v[132:135], v[72:75], a[96:99], v[132:135]
buffer_load_dwordx4 v113, s[84:87], s48 offen offset:1024 lds
v_mfma_f32_16x16x32_bf16 v[132:135], v[76:79], a[112:115], v[132:135]
v_mfma_f32_16x16x32_bf16 v[148:151], v[72:75], a[100:103], v[148:151]
v_mfma_f32_16x16x32_bf16 v[148:151], v[76:79], a[116:119], v[148:151]
buffer_load_dwordx4 v114, s[84:87], s48 offen offset:2048 lds
s_waitcnt vmcnt(17)
v_mfma_f32_16x16x32_bf16 v[164:167], v[72:75], a[104:107], v[164:167]
v_mfma_f32_16x16x32_bf16 v[164:167], v[76:79], a[120:123], v[164:167]
s_waitcnt lgkmcnt(0)
s_barrier
v_mfma_f32_16x16x32_bf16 v[180:183], v[72:75], a[108:111], v[180:183]
ds_read_b64_tr_b16 v[64:65], v96 offset:32768
ds_read_b128 a[160:163], v38 offset:0
v_mfma_f32_16x16x32_bf16 v[180:183], v[76:79], a[124:127], v[180:183]
)"
R"(ds_read_b64_tr_b16 v[66:67], v96 offset:33024
v_mfma_f32_16x16x32_bf16 v[136:139], v[80:83], a[96:99], v[136:139]
ds_read_b64_tr_b16 v[68:69], v96 offset:40960
ds_read_b128 a[164:167], v38 offset:2048
v_mfma_f32_16x16x32_bf16 v[136:139], v[84:87], a[112:115], v[136:139]
ds_read_b64_tr_b16 v[70:71], v96 offset:41216
ds_read_b128 a[168:171], v38 offset:4096
v_mfma_f32_16x16x32_bf16 v[152:155], v[80:83], a[100:103], v[152:155]
ds_read_b64_tr_b16 v[72:73], v97 offset:32768
ds_read_b128 a[172:175], v38 offset:6144
v_mfma_f32_16x16x32_bf16 v[152:155], v[84:87], a[116:119], v[152:155]
ds_read_b64_tr_b16 v[74:75], v97 offset:33024
ds_read_b128 a[176:179], v39 offset:0
v_mfma_f32_16x16x32_bf16 v[168:171], v[80:83], a[104:107], v[168:171]
ds_read_b64_tr_b16 v[76:77], v97 offset:40960
ds_read_b128 a[180:183], v39 offset:2048
v_mfma_f32_16x16x32_bf16 v[168:171], v[84:87], a[120:123], v[168:171]
ds_read_b64_tr_b16 v[78:79], v97 offset:41216
ds_read_b128 a[184:187], v39 offset:4096
v_mfma_f32_16x16x32_bf16 v[184:187], v[80:83], a[108:111], v[184:187]
buffer_load_dwordx4 v115, s[84:87], s48 offen offset:3072 lds
PAD29_%=:
s_cbranch_execz PAD11_%=
v_mfma_f32_16x16x32_bf16 v[184:187], v[84:87], a[124:127], v[184:187]
v_mfma_f32_16x16x32_bf16 v[140:143], v[88:91], a[96:99], v[140:143]
ds_read_b64_tr_b16 v[80:81], v96 offset:33280
ds_read_b128 a[188:191], v39 offset:6144
v_mfma_f32_16x16x32_bf16 v[140:143], v[92:95], a[112:115], v[140:143]
ds_read_b64_tr_b16 v[82:83], v96 offset:33536
v_mfma_f32_16x16x32_bf16 v[156:159], v[88:91], a[100:103], v[156:159]
v_mfma_f32_16x16x32_bf16 v[156:159], v[92:95], a[116:119], v[156:159]
v_mfma_f32_16x16x32_bf16 v[172:175], v[88:91], a[104:107], v[172:175]
ds_read_b64_tr_b16 v[84:85], v96 offset:41472
v_mfma_f32_16x16x32_bf16 v[172:175], v[92:95], a[120:123], v[172:175]
ds_read_b64_tr_b16 v[86:87], v96 offset:41728
v_mfma_f32_16x16x32_bf16 v[188:191], v[88:91], a[108:111], v[188:191]
v_mfma_f32_16x16x32_bf16 v[188:191], v[92:95], a[124:127], v[188:191]
s_waitcnt lgkmcnt(14)
v_mfma_f32_16x16x32_bf16 v[192:195], v[64:67], a[128:131], v[192:195]
ds_read_b64_tr_b16 v[88:89], v97 offset:33280
v_mfma_f32_16x16x32_bf16 v[192:195], v[68:71], a[144:147], v[192:195]
ds_read_b64_tr_b16 v[90:91], v97 offset:33536
v_mfma_f32_16x16x32_bf16 v[208:211], v[64:67], a[132:135], v[208:211]
v_mfma_f32_16x16x32_bf16 v[208:211], v[68:71], a[148:151], v[208:211]
v_mfma_f32_16x16x32_bf16 v[224:227], v[64:67], a[136:139], v[224:227]
ds_read_b64_tr_b16 v[92:93], v97 offset:41472
s_waitcnt lgkmcnt(6)
s_add_u32 m0, s75, 65536
v_mfma_f32_16x16x32_bf16 v[224:227], v[68:71], a[152:155], v[224:227]
ds_read_b64_tr_b16 v[94:95], v97 offset:41728
s_xor_b32 s48, s66, 3145728
v_mfma_f32_16x16x32_bf16 v[240:243], v[64:67], a[140:143], v[240:243]
buffer_load_dwordx4 v104, s[80:83], s48 offen offset:0 lds
v_mfma_f32_16x16x32_bf16 v[240:243], v[68:71], a[156:159], v[240:243]
v_mfma_f32_16x16x32_bf16 v[196:199], v[72:75], a[128:131], v[196:199]
ds_read_b64_tr_b16 v[64:65], v96 offset:36864
s_cmp_lg_u32 %3, 0
s_cbranch_scc1 W0S81_%=
s_mov_b64 exec, 15
global_load_dword v24, v100, s[92:93] offset:0 sc1
s_mov_b64 exec, -1
W0S81_%=:
v_mfma_f32_16x16x32_bf16 v[196:199], v[76:79], a[144:147], v[196:199]
ds_read_b64_tr_b16 v[66:67], v96 offset:37120
v_mfma_f32_16x16x32_bf16 v[212:215], v[72:75], a[132:135], v[212:215]
buffer_load_dwordx4 v105, s[80:83], s48 offen offset:1024 lds
v_mfma_f32_16x16x32_bf16 v[212:215], v[76:79], a[148:151], v[212:215]
buffer_load_dwordx4 v106, s[80:83], s48 offen offset:2048 lds
v_mfma_f32_16x16x32_bf16 v[228:231], v[72:75], a[136:139], v[228:231]
ds_read_b64_tr_b16 v[68:69], v96 offset:45056
v_mfma_f32_16x16x32_bf16 v[228:231], v[76:79], a[152:155], v[228:231]
ds_read_b64_tr_b16 v[70:71], v96 offset:45312
v_mfma_f32_16x16x32_bf16 v[244:247], v[72:75], a[140:143], v[244:247]
v_mfma_f32_16x16x32_bf16 v[244:247], v[76:79], a[156:159], v[244:247]
buffer_load_dwordx4 v107, s[80:83], s48 offen offset:3072 lds
s_waitcnt lgkmcnt(8)
v_mfma_f32_16x16x32_bf16 v[200:203], v[80:83], a[128:131], v[200:203]
ds_read_b64_tr_b16 v[72:73], v97 offset:36864
s_waitcnt vmcnt(12)
v_mfma_f32_16x16x32_bf16 v[200:203], v[84:87], a[144:147], v[200:203]
ds_read_b64_tr_b16 v[74:75], v97 offset:37120
ds_read_b128 a[192:195], v38 offset:32768
v_mfma_f32_16x16x32_bf16 v[216:219], v[80:83], a[132:135], v[216:219]
s_add_u32 m0, s75, 69632
s_xor_b32 s48, s66, 3145856
v_mfma_f32_16x16x32_bf16 v[216:219], v[84:87], a[148:151], v[216:219]
buffer_load_dwordx4 v104, s[80:83], s48 offen offset:0 lds
v_mfma_f32_16x16x32_bf16 v[232:235], v[80:83], a[136:139], v[232:235]
ds_read_b64_tr_b16 v[76:77], v97 offset:45056
ds_read_b128 a[196:199], v38 offset:34816
v_mfma_f32_16x16x32_bf16 v[232:235], v[84:87], a[152:155], v[232:235]
ds_read_b64_tr_b16 v[78:79], v97 offset:45312
ds_read_b128 a[200:203], v38 offset:36864
v_mfma_f32_16x16x32_bf16 v[248:251], v[80:83], a[140:143], v[248:251]
buffer_load_dwordx4 v105, s[80:83], s48 offen offset:1024 lds
v_mfma_f32_16x16x32_bf16 v[248:251], v[84:87], a[156:159], v[248:251]
s_waitcnt lgkmcnt(11)
v_mfma_f32_16x16x32_bf16 v[204:207], v[88:91], a[128:131], v[204:207]
ds_read_b64_tr_b16 v[80:81], v96 offset:37376
ds_read_b128 a[204:207], v38 offset:38912
v_mfma_f32_16x16x32_bf16 v[204:207], v[92:95], a[144:147], v[204:207]
ds_read_b64_tr_b16 v[82:83], v96 offset:37632
ds_read_b128 a[208:211], v39 offset:32768
v_mfma_f32_16x16x32_bf16 v[220:223], v[88:91], a[132:135], v[220:223]
buffer_load_dwordx4 v106, s[80:83], s48 offen offset:2048 lds
v_mfma_f32_16x16x32_bf16 v[220:223], v[92:95], a[148:151], v[220:223]
v_mfma_f32_16x16x32_bf16 v[236:239], v[88:91], a[136:139], v[236:239]
ds_read_b64_tr_b16 v[84:85], v96 offset:45568
ds_read_b128 a[212:215], v39 offset:34816
v_mfma_f32_16x16x32_bf16 v[236:239], v[92:95], a[152:155], v[236:239]
ds_read_b64_tr_b16 v[86:87], v96 offset:45824
ds_read_b128 a[216:219], v39 offset:36864
v_mfma_f32_16x16x32_bf16 v[252:255], v[88:91], a[140:143], v[252:255]
buffer_load_dwordx4 v107, s[80:83], s48 offen offset:3072 lds
v_mfma_f32_16x16x32_bf16 v[252:255], v[92:95], a[156:159], v[252:255]
s_waitcnt lgkmcnt(14)
v_mfma_f32_16x16x32_bf16 v[128:131], v[64:67], a[128:131], v[128:131]
ds_read_b64_tr_b16 v[88:89], v97 offset:37376
ds_read_b128 a[220:223], v39 offset:38912
v_mfma_f32_16x16x32_bf16 v[128:131], v[68:71], a[144:147], v[128:131]
ds_read_b64_tr_b16 v[90:91], v97 offset:37632
v_mfma_f32_16x16x32_bf16 v[144:147], v[64:67], a[132:135], v[144:147]
v_mfma_f32_16x16x32_bf16 v[144:147], v[68:71], a[148:151], v[144:147]
v_mfma_f32_16x16x32_bf16 v[160:163], v[64:67], a[136:139], v[160:163]
ds_read_b64_tr_b16 v[92:93], v97 offset:45568
v_mfma_f32_16x16x32_bf16 v[160:163], v[68:71], a[152:155], v[160:163]
ds_read_b64_tr_b16 v[94:95], v97 offset:45824
v_mfma_f32_16x16x32_bf16 v[176:179], v[64:67], a[140:143], v[176:179]
v_mfma_f32_16x16x32_bf16 v[176:179], v[68:71], a[156:159], v[176:179]
s_waitcnt lgkmcnt(14)
v_mfma_f32_16x16x32_bf16 v[132:135], v[72:75], a[128:131], v[132:135]
ds_read_b64_tr_b16 v[64:65], v96 offset:49152
v_mfma_f32_16x16x32_bf16 v[132:135], v[76:79], a[144:147], v[132:135]
ds_read_b64_tr_b16 v[66:67], v96 offset:49408
v_mfma_f32_16x16x32_bf16 v[148:151], v[72:75], a[132:135], v[148:151]
v_mfma_f32_16x16x32_bf16 v[148:151], v[76:79], a[148:151], v[148:151]
v_mfma_f32_16x16x32_bf16 v[164:167], v[72:75], a[136:139], v[164:167]
ds_read_b64_tr_b16 v[68:69], v96 offset:57344
v_mfma_f32_16x16x32_bf16 v[164:167], v[76:79], a[152:155], v[164:167]
ds_read_b64_tr_b16 v[70:71], v96 offset:57600
v_mfma_f32_16x16x32_bf16 v[180:183], v[72:75], a[140:143], v[180:183]
v_mfma_f32_16x16x32_bf16 v[180:183], v[76:79], a[156:159], v[180:183]
s_waitcnt lgkmcnt(10)
v_mfma_f32_16x16x32_bf16 v[136:139], v[80:83], a[128:131], v[136:139]
ds_read_b64_tr_b16 v[72:73], v97 offset:49152
v_mfma_f32_16x16x32_bf16 v[136:139], v[84:87], a[144:147], v[136:139]
ds_read_b64_tr_b16 v[74:75], v97 offset:49408
v_mfma_f32_16x16x32_bf16 v[152:155], v[80:83], a[132:135], v[152:155]
v_mfma_f32_16x16x32_bf16 v[152:155], v[84:87], a[148:151], v[152:155]
s_waitcnt vmcnt(8)
v_mfma_f32_16x16x32_bf16 v[168:171], v[80:83], a[136:139], v[168:171]
ds_read_b64_tr_b16 v[76:77], v97 offset:57344
ds_read_b128 a[224:227], v40 offset:0
v_mfma_f32_16x16x32_bf16 v[168:171], v[84:87], a[152:155], v[168:171]
ds_read_b64_tr_b16 v[78:79], v97 offset:57600
ds_read_b128 a[228:231], v40 offset:2048
v_mfma_f32_16x16x32_bf16 v[184:187], v[80:83], a[140:143], v[184:187]
v_mfma_f32_16x16x32_bf16 v[184:187], v[84:87], a[156:159], v[184:187]
s_waitcnt lgkmcnt(10)
v_mfma_f32_16x16x32_bf16 v[140:143], v[88:91], a[128:131], v[140:143]
ds_read_b64_tr_b16 v[80:81], v96 offset:49664
ds_read_b128 a[232:235], v40 offset:4096
v_mfma_f32_16x16x32_bf16 v[140:143], v[92:95], a[144:147], v[140:143]
ds_read_b64_tr_b16 v[82:83], v96 offset:49920
ds_read_b128 a[236:239], v40 offset:6144
v_mfma_f32_16x16x32_bf16 v[156:159], v[88:91], a[132:135], v[156:159]
s_cmp_lg_u32 %3, 0
s_cbranch_scc1 W0S82_%=
s_waitcnt vmcnt(7)
v_cmp_ne_u32 vcc, s55, v24
s_and_b32 s91, vcc_lo, 5
s_cbranch_scc1 HSP2S_%=
HOK2S_%=:
v_readlane_b32 s54, v24, 1
v_readlane_b32 s90, v24, 3
W0S82_%=:
v_mfma_f32_16x16x32_bf16 v[156:159], v[92:95], a[148:151], v[156:159]
s_lshl_b32 s48, s28, 8
s_mov_b64 s[80:81], s[12:13]
v_mfma_f32_16x16x32_bf16 v[172:175], v[88:91], a[136:139], v[172:175]
ds_read_b64_tr_b16 v[84:85], v96 offset:57856
ds_read_b128 a[240:243], v41 offset:0
v_mfma_f32_16x16x32_bf16 v[172:175], v[92:95], a[152:155], v[172:175]
ds_read_b64_tr_b16 v[86:87], v96 offset:58112
ds_read_b128 a[244:247], v41 offset:2048
v_mfma_f32_16x16x32_bf16 v[188:191], v[88:91], a[140:143], v[188:191]
s_waitcnt vmcnt(0)
s_barrier
v_mfma_f32_16x16x32_bf16 v[188:191], v[92:95], a[156:159], v[188:191]
s_cmp_lg_u32 %3, 0
s_cbranch_scc1 W0S83_%=
v_mov_b32 v24, 1
s_mov_b64 exec, 1
global_atomic_add v101, v24, s[92:93] sc1
s_mov_b64 exec, -1
W0S83_%=:
s_waitcnt lgkmcnt(14)
v_mfma_f32_16x16x32_bf16 v[192:195], v[64:67], a[160:163], v[192:195]
ds_read_b64_tr_b16 v[88:89], v97 offset:49664
ds_read_b128 a[248:251], v41 offset:4096
v_mfma_f32_16x16x32_bf16 v[192:195], v[68:71], a[176:179], v[192:195]
ds_read_b64_tr_b16 v[90:91], v97 offset:49920
ds_read_b128 a[252:255], v41 offset:6144
v_mfma_f32_16x16x32_bf16 v[208:211], v[64:67], a[164:167], v[208:211]
s_add_u32 s80, s80, s48
s_addc_u32 s81, s81, 0
v_mfma_f32_16x16x32_bf16 v[208:211], v[68:71], a[180:183], v[208:211]
s_and_b32 s81, s81, 0xffff
s_sub_u32 s82, 0x400000, s48
v_mfma_f32_16x16x32_bf16 v[224:227], v[64:67], a[168:171], v[224:227]
ds_read_b64_tr_b16 v[92:93], v97 offset:57856
s_xor_b32 s66, s66, s70
v_mfma_f32_16x16x32_bf16 v[224:227], v[68:71], a[184:187], v[224:227]
ds_read_b64_tr_b16 v[94:95], v97 offset:58112
s_add_u32 m0, s75, 98304
v_mfma_f32_16x16x32_bf16 v[240:243], v[64:67], a[172:175], v[240:243]
s_xor_b32 s48, s66, 0
v_mfma_f32_16x16x32_bf16 v[240:243], v[68:71], a[188:191], v[240:243]
buffer_load_dwordx4 v104, s[80:83], s48 offen offset:0 lds
s_waitcnt lgkmcnt(14)
v_mfma_f32_16x16x32_bf16 v[196:199], v[72:75], a[160:163], v[196:199]
ds_read_b64_tr_b16 v[64:65], v96 offset:53248
v_mfma_f32_16x16x32_bf16 v[196:199], v[76:79], a[176:179], v[196:199]
ds_read_b64_tr_b16 v[66:67], v96 offset:53504
v_mfma_f32_16x16x32_bf16 v[212:215], v[72:75], a[164:167], v[212:215]
buffer_load_dwordx4 v105, s[80:83], s48 offen offset:1024 lds
v_mfma_f32_16x16x32_bf16 v[212:215], v[76:79], a[180:183], v[212:215]
v_mfma_f32_16x16x32_bf16 v[228:231], v[72:75], a[168:171], v[228:231]
ds_read_b64_tr_b16 v[68:69], v96 offset:61440
v_mfma_f32_16x16x32_bf16 v[228:231], v[76:79], a[184:187], v[228:231]
ds_read_b64_tr_b16 v[70:71], v96 offset:61696
v_mfma_f32_16x16x32_bf16 v[244:247], v[72:75], a[172:175], v[244:247]
buffer_load_dwordx4 v106, s[80:83], s48 offen offset:2048 lds
v_mfma_f32_16x16x32_bf16 v[244:247], v[76:79], a[188:191], v[244:247]
s_waitcnt lgkmcnt(11)
v_mfma_f32_16x16x32_bf16 v[200:203], v[80:83], a[160:163], v[200:203]
ds_read_b64_tr_b16 v[72:73], v97 offset:53248
v_mfma_f32_16x16x32_bf16 v[200:203], v[84:87], a[176:179], v[200:203]
ds_read_b64_tr_b16 v[74:75], v97 offset:53504
v_mfma_f32_16x16x32_bf16 v[216:219], v[80:83], a[164:167], v[216:219]
buffer_load_dwordx4 v107, s[80:83], s48 offen offset:3072 lds
s_add_u32 m0, s75, 102400
v_mfma_f32_16x16x32_bf16 v[216:219], v[84:87], a[180:183], v[216:219]
s_xor_b32 s48, s66, 262144
v_mfma_f32_16x16x32_bf16 v[232:235], v[80:83], a[168:171], v[232:235]
ds_read_b64_tr_b16 v[76:77], v97 offset:61440
v_mfma_f32_16x16x32_bf16 v[232:235], v[84:87], a[184:187], v[232:235]
ds_read_b64_tr_b16 v[78:79], v97 offset:61696
v_mfma_f32_16x16x32_bf16 v[248:251], v[80:83], a[172:175], v[248:251]
buffer_load_dwordx4 v104, s[80:83], s48 offen offset:0 lds
v_mfma_f32_16x16x32_bf16 v[248:251], v[84:87], a[188:191], v[248:251]
s_waitcnt lgkmcnt(8)
v_mfma_f32_16x16x32_bf16 v[204:207], v[88:91], a[160:163], v[204:207]
ds_read_b64_tr_b16 v[80:81], v96 offset:53760
v_mfma_f32_16x16x32_bf16 v[204:207], v[92:95], a[176:179], v[204:207]
ds_read_b64_tr_b16 v[82:83], v96 offset:54016
v_mfma_f32_16x16x32_bf16 v[220:223], v[88:91], a[164:167], v[220:223]
buffer_load_dwordx4 v105, s[80:83], s48 offen offset:1024 lds
v_mfma_f32_16x16x32_bf16 v[220:223], v[92:95], a[180:183], v[220:223]
v_mfma_f32_16x16x32_bf16 v[236:239], v[88:91], a[168:171], v[236:239]
ds_read_b64_tr_b16 v[84:85], v96 offset:61952
v_mfma_f32_16x16x32_bf16 v[236:239], v[92:95], a[184:187], v[236:239]
ds_read_b64_tr_b16 v[86:87], v96 offset:62208
v_mfma_f32_16x16x32_bf16 v[252:255], v[88:91], a[172:175], v[252:255]
buffer_load_dwordx4 v106, s[80:83], s48 offen offset:2048 lds
v_mfma_f32_16x16x32_bf16 v[252:255], v[92:95], a[188:191], v[252:255]
s_waitcnt lgkmcnt(8)
v_mfma_f32_16x16x32_bf16 v[128:131], v[64:67], a[160:163], v[128:131]
ds_read_b64_tr_b16 v[88:89], v97 offset:53760
v_mfma_f32_16x16x32_bf16 v[128:131], v[68:71], a[176:179], v[128:131]
ds_read_b64_tr_b16 v[90:91], v97 offset:54016
v_mfma_f32_16x16x32_bf16 v[144:147], v[64:67], a[164:167], v[144:147]
buffer_load_dwordx4 v107, s[80:83], s48 offen offset:3072 lds
v_mfma_f32_16x16x32_bf16 v[144:147], v[68:71], a[180:183], v[144:147]
v_mfma_f32_16x16x32_bf16 v[160:163], v[64:67], a[168:171], v[160:163]
ds_read_b64_tr_b16 v[92:93], v97 offset:61952
v_mfma_f32_16x16x32_bf16 v[160:163], v[68:71], a[184:187], v[160:163]
ds_read_b64_tr_b16 v[94:95], v97 offset:62208
v_mfma_f32_16x16x32_bf16 v[176:179], v[64:67], a[172:175], v[176:179]
global_load_dword v23, v23, s[50:51]
v_mfma_f32_16x16x32_bf16 v[176:179], v[68:71], a[188:191], v[176:179]
s_waitcnt lgkmcnt(8)
v_mfma_f32_16x16x32_bf16 v[132:135], v[72:75], a[160:163], v[132:135]
v_mfma_f32_16x16x32_bf16 v[132:135], v[76:79], a[176:179], v[132:135]
v_mfma_f32_16x16x32_bf16 v[148:151], v[72:75], a[164:167], v[148:151]
v_mfma_f32_16x16x32_bf16 v[148:151], v[76:79], a[180:183], v[148:151]
v_mfma_f32_16x16x32_bf16 v[164:167], v[72:75], a[168:171], v[164:167]
v_mfma_f32_16x16x32_bf16 v[164:167], v[76:79], a[184:187], v[164:167]
s_waitcnt lgkmcnt(0)
s_barrier
v_mfma_f32_16x16x32_bf16 v[180:183], v[72:75], a[172:175], v[180:183]
ds_read_b64_tr_b16 v[64:65], v98 offset:0
v_mfma_f32_16x16x32_bf16 v[180:183], v[76:79], a[188:191], v[180:183]
ds_read_b64_tr_b16 v[66:67], v98 offset:256
v_mfma_f32_16x16x32_bf16 v[136:139], v[80:83], a[160:163], v[136:139]
ds_read_b64_tr_b16 v[68:69], v98 offset:8192
v_mfma_f32_16x16x32_bf16 v[136:139], v[84:87], a[176:179], v[136:139]
ds_read_b64_tr_b16 v[70:71], v98 offset:8448
v_mfma_f32_16x16x32_bf16 v[152:155], v[80:83], a[164:167], v[152:155]
ds_read_b64_tr_b16 v[72:73], v99 offset:0
v_mfma_f32_16x16x32_bf16 v[152:155], v[84:87], a[180:183], v[152:155]
ds_read_b64_tr_b16 v[74:75], v99 offset:256
v_mfma_f32_16x16x32_bf16 v[168:171], v[80:83], a[168:171], v[168:171]
ds_read_b64_tr_b16 v[76:77], v99 offset:8192
v_mfma_f32_16x16x32_bf16 v[168:171], v[84:87], a[184:187], v[168:171]
ds_read_b64_tr_b16 v[78:79], v99 offset:8448
v_mfma_f32_16x16x32_bf16 v[184:187], v[80:83], a[172:175], v[184:187]
v_mfma_f32_16x16x32_bf16 v[184:187], v[84:87], a[188:191], v[184:187]
v_mfma_f32_16x16x32_bf16 v[140:143], v[88:91], a[160:163], v[140:143]
ds_read_b64_tr_b16 v[80:81], v98 offset:512
v_mfma_f32_16x16x32_bf16 v[140:143], v[92:95], a[176:179], v[140:143]
ds_read_b64_tr_b16 v[82:83], v98 offset:768
v_mfma_f32_16x16x32_bf16 v[156:159], v[88:91], a[164:167], v[156:159]
v_mfma_f32_16x16x32_bf16 v[156:159], v[92:95], a[180:183], v[156:159]
v_mfma_f32_16x16x32_bf16 v[172:175], v[88:91], a[168:171], v[172:175]
ds_read_b64_tr_b16 v[84:85], v98 offset:8704
v_mfma_f32_16x16x32_bf16 v[172:175], v[92:95], a[184:187], v[172:175]
ds_read_b64_tr_b16 v[86:87], v98 offset:8960
v_mfma_f32_16x16x32_bf16 v[188:191], v[88:91], a[172:175], v[188:191]
v_mfma_f32_16x16x32_bf16 v[188:191], v[92:95], a[188:191], v[188:191]
s_waitcnt lgkmcnt(8)
v_mfma_f32_16x16x32_bf16 v[192:195], v[64:67], a[192:195], v[192:195]
ds_read_b64_tr_b16 v[88:89], v99 offset:512
v_mfma_f32_16x16x32_bf16 v[192:195], v[68:71], a[208:211], v[192:195]
ds_read_b64_tr_b16 v[90:91], v99 offset:768
v_mfma_f32_16x16x32_bf16 v[208:211], v[64:67], a[196:199], v[208:211]
v_mfma_f32_16x16x32_bf16 v[208:211], v[68:71], a[212:215], v[208:211]
v_mfma_f32_16x16x32_bf16 v[224:227], v[64:67], a[200:203], v[224:227]
ds_read_b64_tr_b16 v[92:93], v99 offset:8704
v_mfma_f32_16x16x32_bf16 v[224:227], v[68:71], a[216:219], v[224:227]
ds_read_b64_tr_b16 v[94:95], v99 offset:8960
v_mfma_f32_16x16x32_bf16 v[240:243], v[64:67], a[204:207], v[240:243]
v_mfma_f32_16x16x32_bf16 v[240:243], v[68:71], a[220:223], v[240:243]
s_waitcnt lgkmcnt(8)
v_mfma_f32_16x16x32_bf16 v[196:199], v[72:75], a[192:195], v[196:199]
ds_read_b64_tr_b16 v[64:65], v98 offset:16384
v_mfma_f32_16x16x32_bf16 v[196:199], v[76:79], a[208:211], v[196:199]
ds_read_b64_tr_b16 v[66:67], v98 offset:16640
v_mfma_f32_16x16x32_bf16 v[212:215], v[72:75], a[196:199], v[212:215]
v_mfma_f32_16x16x32_bf16 v[212:215], v[76:79], a[212:215], v[212:215]
v_mfma_f32_16x16x32_bf16 v[228:231], v[72:75], a[200:203], v[228:231]
ds_read_b64_tr_b16 v[68:69], v98 offset:24576
v_mfma_f32_16x16x32_bf16 v[228:231], v[76:79], a[216:219], v[228:231]
ds_read_b64_tr_b16 v[70:71], v98 offset:24832
v_mfma_f32_16x16x32_bf16 v[244:247], v[72:75], a[204:207], v[244:247]
v_mfma_f32_16x16x32_bf16 v[244:247], v[76:79], a[220:223], v[244:247]
s_waitcnt lgkmcnt(8)
v_mfma_f32_16x16x32_bf16 v[200:203], v[80:83], a[192:195], v[200:203]
ds_read_b64_tr_b16 v[72:73], v99 offset:16384
v_mfma_f32_16x16x32_bf16 v[200:203], v[84:87], a[208:211], v[200:203]
ds_read_b64_tr_b16 v[74:75], v99 offset:16640
v_mfma_f32_16x16x32_bf16 v[216:219], v[80:83], a[196:199], v[216:219]
global_load_dword v1, v1, s[14:15]
v_mfma_f32_16x16x32_bf16 v[216:219], v[84:87], a[212:215], v[216:219]
v_mfma_f32_16x16x32_bf16 v[232:235], v[80:83], a[200:203], v[232:235]
ds_read_b64_tr_b16 v[76:77], v99 offset:24576
v_mfma_f32_16x16x32_bf16 v[232:235], v[84:87], a[216:219], v[232:235]
ds_read_b64_tr_b16 v[78:79], v99 offset:24832
v_mfma_f32_16x16x32_bf16 v[248:251], v[80:83], a[204:207], v[248:251]
v_mfma_f32_16x16x32_bf16 v[248:251], v[84:87], a[220:223], v[248:251]
s_waitcnt lgkmcnt(8)
v_mfma_f32_16x16x32_bf16 v[204:207], v[88:91], a[192:195], v[204:207]
ds_read_b64_tr_b16 v[80:81], v98 offset:16896
v_mfma_f32_16x16x32_bf16 v[204:207], v[92:95], a[208:211], v[204:207]
ds_read_b64_tr_b16 v[82:83], v98 offset:17152
v_mfma_f32_16x16x32_bf16 v[220:223], v[88:91], a[196:199], v[220:223]
v_mfma_f32_16x16x32_bf16 v[220:223], v[92:95], a[212:215], v[220:223]
v_mfma_f32_16x16x32_bf16 v[236:239], v[88:91], a[200:203], v[236:239]
ds_read_b64_tr_b16 v[84:85], v98 offset:25088
v_mfma_f32_16x16x32_bf16 v[236:239], v[92:95], a[216:219], v[236:239]
ds_read_b64_tr_b16 v[86:87], v98 offset:25344
v_mfma_f32_16x16x32_bf16 v[252:255], v[88:91], a[204:207], v[252:255]
v_mfma_f32_16x16x32_bf16 v[252:255], v[92:95], a[220:223], v[252:255]
s_waitcnt lgkmcnt(8)
v_mfma_f32_16x16x32_bf16 v[192:195], v[64:67], a[224:227], v[192:195]
ds_read_b64_tr_b16 v[88:89], v99 offset:16896
v_mfma_f32_16x16x32_bf16 v[192:195], v[68:71], a[240:243], v[192:195]
ds_read_b64_tr_b16 v[90:91], v99 offset:17152
v_mfma_f32_16x16x32_bf16 v[208:211], v[64:67], a[228:231], v[208:211]
v_mfma_f32_16x16x32_bf16 v[208:211], v[68:71], a[244:247], v[208:211]
v_mfma_f32_16x16x32_bf16 v[224:227], v[64:67], a[232:235], v[224:227]
ds_read_b64_tr_b16 v[92:93], v99 offset:25088
v_mfma_f32_16x16x32_bf16 v[224:227], v[68:71], a[248:251], v[224:227]
ds_read_b64_tr_b16 v[94:95], v99 offset:25344
v_mfma_f32_16x16x32_bf16 v[240:243], v[64:67], a[236:239], v[240:243]
v_mfma_f32_16x16x32_bf16 v[240:243], v[68:71], a[252:255], v[240:243]
s_waitcnt lgkmcnt(8)
v_mfma_f32_16x16x32_bf16 v[196:199], v[72:75], a[224:227], v[196:199]
ds_read_b64_tr_b16 v[64:65], v98 offset:4096
v_mfma_f32_16x16x32_bf16 v[196:199], v[76:79], a[240:243], v[196:199]
ds_read_b64_tr_b16 v[66:67], v98 offset:4352
v_mfma_f32_16x16x32_bf16 v[212:215], v[72:75], a[228:231], v[212:215]
v_mfma_f32_16x16x32_bf16 v[212:215], v[76:79], a[244:247], v[212:215]
v_mfma_f32_16x16x32_bf16 v[228:231], v[72:75], a[232:235], v[228:231]
ds_read_b64_tr_b16 v[68:69], v98 offset:12288
v_mfma_f32_16x16x32_bf16 v[228:231], v[76:79], a[248:251], v[228:231]
ds_read_b64_tr_b16 v[70:71], v98 offset:12544
v_mfma_f32_16x16x32_bf16 v[244:247], v[72:75], a[236:239], v[244:247]
v_mfma_f32_16x16x32_bf16 v[244:247], v[76:79], a[252:255], v[244:247]
s_waitcnt lgkmcnt(8)
)"
R"(v_mfma_f32_16x16x32_bf16 v[200:203], v[80:83], a[224:227], v[200:203]
ds_read_b64_tr_b16 v[72:73], v99 offset:4096
v_mfma_f32_16x16x32_bf16 v[200:203], v[84:87], a[240:243], v[200:203]
ds_read_b64_tr_b16 v[74:75], v99 offset:4352
v_mfma_f32_16x16x32_bf16 v[216:219], v[80:83], a[228:231], v[216:219]
v_mfma_f32_16x16x32_bf16 v[216:219], v[84:87], a[244:247], v[216:219]
v_mfma_f32_16x16x32_bf16 v[232:235], v[80:83], a[232:235], v[232:235]
ds_read_b64_tr_b16 v[76:77], v99 offset:12288
v_mfma_f32_16x16x32_bf16 v[232:235], v[84:87], a[248:251], v[232:235]
ds_read_b64_tr_b16 v[78:79], v99 offset:12544
v_mfma_f32_16x16x32_bf16 v[248:251], v[80:83], a[236:239], v[248:251]
v_mfma_f32_16x16x32_bf16 v[248:251], v[84:87], a[252:255], v[248:251]
s_waitcnt lgkmcnt(8)
v_mfma_f32_16x16x32_bf16 v[204:207], v[88:91], a[224:227], v[204:207]
ds_read_b64_tr_b16 v[80:81], v98 offset:4608
v_mfma_f32_16x16x32_bf16 v[204:207], v[92:95], a[240:243], v[204:207]
ds_read_b64_tr_b16 v[82:83], v98 offset:4864
v_mfma_f32_16x16x32_bf16 v[220:223], v[88:91], a[228:231], v[220:223]
v_mfma_f32_16x16x32_bf16 v[220:223], v[92:95], a[244:247], v[220:223]
v_mfma_f32_16x16x32_bf16 v[236:239], v[88:91], a[232:235], v[236:239]
ds_read_b64_tr_b16 v[84:85], v98 offset:12800
v_mfma_f32_16x16x32_bf16 v[236:239], v[92:95], a[248:251], v[236:239]
ds_read_b64_tr_b16 v[86:87], v98 offset:13056
v_mfma_f32_16x16x32_bf16 v[252:255], v[88:91], a[236:239], v[252:255]
v_mfma_f32_16x16x32_bf16 v[252:255], v[92:95], a[252:255], v[252:255]
s_waitcnt lgkmcnt(8)
v_mfma_f32_16x16x32_bf16 v[128:131], v[64:67], a[192:195], v[128:131]
ds_read_b64_tr_b16 v[88:89], v99 offset:4608
v_mfma_f32_16x16x32_bf16 v[128:131], v[68:71], a[208:211], v[128:131]
ds_read_b64_tr_b16 v[90:91], v99 offset:4864
v_mfma_f32_16x16x32_bf16 v[144:147], v[64:67], a[196:199], v[144:147]
v_mfma_f32_16x16x32_bf16 v[144:147], v[68:71], a[212:215], v[144:147]
v_mfma_f32_16x16x32_bf16 v[160:163], v[64:67], a[200:203], v[160:163]
ds_read_b64_tr_b16 v[92:93], v99 offset:12800
v_cvt_pk_bf16_f32 v32, v192, v193
v_mfma_f32_16x16x32_bf16 v[160:163], v[68:71], a[216:219], v[160:163]
ds_read_b64_tr_b16 v[94:95], v99 offset:13056
v_cvt_pk_bf16_f32 v33, v194, v195
v_mfma_f32_16x16x32_bf16 v[176:179], v[64:67], a[204:207], v[176:179]
v_cvt_pk_bf16_f32 v34, v196, v197
v_cvt_pk_bf16_f32 v35, v198, v199
v_mfma_f32_16x16x32_bf16 v[176:179], v[68:71], a[220:223], v[176:179]
v_cvt_pk_bf16_f32 v36, v200, v201
s_waitcnt lgkmcnt(8)
v_mfma_f32_16x16x32_bf16 v[132:135], v[72:75], a[192:195], v[132:135]
ds_read_b64_tr_b16 v[64:65], v98 offset:20480
v_mfma_f32_16x16x32_bf16 v[132:135], v[76:79], a[208:211], v[132:135]
ds_read_b64_tr_b16 v[66:67], v98 offset:20736
v_cvt_pk_bf16_f32 v37, v202, v203
v_mfma_f32_16x16x32_bf16 v[148:151], v[72:75], a[196:199], v[148:151]
v_mfma_f32_16x16x32_bf16 v[148:151], v[76:79], a[212:215], v[148:151]
v_cvt_pk_bf16_f32 v38, v204, v205
v_mfma_f32_16x16x32_bf16 v[164:167], v[72:75], a[200:203], v[164:167]
ds_read_b64_tr_b16 v[68:69], v98 offset:28672
v_mfma_f32_16x16x32_bf16 v[164:167], v[76:79], a[216:219], v[164:167]
ds_read_b64_tr_b16 v[70:71], v98 offset:28928
v_cvt_pk_bf16_f32 v39, v206, v207
v_mfma_f32_16x16x32_bf16 v[180:183], v[72:75], a[204:207], v[180:183]
v_mfma_f32_16x16x32_bf16 v[180:183], v[76:79], a[220:223], v[180:183]
v_cvt_pk_bf16_f32 v40, v208, v209
s_waitcnt lgkmcnt(8)
v_mfma_f32_16x16x32_bf16 v[136:139], v[80:83], a[192:195], v[136:139]
ds_read_b64_tr_b16 v[72:73], v99 offset:20480
v_cvt_pk_bf16_f32 v41, v210, v211
v_mfma_f32_16x16x32_bf16 v[136:139], v[84:87], a[208:211], v[136:139]
ds_read_b64_tr_b16 v[74:75], v99 offset:20736
v_mfma_f32_16x16x32_bf16 v[152:155], v[80:83], a[196:199], v[152:155]
v_cvt_pk_bf16_f32 v42, v212, v213
v_mfma_f32_16x16x32_bf16 v[152:155], v[84:87], a[212:215], v[152:155]
v_mfma_f32_16x16x32_bf16 v[168:171], v[80:83], a[200:203], v[168:171]
ds_read_b64_tr_b16 v[76:77], v99 offset:28672
v_cvt_pk_bf16_f32 v43, v214, v215
v_mfma_f32_16x16x32_bf16 v[168:171], v[84:87], a[216:219], v[168:171]
ds_read_b64_tr_b16 v[78:79], v99 offset:28928
v_mfma_f32_16x16x32_bf16 v[184:187], v[80:83], a[204:207], v[184:187]
s_add_u32 m0, s75, 0
s_xor_b32 s48, s66, 128
v_mfma_f32_16x16x32_bf16 v[184:187], v[84:87], a[220:223], v[184:187]
buffer_load_dwordx4 v104, s[80:83], s48 offen offset:0 lds
s_waitcnt lgkmcnt(8)
v_mfma_f32_16x16x32_bf16 v[140:143], v[88:91], a[192:195], v[140:143]
ds_read_b64_tr_b16 v[80:81], v98 offset:20992
v_cvt_pk_bf16_f32 v44, v216, v217
v_mfma_f32_16x16x32_bf16 v[140:143], v[92:95], a[208:211], v[140:143]
ds_read_b64_tr_b16 v[82:83], v98 offset:21248
v_cvt_pk_bf16_f32 v45, v218, v219
v_mfma_f32_16x16x32_bf16 v[156:159], v[88:91], a[196:199], v[156:159]
v_cvt_pk_bf16_f32 v46, v220, v221
v_mfma_f32_16x16x32_bf16 v[156:159], v[92:95], a[212:215], v[156:159]
global_store_dwordx4 v22, v[32:35], s[94:95] offset:-4096 sc1
v_cvt_pk_bf16_f32 v47, v222, v223
v_mfma_f32_16x16x32_bf16 v[172:175], v[88:91], a[200:203], v[172:175]
ds_read_b64_tr_b16 v[84:85], v98 offset:29184
v_mfma_f32_16x16x32_bf16 v[172:175], v[92:95], a[216:219], v[172:175]
ds_read_b64_tr_b16 v[86:87], v98 offset:29440
v_cvt_pk_bf16_f32 v48, v224, v225
v_mfma_f32_16x16x32_bf16 v[188:191], v[88:91], a[204:207], v[188:191]
v_mfma_f32_16x16x32_bf16 v[188:191], v[92:95], a[220:223], v[188:191]
v_cvt_pk_bf16_f32 v49, v226, v227
s_waitcnt lgkmcnt(8)
v_mfma_f32_16x16x32_bf16 v[128:131], v[64:67], a[224:227], v[128:131]
ds_read_b64_tr_b16 v[88:89], v99 offset:20992
v_mfma_f32_16x16x32_bf16 v[128:131], v[68:71], a[240:243], v[128:131]
ds_read_b64_tr_b16 v[90:91], v99 offset:21248
v_cvt_pk_bf16_f32 v50, v228, v229
v_mfma_f32_16x16x32_bf16 v[144:147], v[64:67], a[228:231], v[144:147]
global_store_dwordx4 v22, v[36:39], s[94:95] offset:-3072 sc1
v_mfma_f32_16x16x32_bf16 v[144:147], v[68:71], a[244:247], v[144:147]
v_cvt_pk_bf16_f32 v51, v230, v231
v_mfma_f32_16x16x32_bf16 v[160:163], v[64:67], a[232:235], v[160:163]
ds_read_b64_tr_b16 v[92:93], v99 offset:29184
v_mfma_f32_16x16x32_bf16 v[160:163], v[68:71], a[248:251], v[160:163]
ds_read_b64_tr_b16 v[94:95], v99 offset:29440
v_cvt_pk_bf16_f32 v52, v232, v233
v_mfma_f32_16x16x32_bf16 v[176:179], v[64:67], a[236:239], v[176:179]
v_mfma_f32_16x16x32_bf16 v[176:179], v[68:71], a[252:255], v[176:179]
global_store_dwordx4 v22, v[40:43], s[94:95] offset:0 sc1
s_waitcnt lgkmcnt(8)
v_mfma_f32_16x16x32_bf16 v[132:135], v[72:75], a[224:227], v[132:135]
v_cvt_pk_bf16_f32 v53, v234, v235
v_cvt_pk_bf16_f32 v54, v236, v237
v_mfma_f32_16x16x32_bf16 v[132:135], v[76:79], a[240:243], v[132:135]
v_mfma_f32_16x16x32_bf16 v[148:151], v[72:75], a[228:231], v[148:151]
v_cvt_pk_bf16_f32 v55, v238, v239
v_mfma_f32_16x16x32_bf16 v[148:151], v[76:79], a[244:247], v[148:151]
v_mfma_f32_16x16x32_bf16 v[164:167], v[72:75], a[232:235], v[164:167]
v_cvt_pk_bf16_f32 v56, v240, v241
v_mfma_f32_16x16x32_bf16 v[164:167], v[76:79], a[248:251], v[164:167]
s_waitcnt vmcnt(6) lgkmcnt(0)
s_barrier
v_mfma_f32_16x16x32_bf16 v[180:183], v[72:75], a[236:239], v[180:183]
ds_read_b64_tr_b16 v[64:65], v98 offset:32768
v_cvt_pk_bf16_f32 v57, v242, v243
v_mfma_f32_16x16x32_bf16 v[180:183], v[76:79], a[252:255], v[180:183]
ds_read_b64_tr_b16 v[66:67], v98 offset:33024
v_mfma_f32_16x16x32_bf16 v[136:139], v[80:83], a[224:227], v[136:139]
ds_read_b64_tr_b16 v[68:69], v98 offset:36864
v_cvt_pk_bf16_f32 v58, v244, v245
v_mfma_f32_16x16x32_bf16 v[136:139], v[84:87], a[240:243], v[136:139]
ds_read_b64_tr_b16 v[70:71], v98 offset:37120
v_mfma_f32_16x16x32_bf16 v[152:155], v[80:83], a[228:231], v[152:155]
ds_read_b64_tr_b16 v[72:73], v99 offset:32768
v_cvt_pk_bf16_f32 v59, v246, v247
v_mfma_f32_16x16x32_bf16 v[152:155], v[84:87], a[244:247], v[152:155]
ds_read_b64_tr_b16 v[74:75], v99 offset:33024
v_cvt_pk_bf16_f32 v60, v248, v249
v_mfma_f32_16x16x32_bf16 v[168:171], v[80:83], a[232:235], v[168:171]
ds_read_b64_tr_b16 v[76:77], v99 offset:36864
v_mfma_f32_16x16x32_bf16 v[168:171], v[84:87], a[248:251], v[168:171]
ds_read_b64_tr_b16 v[78:79], v99 offset:37120
v_cvt_pk_bf16_f32 v61, v250, v251
v_mfma_f32_16x16x32_bf16 v[184:187], v[80:83], a[236:239], v[184:187]
buffer_load_dwordx4 v105, s[80:83], s48 offen offset:1024 lds
v_mfma_f32_16x16x32_bf16 v[184:187], v[84:87], a[252:255], v[184:187]
v_cvt_pk_bf16_f32 v62, v252, v253
v_mfma_f32_16x16x32_bf16 v[140:143], v[88:91], a[224:227], v[140:143]
ds_read_b64_tr_b16 v[80:81], v98 offset:33280
v_mfma_f32_16x16x32_bf16 v[140:143], v[92:95], a[240:243], v[140:143]
ds_read_b64_tr_b16 v[82:83], v98 offset:33536
v_cvt_pk_bf16_f32 v63, v254, v255
v_mfma_f32_16x16x32_bf16 v[156:159], v[88:91], a[228:231], v[156:159]
buffer_load_dwordx4 v106, s[80:83], s48 offen offset:2048 lds
v_mfma_f32_16x16x32_bf16 v[156:159], v[92:95], a[244:247], v[156:159]
v_mfma_f32_16x16x32_bf16 v[172:175], v[88:91], a[232:235], v[172:175]
ds_read_b64_tr_b16 v[84:85], v98 offset:37376
v_mfma_f32_16x16x32_bf16 v[172:175], v[92:95], a[248:251], v[172:175]
ds_read_b64_tr_b16 v[86:87], v98 offset:37632
PAD30_%=:
s_cbranch_execz PAD12_%=
v_mfma_f32_16x16x32_bf16 v[188:191], v[88:91], a[236:239], v[188:191]
buffer_load_dwordx4 v107, s[80:83], s48 offen offset:3072 lds
s_add_u32 m0, s75, 4096
v_mfma_f32_16x16x32_bf16 v[188:191], v[92:95], a[252:255], v[188:191]
s_xor_b32 s48, s66, 262272
s_waitcnt lgkmcnt(8)
v_mfma_f32_16x16x32_bf16 v[192:195], a[0:3], v[64:67], 0
ds_read_b64_tr_b16 v[88:89], v99 offset:33280
v_mfma_f32_16x16x32_bf16 v[192:195], a[16:19], v[68:71], v[192:195]
ds_read_b64_tr_b16 v[90:91], v99 offset:33536
v_mfma_f32_16x16x32_bf16 v[208:211], a[4:7], v[64:67], 0
buffer_load_dwordx4 v104, s[80:83], s48 offen offset:0 lds
v_mfma_f32_16x16x32_bf16 v[208:211], a[20:23], v[68:71], v[208:211]
v_mfma_f32_16x16x32_bf16 v[224:227], a[8:11], v[64:67], 0
ds_read_b64_tr_b16 v[92:93], v99 offset:37376
v_mul_f32 v128, s98, v128
v_mfma_f32_16x16x32_bf16 v[224:227], a[24:27], v[68:71], v[224:227]
ds_read_b64_tr_b16 v[94:95], v99 offset:37632
v_mul_f32 v129, s98, v129
v_mfma_f32_16x16x32_bf16 v[240:243], a[12:15], v[64:67], 0
buffer_load_dwordx4 v105, s[80:83], s48 offen offset:1024 lds
v_cvt_pk_bf16_f32 v2, v128, v129
v_mfma_f32_16x16x32_bf16 v[240:243], a[28:31], v[68:71], v[240:243]
v_mul_f32 v130, s98, v130
s_waitcnt lgkmcnt(8)
v_mfma_f32_16x16x32_bf16 v[196:199], a[0:3], v[72:75], 0
ds_read_b64_tr_b16 v[64:65], v98 offset:40960
v_mul_f32 v131, s98, v131
v_mfma_f32_16x16x32_bf16 v[196:199], a[16:19], v[76:79], v[196:199]
ds_read_b64_tr_b16 v[66:67], v98 offset:41216
v_cvt_pk_bf16_f32 v3, v130, v131
v_mfma_f32_16x16x32_bf16 v[212:215], a[4:7], v[72:75], 0
buffer_load_dwordx4 v106, s[80:83], s48 offen offset:2048 lds
v_mul_f32 v132, s98, v132
v_mfma_f32_16x16x32_bf16 v[212:215], a[20:23], v[76:79], v[212:215]
v_mul_f32 v133, s98, v133
v_cvt_pk_bf16_f32 v4, v132, v133
v_mfma_f32_16x16x32_bf16 v[228:231], a[8:11], v[72:75], 0
ds_read_b64_tr_b16 v[68:69], v98 offset:45056
v_mul_f32 v134, s98, v134
v_mfma_f32_16x16x32_bf16 v[228:231], a[24:27], v[76:79], v[228:231]
ds_read_b64_tr_b16 v[70:71], v98 offset:45312
v_mul_f32 v135, s98, v135
v_mfma_f32_16x16x32_bf16 v[244:247], a[12:15], v[72:75], 0
buffer_load_dwordx4 v107, s[80:83], s48 offen offset:3072 lds
v_cvt_pk_bf16_f32 v5, v134, v135
v_mfma_f32_16x16x32_bf16 v[244:247], a[28:31], v[76:79], v[244:247]
v_mul_f32 v136, s98, v136
s_waitcnt lgkmcnt(8)
v_mfma_f32_16x16x32_bf16 v[200:203], a[0:3], v[80:83], 0
ds_read_b64_tr_b16 v[72:73], v99 offset:40960
v_mul_f32 v137, s98, v137
v_mfma_f32_16x16x32_bf16 v[200:203], a[16:19], v[84:87], v[200:203]
ds_read_b64_tr_b16 v[74:75], v99 offset:41216
v_cvt_pk_bf16_f32 v6, v136, v137
v_mfma_f32_16x16x32_bf16 v[216:219], a[4:7], v[80:83], 0
global_store_dwordx4 v22, v[44:47], s[94:95] offset:1024 sc1
v_mul_f32 v138, s98, v138
v_mfma_f32_16x16x32_bf16 v[216:219], a[20:23], v[84:87], v[216:219]
v_mul_f32 v139, s98, v139
v_cvt_pk_bf16_f32 v7, v138, v139
v_mfma_f32_16x16x32_bf16 v[232:235], a[8:11], v[80:83], 0
ds_read_b64_tr_b16 v[76:77], v99 offset:45056
v_mul_f32 v140, s98, v140
v_mfma_f32_16x16x32_bf16 v[232:235], a[24:27], v[84:87], v[232:235]
ds_read_b64_tr_b16 v[78:79], v99 offset:45312
v_mul_f32 v141, s98, v141
v_mfma_f32_16x16x32_bf16 v[248:251], a[12:15], v[80:83], 0
global_store_dwordx4 v22, v[48:51], s[96:97] offset:-4096 sc1
v_cvt_pk_bf16_f32 v8, v140, v141
v_mfma_f32_16x16x32_bf16 v[248:251], a[28:31], v[84:87], v[248:251]
v_mul_f32 v142, s98, v142
s_waitcnt lgkmcnt(8)
v_mfma_f32_16x16x32_bf16 v[204:207], a[0:3], v[88:91], 0
ds_read_b64_tr_b16 v[80:81], v98 offset:41472
v_mul_f32 v143, s98, v143
v_mfma_f32_16x16x32_bf16 v[204:207], a[16:19], v[92:95], v[204:207]
ds_read_b64_tr_b16 v[82:83], v98 offset:41728
v_cvt_pk_bf16_f32 v9, v142, v143
v_mfma_f32_16x16x32_bf16 v[220:223], a[4:7], v[88:91], 0
v_mul_f32 v144, s98, v144
v_mul_f32 v145, s98, v145
v_mfma_f32_16x16x32_bf16 v[220:223], a[20:23], v[92:95], v[220:223]
v_cvt_pk_bf16_f32 v10, v144, v145
v_mul_f32 v146, s98, v146
v_mfma_f32_16x16x32_bf16 v[236:239], a[8:11], v[88:91], 0
ds_read_b64_tr_b16 v[84:85], v98 offset:45568
s_lshl_b32 s48, s28, 8
v_mfma_f32_16x16x32_bf16 v[236:239], a[24:27], v[92:95], v[236:239]
ds_read_b64_tr_b16 v[86:87], v98 offset:45824
s_mov_b64 s[84:85], s[14:15]
v_mfma_f32_16x16x32_bf16 v[252:255], a[12:15], v[88:91], 0
s_add_u32 s84, s84, s48
s_addc_u32 s85, s85, 0
v_mfma_f32_16x16x32_bf16 v[252:255], a[28:31], v[92:95], v[252:255]
s_and_b32 s85, s85, 0xffff
s_waitcnt lgkmcnt(8)
v_mfma_f32_16x16x32_bf16 v[192:195], a[32:35], v[64:67], v[192:195]
ds_read_b64_tr_b16 v[88:89], v99 offset:41472
s_sub_u32 s86, 0x400000, s48
v_mfma_f32_16x16x32_bf16 v[192:195], a[48:51], v[68:71], v[192:195]
ds_read_b64_tr_b16 v[90:91], v99 offset:41728
s_add_u32 m0, s75, 131072
v_mfma_f32_16x16x32_bf16 v[208:211], a[36:39], v[64:67], v[208:211]
s_xor_b32 s48, s65, 0
v_mul_f32 v147, s98, v147
v_mfma_f32_16x16x32_bf16 v[208:211], a[52:55], v[68:71], v[208:211]
buffer_load_dwordx4 v104, s[84:87], s48 offen offset:0 lds
v_cvt_pk_bf16_f32 v11, v146, v147
v_mfma_f32_16x16x32_bf16 v[224:227], a[40:43], v[64:67], v[224:227]
ds_read_b64_tr_b16 v[92:93], v99 offset:45568
v_mul_f32 v148, s98, v148
v_mfma_f32_16x16x32_bf16 v[224:227], a[56:59], v[68:71], v[224:227]
ds_read_b64_tr_b16 v[94:95], v99 offset:45824
v_mul_f32 v149, s98, v149
v_mfma_f32_16x16x32_bf16 v[240:243], a[44:47], v[64:67], v[240:243]
buffer_load_dwordx4 v105, s[84:87], s48 offen offset:1024 lds
v_cvt_pk_bf16_f32 v12, v148, v149
v_mfma_f32_16x16x32_bf16 v[240:243], a[60:63], v[68:71], v[240:243]
v_mul_f32 v150, s98, v150
s_waitcnt lgkmcnt(8)
v_mfma_f32_16x16x32_bf16 v[196:199], a[32:35], v[72:75], v[196:199]
ds_read_b64_tr_b16 v[64:65], v98 offset:49152
s_and_b32 s77, s30, 4
v_mfma_f32_16x16x32_bf16 v[196:199], a[48:51], v[76:79], v[196:199]
ds_read_b64_tr_b16 v[66:67], v98 offset:49408
s_lshl_b32 s77, s77, 11
v_mfma_f32_16x16x32_bf16 v[212:215], a[36:39], v[72:75], v[212:215]
v_or_b32 v115, s77, v22
v_xor_b32 v112, 4096, v115
v_mfma_f32_16x16x32_bf16 v[212:215], a[52:55], v[76:79], v[212:215]
v_xor_b32 v113, 12288, v115
v_xor_b32 v114, 20480, v115
v_mfma_f32_16x16x32_bf16 v[228:231], a[40:43], v[72:75], v[228:231]
ds_read_b64_tr_b16 v[68:69], v98 offset:53248
v_xor_b32 v115, 28672, v115
v_mfma_f32_16x16x32_bf16 v[228:231], a[56:59], v[76:79], v[228:231]
ds_read_b64_tr_b16 v[70:71], v98 offset:53504
s_lshl_b32 s77, s29, 2
v_mfma_f32_16x16x32_bf16 v[244:247], a[44:47], v[72:75], v[244:247]
s_add_u32 s77, s77, %2
s_lshl_b32 s77, s77, 15
v_mfma_f32_16x16x32_bf16 v[244:247], a[60:63], v[76:79], v[244:247]
s_mov_b64 s[44:45], s[10:11]
s_waitcnt lgkmcnt(8)
v_mfma_f32_16x16x32_bf16 v[200:203], a[32:35], v[80:83], v[200:203]
ds_read_b64_tr_b16 v[72:73], v99 offset:49152
v_mul_f32 v151, s98, v151
v_mfma_f32_16x16x32_bf16 v[200:203], a[48:51], v[84:87], v[200:203]
ds_read_b64_tr_b16 v[74:75], v99 offset:49408
v_cvt_pk_bf16_f32 v13, v150, v151
v_mfma_f32_16x16x32_bf16 v[216:219], a[36:39], v[80:83], v[216:219]
s_add_u32 s44, s44, s77
s_addc_u32 s45, s45, 0
v_mfma_f32_16x16x32_bf16 v[216:219], a[52:55], v[84:87], v[216:219]
s_lshl_b32 s77, s29, 2
s_sub_u32 s77, 60, s77
v_mfma_f32_16x16x32_bf16 v[232:235], a[40:43], v[80:83], v[232:235]
ds_read_b64_tr_b16 v[76:77], v99 offset:53248
s_add_u32 s77, s77, %2
v_mfma_f32_16x16x32_bf16 v[232:235], a[56:59], v[84:87], v[232:235]
ds_read_b64_tr_b16 v[78:79], v99 offset:53504
s_lshl_b32 s77, s77, 15
s_cmp_lg_u32 %3, 0
s_cbranch_scc1 W0S84_%=
s_mov_b64 exec, 1
global_load_dword v24, v101, s[92:93] offset:0 sc1
s_mov_b64 exec, -1
W0S84_%=:
v_mfma_f32_16x16x32_bf16 v[248:251], a[44:47], v[80:83], v[248:251]
buffer_load_dwordx4 v106, s[84:87], s48 offen offset:2048 lds
s_mov_b64 s[46:47], s[10:11]
v_mfma_f32_16x16x32_bf16 v[248:251], a[60:63], v[84:87], v[248:251]
v_mul_f32 v152, s98, v152
s_waitcnt lgkmcnt(8)
v_mfma_f32_16x16x32_bf16 v[204:207], a[32:35], v[88:91], v[204:207]
ds_read_b64_tr_b16 v[80:81], v98 offset:49664
v_mul_f32 v153, s98, v153
v_mfma_f32_16x16x32_bf16 v[204:207], a[48:51], v[92:95], v[204:207]
ds_read_b64_tr_b16 v[82:83], v98 offset:49920
v_cvt_pk_bf16_f32 v14, v152, v153
v_mfma_f32_16x16x32_bf16 v[220:223], a[36:39], v[88:91], v[220:223]
s_add_u32 s46, s46, s77
s_addc_u32 s47, s47, 0
v_mfma_f32_16x16x32_bf16 v[220:223], a[52:55], v[92:95], v[220:223]
buffer_load_dwordx4 v107, s[84:87], s48 offen offset:3072 lds
s_add_u32 m0, s75, 135168
v_mfma_f32_16x16x32_bf16 v[236:239], a[40:43], v[88:91], v[236:239]
ds_read_b64_tr_b16 v[84:85], v98 offset:53760
s_xor_b32 s48, s65, 262144
v_mfma_f32_16x16x32_bf16 v[236:239], a[56:59], v[92:95], v[236:239]
ds_read_b64_tr_b16 v[86:87], v98 offset:54016
v_mul_f32 v154, s98, v154
v_mfma_f32_16x16x32_bf16 v[252:255], a[44:47], v[88:91], v[252:255]
buffer_load_dwordx4 v104, s[84:87], s48 offen offset:0 lds
v_mul_f32 v155, s98, v155
v_mfma_f32_16x16x32_bf16 v[252:255], a[60:63], v[92:95], v[252:255]
v_cvt_pk_bf16_f32 v15, v154, v155
s_waitcnt lgkmcnt(8)
v_mfma_f32_16x16x32_bf16 v[192:195], a[64:67], v[64:67], v[192:195]
ds_read_b64_tr_b16 v[88:89], v99 offset:49664
v_mul_f32 v156, s98, v156
v_mfma_f32_16x16x32_bf16 v[192:195], a[80:83], v[68:71], v[192:195]
ds_read_b64_tr_b16 v[90:91], v99 offset:49920
v_mul_f32 v157, s98, v157
v_mfma_f32_16x16x32_bf16 v[208:211], a[68:71], v[64:67], v[208:211]
buffer_load_dwordx4 v105, s[84:87], s48 offen offset:1024 lds
v_cvt_pk_bf16_f32 v16, v156, v157
v_mfma_f32_16x16x32_bf16 v[208:211], a[84:87], v[68:71], v[208:211]
v_mul_f32 v158, s98, v158
v_mul_f32 v159, s98, v159
v_mfma_f32_16x16x32_bf16 v[224:227], a[72:75], v[64:67], v[224:227]
ds_read_b64_tr_b16 v[92:93], v99 offset:53760
v_cvt_pk_bf16_f32 v17, v158, v159
v_mfma_f32_16x16x32_bf16 v[224:227], a[88:91], v[68:71], v[224:227]
ds_read_b64_tr_b16 v[94:95], v99 offset:54016
v_mul_f32 v160, s98, v160
v_mfma_f32_16x16x32_bf16 v[240:243], a[76:79], v[64:67], v[240:243]
buffer_load_dwordx4 v106, s[84:87], s48 offen offset:2048 lds
v_mul_f32 v161, s98, v161
v_mfma_f32_16x16x32_bf16 v[240:243], a[92:95], v[68:71], v[240:243]
v_cvt_pk_bf16_f32 v18, v160, v161
s_waitcnt lgkmcnt(8)
v_mfma_f32_16x16x32_bf16 v[196:199], a[64:67], v[72:75], v[196:199]
ds_read_b64_tr_b16 v[64:65], v98 offset:57344
v_mul_f32 v162, s98, v162
v_mfma_f32_16x16x32_bf16 v[196:199], a[80:83], v[76:79], v[196:199]
ds_read_b64_tr_b16 v[66:67], v98 offset:57600
v_mul_f32 v163, s98, v163
v_mfma_f32_16x16x32_bf16 v[212:215], a[68:71], v[72:75], v[212:215]
buffer_load_dwordx4 v107, s[84:87], s48 offen offset:3072 lds
)"
R"(v_cvt_pk_bf16_f32 v19, v162, v163
v_mfma_f32_16x16x32_bf16 v[212:215], a[84:87], v[76:79], v[212:215]
v_mul_f32 v164, s98, v164
v_mul_f32 v165, s98, v165
v_mfma_f32_16x16x32_bf16 v[228:231], a[72:75], v[72:75], v[228:231]
ds_read_b64_tr_b16 v[68:69], v98 offset:61440
v_cvt_pk_bf16_f32 v20, v164, v165
v_mfma_f32_16x16x32_bf16 v[228:231], a[88:91], v[76:79], v[228:231]
ds_read_b64_tr_b16 v[70:71], v98 offset:61696
v_mul_f32 v166, s98, v166
v_mfma_f32_16x16x32_bf16 v[244:247], a[76:79], v[72:75], v[244:247]
global_store_dwordx4 v22, v[52:55], s[96:97] offset:-3072 sc1
v_mul_f32 v167, s98, v167
v_mfma_f32_16x16x32_bf16 v[244:247], a[92:95], v[76:79], v[244:247]
v_cvt_pk_bf16_f32 v21, v166, v167
s_waitcnt lgkmcnt(8)
v_mfma_f32_16x16x32_bf16 v[200:203], a[64:67], v[80:83], v[200:203]
ds_read_b64_tr_b16 v[72:73], v99 offset:57344
v_mul_f32 v168, s98, v168
v_mfma_f32_16x16x32_bf16 v[200:203], a[80:83], v[84:87], v[200:203]
ds_read_b64_tr_b16 v[74:75], v99 offset:57600
v_mul_f32 v169, s98, v169
v_mfma_f32_16x16x32_bf16 v[216:219], a[68:71], v[80:83], v[216:219]
v_cvt_pk_bf16_f32 v28, v168, v169
v_mul_f32 v170, s98, v170
v_mfma_f32_16x16x32_bf16 v[216:219], a[84:87], v[84:87], v[216:219]
global_store_dwordx4 v22, v[56:59], s[96:97] offset:0 sc1
v_mul_f32 v171, s98, v171
v_mfma_f32_16x16x32_bf16 v[232:235], a[72:75], v[80:83], v[232:235]
ds_read_b64_tr_b16 v[76:77], v99 offset:61440
v_cvt_pk_bf16_f32 v29, v170, v171
v_mfma_f32_16x16x32_bf16 v[232:235], a[88:91], v[84:87], v[232:235]
ds_read_b64_tr_b16 v[78:79], v99 offset:61696
v_mul_f32 v172, s98, v172
v_mfma_f32_16x16x32_bf16 v[248:251], a[76:79], v[80:83], v[248:251]
v_mfma_f32_16x16x32_bf16 v[248:251], a[92:95], v[84:87], v[248:251]
v_mul_f32 v173, s98, v173
s_waitcnt lgkmcnt(8)
v_mfma_f32_16x16x32_bf16 v[204:207], a[64:67], v[88:91], v[204:207]
ds_read_b64_tr_b16 v[80:81], v98 offset:57856
v_cvt_pk_bf16_f32 v30, v172, v173
v_mfma_f32_16x16x32_bf16 v[204:207], a[80:83], v[92:95], v[204:207]
ds_read_b64_tr_b16 v[82:83], v98 offset:58112
v_mul_f32 v174, s98, v174
v_mfma_f32_16x16x32_bf16 v[220:223], a[68:71], v[88:91], v[220:223]
global_store_dwordx4 v22, v[60:63], s[96:97] offset:1024 sc1
v_mfma_f32_16x16x32_bf16 v[220:223], a[84:87], v[92:95], v[220:223]
v_mul_f32 v175, s98, v175
v_mfma_f32_16x16x32_bf16 v[236:239], a[72:75], v[88:91], v[236:239]
ds_read_b64_tr_b16 v[84:85], v98 offset:61952
s_add_u32 m0, s75, 32768
v_mfma_f32_16x16x32_bf16 v[236:239], a[88:91], v[92:95], v[236:239]
ds_read_b64_tr_b16 v[86:87], v98 offset:62208
s_xor_b32 s48, s66, 2097152
v_mfma_f32_16x16x32_bf16 v[252:255], a[76:79], v[88:91], v[252:255]
v_cvt_pk_bf16_f32 v31, v174, v175
v_mul_f32 v176, s98, v176
v_mfma_f32_16x16x32_bf16 v[252:255], a[92:95], v[92:95], v[252:255]
buffer_load_dwordx4 v104, s[80:83], s48 offen offset:0 lds
s_waitcnt lgkmcnt(8)
v_mfma_f32_16x16x32_bf16 v[192:195], a[96:99], v[64:67], v[192:195]
ds_read_b64_tr_b16 v[88:89], v99 offset:57856
v_mul_f32 v177, s98, v177
v_mfma_f32_16x16x32_bf16 v[192:195], a[112:115], v[68:71], v[192:195]
ds_read_b64_tr_b16 v[90:91], v99 offset:58112
v_cvt_pk_bf16_f32 v120, v176, v177
v_mfma_f32_16x16x32_bf16 v[208:211], a[100:103], v[64:67], v[208:211]
v_mul_f32 v178, s98, v178
v_mul_f32 v179, s98, v179
v_mfma_f32_16x16x32_bf16 v[208:211], a[116:119], v[68:71], v[208:211]
v_mfma_f32_16x16x32_bf16 v[224:227], a[104:107], v[64:67], v[224:227]
ds_read_b64_tr_b16 v[92:93], v99 offset:61952
v_cvt_pk_bf16_f32 v121, v178, v179
v_mfma_f32_16x16x32_bf16 v[224:227], a[120:123], v[68:71], v[224:227]
ds_read_b64_tr_b16 v[94:95], v99 offset:62208
v_mul_f32 v180, s98, v180
v_mfma_f32_16x16x32_bf16 v[240:243], a[108:111], v[64:67], v[240:243]
buffer_load_dwordx4 v105, s[80:83], s48 offen offset:1024 lds
v_mul_f32 v181, s98, v181
v_mfma_f32_16x16x32_bf16 v[240:243], a[124:127], v[68:71], v[240:243]
s_cmp_lg_u32 %3, 0
s_cbranch_scc1 W0S85_%=
s_waitcnt vmcnt(11)
v_cmp_ne_u32 vcc, s54, v24
s_and_b32 s91, vcc_lo, 1
s_cbranch_scc1 HSP0S_%=
HOK0S_%=:
W0S85_%=:
s_waitcnt lgkmcnt(8)
v_mfma_f32_16x16x32_bf16 v[196:199], a[96:99], v[72:75], v[196:199]
buffer_load_dwordx4 v106, s[80:83], s48 offen offset:2048 lds
v_cvt_pk_bf16_f32 v122, v180, v181
v_mfma_f32_16x16x32_bf16 v[196:199], a[112:115], v[76:79], v[196:199]
v_mul_f32 v182, s98, v182
v_mfma_f32_16x16x32_bf16 v[212:215], a[100:103], v[72:75], v[212:215]
v_mul_f32 v183, s98, v183
v_mfma_f32_16x16x32_bf16 v[212:215], a[116:119], v[76:79], v[212:215]
v_cvt_pk_bf16_f32 v123, v182, v183
v_mfma_f32_16x16x32_bf16 v[228:231], a[104:107], v[72:75], v[228:231]
buffer_load_dwordx4 v107, s[80:83], s48 offen offset:3072 lds
v_mfma_f32_16x16x32_bf16 v[228:231], a[120:123], v[76:79], v[228:231]
s_waitcnt vmcnt(17) lgkmcnt(0)
s_barrier
v_mfma_f32_16x16x32_bf16 v[244:247], a[108:111], v[72:75], v[244:247]
ds_read_b64_tr_b16 v[64:65], v96 offset:0
s_add_u32 m0, s75, 36864
v_mfma_f32_16x16x32_bf16 v[244:247], a[124:127], v[76:79], v[244:247]
ds_read_b64_tr_b16 v[66:67], v96 offset:256
v_mfma_f32_16x16x32_bf16 v[200:203], a[96:99], v[80:83], v[200:203]
ds_read_b64_tr_b16 v[68:69], v96 offset:4096
s_xor_b32 s48, s66, 2359296
v_mfma_f32_16x16x32_bf16 v[200:203], a[112:115], v[84:87], v[200:203]
ds_read_b64_tr_b16 v[70:71], v96 offset:4352
v_mul_f32 v184, s98, v184
v_mfma_f32_16x16x32_bf16 v[216:219], a[100:103], v[80:83], v[216:219]
ds_read_b64_tr_b16 v[72:73], v97 offset:0
v_mul_f32 v185, s98, v185
v_mfma_f32_16x16x32_bf16 v[216:219], a[116:119], v[84:87], v[216:219]
ds_read_b64_tr_b16 v[74:75], v97 offset:256
v_cvt_pk_bf16_f32 v124, v184, v185
v_mfma_f32_16x16x32_bf16 v[232:235], a[104:107], v[80:83], v[232:235]
ds_read_b64_tr_b16 v[76:77], v97 offset:4096
v_mul_f32 v186, s98, v186
v_mfma_f32_16x16x32_bf16 v[232:235], a[120:123], v[84:87], v[232:235]
ds_read_b64_tr_b16 v[78:79], v97 offset:4352
v_mul_f32 v187, s98, v187
v_mfma_f32_16x16x32_bf16 v[248:251], a[108:111], v[80:83], v[248:251]
buffer_load_dwordx4 v104, s[80:83], s48 offen offset:0 lds
v_cvt_pk_bf16_f32 v125, v186, v187
v_mfma_f32_16x16x32_bf16 v[248:251], a[124:127], v[84:87], v[248:251]
v_mul_f32 v188, s98, v188
v_mfma_f32_16x16x32_bf16 v[204:207], a[96:99], v[88:91], v[204:207]
ds_read_b64_tr_b16 v[80:81], v96 offset:512
v_mul_f32 v189, s98, v189
v_mfma_f32_16x16x32_bf16 v[204:207], a[112:115], v[92:95], v[204:207]
ds_read_b64_tr_b16 v[82:83], v96 offset:768
v_cvt_pk_bf16_f32 v126, v188, v189
v_mfma_f32_16x16x32_bf16 v[220:223], a[100:103], v[88:91], v[220:223]
buffer_load_dwordx4 v105, s[80:83], s48 offen offset:1024 lds
v_mul_f32 v190, s98, v190
v_mfma_f32_16x16x32_bf16 v[220:223], a[116:119], v[92:95], v[220:223]
v_mul_f32 v191, s98, v191
v_cvt_pk_bf16_f32 v127, v190, v191
v_mfma_f32_16x16x32_bf16 v[236:239], a[104:107], v[88:91], v[236:239]
ds_read_b64_tr_b16 v[84:85], v96 offset:4608
v_mfma_f32_16x16x32_bf16 v[236:239], a[120:123], v[92:95], v[236:239]
ds_read_b64_tr_b16 v[86:87], v96 offset:4864
v_mfma_f32_16x16x32_bf16 v[252:255], a[108:111], v[88:91], v[252:255]
buffer_load_dwordx4 v106, s[80:83], s48 offen offset:2048 lds
v_mfma_f32_16x16x32_bf16 v[252:255], a[124:127], v[92:95], v[252:255]
s_waitcnt lgkmcnt(8)
v_mfma_f32_16x16x32_bf16 v[128:131], a[0:3], v[64:67], 0
ds_read_b64_tr_b16 v[88:89], v97 offset:512
v_mfma_f32_16x16x32_bf16 v[128:131], a[16:19], v[68:71], v[128:131]
ds_read_b64_tr_b16 v[90:91], v97 offset:768
v_mfma_f32_16x16x32_bf16 v[144:147], a[4:7], v[64:67], 0
buffer_load_dwordx4 v107, s[80:83], s48 offen offset:3072 lds
v_mfma_f32_16x16x32_bf16 v[144:147], a[20:23], v[68:71], v[144:147]
v_mfma_f32_16x16x32_bf16 v[160:163], a[8:11], v[64:67], 0
ds_read_b64_tr_b16 v[92:93], v97 offset:4608
v_mfma_f32_16x16x32_bf16 v[160:163], a[24:27], v[68:71], v[160:163]
ds_read_b64_tr_b16 v[94:95], v97 offset:4864
v_mfma_f32_16x16x32_bf16 v[176:179], a[12:15], v[64:67], 0
global_store_dwordx4 v22, v[2:5], s[94:95] offset:-2048 sc1
v_mfma_f32_16x16x32_bf16 v[176:179], a[28:31], v[68:71], v[176:179]
s_waitcnt lgkmcnt(8)
v_mfma_f32_16x16x32_bf16 v[132:135], a[0:3], v[72:75], 0
ds_read_b64_tr_b16 v[64:65], v96 offset:8192
v_mfma_f32_16x16x32_bf16 v[132:135], a[16:19], v[76:79], v[132:135]
ds_read_b64_tr_b16 v[66:67], v96 offset:8448
v_mfma_f32_16x16x32_bf16 v[148:151], a[4:7], v[72:75], 0
global_store_dwordx4 v22, v[6:9], s[94:95] offset:-1024 sc1
v_mfma_f32_16x16x32_bf16 v[148:151], a[20:23], v[76:79], v[148:151]
v_mfma_f32_16x16x32_bf16 v[164:167], a[8:11], v[72:75], 0
ds_read_b64_tr_b16 v[68:69], v96 offset:12288
v_mfma_f32_16x16x32_bf16 v[164:167], a[24:27], v[76:79], v[164:167]
ds_read_b64_tr_b16 v[70:71], v96 offset:12544
v_mfma_f32_16x16x32_bf16 v[180:183], a[12:15], v[72:75], 0
v_mfma_f32_16x16x32_bf16 v[180:183], a[28:31], v[76:79], v[180:183]
s_waitcnt lgkmcnt(8)
v_mfma_f32_16x16x32_bf16 v[136:139], a[0:3], v[80:83], 0
ds_read_b64_tr_b16 v[72:73], v97 offset:8192
v_mfma_f32_16x16x32_bf16 v[136:139], a[16:19], v[84:87], v[136:139]
ds_read_b64_tr_b16 v[74:75], v97 offset:8448
v_mfma_f32_16x16x32_bf16 v[152:155], a[4:7], v[80:83], 0
global_store_dwordx4 v22, v[10:13], s[94:95] offset:2048 sc1
v_mfma_f32_16x16x32_bf16 v[152:155], a[20:23], v[84:87], v[152:155]
v_mfma_f32_16x16x32_bf16 v[168:171], a[8:11], v[80:83], 0
ds_read_b64_tr_b16 v[76:77], v97 offset:12288
v_mfma_f32_16x16x32_bf16 v[168:171], a[24:27], v[84:87], v[168:171]
ds_read_b64_tr_b16 v[78:79], v97 offset:12544
v_mfma_f32_16x16x32_bf16 v[184:187], a[12:15], v[80:83], 0
global_store_dwordx4 v22, v[14:17], s[94:95] offset:3072 sc1
v_mfma_f32_16x16x32_bf16 v[184:187], a[28:31], v[84:87], v[184:187]
s_waitcnt lgkmcnt(8)
v_mfma_f32_16x16x32_bf16 v[140:143], a[0:3], v[88:91], 0
ds_read_b64_tr_b16 v[80:81], v96 offset:8704
v_mfma_f32_16x16x32_bf16 v[140:143], a[16:19], v[92:95], v[140:143]
ds_read_b64_tr_b16 v[82:83], v96 offset:8960
v_mfma_f32_16x16x32_bf16 v[156:159], a[4:7], v[88:91], 0
s_add_u32 m0, s75, 65536
s_xor_b32 s48, s66, 2097280
v_mfma_f32_16x16x32_bf16 v[156:159], a[20:23], v[92:95], v[156:159]
v_mfma_f32_16x16x32_bf16 v[172:175], a[8:11], v[88:91], 0
ds_read_b64_tr_b16 v[84:85], v96 offset:12800
v_mfma_f32_16x16x32_bf16 v[172:175], a[24:27], v[92:95], v[172:175]
ds_read_b64_tr_b16 v[86:87], v96 offset:13056
v_mfma_f32_16x16x32_bf16 v[188:191], a[12:15], v[88:91], 0
buffer_load_dwordx4 v104, s[80:83], s48 offen offset:0 lds
v_mfma_f32_16x16x32_bf16 v[188:191], a[28:31], v[92:95], v[188:191]
s_waitcnt lgkmcnt(8)
v_mfma_f32_16x16x32_bf16 v[128:131], a[32:35], v[64:67], v[128:131]
ds_read_b64_tr_b16 v[88:89], v97 offset:8704
v_mfma_f32_16x16x32_bf16 v[128:131], a[48:51], v[68:71], v[128:131]
ds_read_b64_tr_b16 v[90:91], v97 offset:8960
v_mfma_f32_16x16x32_bf16 v[144:147], a[36:39], v[64:67], v[144:147]
buffer_load_dwordx4 v105, s[80:83], s48 offen offset:1024 lds
v_mfma_f32_16x16x32_bf16 v[144:147], a[52:55], v[68:71], v[144:147]
v_mfma_f32_16x16x32_bf16 v[160:163], a[40:43], v[64:67], v[160:163]
ds_read_b64_tr_b16 v[92:93], v97 offset:12800
v_mfma_f32_16x16x32_bf16 v[160:163], a[56:59], v[68:71], v[160:163]
ds_read_b64_tr_b16 v[94:95], v97 offset:13056
v_mfma_f32_16x16x32_bf16 v[176:179], a[44:47], v[64:67], v[176:179]
buffer_load_dwordx4 v106, s[80:83], s48 offen offset:2048 lds
v_mfma_f32_16x16x32_bf16 v[176:179], a[60:63], v[68:71], v[176:179]
s_waitcnt lgkmcnt(8)
v_mfma_f32_16x16x32_bf16 v[132:135], a[32:35], v[72:75], v[132:135]
ds_read_b64_tr_b16 v[64:65], v96 offset:16384
v_mfma_f32_16x16x32_bf16 v[132:135], a[48:51], v[76:79], v[132:135]
ds_read_b64_tr_b16 v[66:67], v96 offset:16640
v_mfma_f32_16x16x32_bf16 v[148:151], a[36:39], v[72:75], v[148:151]
buffer_load_dwordx4 v107, s[80:83], s48 offen offset:3072 lds
s_add_u32 m0, s75, 69632
v_mfma_f32_16x16x32_bf16 v[148:151], a[52:55], v[76:79], v[148:151]
s_xor_b32 s48, s66, 2359424
v_mfma_f32_16x16x32_bf16 v[164:167], a[40:43], v[72:75], v[164:167]
ds_read_b64_tr_b16 v[68:69], v96 offset:20480
v_mfma_f32_16x16x32_bf16 v[164:167], a[56:59], v[76:79], v[164:167]
ds_read_b64_tr_b16 v[70:71], v96 offset:20736
v_mfma_f32_16x16x32_bf16 v[180:183], a[44:47], v[72:75], v[180:183]
buffer_load_dwordx4 v104, s[80:83], s48 offen offset:0 lds
v_mfma_f32_16x16x32_bf16 v[180:183], a[60:63], v[76:79], v[180:183]
s_waitcnt lgkmcnt(8)
v_mfma_f32_16x16x32_bf16 v[136:139], a[32:35], v[80:83], v[136:139]
ds_read_b64_tr_b16 v[72:73], v97 offset:16384
v_mfma_f32_16x16x32_bf16 v[136:139], a[48:51], v[84:87], v[136:139]
ds_read_b64_tr_b16 v[74:75], v97 offset:16640
v_mfma_f32_16x16x32_bf16 v[152:155], a[36:39], v[80:83], v[152:155]
buffer_load_dwordx4 v105, s[80:83], s48 offen offset:1024 lds
v_mfma_f32_16x16x32_bf16 v[152:155], a[52:55], v[84:87], v[152:155]
v_mfma_f32_16x16x32_bf16 v[168:171], a[40:43], v[80:83], v[168:171]
ds_read_b64_tr_b16 v[76:77], v97 offset:20480
v_mfma_f32_16x16x32_bf16 v[168:171], a[56:59], v[84:87], v[168:171]
ds_read_b64_tr_b16 v[78:79], v97 offset:20736
v_mfma_f32_16x16x32_bf16 v[184:187], a[44:47], v[80:83], v[184:187]
buffer_load_dwordx4 v106, s[80:83], s48 offen offset:2048 lds
v_mfma_f32_16x16x32_bf16 v[184:187], a[60:63], v[84:87], v[184:187]
s_waitcnt lgkmcnt(8)
v_mfma_f32_16x16x32_bf16 v[140:143], a[32:35], v[88:91], v[140:143]
ds_read_b64_tr_b16 v[80:81], v96 offset:16896
v_mfma_f32_16x16x32_bf16 v[140:143], a[48:51], v[92:95], v[140:143]
ds_read_b64_tr_b16 v[82:83], v96 offset:17152
v_mfma_f32_16x16x32_bf16 v[156:159], a[36:39], v[88:91], v[156:159]
buffer_load_dwordx4 v107, s[80:83], s48 offen offset:3072 lds
v_mfma_f32_16x16x32_bf16 v[156:159], a[52:55], v[92:95], v[156:159]
v_mfma_f32_16x16x32_bf16 v[172:175], a[40:43], v[88:91], v[172:175]
ds_read_b64_tr_b16 v[84:85], v96 offset:20992
v_mfma_f32_16x16x32_bf16 v[172:175], a[56:59], v[92:95], v[172:175]
ds_read_b64_tr_b16 v[86:87], v96 offset:21248
v_mfma_f32_16x16x32_bf16 v[188:191], a[44:47], v[88:91], v[188:191]
global_store_dwordx4 v22, v[18:21], s[96:97] offset:-2048 sc1
v_mfma_f32_16x16x32_bf16 v[188:191], a[60:63], v[92:95], v[188:191]
s_waitcnt lgkmcnt(8)
v_mfma_f32_16x16x32_bf16 v[128:131], a[64:67], v[64:67], v[128:131]
ds_read_b64_tr_b16 v[88:89], v97 offset:16896
v_mfma_f32_16x16x32_bf16 v[128:131], a[80:83], v[68:71], v[128:131]
ds_read_b64_tr_b16 v[90:91], v97 offset:17152
v_mfma_f32_16x16x32_bf16 v[144:147], a[68:71], v[64:67], v[144:147]
v_mfma_f32_16x16x32_bf16 v[144:147], a[84:87], v[68:71], v[144:147]
global_store_dwordx4 v22, v[28:31], s[96:97] offset:-1024 sc1
v_mfma_f32_16x16x32_bf16 v[160:163], a[72:75], v[64:67], v[160:163]
ds_read_b64_tr_b16 v[92:93], v97 offset:20992
v_mfma_f32_16x16x32_bf16 v[160:163], a[88:91], v[68:71], v[160:163]
ds_read_b64_tr_b16 v[94:95], v97 offset:21248
v_mfma_f32_16x16x32_bf16 v[176:179], a[76:79], v[64:67], v[176:179]
v_mfma_f32_16x16x32_bf16 v[176:179], a[92:95], v[68:71], v[176:179]
s_waitcnt lgkmcnt(8)
v_mfma_f32_16x16x32_bf16 v[132:135], a[64:67], v[72:75], v[132:135]
ds_read_b64_tr_b16 v[64:65], v96 offset:24576
v_mfma_f32_16x16x32_bf16 v[132:135], a[80:83], v[76:79], v[132:135]
ds_read_b64_tr_b16 v[66:67], v96 offset:24832
v_mfma_f32_16x16x32_bf16 v[148:151], a[68:71], v[72:75], v[148:151]
global_store_dwordx4 v22, v[120:123], s[96:97] offset:2048 sc1
v_mfma_f32_16x16x32_bf16 v[148:151], a[84:87], v[76:79], v[148:151]
v_mfma_f32_16x16x32_bf16 v[164:167], a[72:75], v[72:75], v[164:167]
ds_read_b64_tr_b16 v[68:69], v96 offset:28672
v_mfma_f32_16x16x32_bf16 v[164:167], a[88:91], v[76:79], v[164:167]
ds_read_b64_tr_b16 v[70:71], v96 offset:28928
v_mfma_f32_16x16x32_bf16 v[180:183], a[76:79], v[72:75], v[180:183]
v_mfma_f32_16x16x32_bf16 v[180:183], a[92:95], v[76:79], v[180:183]
global_store_dwordx4 v22, v[124:127], s[96:97] offset:3072 sc1
s_waitcnt lgkmcnt(8)
v_mfma_f32_16x16x32_bf16 v[136:139], a[64:67], v[80:83], v[136:139]
ds_read_b64_tr_b16 v[72:73], v97 offset:24576
v_mfma_f32_16x16x32_bf16 v[136:139], a[80:83], v[84:87], v[136:139]
ds_read_b64_tr_b16 v[74:75], v97 offset:24832
v_mfma_f32_16x16x32_bf16 v[152:155], a[68:71], v[80:83], v[152:155]
s_add_u32 s94, s94, 0x1000000
s_addc_u32 s95, s95, 0
v_mfma_f32_16x16x32_bf16 v[152:155], a[84:87], v[84:87], v[152:155]
s_add_u32 s96, s96, 0x1000000
s_addc_u32 s97, s97, 0
v_mfma_f32_16x16x32_bf16 v[168:171], a[72:75], v[80:83], v[168:171]
ds_read_b64_tr_b16 v[76:77], v97 offset:28672
v_mfma_f32_16x16x32_bf16 v[168:171], a[88:91], v[84:87], v[168:171]
ds_read_b64_tr_b16 v[78:79], v97 offset:28928
v_mfma_f32_16x16x32_bf16 v[184:187], a[76:79], v[80:83], v[184:187]
global_load_dwordx4 a[0:3], v112, s[44:45] offset:-4096 sc1
v_mfma_f32_16x16x32_bf16 v[184:187], a[92:95], v[84:87], v[184:187]
s_waitcnt lgkmcnt(8)
v_mfma_f32_16x16x32_bf16 v[140:143], a[64:67], v[88:91], v[140:143]
ds_read_b64_tr_b16 v[80:81], v96 offset:25088
v_mfma_f32_16x16x32_bf16 v[140:143], a[80:83], v[92:95], v[140:143]
ds_read_b64_tr_b16 v[82:83], v96 offset:25344
v_mfma_f32_16x16x32_bf16 v[156:159], a[68:71], v[88:91], v[156:159]
global_load_dwordx4 a[4:7], v114, s[44:45] offset:-4096 sc1
v_mfma_f32_16x16x32_bf16 v[156:159], a[84:87], v[92:95], v[156:159]
v_mfma_f32_16x16x32_bf16 v[172:175], a[72:75], v[88:91], v[172:175]
ds_read_b64_tr_b16 v[84:85], v96 offset:29184
v_mfma_f32_16x16x32_bf16 v[172:175], a[88:91], v[92:95], v[172:175]
ds_read_b64_tr_b16 v[86:87], v96 offset:29440
v_mfma_f32_16x16x32_bf16 v[188:191], a[76:79], v[88:91], v[188:191]
global_load_dwordx4 a[8:11], v112, s[46:47] offset:-4096 sc1
v_mfma_f32_16x16x32_bf16 v[188:191], a[92:95], v[92:95], v[188:191]
s_waitcnt lgkmcnt(8)
v_mfma_f32_16x16x32_bf16 v[128:131], a[96:99], v[64:67], v[128:131]
ds_read_b64_tr_b16 v[88:89], v97 offset:25088
v_mfma_f32_16x16x32_bf16 v[128:131], a[112:115], v[68:71], v[128:131]
ds_read_b64_tr_b16 v[90:91], v97 offset:25344
v_mfma_f32_16x16x32_bf16 v[144:147], a[100:103], v[64:67], v[144:147]
global_load_dwordx4 a[12:15], v114, s[46:47] offset:-4096 sc1
v_mfma_f32_16x16x32_bf16 v[144:147], a[116:119], v[68:71], v[144:147]
v_mfma_f32_16x16x32_bf16 v[160:163], a[104:107], v[64:67], v[160:163]
ds_read_b64_tr_b16 v[92:93], v97 offset:29184
v_mfma_f32_16x16x32_bf16 v[160:163], a[120:123], v[68:71], v[160:163]
ds_read_b64_tr_b16 v[94:95], v97 offset:29440
v_mfma_f32_16x16x32_bf16 v[176:179], a[108:111], v[64:67], v[176:179]
global_load_dwordx4 a[16:19], v112, s[44:45] offset:-3072 sc1
PAD31_%=:
s_cbranch_execz PAD13_%=
v_mfma_f32_16x16x32_bf16 v[176:179], a[124:127], v[68:71], v[176:179]
s_waitcnt lgkmcnt(8)
v_mfma_f32_16x16x32_bf16 v[132:135], a[96:99], v[72:75], v[132:135]
v_mfma_f32_16x16x32_bf16 v[132:135], a[112:115], v[76:79], v[132:135]
global_load_dwordx4 a[20:23], v114, s[44:45] offset:-3072 sc1
v_mfma_f32_16x16x32_bf16 v[148:151], a[100:103], v[72:75], v[148:151]
v_mfma_f32_16x16x32_bf16 v[148:151], a[116:119], v[76:79], v[148:151]
v_mfma_f32_16x16x32_bf16 v[164:167], a[104:107], v[72:75], v[164:167]
v_mfma_f32_16x16x32_bf16 v[164:167], a[120:123], v[76:79], v[164:167]
s_waitcnt vmcnt(22) lgkmcnt(0)
s_barrier
v_mfma_f32_16x16x32_bf16 v[180:183], a[108:111], v[72:75], v[180:183]
ds_read_b64_tr_b16 v[64:65], v96 offset:32768
v_mfma_f32_16x16x32_bf16 v[180:183], a[124:127], v[76:79], v[180:183]
ds_read_b64_tr_b16 v[66:67], v96 offset:33024
v_mfma_f32_16x16x32_bf16 v[136:139], a[96:99], v[80:83], v[136:139]
ds_read_b64_tr_b16 v[68:69], v96 offset:36864
v_mfma_f32_16x16x32_bf16 v[136:139], a[112:115], v[84:87], v[136:139]
ds_read_b64_tr_b16 v[70:71], v96 offset:37120
v_mfma_f32_16x16x32_bf16 v[152:155], a[100:103], v[80:83], v[152:155]
ds_read_b64_tr_b16 v[72:73], v97 offset:32768
v_mfma_f32_16x16x32_bf16 v[152:155], a[116:119], v[84:87], v[152:155]
ds_read_b64_tr_b16 v[74:75], v97 offset:33024
v_mfma_f32_16x16x32_bf16 v[168:171], a[104:107], v[80:83], v[168:171]
ds_read_b64_tr_b16 v[76:77], v97 offset:36864
v_mfma_f32_16x16x32_bf16 v[168:171], a[120:123], v[84:87], v[168:171]
ds_read_b64_tr_b16 v[78:79], v97 offset:37120
v_mfma_f32_16x16x32_bf16 v[184:187], a[108:111], v[80:83], v[184:187]
global_load_dwordx4 a[24:27], v112, s[46:47] offset:-3072 sc1
v_mfma_f32_16x16x32_bf16 v[184:187], a[124:127], v[84:87], v[184:187]
v_mfma_f32_16x16x32_bf16 v[140:143], a[96:99], v[88:91], v[140:143]
ds_read_b64_tr_b16 v[80:81], v96 offset:33280
v_mfma_f32_16x16x32_bf16 v[140:143], a[112:115], v[92:95], v[140:143]
ds_read_b64_tr_b16 v[82:83], v96 offset:33536
v_mfma_f32_16x16x32_bf16 v[156:159], a[100:103], v[88:91], v[156:159]
global_load_dwordx4 a[28:31], v114, s[46:47] offset:-3072 sc1
v_mfma_f32_16x16x32_bf16 v[156:159], a[116:119], v[92:95], v[156:159]
v_mfma_f32_16x16x32_bf16 v[172:175], a[104:107], v[88:91], v[172:175]
ds_read_b64_tr_b16 v[84:85], v96 offset:37376
v_mfma_f32_16x16x32_bf16 v[172:175], a[120:123], v[92:95], v[172:175]
ds_read_b64_tr_b16 v[86:87], v96 offset:37632
v_mfma_f32_16x16x32_bf16 v[188:191], a[108:111], v[88:91], v[188:191]
global_load_dwordx4 a[32:35], v112, s[44:45] offset:-2048 sc1
v_mfma_f32_16x16x32_bf16 v[188:191], a[124:127], v[92:95], v[188:191]
s_waitcnt lgkmcnt(8)
v_mfma_f32_16x16x32_bf16 v[192:195], a[128:131], v[64:67], v[192:195]
ds_read_b64_tr_b16 v[88:89], v97 offset:33280
v_mfma_f32_16x16x32_bf16 v[192:195], a[144:147], v[68:71], v[192:195]
ds_read_b64_tr_b16 v[90:91], v97 offset:33536
v_mfma_f32_16x16x32_bf16 v[208:211], a[132:135], v[64:67], v[208:211]
)"
R"(global_load_dwordx4 a[36:39], v114, s[44:45] offset:-2048 sc1
v_mfma_f32_16x16x32_bf16 v[208:211], a[148:151], v[68:71], v[208:211]
v_mfma_f32_16x16x32_bf16 v[224:227], a[136:139], v[64:67], v[224:227]
ds_read_b64_tr_b16 v[92:93], v97 offset:37376
v_mfma_f32_16x16x32_bf16 v[224:227], a[152:155], v[68:71], v[224:227]
ds_read_b64_tr_b16 v[94:95], v97 offset:37632
v_mfma_f32_16x16x32_bf16 v[240:243], a[140:143], v[64:67], v[240:243]
global_load_dwordx4 a[40:43], v112, s[46:47] offset:-2048 sc1
v_mfma_f32_16x16x32_bf16 v[240:243], a[156:159], v[68:71], v[240:243]
s_waitcnt lgkmcnt(8)
v_mfma_f32_16x16x32_bf16 v[196:199], a[128:131], v[72:75], v[196:199]
ds_read_b64_tr_b16 v[64:65], v96 offset:40960
v_mfma_f32_16x16x32_bf16 v[196:199], a[144:147], v[76:79], v[196:199]
ds_read_b64_tr_b16 v[66:67], v96 offset:41216
v_mfma_f32_16x16x32_bf16 v[212:215], a[132:135], v[72:75], v[212:215]
global_load_dwordx4 a[44:47], v114, s[46:47] offset:-2048 sc1
v_mfma_f32_16x16x32_bf16 v[212:215], a[148:151], v[76:79], v[212:215]
v_mfma_f32_16x16x32_bf16 v[228:231], a[136:139], v[72:75], v[228:231]
ds_read_b64_tr_b16 v[68:69], v96 offset:45056
v_mfma_f32_16x16x32_bf16 v[228:231], a[152:155], v[76:79], v[228:231]
ds_read_b64_tr_b16 v[70:71], v96 offset:45312
v_mfma_f32_16x16x32_bf16 v[244:247], a[140:143], v[72:75], v[244:247]
global_load_dwordx4 a[48:51], v112, s[44:45] offset:-1024 sc1
v_mfma_f32_16x16x32_bf16 v[244:247], a[156:159], v[76:79], v[244:247]
s_waitcnt lgkmcnt(8)
v_mfma_f32_16x16x32_bf16 v[200:203], a[128:131], v[80:83], v[200:203]
ds_read_b64_tr_b16 v[72:73], v97 offset:40960
v_mfma_f32_16x16x32_bf16 v[200:203], a[144:147], v[84:87], v[200:203]
ds_read_b64_tr_b16 v[74:75], v97 offset:41216
v_mfma_f32_16x16x32_bf16 v[216:219], a[132:135], v[80:83], v[216:219]
global_load_dwordx4 a[52:55], v114, s[44:45] offset:-1024 sc1
v_mfma_f32_16x16x32_bf16 v[216:219], a[148:151], v[84:87], v[216:219]
v_mfma_f32_16x16x32_bf16 v[232:235], a[136:139], v[80:83], v[232:235]
ds_read_b64_tr_b16 v[76:77], v97 offset:45056
v_mfma_f32_16x16x32_bf16 v[232:235], a[152:155], v[84:87], v[232:235]
ds_read_b64_tr_b16 v[78:79], v97 offset:45312
v_mfma_f32_16x16x32_bf16 v[248:251], a[140:143], v[80:83], v[248:251]
global_load_dwordx4 a[56:59], v112, s[46:47] offset:-1024 sc1
v_mfma_f32_16x16x32_bf16 v[248:251], a[156:159], v[84:87], v[248:251]
s_waitcnt lgkmcnt(8)
v_mfma_f32_16x16x32_bf16 v[204:207], a[128:131], v[88:91], v[204:207]
ds_read_b64_tr_b16 v[80:81], v96 offset:41472
v_mfma_f32_16x16x32_bf16 v[204:207], a[144:147], v[92:95], v[204:207]
ds_read_b64_tr_b16 v[82:83], v96 offset:41728
v_mfma_f32_16x16x32_bf16 v[220:223], a[132:135], v[88:91], v[220:223]
global_load_dwordx4 a[60:63], v114, s[46:47] offset:-1024 sc1
v_mfma_f32_16x16x32_bf16 v[220:223], a[148:151], v[92:95], v[220:223]
v_mfma_f32_16x16x32_bf16 v[236:239], a[136:139], v[88:91], v[236:239]
ds_read_b64_tr_b16 v[84:85], v96 offset:45568
v_mfma_f32_16x16x32_bf16 v[236:239], a[152:155], v[92:95], v[236:239]
ds_read_b64_tr_b16 v[86:87], v96 offset:45824
v_mfma_f32_16x16x32_bf16 v[252:255], a[140:143], v[88:91], v[252:255]
global_load_dwordx4 a[64:67], v112, s[44:45] offset:0 sc1
v_mfma_f32_16x16x32_bf16 v[252:255], a[156:159], v[92:95], v[252:255]
s_waitcnt lgkmcnt(8)
v_mfma_f32_16x16x32_bf16 v[192:195], a[160:163], v[64:67], v[192:195]
ds_read_b64_tr_b16 v[88:89], v97 offset:41472
v_mfma_f32_16x16x32_bf16 v[192:195], a[176:179], v[68:71], v[192:195]
ds_read_b64_tr_b16 v[90:91], v97 offset:41728
v_mfma_f32_16x16x32_bf16 v[208:211], a[164:167], v[64:67], v[208:211]
global_load_dwordx4 a[68:71], v114, s[44:45] offset:0 sc1
v_mfma_f32_16x16x32_bf16 v[208:211], a[180:183], v[68:71], v[208:211]
v_mfma_f32_16x16x32_bf16 v[224:227], a[168:171], v[64:67], v[224:227]
ds_read_b64_tr_b16 v[92:93], v97 offset:45568
v_mfma_f32_16x16x32_bf16 v[224:227], a[184:187], v[68:71], v[224:227]
ds_read_b64_tr_b16 v[94:95], v97 offset:45824
v_mfma_f32_16x16x32_bf16 v[240:243], a[172:175], v[64:67], v[240:243]
global_load_dwordx4 a[72:75], v112, s[46:47] offset:0 sc1
v_mfma_f32_16x16x32_bf16 v[240:243], a[188:191], v[68:71], v[240:243]
s_waitcnt lgkmcnt(8)
v_mfma_f32_16x16x32_bf16 v[196:199], a[160:163], v[72:75], v[196:199]
ds_read_b64_tr_b16 v[64:65], v96 offset:49152
v_mfma_f32_16x16x32_bf16 v[196:199], a[176:179], v[76:79], v[196:199]
ds_read_b64_tr_b16 v[66:67], v96 offset:49408
v_mfma_f32_16x16x32_bf16 v[212:215], a[164:167], v[72:75], v[212:215]
global_load_dwordx4 a[76:79], v114, s[46:47] offset:0 sc1
v_mfma_f32_16x16x32_bf16 v[212:215], a[180:183], v[76:79], v[212:215]
v_mfma_f32_16x16x32_bf16 v[228:231], a[168:171], v[72:75], v[228:231]
ds_read_b64_tr_b16 v[68:69], v96 offset:53248
v_mfma_f32_16x16x32_bf16 v[228:231], a[184:187], v[76:79], v[228:231]
ds_read_b64_tr_b16 v[70:71], v96 offset:53504
v_mfma_f32_16x16x32_bf16 v[244:247], a[172:175], v[72:75], v[244:247]
global_load_dwordx4 a[80:83], v112, s[44:45] offset:1024 sc1
v_mfma_f32_16x16x32_bf16 v[244:247], a[188:191], v[76:79], v[244:247]
s_waitcnt lgkmcnt(8)
v_mfma_f32_16x16x32_bf16 v[200:203], a[160:163], v[80:83], v[200:203]
ds_read_b64_tr_b16 v[72:73], v97 offset:49152
v_mfma_f32_16x16x32_bf16 v[200:203], a[176:179], v[84:87], v[200:203]
ds_read_b64_tr_b16 v[74:75], v97 offset:49408
v_mfma_f32_16x16x32_bf16 v[216:219], a[164:167], v[80:83], v[216:219]
global_load_dwordx4 a[84:87], v114, s[44:45] offset:1024 sc1
v_mfma_f32_16x16x32_bf16 v[216:219], a[180:183], v[84:87], v[216:219]
v_mfma_f32_16x16x32_bf16 v[232:235], a[168:171], v[80:83], v[232:235]
ds_read_b64_tr_b16 v[76:77], v97 offset:53248
v_mfma_f32_16x16x32_bf16 v[232:235], a[184:187], v[84:87], v[232:235]
ds_read_b64_tr_b16 v[78:79], v97 offset:53504
v_mfma_f32_16x16x32_bf16 v[248:251], a[172:175], v[80:83], v[248:251]
global_load_dwordx4 a[88:91], v112, s[46:47] offset:1024 sc1
v_mfma_f32_16x16x32_bf16 v[248:251], a[188:191], v[84:87], v[248:251]
s_waitcnt lgkmcnt(8)
v_mfma_f32_16x16x32_bf16 v[204:207], a[160:163], v[88:91], v[204:207]
ds_read_b64_tr_b16 v[80:81], v96 offset:49664
s_mov_b64 s[80:81], s[16:17]
v_mfma_f32_16x16x32_bf16 v[204:207], a[176:179], v[92:95], v[204:207]
ds_read_b64_tr_b16 v[82:83], v96 offset:49920
v_mfma_f32_16x16x32_bf16 v[220:223], a[164:167], v[88:91], v[220:223]
global_load_dwordx4 a[92:95], v114, s[46:47] offset:1024 sc1
v_mfma_f32_16x16x32_bf16 v[220:223], a[180:183], v[92:95], v[220:223]
s_add_u32 s80, s80, s99
s_addc_u32 s81, s81, 0
v_mfma_f32_16x16x32_bf16 v[236:239], a[168:171], v[88:91], v[236:239]
ds_read_b64_tr_b16 v[84:85], v96 offset:53760
s_and_b32 s81, s81, 0xffff
v_mfma_f32_16x16x32_bf16 v[236:239], a[184:187], v[92:95], v[236:239]
ds_read_b64_tr_b16 v[86:87], v96 offset:54016
s_sub_u32 s82, 1835008, s99
v_mfma_f32_16x16x32_bf16 v[252:255], a[172:175], v[88:91], v[252:255]
s_add_u32 m0, s75, 98304
s_xor_b32 s48, s67, 0
v_mfma_f32_16x16x32_bf16 v[252:255], a[188:191], v[92:95], v[252:255]
buffer_load_dwordx4 v108, s[80:83], s48 offen offset:0 lds
s_waitcnt lgkmcnt(8)
v_mfma_f32_16x16x32_bf16 v[192:195], a[192:195], v[64:67], v[192:195]
ds_read_b64_tr_b16 v[88:89], v97 offset:49664
v_mfma_f32_16x16x32_bf16 v[192:195], a[208:211], v[68:71], v[192:195]
ds_read_b64_tr_b16 v[90:91], v97 offset:49920
v_mfma_f32_16x16x32_bf16 v[208:211], a[196:199], v[64:67], v[208:211]
buffer_load_dwordx4 v109, s[80:83], s48 offen offset:1024 lds
v_mfma_f32_16x16x32_bf16 v[208:211], a[212:215], v[68:71], v[208:211]
v_mfma_f32_16x16x32_bf16 v[224:227], a[200:203], v[64:67], v[224:227]
ds_read_b64_tr_b16 v[92:93], v97 offset:53760
v_mfma_f32_16x16x32_bf16 v[224:227], a[216:219], v[68:71], v[224:227]
ds_read_b64_tr_b16 v[94:95], v97 offset:54016
v_mfma_f32_16x16x32_bf16 v[240:243], a[204:207], v[64:67], v[240:243]
buffer_load_dwordx4 v110, s[80:83], s48 offen offset:2048 lds
v_mfma_f32_16x16x32_bf16 v[240:243], a[220:223], v[68:71], v[240:243]
s_waitcnt lgkmcnt(8)
v_mfma_f32_16x16x32_bf16 v[196:199], a[192:195], v[72:75], v[196:199]
ds_read_b64_tr_b16 v[64:65], v96 offset:57344
v_mfma_f32_16x16x32_bf16 v[196:199], a[208:211], v[76:79], v[196:199]
ds_read_b64_tr_b16 v[66:67], v96 offset:57600
v_mfma_f32_16x16x32_bf16 v[212:215], a[196:199], v[72:75], v[212:215]
buffer_load_dwordx4 v111, s[80:83], s48 offen offset:3072 lds
s_mov_b64 s[40:41], s[18:19]
v_mfma_f32_16x16x32_bf16 v[212:215], a[212:215], v[76:79], v[212:215]
s_add_u32 s40, s40, s99
s_addc_u32 s41, s41, 0
v_mfma_f32_16x16x32_bf16 v[228:231], a[200:203], v[72:75], v[228:231]
ds_read_b64_tr_b16 v[68:69], v96 offset:61440
s_and_b32 s41, s41, 0xffff
v_mfma_f32_16x16x32_bf16 v[228:231], a[216:219], v[76:79], v[228:231]
ds_read_b64_tr_b16 v[70:71], v96 offset:61696
s_sub_u32 s42, 1835008, s99
v_mfma_f32_16x16x32_bf16 v[244:247], a[204:207], v[72:75], v[244:247]
s_add_u32 m0, s75, 102400
s_xor_b32 s48, s67, 0
v_mfma_f32_16x16x32_bf16 v[244:247], a[220:223], v[76:79], v[244:247]
buffer_load_dwordx4 v108, s[40:43], s48 offen offset:0 lds
s_waitcnt lgkmcnt(8)
v_mfma_f32_16x16x32_bf16 v[200:203], a[192:195], v[80:83], v[200:203]
ds_read_b64_tr_b16 v[72:73], v97 offset:57344
v_mfma_f32_16x16x32_bf16 v[200:203], a[208:211], v[84:87], v[200:203]
ds_read_b64_tr_b16 v[74:75], v97 offset:57600
v_mfma_f32_16x16x32_bf16 v[216:219], a[196:199], v[80:83], v[216:219]
buffer_load_dwordx4 v109, s[40:43], s48 offen offset:1024 lds
v_mfma_f32_16x16x32_bf16 v[216:219], a[212:215], v[84:87], v[216:219]
v_mfma_f32_16x16x32_bf16 v[232:235], a[200:203], v[80:83], v[232:235]
ds_read_b64_tr_b16 v[76:77], v97 offset:61440
v_mfma_f32_16x16x32_bf16 v[232:235], a[216:219], v[84:87], v[232:235]
ds_read_b64_tr_b16 v[78:79], v97 offset:61696
v_mfma_f32_16x16x32_bf16 v[248:251], a[204:207], v[80:83], v[248:251]
buffer_load_dwordx4 v110, s[40:43], s48 offen offset:2048 lds
v_mfma_f32_16x16x32_bf16 v[248:251], a[220:223], v[84:87], v[248:251]
s_waitcnt lgkmcnt(8)
v_mfma_f32_16x16x32_bf16 v[204:207], a[192:195], v[88:91], v[204:207]
ds_read_b64_tr_b16 v[80:81], v96 offset:57856
v_mfma_f32_16x16x32_bf16 v[204:207], a[208:211], v[92:95], v[204:207]
ds_read_b64_tr_b16 v[82:83], v96 offset:58112
v_mfma_f32_16x16x32_bf16 v[220:223], a[196:199], v[88:91], v[220:223]
buffer_load_dwordx4 v111, s[40:43], s48 offen offset:3072 lds
v_mfma_f32_16x16x32_bf16 v[220:223], a[212:215], v[92:95], v[220:223]
s_add_u32 m0, s75, 0
s_xor_b32 s48, s67, 0
v_mfma_f32_16x16x32_bf16 v[236:239], a[200:203], v[88:91], v[236:239]
ds_read_b64_tr_b16 v[84:85], v96 offset:61952
s_add_u32 s48, s48, 458752
v_mfma_f32_16x16x32_bf16 v[236:239], a[216:219], v[92:95], v[236:239]
ds_read_b64_tr_b16 v[86:87], v96 offset:62208
v_mfma_f32_16x16x32_bf16 v[252:255], a[204:207], v[88:91], v[252:255]
buffer_load_dwordx4 v108, s[80:83], s48 offen offset:0 lds
v_mfma_f32_16x16x32_bf16 v[252:255], a[220:223], v[92:95], v[252:255]
s_waitcnt lgkmcnt(8)
v_mfma_f32_16x16x32_bf16 v[192:195], a[224:227], v[64:67], v[192:195]
ds_read_b64_tr_b16 v[88:89], v97 offset:57856
v_mfma_f32_16x16x32_bf16 v[192:195], a[240:243], v[68:71], v[192:195]
ds_read_b64_tr_b16 v[90:91], v97 offset:58112
v_mfma_f32_16x16x32_bf16 v[208:211], a[228:231], v[64:67], v[208:211]
buffer_load_dwordx4 v109, s[80:83], s48 offen offset:1024 lds
v_mfma_f32_16x16x32_bf16 v[208:211], a[244:247], v[68:71], v[208:211]
v_mfma_f32_16x16x32_bf16 v[224:227], a[232:235], v[64:67], v[224:227]
ds_read_b64_tr_b16 v[92:93], v97 offset:61952
v_mfma_f32_16x16x32_bf16 v[224:227], a[248:251], v[68:71], v[224:227]
ds_read_b64_tr_b16 v[94:95], v97 offset:62208
v_mfma_f32_16x16x32_bf16 v[240:243], a[236:239], v[64:67], v[240:243]
buffer_load_dwordx4 v110, s[80:83], s48 offen offset:2048 lds
v_mfma_f32_16x16x32_bf16 v[240:243], a[252:255], v[68:71], v[240:243]
s_waitcnt lgkmcnt(8)
v_mfma_f32_16x16x32_bf16 v[196:199], a[224:227], v[72:75], v[196:199]
v_mfma_f32_16x16x32_bf16 v[196:199], a[240:243], v[76:79], v[196:199]
v_mfma_f32_16x16x32_bf16 v[212:215], a[228:231], v[72:75], v[212:215]
buffer_load_dwordx4 v111, s[80:83], s48 offen offset:3072 lds
v_mfma_f32_16x16x32_bf16 v[212:215], a[244:247], v[76:79], v[212:215]
s_add_u32 m0, s75, 4096
s_xor_b32 s48, s67, 0
v_mfma_f32_16x16x32_bf16 v[228:231], a[232:235], v[72:75], v[228:231]
s_add_u32 s48, s48, 458752
v_mfma_f32_16x16x32_bf16 v[228:231], a[248:251], v[76:79], v[228:231]
s_waitcnt vmcnt(40) lgkmcnt(0)
s_barrier
v_mfma_f32_16x16x32_bf16 v[244:247], a[236:239], v[72:75], v[244:247]
ds_read_b64_tr_b16 v[64:65], v98 offset:0
v_mfma_f32_16x16x32_bf16 v[244:247], a[252:255], v[76:79], v[244:247]
ds_read_b64_tr_b16 v[66:67], v98 offset:256
v_mfma_f32_16x16x32_bf16 v[200:203], a[224:227], v[80:83], v[200:203]
ds_read_b64_tr_b16 v[68:69], v98 offset:4096
s_and_b32 s12, s26, 0xfc
v_mfma_f32_16x16x32_bf16 v[200:203], a[240:243], v[84:87], v[200:203]
ds_read_b64_tr_b16 v[70:71], v98 offset:4352
s_lshl1_add_u32 s12, s12, %2
v_mfma_f32_16x16x32_bf16 v[216:219], a[228:231], v[80:83], v[216:219]
ds_read_b64_tr_b16 v[72:73], v99 offset:0
s_lshl_b32 s12, s12, 14
v_mfma_f32_16x16x32_bf16 v[216:219], a[244:247], v[84:87], v[216:219]
ds_read_b64_tr_b16 v[74:75], v99 offset:256
s_and_b32 s13, s26, 3
v_mfma_f32_16x16x32_bf16 v[232:235], a[232:235], v[80:83], v[232:235]
ds_read_b64_tr_b16 v[76:77], v99 offset:4096
s_lshl2_add_u32 s13, s13, s31
v_mfma_f32_16x16x32_bf16 v[232:235], a[248:251], v[84:87], v[232:235]
ds_read_b64_tr_b16 v[78:79], v99 offset:4352
s_lshl_b32 s13, s13, 6
v_mfma_f32_16x16x32_bf16 v[248:251], a[236:239], v[80:83], v[248:251]
buffer_load_dwordx4 v108, s[40:43], s48 offen offset:0 lds
s_add_u32 s12, s12, s13
v_mfma_f32_16x16x32_bf16 v[248:251], a[252:255], v[84:87], v[248:251]
v_mfma_f32_16x16x32_bf16 v[204:207], a[224:227], v[88:91], v[204:207]
ds_read_b64_tr_b16 v[80:81], v98 offset:512
v_mfma_f32_16x16x32_bf16 v[204:207], a[240:243], v[92:95], v[204:207]
ds_read_b64_tr_b16 v[82:83], v98 offset:768
v_mfma_f32_16x16x32_bf16 v[220:223], a[228:231], v[88:91], v[220:223]
buffer_load_dwordx4 v109, s[40:43], s48 offen offset:1024 lds
v_mfma_f32_16x16x32_bf16 v[220:223], a[244:247], v[92:95], v[220:223]
s_add_u32 s4, s4, s12
s_addc_u32 s5, s5, 0
v_mfma_f32_16x16x32_bf16 v[236:239], a[232:235], v[88:91], v[236:239]
ds_read_b64_tr_b16 v[84:85], v98 offset:4608
v_mfma_f32_16x16x32_bf16 v[236:239], a[248:251], v[92:95], v[236:239]
ds_read_b64_tr_b16 v[86:87], v98 offset:4864
v_mfma_f32_16x16x32_bf16 v[252:255], a[236:239], v[88:91], v[252:255]
buffer_load_dwordx4 v110, s[40:43], s48 offen offset:2048 lds
v_mfma_f32_16x16x32_bf16 v[252:255], a[252:255], v[92:95], v[252:255]
s_waitcnt lgkmcnt(8)
v_mfma_f32_16x16x32_bf16 v[128:131], a[128:131], v[64:67], v[128:131]
ds_read_b64_tr_b16 v[88:89], v99 offset:512
v_mfma_f32_16x16x32_bf16 v[128:131], a[144:147], v[68:71], v[128:131]
ds_read_b64_tr_b16 v[90:91], v99 offset:768
v_mfma_f32_16x16x32_bf16 v[144:147], a[132:135], v[64:67], v[144:147]
buffer_load_dwordx4 v111, s[40:43], s48 offen offset:3072 lds
v_mfma_f32_16x16x32_bf16 v[144:147], a[148:151], v[68:71], v[144:147]
s_add_u32 s24, s24, s12
s_addc_u32 s25, s25, 0
v_mfma_f32_16x16x32_bf16 v[160:163], a[136:139], v[64:67], v[160:163]
ds_read_b64_tr_b16 v[92:93], v99 offset:4608
v_cvt_pk_bf16_f32 v32, v192, v193
v_mfma_f32_16x16x32_bf16 v[160:163], a[152:155], v[68:71], v[160:163]
ds_read_b64_tr_b16 v[94:95], v99 offset:4864
v_cvt_pk_bf16_f32 v33, v194, v195
v_mfma_f32_16x16x32_bf16 v[176:179], a[140:143], v[64:67], v[176:179]
global_load_dwordx4 a[96:99], v112, s[44:45] offset:2048 sc1
v_cvt_pk_bf16_f32 v34, v208, v209
v_mfma_f32_16x16x32_bf16 v[176:179], a[156:159], v[68:71], v[176:179]
v_cvt_pk_bf16_f32 v35, v210, v211
s_waitcnt lgkmcnt(8)
v_mfma_f32_16x16x32_bf16 v[132:135], a[128:131], v[72:75], v[132:135]
ds_read_b64_tr_b16 v[64:65], v98 offset:8192
v_mfma_f32_16x16x32_bf16 v[132:135], a[144:147], v[76:79], v[132:135]
ds_read_b64_tr_b16 v[66:67], v98 offset:8448
v_mfma_f32_16x16x32_bf16 v[148:151], a[132:135], v[72:75], v[148:151]
global_load_dwordx4 a[100:103], v114, s[44:45] offset:2048 sc1
v_mfma_f32_16x16x32_bf16 v[148:151], a[148:151], v[76:79], v[148:151]
v_cvt_pk_bf16_f32 v36, v224, v225
v_mfma_f32_16x16x32_bf16 v[164:167], a[136:139], v[72:75], v[164:167]
ds_read_b64_tr_b16 v[68:69], v98 offset:12288
v_mfma_f32_16x16x32_bf16 v[164:167], a[152:155], v[76:79], v[164:167]
ds_read_b64_tr_b16 v[70:71], v98 offset:12544
v_mfma_f32_16x16x32_bf16 v[180:183], a[140:143], v[72:75], v[180:183]
global_load_dwordx4 a[104:107], v112, s[46:47] offset:2048 sc1
v_mfma_f32_16x16x32_bf16 v[180:183], a[156:159], v[76:79], v[180:183]
v_cvt_pk_bf16_f32 v37, v226, v227
s_waitcnt lgkmcnt(8)
v_mfma_f32_16x16x32_bf16 v[136:139], a[128:131], v[80:83], v[136:139]
ds_read_b64_tr_b16 v[72:73], v99 offset:8192
v_mfma_f32_16x16x32_bf16 v[136:139], a[144:147], v[84:87], v[136:139]
ds_read_b64_tr_b16 v[74:75], v99 offset:8448
v_mfma_f32_16x16x32_bf16 v[152:155], a[132:135], v[80:83], v[152:155]
global_load_dwordx4 a[108:111], v114, s[46:47] offset:2048 sc1
v_mfma_f32_16x16x32_bf16 v[152:155], a[148:151], v[84:87], v[152:155]
v_cvt_pk_bf16_f32 v38, v240, v241
v_mfma_f32_16x16x32_bf16 v[168:171], a[136:139], v[80:83], v[168:171]
ds_read_b64_tr_b16 v[76:77], v99 offset:12288
v_mfma_f32_16x16x32_bf16 v[168:171], a[152:155], v[84:87], v[168:171]
ds_read_b64_tr_b16 v[78:79], v99 offset:12544
v_mfma_f32_16x16x32_bf16 v[184:187], a[140:143], v[80:83], v[184:187]
global_load_dwordx4 a[112:115], v112, s[44:45] offset:3072 sc1
v_mfma_f32_16x16x32_bf16 v[184:187], a[156:159], v[84:87], v[184:187]
v_cvt_pk_bf16_f32 v39, v242, v243
s_waitcnt lgkmcnt(8)
v_mfma_f32_16x16x32_bf16 v[140:143], a[128:131], v[88:91], v[140:143]
ds_read_b64_tr_b16 v[80:81], v98 offset:8704
v_mfma_f32_16x16x32_bf16 v[140:143], a[144:147], v[92:95], v[140:143]
ds_read_b64_tr_b16 v[82:83], v98 offset:8960
v_mfma_f32_16x16x32_bf16 v[156:159], a[132:135], v[88:91], v[156:159]
global_load_dwordx4 a[116:119], v114, s[44:45] offset:3072 sc1
v_mfma_f32_16x16x32_bf16 v[156:159], a[148:151], v[92:95], v[156:159]
v_cvt_pk_bf16_f32 v40, v196, v197
v_mfma_f32_16x16x32_bf16 v[172:175], a[136:139], v[88:91], v[172:175]
ds_read_b64_tr_b16 v[84:85], v98 offset:12800
v_mfma_f32_16x16x32_bf16 v[172:175], a[152:155], v[92:95], v[172:175]
ds_read_b64_tr_b16 v[86:87], v98 offset:13056
v_mfma_f32_16x16x32_bf16 v[188:191], a[140:143], v[88:91], v[188:191]
global_load_dwordx4 a[120:123], v112, s[46:47] offset:3072 sc1
v_cvt_pk_bf16_f32 v41, v198, v199
v_mfma_f32_16x16x32_bf16 v[188:191], a[156:159], v[92:95], v[188:191]
s_waitcnt lgkmcnt(8)
v_mfma_f32_16x16x32_bf16 v[128:131], a[160:163], v[64:67], v[128:131]
ds_read_b64_tr_b16 v[88:89], v99 offset:8704
v_mfma_f32_16x16x32_bf16 v[128:131], a[176:179], v[68:71], v[128:131]
ds_read_b64_tr_b16 v[90:91], v99 offset:8960
v_mfma_f32_16x16x32_bf16 v[144:147], a[164:167], v[64:67], v[144:147]
global_store_dwordx4 v117, v[32:35], s[94:95] offset:-4096 sc1
v_cvt_pk_bf16_f32 v42, v212, v213
v_mfma_f32_16x16x32_bf16 v[144:147], a[180:183], v[68:71], v[144:147]
s_add_u32 s6, s4, 0x10000
s_addc_u32 s7, s5, 0
v_mfma_f32_16x16x32_bf16 v[160:163], a[168:171], v[64:67], v[160:163]
ds_read_b64_tr_b16 v[92:93], v99 offset:12800
v_mfma_f32_16x16x32_bf16 v[160:163], a[184:187], v[68:71], v[160:163]
ds_read_b64_tr_b16 v[94:95], v99 offset:13056
v_mfma_f32_16x16x32_bf16 v[176:179], a[172:175], v[64:67], v[176:179]
v_cvt_pk_bf16_f32 v43, v214, v215
v_mfma_f32_16x16x32_bf16 v[176:179], a[188:191], v[68:71], v[176:179]
global_load_dwordx4 a[124:127], v114, s[46:47] offset:3072 sc1
s_waitcnt lgkmcnt(8)
v_mfma_f32_16x16x32_bf16 v[132:135], a[160:163], v[72:75], v[132:135]
ds_read_b64_tr_b16 v[64:65], v98 offset:16384
s_add_u32 m0, s75, 32768
v_mfma_f32_16x16x32_bf16 v[132:135], a[176:179], v[76:79], v[132:135]
ds_read_b64_tr_b16 v[66:67], v98 offset:16640
s_xor_b32 s48, s65, 2097152
v_mfma_f32_16x16x32_bf16 v[148:151], a[164:167], v[72:75], v[148:151]
v_cvt_pk_bf16_f32 v44, v228, v229
v_mfma_f32_16x16x32_bf16 v[148:151], a[180:183], v[76:79], v[148:151]
buffer_load_dwordx4 v104, s[84:87], s48 offen offset:0 lds
v_mfma_f32_16x16x32_bf16 v[164:167], a[168:171], v[72:75], v[164:167]
ds_read_b64_tr_b16 v[68:69], v98 offset:20480
v_mfma_f32_16x16x32_bf16 v[164:167], a[184:187], v[76:79], v[164:167]
ds_read_b64_tr_b16 v[70:71], v98 offset:20736
v_mfma_f32_16x16x32_bf16 v[180:183], a[172:175], v[72:75], v[180:183]
v_cvt_pk_bf16_f32 v45, v230, v231
v_mfma_f32_16x16x32_bf16 v[180:183], a[188:191], v[76:79], v[180:183]
buffer_load_dwordx4 v105, s[84:87], s48 offen offset:1024 lds
s_waitcnt lgkmcnt(8)
v_mfma_f32_16x16x32_bf16 v[136:139], a[160:163], v[80:83], v[136:139]
ds_read_b64_tr_b16 v[72:73], v99 offset:16384
v_mfma_f32_16x16x32_bf16 v[136:139], a[176:179], v[84:87], v[136:139]
ds_read_b64_tr_b16 v[74:75], v99 offset:16640
v_mfma_f32_16x16x32_bf16 v[152:155], a[164:167], v[80:83], v[152:155]
v_cvt_pk_bf16_f32 v46, v244, v245
v_mfma_f32_16x16x32_bf16 v[152:155], a[180:183], v[84:87], v[152:155]
global_store_dwordx4 v117, v[36:39], s[96:97] offset:-4096 sc1
v_mfma_f32_16x16x32_bf16 v[168:171], a[168:171], v[80:83], v[168:171]
ds_read_b64_tr_b16 v[76:77], v99 offset:20480
v_mfma_f32_16x16x32_bf16 v[168:171], a[184:187], v[84:87], v[168:171]
ds_read_b64_tr_b16 v[78:79], v99 offset:20736
v_cvt_pk_bf16_f32 v47, v246, v247
v_mfma_f32_16x16x32_bf16 v[184:187], a[172:175], v[80:83], v[184:187]
s_add_u32 s8, s24, 0x10000
s_addc_u32 s9, s25, 0
v_mfma_f32_16x16x32_bf16 v[184:187], a[188:191], v[84:87], v[184:187]
s_and_b32 s101, s26, 3
s_waitcnt lgkmcnt(8)
v_mfma_f32_16x16x32_bf16 v[140:143], a[160:163], v[88:91], v[140:143]
ds_read_b64_tr_b16 v[80:81], v98 offset:16896
s_lshl_b32 s101, s101, 8
v_mfma_f32_16x16x32_bf16 v[140:143], a[176:179], v[92:95], v[140:143]
ds_read_b64_tr_b16 v[82:83], v98 offset:17152
v_cvt_pk_bf16_f32 v48, v200, v201
v_mfma_f32_16x16x32_bf16 v[156:159], a[164:167], v[88:91], v[156:159]
buffer_load_dwordx4 v106, s[84:87], s48 offen offset:2048 lds
v_mfma_f32_16x16x32_bf16 v[156:159], a[180:183], v[92:95], v[156:159]
)"
R"(s_add_u32 s20, s22, s101
s_addc_u32 s21, s23, 0
v_mfma_f32_16x16x32_bf16 v[172:175], a[168:171], v[88:91], v[172:175]
ds_read_b64_tr_b16 v[84:85], v98 offset:20992
s_and_b32 s21, s21, 0xffff
v_mfma_f32_16x16x32_bf16 v[172:175], a[184:187], v[92:95], v[172:175]
ds_read_b64_tr_b16 v[86:87], v98 offset:21248
v_cvt_pk_bf16_f32 v49, v202, v203
v_mfma_f32_16x16x32_bf16 v[188:191], a[172:175], v[88:91], v[188:191]
buffer_load_dwordx4 v107, s[84:87], s48 offen offset:3072 lds
s_sub_u32 s22, 0x1c0000, s101
v_mfma_f32_16x16x32_bf16 v[188:191], a[188:191], v[92:95], v[188:191]
s_mov_b32 s23, 0x20000
s_waitcnt lgkmcnt(8)
v_mfma_f32_16x16x32_bf16 v[128:131], a[192:195], v[64:67], v[128:131]
ds_read_b64_tr_b16 v[88:89], v99 offset:16896
s_and_b32 s101, s26, 0xfc
v_mfma_f32_16x16x32_bf16 v[128:131], a[208:211], v[68:71], v[128:131]
ds_read_b64_tr_b16 v[90:91], v99 offset:17152
v_cvt_pk_bf16_f32 v50, v216, v217
v_mfma_f32_16x16x32_bf16 v[144:147], a[196:199], v[64:67], v[144:147]
global_store_dwordx4 v117, v[40:43], s[94:95] offset:-3072 sc1
s_add_u32 s101, s101, %2
v_mfma_f32_16x16x32_bf16 v[144:147], a[212:215], v[68:71], v[144:147]
s_mul_i32 s101, s101, 376832
s_mov_b64 s[12:13], s[10:11]
v_mfma_f32_16x16x32_bf16 v[160:163], a[200:203], v[64:67], v[160:163]
ds_read_b64_tr_b16 v[92:93], v99 offset:20992
s_add_u32 m0, s75, 36864
v_mfma_f32_16x16x32_bf16 v[160:163], a[216:219], v[68:71], v[160:163]
ds_read_b64_tr_b16 v[94:95], v99 offset:21248
s_xor_b32 s48, s65, 2359296
v_mfma_f32_16x16x32_bf16 v[176:179], a[204:207], v[64:67], v[176:179]
v_cvt_pk_bf16_f32 v51, v218, v219
v_mfma_f32_16x16x32_bf16 v[176:179], a[220:223], v[68:71], v[176:179]
buffer_load_dwordx4 v104, s[84:87], s48 offen offset:0 lds
s_waitcnt lgkmcnt(8)
v_mfma_f32_16x16x32_bf16 v[132:135], a[192:195], v[72:75], v[132:135]
ds_read_b64_tr_b16 v[64:65], v98 offset:24576
v_mfma_f32_16x16x32_bf16 v[132:135], a[208:211], v[76:79], v[132:135]
ds_read_b64_tr_b16 v[66:67], v98 offset:24832
v_cvt_pk_bf16_f32 v52, v232, v233
v_mfma_f32_16x16x32_bf16 v[148:151], a[196:199], v[72:75], v[148:151]
s_add_u32 s12, s12, 0x3400000
s_addc_u32 s13, s13, 0
v_mfma_f32_16x16x32_bf16 v[148:151], a[212:215], v[76:79], v[148:151]
buffer_load_dwordx4 v105, s[84:87], s48 offen offset:1024 lds
v_mfma_f32_16x16x32_bf16 v[164:167], a[200:203], v[72:75], v[164:167]
ds_read_b64_tr_b16 v[68:69], v98 offset:28672
v_mfma_f32_16x16x32_bf16 v[164:167], a[216:219], v[76:79], v[164:167]
ds_read_b64_tr_b16 v[70:71], v98 offset:28928
v_cvt_pk_bf16_f32 v53, v234, v235
v_mfma_f32_16x16x32_bf16 v[180:183], a[204:207], v[72:75], v[180:183]
s_add_u32 s12, s12, s101
s_addc_u32 s13, s13, 0
v_mfma_f32_16x16x32_bf16 v[180:183], a[220:223], v[76:79], v[180:183]
global_store_dwordx4 v117, v[44:47], s[96:97] offset:-3072 sc1
s_waitcnt lgkmcnt(8)
v_mfma_f32_16x16x32_bf16 v[136:139], a[192:195], v[80:83], v[136:139]
ds_read_b64_tr_b16 v[72:73], v99 offset:24576
v_cvt_pk_bf16_f32 v54, v248, v249
v_mfma_f32_16x16x32_bf16 v[136:139], a[208:211], v[84:87], v[136:139]
ds_read_b64_tr_b16 v[74:75], v99 offset:24832
s_and_b32 s13, s13, 0xffff
v_mfma_f32_16x16x32_bf16 v[152:155], a[196:199], v[80:83], v[152:155]
s_mov_b32 s14, 262144
s_mov_b32 s15, 0x20000
v_mfma_f32_16x16x32_bf16 v[152:155], a[212:215], v[84:87], v[152:155]
s_lshr_b32 s101, s26, 2
s_sub_u32 s59, 15, s101
v_mfma_f32_16x16x32_bf16 v[168:171], a[200:203], v[80:83], v[168:171]
ds_read_b64_tr_b16 v[76:77], v99 offset:28672
v_cvt_pk_bf16_f32 v55, v250, v251
v_mfma_f32_16x16x32_bf16 v[168:171], a[216:219], v[84:87], v[168:171]
ds_read_b64_tr_b16 v[78:79], v99 offset:28928
s_min_u32 s59, s59, s101
v_mfma_f32_16x16x32_bf16 v[184:187], a[204:207], v[80:83], v[184:187]
buffer_load_dwordx4 v106, s[84:87], s48 offen offset:2048 lds
s_lshl_b32 s59, s59, 7
v_mfma_f32_16x16x32_bf16 v[184:187], a[220:223], v[84:87], v[184:187]
s_add_u32 s59, s59, 4096
s_waitcnt lgkmcnt(8)
v_mfma_f32_16x16x32_bf16 v[140:143], a[192:195], v[88:91], v[140:143]
ds_read_b64_tr_b16 v[80:81], v98 offset:25088
v_cvt_pk_bf16_f32 v56, v204, v205
v_mfma_f32_16x16x32_bf16 v[140:143], a[208:211], v[92:95], v[140:143]
ds_read_b64_tr_b16 v[82:83], v98 offset:25344
s_mov_b32 s101, 0
v_mfma_f32_16x16x32_bf16 v[156:159], a[196:199], v[88:91], v[156:159]
buffer_load_dwordx4 v107, s[84:87], s48 offen offset:3072 lds
s_mov_b32 s100, 14
v_mfma_f32_16x16x32_bf16 v[156:159], a[212:215], v[92:95], v[156:159]
s_mov_b32 s89, 1
s_mov_b32 s99, 1
v_mfma_f32_16x16x32_bf16 v[172:175], a[200:203], v[88:91], v[172:175]
ds_read_b64_tr_b16 v[84:85], v98 offset:29184
v_cvt_pk_bf16_f32 v57, v206, v207
PAD32_%=:
s_cbranch_execz PAD14_%=
v_mfma_f32_16x16x32_bf16 v[172:175], a[216:219], v[92:95], v[172:175]
ds_read_b64_tr_b16 v[86:87], v98 offset:29440
v_mfma_f32_16x16x32_bf16 v[188:191], a[204:207], v[88:91], v[188:191]
global_load_dwordx4 a[128:131], v113, s[44:45] offset:-4096 sc1
v_mfma_f32_16x16x32_bf16 v[188:191], a[220:223], v[92:95], v[188:191]
s_waitcnt lgkmcnt(8)
v_mfma_f32_16x16x32_bf16 v[128:131], a[224:227], v[64:67], v[128:131]
ds_read_b64_tr_b16 v[88:89], v99 offset:25088
v_cvt_pk_bf16_f32 v58, v220, v221
v_mfma_f32_16x16x32_bf16 v[128:131], a[240:243], v[68:71], v[128:131]
ds_read_b64_tr_b16 v[90:91], v99 offset:25344
v_mfma_f32_16x16x32_bf16 v[144:147], a[228:231], v[64:67], v[144:147]
global_store_dwordx4 v117, v[48:51], s[94:95] offset:-2048 sc1
v_mfma_f32_16x16x32_bf16 v[144:147], a[244:247], v[68:71], v[144:147]
v_mfma_f32_16x16x32_bf16 v[160:163], a[232:235], v[64:67], v[160:163]
ds_read_b64_tr_b16 v[92:93], v99 offset:29184
v_cvt_pk_bf16_f32 v59, v222, v223
v_mfma_f32_16x16x32_bf16 v[160:163], a[248:251], v[68:71], v[160:163]
ds_read_b64_tr_b16 v[94:95], v99 offset:29440
v_mfma_f32_16x16x32_bf16 v[176:179], a[236:239], v[64:67], v[176:179]
v_mfma_f32_16x16x32_bf16 v[176:179], a[252:255], v[68:71], v[176:179]
global_load_dwordx4 a[132:135], v115, s[44:45] offset:-4096 sc1
s_waitcnt lgkmcnt(8)
v_mfma_f32_16x16x32_bf16 v[132:135], a[224:227], v[72:75], v[132:135]
v_cvt_pk_bf16_f32 v60, v236, v237
v_mfma_f32_16x16x32_bf16 v[132:135], a[240:243], v[76:79], v[132:135]
v_mfma_f32_16x16x32_bf16 v[148:151], a[228:231], v[72:75], v[148:151]
v_mfma_f32_16x16x32_bf16 v[148:151], a[244:247], v[76:79], v[148:151]
global_load_dwordx4 a[136:139], v113, s[46:47] offset:-4096 sc1
v_cvt_pk_bf16_f32 v61, v238, v239
v_mfma_f32_16x16x32_bf16 v[164:167], a[232:235], v[72:75], v[164:167]
v_mfma_f32_16x16x32_bf16 v[164:167], a[248:251], v[76:79], v[164:167]
s_waitcnt lgkmcnt(0)
s_barrier
v_mfma_f32_16x16x32_bf16 v[180:183], a[236:239], v[72:75], v[180:183]
ds_read_b64_tr_b16 v[64:65], v26 offset:0
v_mfma_f32_16x16x32_bf16 v[180:183], a[252:255], v[76:79], v[180:183]
ds_read_b64_tr_b16 v[66:67], v26 offset:256
v_mfma_f32_16x16x32_bf16 v[136:139], a[224:227], v[80:83], v[136:139]
ds_read_b64_tr_b16 v[68:69], v26 offset:4096
v_cvt_pk_bf16_f32 v62, v252, v253
v_mfma_f32_16x16x32_bf16 v[136:139], a[240:243], v[84:87], v[136:139]
ds_read_b64_tr_b16 v[70:71], v26 offset:4352
v_mfma_f32_16x16x32_bf16 v[152:155], a[228:231], v[80:83], v[152:155]
ds_read_b64_tr_b16 v[72:73], v27 offset:0
v_mfma_f32_16x16x32_bf16 v[152:155], a[244:247], v[84:87], v[152:155]
ds_read_b64_tr_b16 v[74:75], v27 offset:256
v_cvt_pk_bf16_f32 v63, v254, v255
v_mfma_f32_16x16x32_bf16 v[168:171], a[232:235], v[80:83], v[168:171]
ds_read_b64_tr_b16 v[76:77], v27 offset:4096
v_mfma_f32_16x16x32_bf16 v[168:171], a[248:251], v[84:87], v[168:171]
ds_read_b64_tr_b16 v[78:79], v27 offset:4352
v_mfma_f32_16x16x32_bf16 v[184:187], a[236:239], v[80:83], v[184:187]
global_store_dwordx4 v117, v[52:55], s[96:97] offset:-2048 sc1
v_mfma_f32_16x16x32_bf16 v[184:187], a[252:255], v[84:87], v[184:187]
v_mfma_f32_16x16x32_bf16 v[140:143], a[224:227], v[88:91], v[140:143]
ds_read_b64_tr_b16 v[80:81], v26 offset:512
v_mfma_f32_16x16x32_bf16 v[140:143], a[240:243], v[92:95], v[140:143]
ds_read_b64_tr_b16 v[82:83], v26 offset:768
v_mfma_f32_16x16x32_bf16 v[156:159], a[228:231], v[88:91], v[156:159]
global_load_dwordx4 a[140:143], v115, s[46:47] offset:-4096 sc1
v_mfma_f32_16x16x32_bf16 v[156:159], a[244:247], v[92:95], v[156:159]
v_mfma_f32_16x16x32_bf16 v[172:175], a[232:235], v[88:91], v[172:175]
ds_read_b64_tr_b16 v[84:85], v26 offset:4608
v_mfma_f32_16x16x32_bf16 v[172:175], a[248:251], v[92:95], v[172:175]
ds_read_b64_tr_b16 v[86:87], v26 offset:4864
v_mfma_f32_16x16x32_bf16 v[188:191], a[236:239], v[88:91], v[188:191]
global_store_dwordx4 v117, v[56:59], s[94:95] offset:-1024 sc1
v_mfma_f32_16x16x32_bf16 v[188:191], a[252:255], v[92:95], v[188:191]
s_waitcnt vmcnt(59) lgkmcnt(8)
v_mfma_f32_16x16x32_bf16 v[192:195], v[64:67], a[0:3], 0
ds_read_b64_tr_b16 v[88:89], v27 offset:512
v_mfma_f32_16x16x32_bf16 v[192:195], v[68:71], a[16:19], v[192:195]
ds_read_b64_tr_b16 v[90:91], v27 offset:768
v_mfma_f32_16x16x32_bf16 v[208:211], v[64:67], a[4:7], 0
v_mfma_f32_16x16x32_bf16 v[208:211], v[68:71], a[20:23], v[208:211]
v_mfma_f32_16x16x32_bf16 v[224:227], v[64:67], a[8:11], 0
ds_read_b64_tr_b16 v[92:93], v27 offset:4608
v_cvt_pk_bf16_f32 v2, v128, v129
v_mfma_f32_16x16x32_bf16 v[224:227], v[68:71], a[24:27], v[224:227]
ds_read_b64_tr_b16 v[94:95], v27 offset:4864
v_cvt_pk_bf16_f32 v3, v130, v131
v_mfma_f32_16x16x32_bf16 v[240:243], v[64:67], a[12:15], 0
global_load_dwordx4 a[144:147], v113, s[44:45] offset:-3072 sc1
v_cvt_pk_bf16_f32 v4, v144, v145
v_mfma_f32_16x16x32_bf16 v[240:243], v[68:71], a[28:31], v[240:243]
v_cvt_pk_bf16_f32 v5, v146, v147
s_waitcnt lgkmcnt(8)
v_mfma_f32_16x16x32_bf16 v[196:199], v[72:75], a[0:3], 0
ds_read_b64_tr_b16 v[64:65], v26 offset:8192
v_mfma_f32_16x16x32_bf16 v[196:199], v[76:79], a[16:19], v[196:199]
ds_read_b64_tr_b16 v[66:67], v26 offset:8448
v_mfma_f32_16x16x32_bf16 v[212:215], v[72:75], a[4:7], 0
global_load_dwordx4 a[148:151], v115, s[44:45] offset:-3072 sc1
v_mfma_f32_16x16x32_bf16 v[212:215], v[76:79], a[20:23], v[212:215]
v_cvt_pk_bf16_f32 v6, v160, v161
v_mfma_f32_16x16x32_bf16 v[228:231], v[72:75], a[8:11], 0
ds_read_b64_tr_b16 v[68:69], v26 offset:12288
v_mfma_f32_16x16x32_bf16 v[228:231], v[76:79], a[24:27], v[228:231]
ds_read_b64_tr_b16 v[70:71], v26 offset:12544
v_mfma_f32_16x16x32_bf16 v[244:247], v[72:75], a[12:15], 0
global_store_dwordx4 v117, v[60:63], s[96:97] offset:-1024 sc1
s_nop 0
v_mfma_f32_16x16x32_bf16 v[244:247], v[76:79], a[28:31], v[244:247]
s_nop 0
s_waitcnt lgkmcnt(8)
v_mfma_f32_16x16x32_bf16 v[200:203], v[80:83], a[0:3], 0
ds_read_b64_tr_b16 v[72:73], v27 offset:8192
v_xor_b32 v117, 0x1000, v117
v_mfma_f32_16x16x32_bf16 v[200:203], v[84:87], a[16:19], v[200:203]
ds_read_b64_tr_b16 v[74:75], v27 offset:8448
v_cvt_pk_bf16_f32 v7, v162, v163
v_mfma_f32_16x16x32_bf16 v[216:219], v[80:83], a[4:7], 0
global_load_dwordx4 a[152:155], v113, s[46:47] offset:-3072 sc1
v_mfma_f32_16x16x32_bf16 v[216:219], v[84:87], a[20:23], v[216:219]
v_cvt_pk_bf16_f32 v8, v176, v177
v_mfma_f32_16x16x32_bf16 v[232:235], v[80:83], a[8:11], 0
ds_read_b64_tr_b16 v[76:77], v27 offset:12288
v_mfma_f32_16x16x32_bf16 v[232:235], v[84:87], a[24:27], v[232:235]
ds_read_b64_tr_b16 v[78:79], v27 offset:12544
v_mfma_f32_16x16x32_bf16 v[248:251], v[80:83], a[12:15], 0
global_load_dwordx4 a[156:159], v115, s[46:47] offset:-3072 sc1
v_mfma_f32_16x16x32_bf16 v[248:251], v[84:87], a[28:31], v[248:251]
v_cvt_pk_bf16_f32 v9, v178, v179
s_waitcnt lgkmcnt(8)
v_mfma_f32_16x16x32_bf16 v[204:207], v[88:91], a[0:3], 0
ds_read_b64_tr_b16 v[80:81], v26 offset:8704
v_mfma_f32_16x16x32_bf16 v[204:207], v[92:95], a[16:19], v[204:207]
ds_read_b64_tr_b16 v[82:83], v26 offset:8960
v_mfma_f32_16x16x32_bf16 v[220:223], v[88:91], a[4:7], 0
global_load_dwordx4 a[160:163], v113, s[44:45] offset:-2048 sc1
v_mfma_f32_16x16x32_bf16 v[220:223], v[92:95], a[20:23], v[220:223]
v_cvt_pk_bf16_f32 v10, v132, v133
v_mfma_f32_16x16x32_bf16 v[236:239], v[88:91], a[8:11], 0
ds_read_b64_tr_b16 v[84:85], v26 offset:12800
v_mfma_f32_16x16x32_bf16 v[236:239], v[92:95], a[24:27], v[236:239]
ds_read_b64_tr_b16 v[86:87], v26 offset:13056
v_mfma_f32_16x16x32_bf16 v[252:255], v[88:91], a[12:15], 0
v_cvt_pk_bf16_f32 v11, v134, v135
v_mfma_f32_16x16x32_bf16 v[252:255], v[92:95], a[28:31], v[252:255]
global_load_dwordx4 a[164:167], v115, s[44:45] offset:-2048 sc1
s_waitcnt vmcnt(58) lgkmcnt(8)
v_mfma_f32_16x16x32_bf16 v[192:195], v[64:67], a[32:35], v[192:195]
ds_read_b64_tr_b16 v[88:89], v27 offset:8704
v_mfma_f32_16x16x32_bf16 v[192:195], v[68:71], a[48:51], v[192:195]
ds_read_b64_tr_b16 v[90:91], v27 offset:8960
v_mfma_f32_16x16x32_bf16 v[208:211], v[64:67], a[36:39], v[208:211]
v_cvt_pk_bf16_f32 v12, v148, v149
v_mfma_f32_16x16x32_bf16 v[208:211], v[68:71], a[52:55], v[208:211]
global_store_dwordx4 v117, v[2:5], s[94:95] offset:-4096 sc1
v_mfma_f32_16x16x32_bf16 v[224:227], v[64:67], a[40:43], v[224:227]
ds_read_b64_tr_b16 v[92:93], v27 offset:12800
v_mfma_f32_16x16x32_bf16 v[224:227], v[68:71], a[56:59], v[224:227]
ds_read_b64_tr_b16 v[94:95], v27 offset:13056
v_bfe_u32 v5, v22, 6, 2
v_mfma_f32_16x16x32_bf16 v[240:243], v[64:67], a[44:47], v[240:243]
v_cvt_pk_bf16_f32 v13, v150, v151
v_mfma_f32_16x16x32_bf16 v[240:243], v[68:71], a[60:63], v[240:243]
s_waitcnt lgkmcnt(8)
v_mfma_f32_16x16x32_bf16 v[196:199], v[72:75], a[32:35], v[196:199]
ds_read_b64_tr_b16 v[64:65], v26 offset:16384
v_mfma_f32_16x16x32_bf16 v[196:199], v[76:79], a[48:51], v[196:199]
ds_read_b64_tr_b16 v[66:67], v26 offset:16640
v_mfma_f32_16x16x32_bf16 v[212:215], v[72:75], a[36:39], v[212:215]
global_load_dwordx4 a[168:171], v113, s[46:47] offset:-2048 sc1
v_cvt_pk_bf16_f32 v14, v164, v165
v_mfma_f32_16x16x32_bf16 v[212:215], v[76:79], a[52:55], v[212:215]
v_mfma_f32_16x16x32_bf16 v[228:231], v[72:75], a[40:43], v[228:231]
ds_read_b64_tr_b16 v[68:69], v26 offset:20480
v_mfma_f32_16x16x32_bf16 v[228:231], v[76:79], a[56:59], v[228:231]
ds_read_b64_tr_b16 v[70:71], v26 offset:20736
v_mfma_f32_16x16x32_bf16 v[244:247], v[72:75], a[44:47], v[244:247]
global_load_dwordx4 a[172:175], v115, s[46:47] offset:-2048 sc1
v_cvt_pk_bf16_f32 v15, v166, v167
v_mfma_f32_16x16x32_bf16 v[244:247], v[76:79], a[60:63], v[244:247]
s_waitcnt lgkmcnt(8)
v_mfma_f32_16x16x32_bf16 v[200:203], v[80:83], a[32:35], v[200:203]
ds_read_b64_tr_b16 v[72:73], v27 offset:16384
v_mfma_f32_16x16x32_bf16 v[200:203], v[84:87], a[48:51], v[200:203]
ds_read_b64_tr_b16 v[74:75], v27 offset:16640
v_mfma_f32_16x16x32_bf16 v[216:219], v[80:83], a[36:39], v[216:219]
global_store_dwordx4 v117, v[6:9], s[96:97] offset:-4096 sc1
v_cvt_pk_bf16_f32 v16, v180, v181
v_mfma_f32_16x16x32_bf16 v[216:219], v[84:87], a[52:55], v[216:219]
v_mfma_f32_16x16x32_bf16 v[232:235], v[80:83], a[40:43], v[232:235]
ds_read_b64_tr_b16 v[76:77], v27 offset:20480
v_lshrrev_b32 v6, 8, v22
v_mfma_f32_16x16x32_bf16 v[232:235], v[84:87], a[56:59], v[232:235]
ds_read_b64_tr_b16 v[78:79], v27 offset:20736
v_cvt_pk_bf16_f32 v17, v182, v183
v_mfma_f32_16x16x32_bf16 v[248:251], v[80:83], a[44:47], v[248:251]
v_lshrrev_b32 v9, 1, v6
v_lshl_add_u32 v9, v9, 6, v5
v_mfma_f32_16x16x32_bf16 v[248:251], v[84:87], a[60:63], v[248:251]
global_load_dwordx4 a[176:179], v113, s[44:45] offset:-1024 sc1
s_waitcnt lgkmcnt(8)
v_mfma_f32_16x16x32_bf16 v[204:207], v[88:91], a[32:35], v[204:207]
ds_read_b64_tr_b16 v[80:81], v26 offset:16896
v_lshlrev_b32 v9, 6, v9
v_mfma_f32_16x16x32_bf16 v[204:207], v[92:95], a[48:51], v[204:207]
ds_read_b64_tr_b16 v[82:83], v26 offset:17152
v_cvt_pk_bf16_f32 v18, v136, v137
v_mfma_f32_16x16x32_bf16 v[220:223], v[88:91], a[36:39], v[220:223]
v_and_b32 v7, 32, v22
v_bfe_u32 v8, v22, 4, 1
v_mfma_f32_16x16x32_bf16 v[220:223], v[92:95], a[52:55], v[220:223]
global_load_dwordx4 a[180:183], v115, s[44:45] offset:-1024 sc1
v_lshl_add_u32 v7, v8, 3, v7
v_mfma_f32_16x16x32_bf16 v[236:239], v[88:91], a[40:43], v[236:239]
ds_read_b64_tr_b16 v[84:85], v26 offset:20992
v_and_b32 v8, 1, v6
v_mfma_f32_16x16x32_bf16 v[236:239], v[92:95], a[56:59], v[236:239]
ds_read_b64_tr_b16 v[86:87], v26 offset:21248
v_cvt_pk_bf16_f32 v19, v138, v139
v_mfma_f32_16x16x32_bf16 v[252:255], v[88:91], a[44:47], v[252:255]
v_mul_u32_u24 v8, 1040, v8
v_add3_u32 v9, v9, v7, v8
v_mfma_f32_16x16x32_bf16 v[252:255], v[92:95], a[60:63], v[252:255]
global_store_dwordx4 v117, v[10:13], s[94:95] offset:-3072 sc1
s_waitcnt vmcnt(57) lgkmcnt(8)
v_mfma_f32_16x16x32_bf16 v[192:195], v[64:67], a[64:67], v[192:195]
ds_read_b64_tr_b16 v[88:89], v27 offset:16896
v_lshl_add_u32 v9, v9, 2, %2
v_mfma_f32_16x16x32_bf16 v[192:195], v[68:71], a[80:83], v[192:195]
ds_read_b64_tr_b16 v[90:91], v27 offset:17152
v_cvt_pk_bf16_f32 v20, v152, v153
v_mfma_f32_16x16x32_bf16 v[208:211], v[64:67], a[68:71], v[208:211]
v_lshl_add_u32 v9, v9, 5, v5
v_xor_b32 v6, s31, v6
v_mfma_f32_16x16x32_bf16 v[208:211], v[68:71], a[84:87], v[208:211]
v_lshl_add_u32 v9, v9, 4, v6
v_and_b32 v7, 48, v22
v_mfma_f32_16x16x32_bf16 v[224:227], v[64:67], a[72:75], v[224:227]
ds_read_b64_tr_b16 v[92:93], v27 offset:20992
v_mfma_f32_16x16x32_bf16 v[224:227], v[68:71], a[88:91], v[224:227]
ds_read_b64_tr_b16 v[94:95], v27 offset:21248
v_cvt_pk_bf16_f32 v21, v154, v155
v_mfma_f32_16x16x32_bf16 v[240:243], v[64:67], a[76:79], v[240:243]
global_load_dwordx4 a[184:187], v113, s[46:47] offset:-1024 sc1
v_mfma_f32_16x16x32_bf16 v[240:243], v[68:71], a[92:95], v[240:243]
s_waitcnt lgkmcnt(8)
v_mfma_f32_16x16x32_bf16 v[196:199], v[72:75], a[64:67], v[196:199]
ds_read_b64_tr_b16 v[64:65], v26 offset:24576
v_mfma_f32_16x16x32_bf16 v[196:199], v[76:79], a[80:83], v[196:199]
ds_read_b64_tr_b16 v[66:67], v26 offset:24832
v_cvt_pk_bf16_f32 v28, v168, v169
v_mfma_f32_16x16x32_bf16 v[212:215], v[72:75], a[68:71], v[212:215]
global_load_dwordx4 a[188:191], v115, s[46:47] offset:-1024 sc1
v_mfma_f32_16x16x32_bf16 v[212:215], v[76:79], a[84:87], v[212:215]
v_mfma_f32_16x16x32_bf16 v[228:231], v[72:75], a[72:75], v[228:231]
ds_read_b64_tr_b16 v[68:69], v26 offset:28672
v_mfma_f32_16x16x32_bf16 v[228:231], v[76:79], a[88:91], v[228:231]
ds_read_b64_tr_b16 v[70:71], v26 offset:28928
v_cvt_pk_bf16_f32 v29, v170, v171
v_mfma_f32_16x16x32_bf16 v[244:247], v[72:75], a[76:79], v[244:247]
global_load_dwordx4 a[192:195], v113, s[44:45] offset:0 sc1
v_mfma_f32_16x16x32_bf16 v[244:247], v[76:79], a[92:95], v[244:247]
s_waitcnt lgkmcnt(8)
v_mfma_f32_16x16x32_bf16 v[200:203], v[80:83], a[64:67], v[200:203]
ds_read_b64_tr_b16 v[72:73], v27 offset:24576
v_cvt_pk_bf16_f32 v30, v184, v185
v_mfma_f32_16x16x32_bf16 v[200:203], v[84:87], a[80:83], v[200:203]
ds_read_b64_tr_b16 v[74:75], v27 offset:24832
v_mfma_f32_16x16x32_bf16 v[216:219], v[80:83], a[68:71], v[216:219]
global_store_dwordx4 v117, v[14:17], s[96:97] offset:-3072 sc1
v_mfma_f32_16x16x32_bf16 v[216:219], v[84:87], a[84:87], v[216:219]
v_mfma_f32_16x16x32_bf16 v[232:235], v[80:83], a[72:75], v[232:235]
ds_read_b64_tr_b16 v[76:77], v27 offset:28672
v_cvt_pk_bf16_f32 v31, v186, v187
v_mfma_f32_16x16x32_bf16 v[232:235], v[84:87], a[88:91], v[232:235]
ds_read_b64_tr_b16 v[78:79], v27 offset:28928
v_mfma_f32_16x16x32_bf16 v[248:251], v[80:83], a[76:79], v[248:251]
v_mfma_f32_16x16x32_bf16 v[248:251], v[84:87], a[92:95], v[248:251]
global_load_dwordx4 a[196:199], v115, s[44:45] offset:0 sc1
s_waitcnt lgkmcnt(8)
v_mfma_f32_16x16x32_bf16 v[204:207], v[88:91], a[64:67], v[204:207]
ds_read_b64_tr_b16 v[80:81], v26 offset:25088
v_cvt_pk_bf16_f32 v120, v140, v141
v_mfma_f32_16x16x32_bf16 v[204:207], v[92:95], a[80:83], v[204:207]
ds_read_b64_tr_b16 v[82:83], v26 offset:25344
v_mfma_f32_16x16x32_bf16 v[220:223], v[88:91], a[68:71], v[220:223]
v_mfma_f32_16x16x32_bf16 v[220:223], v[92:95], a[84:87], v[220:223]
global_load_dwordx4 a[200:203], v113, s[46:47] offset:0 sc1
v_mfma_f32_16x16x32_bf16 v[236:239], v[88:91], a[72:75], v[236:239]
ds_read_b64_tr_b16 v[84:85], v26 offset:29184
v_cvt_pk_bf16_f32 v121, v142, v143
v_mfma_f32_16x16x32_bf16 v[236:239], v[92:95], a[88:91], v[236:239]
ds_read_b64_tr_b16 v[86:87], v26 offset:29440
v_mfma_f32_16x16x32_bf16 v[252:255], v[88:91], a[76:79], v[252:255]
v_mfma_f32_16x16x32_bf16 v[252:255], v[92:95], a[92:95], v[252:255]
global_store_dwordx4 v117, v[18:21], s[94:95] offset:-2048 sc1
s_waitcnt vmcnt(39) lgkmcnt(8)
v_mfma_f32_16x16x32_bf16 v[192:195], v[64:67], a[96:99], v[192:195]
ds_read_b64_tr_b16 v[88:89], v27 offset:25088
v_cvt_pk_bf16_f32 v122, v156, v157
v_mfma_f32_16x16x32_bf16 v[192:195], v[68:71], a[112:115], v[192:195]
ds_read_b64_tr_b16 v[90:91], v27 offset:25344
v_mfma_f32_16x16x32_bf16 v[208:211], v[64:67], a[100:103], v[208:211]
v_mfma_f32_16x16x32_bf16 v[208:211], v[68:71], a[116:119], v[208:211]
v_mfma_f32_16x16x32_bf16 v[224:227], v[64:67], a[104:107], v[224:227]
ds_read_b64_tr_b16 v[92:93], v27 offset:29184
)"
R"(v_cvt_pk_bf16_f32 v123, v158, v159
v_mfma_f32_16x16x32_bf16 v[224:227], v[68:71], a[120:123], v[224:227]
ds_read_b64_tr_b16 v[94:95], v27 offset:29440
v_mfma_f32_16x16x32_bf16 v[240:243], v[64:67], a[108:111], v[240:243]
global_load_dwordx4 a[204:207], v115, s[46:47] offset:0 sc1
v_mfma_f32_16x16x32_bf16 v[240:243], v[68:71], a[124:127], v[240:243]
v_cvt_pk_bf16_f32 v124, v172, v173
s_waitcnt lgkmcnt(8)
v_mfma_f32_16x16x32_bf16 v[196:199], v[72:75], a[96:99], v[196:199]
v_mfma_f32_16x16x32_bf16 v[196:199], v[76:79], a[112:115], v[196:199]
global_load_dwordx4 a[208:211], v113, s[44:45] offset:1024 sc1
v_mfma_f32_16x16x32_bf16 v[212:215], v[72:75], a[100:103], v[212:215]
v_mfma_f32_16x16x32_bf16 v[212:215], v[76:79], a[116:119], v[212:215]
v_cvt_pk_bf16_f32 v125, v174, v175
v_mfma_f32_16x16x32_bf16 v[228:231], v[72:75], a[104:107], v[228:231]
v_mfma_f32_16x16x32_bf16 v[228:231], v[76:79], a[120:123], v[228:231]
s_waitcnt lgkmcnt(0)
s_barrier
v_mfma_f32_16x16x32_bf16 v[244:247], v[72:75], a[108:111], v[244:247]
ds_read_b64_tr_b16 v[64:65], v98 offset:32768
v_mfma_f32_16x16x32_bf16 v[244:247], v[76:79], a[124:127], v[244:247]
ds_read_b64_tr_b16 v[66:67], v98 offset:33024
v_mfma_f32_16x16x32_bf16 v[200:203], v[80:83], a[96:99], v[200:203]
ds_read_b64_tr_b16 v[68:69], v98 offset:40960
v_cvt_pk_bf16_f32 v126, v188, v189
v_mfma_f32_16x16x32_bf16 v[200:203], v[84:87], a[112:115], v[200:203]
ds_read_b64_tr_b16 v[70:71], v98 offset:41216
v_mfma_f32_16x16x32_bf16 v[216:219], v[80:83], a[100:103], v[216:219]
ds_read_b64_tr_b16 v[72:73], v99 offset:32768
v_mfma_f32_16x16x32_bf16 v[216:219], v[84:87], a[116:119], v[216:219]
ds_read_b64_tr_b16 v[74:75], v99 offset:33024
v_cvt_pk_bf16_f32 v127, v190, v191
v_mfma_f32_16x16x32_bf16 v[232:235], v[80:83], a[104:107], v[232:235]
ds_read_b64_tr_b16 v[76:77], v99 offset:40960
v_mfma_f32_16x16x32_bf16 v[232:235], v[84:87], a[120:123], v[232:235]
ds_read_b64_tr_b16 v[78:79], v99 offset:41216
v_mfma_f32_16x16x32_bf16 v[248:251], v[80:83], a[108:111], v[248:251]
global_store_dwordx4 v117, v[28:31], s[96:97] offset:-2048 sc1
v_mfma_f32_16x16x32_bf16 v[248:251], v[84:87], a[124:127], v[248:251]
v_mfma_f32_16x16x32_bf16 v[204:207], v[88:91], a[96:99], v[204:207]
ds_read_b64_tr_b16 v[80:81], v98 offset:36864
v_mfma_f32_16x16x32_bf16 v[204:207], v[92:95], a[112:115], v[204:207]
ds_read_b64_tr_b16 v[82:83], v98 offset:37120
v_mfma_f32_16x16x32_bf16 v[220:223], v[88:91], a[100:103], v[220:223]
global_load_dwordx4 a[212:215], v115, s[44:45] offset:1024 sc1
v_mfma_f32_16x16x32_bf16 v[220:223], v[92:95], a[116:119], v[220:223]
v_mfma_f32_16x16x32_bf16 v[236:239], v[88:91], a[104:107], v[236:239]
ds_read_b64_tr_b16 v[84:85], v98 offset:45056
v_mfma_f32_16x16x32_bf16 v[236:239], v[92:95], a[120:123], v[236:239]
ds_read_b64_tr_b16 v[86:87], v98 offset:45312
v_mfma_f32_16x16x32_bf16 v[252:255], v[88:91], a[108:111], v[252:255]
global_load_dwordx4 a[216:219], v113, s[46:47] offset:1024 sc1
v_mfma_f32_16x16x32_bf16 v[252:255], v[92:95], a[124:127], v[252:255]
s_waitcnt lgkmcnt(8)
v_mfma_f32_16x16x32_bf16 v[128:131], v[64:67], a[0:3], 0
ds_read_b64_tr_b16 v[88:89], v99 offset:36864
v_mfma_f32_16x16x32_bf16 v[128:131], v[68:71], a[16:19], v[128:131]
ds_read_b64_tr_b16 v[90:91], v99 offset:37120
v_mfma_f32_16x16x32_bf16 v[144:147], v[64:67], a[4:7], 0
global_load_dwordx4 a[220:223], v115, s[46:47] offset:1024 sc1
v_mfma_f32_16x16x32_bf16 v[144:147], v[68:71], a[20:23], v[144:147]
v_mfma_f32_16x16x32_bf16 v[160:163], v[64:67], a[8:11], 0
ds_read_b64_tr_b16 v[92:93], v99 offset:45056
v_mfma_f32_16x16x32_bf16 v[160:163], v[68:71], a[24:27], v[160:163]
ds_read_b64_tr_b16 v[94:95], v99 offset:45312
v_mfma_f32_16x16x32_bf16 v[176:179], v[64:67], a[12:15], 0
global_store_dwordx4 v117, v[120:123], s[94:95] offset:-1024 sc1
v_mfma_f32_16x16x32_bf16 v[176:179], v[68:71], a[28:31], v[176:179]
s_waitcnt lgkmcnt(8)
v_mfma_f32_16x16x32_bf16 v[132:135], v[72:75], a[0:3], 0
ds_read_b64_tr_b16 v[64:65], v98 offset:49152
v_mfma_f32_16x16x32_bf16 v[132:135], v[76:79], a[16:19], v[132:135]
ds_read_b64_tr_b16 v[66:67], v98 offset:49408
s_add_u32 m0, s75, 65536
v_mfma_f32_16x16x32_bf16 v[148:151], v[72:75], a[4:7], 0
s_xor_b32 s48, s67, 917504
v_mfma_f32_16x16x32_bf16 v[148:151], v[76:79], a[20:23], v[148:151]
v_mfma_f32_16x16x32_bf16 v[164:167], v[72:75], a[8:11], 0
ds_read_b64_tr_b16 v[68:69], v98 offset:57344
v_mfma_f32_16x16x32_bf16 v[164:167], v[76:79], a[24:27], v[164:167]
ds_read_b64_tr_b16 v[70:71], v98 offset:57600
v_mfma_f32_16x16x32_bf16 v[180:183], v[72:75], a[12:15], 0
buffer_load_dwordx4 v108, s[80:83], s48 offen offset:0 lds
v_mfma_f32_16x16x32_bf16 v[180:183], v[76:79], a[28:31], v[180:183]
s_waitcnt lgkmcnt(8)
v_mfma_f32_16x16x32_bf16 v[136:139], v[80:83], a[0:3], 0
ds_read_b64_tr_b16 v[72:73], v99 offset:49152
v_mfma_f32_16x16x32_bf16 v[136:139], v[84:87], a[16:19], v[136:139]
ds_read_b64_tr_b16 v[74:75], v99 offset:49408
v_mfma_f32_16x16x32_bf16 v[152:155], v[80:83], a[4:7], 0
buffer_load_dwordx4 v109, s[80:83], s48 offen offset:1024 lds
v_mfma_f32_16x16x32_bf16 v[152:155], v[84:87], a[20:23], v[152:155]
v_mfma_f32_16x16x32_bf16 v[168:171], v[80:83], a[8:11], 0
ds_read_b64_tr_b16 v[76:77], v99 offset:57344
v_mfma_f32_16x16x32_bf16 v[168:171], v[84:87], a[24:27], v[168:171]
ds_read_b64_tr_b16 v[78:79], v99 offset:57600
v_mfma_f32_16x16x32_bf16 v[184:187], v[80:83], a[12:15], 0
buffer_load_dwordx4 v110, s[80:83], s48 offen offset:2048 lds
v_mfma_f32_16x16x32_bf16 v[184:187], v[84:87], a[28:31], v[184:187]
s_waitcnt lgkmcnt(8)
v_mfma_f32_16x16x32_bf16 v[140:143], v[88:91], a[0:3], 0
ds_read_b64_tr_b16 v[80:81], v98 offset:53248
v_mfma_f32_16x16x32_bf16 v[140:143], v[92:95], a[16:19], v[140:143]
ds_read_b64_tr_b16 v[82:83], v98 offset:53504
v_mfma_f32_16x16x32_bf16 v[156:159], v[88:91], a[4:7], 0
buffer_load_dwordx4 v111, s[80:83], s48 offen offset:3072 lds
s_add_u32 m0, s75, 69632
v_mfma_f32_16x16x32_bf16 v[156:159], v[92:95], a[20:23], v[156:159]
s_xor_b32 s48, s67, 917504
v_mfma_f32_16x16x32_bf16 v[172:175], v[88:91], a[8:11], 0
ds_read_b64_tr_b16 v[84:85], v98 offset:61440
v_mfma_f32_16x16x32_bf16 v[172:175], v[92:95], a[24:27], v[172:175]
ds_read_b64_tr_b16 v[86:87], v98 offset:61696
v_mfma_f32_16x16x32_bf16 v[188:191], v[88:91], a[12:15], 0
buffer_load_dwordx4 v108, s[40:43], s48 offen offset:0 lds
v_mfma_f32_16x16x32_bf16 v[188:191], v[92:95], a[28:31], v[188:191]
s_waitcnt lgkmcnt(8)
v_mfma_f32_16x16x32_bf16 v[128:131], v[64:67], a[32:35], v[128:131]
ds_read_b64_tr_b16 v[88:89], v99 offset:53248
v_mfma_f32_16x16x32_bf16 v[128:131], v[68:71], a[48:51], v[128:131]
ds_read_b64_tr_b16 v[90:91], v99 offset:53504
v_mfma_f32_16x16x32_bf16 v[144:147], v[64:67], a[36:39], v[144:147]
buffer_load_dwordx4 v109, s[40:43], s48 offen offset:1024 lds
v_mfma_f32_16x16x32_bf16 v[144:147], v[68:71], a[52:55], v[144:147]
v_mfma_f32_16x16x32_bf16 v[160:163], v[64:67], a[40:43], v[160:163]
ds_read_b64_tr_b16 v[92:93], v99 offset:61440
v_mfma_f32_16x16x32_bf16 v[160:163], v[68:71], a[56:59], v[160:163]
ds_read_b64_tr_b16 v[94:95], v99 offset:61696
v_mfma_f32_16x16x32_bf16 v[176:179], v[64:67], a[44:47], v[176:179]
buffer_load_dwordx4 v110, s[40:43], s48 offen offset:2048 lds
v_mfma_f32_16x16x32_bf16 v[176:179], v[68:71], a[60:63], v[176:179]
s_waitcnt lgkmcnt(8)
v_mfma_f32_16x16x32_bf16 v[132:135], v[72:75], a[32:35], v[132:135]
ds_read_b64_tr_b16 v[64:65], v96 offset:0
v_mfma_f32_16x16x32_bf16 v[132:135], v[76:79], a[48:51], v[132:135]
ds_read_b64_tr_b16 v[66:67], v96 offset:256
v_mfma_f32_16x16x32_bf16 v[148:151], v[72:75], a[36:39], v[148:151]
buffer_load_dwordx4 v111, s[40:43], s48 offen offset:3072 lds
s_add_u32 m0, s75, 131072
v_mfma_f32_16x16x32_bf16 v[148:151], v[76:79], a[52:55], v[148:151]
s_xor_b32 s48, s67, 917504
s_add_u32 s48, s48, 458752
v_mfma_f32_16x16x32_bf16 v[164:167], v[72:75], a[40:43], v[164:167]
ds_read_b64_tr_b16 v[68:69], v96 offset:8192
v_mfma_f32_16x16x32_bf16 v[164:167], v[76:79], a[56:59], v[164:167]
ds_read_b64_tr_b16 v[70:71], v96 offset:8448
v_mfma_f32_16x16x32_bf16 v[180:183], v[72:75], a[44:47], v[180:183]
buffer_load_dwordx4 v108, s[80:83], s48 offen offset:0 lds
v_mfma_f32_16x16x32_bf16 v[180:183], v[76:79], a[60:63], v[180:183]
s_waitcnt lgkmcnt(8)
v_mfma_f32_16x16x32_bf16 v[136:139], v[80:83], a[32:35], v[136:139]
ds_read_b64_tr_b16 v[72:73], v97 offset:0
v_mfma_f32_16x16x32_bf16 v[136:139], v[84:87], a[48:51], v[136:139]
ds_read_b64_tr_b16 v[74:75], v97 offset:256
v_mfma_f32_16x16x32_bf16 v[152:155], v[80:83], a[36:39], v[152:155]
buffer_load_dwordx4 v109, s[80:83], s48 offen offset:1024 lds
v_mfma_f32_16x16x32_bf16 v[152:155], v[84:87], a[52:55], v[152:155]
v_mfma_f32_16x16x32_bf16 v[168:171], v[80:83], a[40:43], v[168:171]
ds_read_b64_tr_b16 v[76:77], v97 offset:8192
v_mfma_f32_16x16x32_bf16 v[168:171], v[84:87], a[56:59], v[168:171]
ds_read_b64_tr_b16 v[78:79], v97 offset:8448
v_mfma_f32_16x16x32_bf16 v[184:187], v[80:83], a[44:47], v[184:187]
buffer_load_dwordx4 v110, s[80:83], s48 offen offset:2048 lds
v_mfma_f32_16x16x32_bf16 v[184:187], v[84:87], a[60:63], v[184:187]
s_waitcnt lgkmcnt(8)
v_mfma_f32_16x16x32_bf16 v[140:143], v[88:91], a[32:35], v[140:143]
ds_read_b64_tr_b16 v[80:81], v96 offset:4096
v_mfma_f32_16x16x32_bf16 v[140:143], v[92:95], a[48:51], v[140:143]
ds_read_b64_tr_b16 v[82:83], v96 offset:4352
v_mfma_f32_16x16x32_bf16 v[156:159], v[88:91], a[36:39], v[156:159]
buffer_load_dwordx4 v111, s[80:83], s48 offen offset:3072 lds
s_add_u32 m0, s75, 135168
v_mfma_f32_16x16x32_bf16 v[156:159], v[92:95], a[52:55], v[156:159]
s_xor_b32 s48, s67, 917504
s_add_u32 s48, s48, 458752
v_mfma_f32_16x16x32_bf16 v[172:175], v[88:91], a[40:43], v[172:175]
ds_read_b64_tr_b16 v[84:85], v96 offset:12288
v_mfma_f32_16x16x32_bf16 v[172:175], v[92:95], a[56:59], v[172:175]
ds_read_b64_tr_b16 v[86:87], v96 offset:12544
v_mfma_f32_16x16x32_bf16 v[188:191], v[88:91], a[44:47], v[188:191]
buffer_load_dwordx4 v108, s[40:43], s48 offen offset:0 lds
v_mfma_f32_16x16x32_bf16 v[188:191], v[92:95], a[60:63], v[188:191]
s_waitcnt lgkmcnt(8)
v_mfma_f32_16x16x32_bf16 v[128:131], v[64:67], a[64:67], v[128:131]
ds_read_b64_tr_b16 v[88:89], v97 offset:4096
v_mfma_f32_16x16x32_bf16 v[128:131], v[68:71], a[80:83], v[128:131]
ds_read_b64_tr_b16 v[90:91], v97 offset:4352
v_mfma_f32_16x16x32_bf16 v[144:147], v[64:67], a[68:71], v[144:147]
buffer_load_dwordx4 v109, s[40:43], s48 offen offset:1024 lds
v_mfma_f32_16x16x32_bf16 v[144:147], v[68:71], a[84:87], v[144:147]
v_mfma_f32_16x16x32_bf16 v[160:163], v[64:67], a[72:75], v[160:163]
ds_read_b64_tr_b16 v[92:93], v97 offset:12288
v_mfma_f32_16x16x32_bf16 v[160:163], v[68:71], a[88:91], v[160:163]
ds_read_b64_tr_b16 v[94:95], v97 offset:12544
v_mfma_f32_16x16x32_bf16 v[176:179], v[64:67], a[76:79], v[176:179]
buffer_load_dwordx4 v110, s[40:43], s48 offen offset:2048 lds
v_mfma_f32_16x16x32_bf16 v[176:179], v[68:71], a[92:95], v[176:179]
s_waitcnt lgkmcnt(8)
v_mfma_f32_16x16x32_bf16 v[132:135], v[72:75], a[64:67], v[132:135]
ds_read_b64_tr_b16 v[64:65], v96 offset:16384
v_mfma_f32_16x16x32_bf16 v[132:135], v[76:79], a[80:83], v[132:135]
ds_read_b64_tr_b16 v[66:67], v96 offset:16640
v_mfma_f32_16x16x32_bf16 v[148:151], v[72:75], a[68:71], v[148:151]
buffer_load_dwordx4 v111, s[40:43], s48 offen offset:3072 lds
v_mfma_f32_16x16x32_bf16 v[148:151], v[76:79], a[84:87], v[148:151]
s_mov_b32 s55, s29
s_lshl_b32 s79, s28, 19
v_mfma_f32_16x16x32_bf16 v[164:167], v[72:75], a[72:75], v[164:167]
ds_read_b64_tr_b16 v[68:69], v96 offset:24576
s_mov_b64 s[36:37], s[10:11]
v_mfma_f32_16x16x32_bf16 v[164:167], v[76:79], a[88:91], v[164:167]
ds_read_b64_tr_b16 v[70:71], v96 offset:24832
v_mfma_f32_16x16x32_bf16 v[180:183], v[72:75], a[76:79], v[180:183]
s_add_u32 s36, s36, 0x1400000
s_addc_u32 s37, s37, 0
v_mfma_f32_16x16x32_bf16 v[180:183], v[76:79], a[92:95], v[180:183]
global_store_dwordx4 v117, v[124:127], s[96:97] offset:-1024 sc1
s_waitcnt lgkmcnt(8)
v_mfma_f32_16x16x32_bf16 v[136:139], v[80:83], a[64:67], v[136:139]
ds_read_b64_tr_b16 v[72:73], v97 offset:16384
s_lshl_b32 s98, s28, 1
v_mfma_f32_16x16x32_bf16 v[136:139], v[84:87], a[80:83], v[136:139]
ds_read_b64_tr_b16 v[74:75], v97 offset:16640
s_add_u32 s91, s28, 24
v_mfma_f32_16x16x32_bf16 v[152:155], v[80:83], a[68:71], v[152:155]
s_add_u32 s36, s36, s79
s_addc_u32 s37, s37, 0
v_mfma_f32_16x16x32_bf16 v[152:155], v[84:87], a[84:87], v[152:155]
s_mov_b64 s[40:41], s[10:11]
s_min_u32 s98, s98, s91
v_mfma_f32_16x16x32_bf16 v[168:171], v[80:83], a[72:75], v[168:171]
ds_read_b64_tr_b16 v[76:77], v97 offset:24576
s_lshl_b32 s98, s98, 11
v_mfma_f32_16x16x32_bf16 v[168:171], v[84:87], a[88:91], v[168:171]
ds_read_b64_tr_b16 v[78:79], v97 offset:24832
s_lshl_b32 s91, s29, 2
PAD33_%=:
s_cbranch_execz PAD15_%=
v_mfma_f32_16x16x32_bf16 v[184:187], v[80:83], a[76:79], v[184:187]
s_add_u32 s40, s40, 0x2400000
s_addc_u32 s41, s41, 0
v_mfma_f32_16x16x32_bf16 v[184:187], v[84:87], a[92:95], v[184:187]
s_add_u32 s91, s91, %2
s_waitcnt lgkmcnt(8)
v_mfma_f32_16x16x32_bf16 v[140:143], v[88:91], a[64:67], v[140:143]
ds_read_b64_tr_b16 v[80:81], v96 offset:20480
s_mul_i32 s91, s91, 376832
v_mfma_f32_16x16x32_bf16 v[140:143], v[92:95], a[80:83], v[140:143]
ds_read_b64_tr_b16 v[82:83], v96 offset:20736
s_add_u32 s91, s91, s98
v_mfma_f32_16x16x32_bf16 v[156:159], v[88:91], a[68:71], v[156:159]
s_add_u32 s40, s40, s79
s_addc_u32 s41, s41, 0
v_mfma_f32_16x16x32_bf16 v[156:159], v[92:95], a[84:87], v[156:159]
s_and_b32 s37, s37, 0xffff
s_mov_b32 s38, 0x80000
v_mfma_f32_16x16x32_bf16 v[172:175], v[88:91], a[72:75], v[172:175]
ds_read_b64_tr_b16 v[84:85], v96 offset:28672
s_mov_b32 s39, 0x20000
v_mfma_f32_16x16x32_bf16 v[172:175], v[92:95], a[88:91], v[172:175]
ds_read_b64_tr_b16 v[86:87], v96 offset:28928
s_and_b32 s41, s41, 0xffff
v_mfma_f32_16x16x32_bf16 v[188:191], v[88:91], a[76:79], v[188:191]
s_mov_b32 s42, 0x80000
s_mov_b32 s43, 0x20000
v_mfma_f32_16x16x32_bf16 v[188:191], v[92:95], a[92:95], v[188:191]
s_lshl_b32 s79, s28, 13
s_waitcnt lgkmcnt(8)
v_mfma_f32_16x16x32_bf16 v[128:131], v[64:67], a[96:99], v[128:131]
ds_read_b64_tr_b16 v[88:89], v97 offset:20480
s_lshl_b32 s54, s55, 2
v_mfma_f32_16x16x32_bf16 v[128:131], v[68:71], a[112:115], v[128:131]
ds_read_b64_tr_b16 v[90:91], v97 offset:20736
s_add_u32 s76, s54, %2
v_mfma_f32_16x16x32_bf16 v[144:147], v[64:67], a[100:103], v[144:147]
s_mul_i32 s76, s76, 376832
s_add_u32 s76, s76, s79
v_mfma_f32_16x16x32_bf16 v[144:147], v[68:71], a[116:119], v[144:147]
s_mov_b64 s[50:51], s[10:11]
s_mov_b64 s[94:95], s[10:11]
v_mfma_f32_16x16x32_bf16 v[160:163], v[64:67], a[104:107], v[160:163]
ds_read_b64_tr_b16 v[92:93], v97 offset:28672
v_mfma_f32_16x16x32_bf16 v[160:163], v[68:71], a[120:123], v[160:163]
ds_read_b64_tr_b16 v[94:95], v97 offset:28928
v_mfma_f32_16x16x32_bf16 v[176:179], v[64:67], a[108:111], v[176:179]
s_add_u32 s50, s50, 0x341d000
s_addc_u32 s51, s51, 0
v_mfma_f32_16x16x32_bf16 v[176:179], v[68:71], a[124:127], v[176:179]
global_load_dwordx4 a[224:227], v113, s[44:45] offset:2048 sc1
s_waitcnt lgkmcnt(8)
v_mfma_f32_16x16x32_bf16 v[132:135], v[72:75], a[96:99], v[132:135]
s_add_u32 s50, s50, s76
s_addc_u32 s51, s51, 0
v_mfma_f32_16x16x32_bf16 v[132:135], v[76:79], a[112:115], v[132:135]
s_sub_u32 s54, 60, s54
s_add_u32 s76, s54, %2
v_mfma_f32_16x16x32_bf16 v[148:151], v[72:75], a[100:103], v[148:151]
s_mul_i32 s76, s76, 376832
s_add_u32 s76, s76, s79
v_mfma_f32_16x16x32_bf16 v[148:151], v[76:79], a[116:119], v[148:151]
global_load_dwordx4 a[228:231], v115, s[44:45] offset:2048 sc1
s_mov_b64 s[52:53], s[10:11]
v_mfma_f32_16x16x32_bf16 v[164:167], v[72:75], a[104:107], v[164:167]
s_add_u32 s52, s52, 0x341d000
s_addc_u32 s53, s53, 0
v_mfma_f32_16x16x32_bf16 v[164:167], v[76:79], a[120:123], v[164:167]
s_waitcnt vmcnt(54) lgkmcnt(0)
s_barrier
v_mfma_f32_16x16x32_bf16 v[180:183], v[72:75], a[108:111], v[180:183]
ds_read_b64_tr_b16 v[64:65], v96 offset:32768
v_mfma_f32_16x16x32_bf16 v[180:183], v[76:79], a[124:127], v[180:183]
ds_read_b64_tr_b16 v[66:67], v96 offset:33024
v_mfma_f32_16x16x32_bf16 v[136:139], v[80:83], a[96:99], v[136:139]
ds_read_b64_tr_b16 v[68:69], v96 offset:36864
v_mfma_f32_16x16x32_bf16 v[136:139], v[84:87], a[112:115], v[136:139]
ds_read_b64_tr_b16 v[70:71], v96 offset:37120
v_mfma_f32_16x16x32_bf16 v[152:155], v[80:83], a[100:103], v[152:155]
ds_read_b64_tr_b16 v[72:73], v97 offset:32768
v_mfma_f32_16x16x32_bf16 v[152:155], v[84:87], a[116:119], v[152:155]
ds_read_b64_tr_b16 v[74:75], v97 offset:33024
v_mfma_f32_16x16x32_bf16 v[168:171], v[80:83], a[104:107], v[168:171]
ds_read_b64_tr_b16 v[76:77], v97 offset:36864
v_mfma_f32_16x16x32_bf16 v[168:171], v[84:87], a[120:123], v[168:171]
ds_read_b64_tr_b16 v[78:79], v97 offset:37120
v_mfma_f32_16x16x32_bf16 v[184:187], v[80:83], a[108:111], v[184:187]
s_add_u32 s52, s52, s76
s_addc_u32 s53, s53, 0
v_mfma_f32_16x16x32_bf16 v[184:187], v[84:87], a[124:127], v[184:187]
s_add_u32 s74, s55, 1
v_mfma_f32_16x16x32_bf16 v[140:143], v[88:91], a[96:99], v[140:143]
ds_read_b64_tr_b16 v[80:81], v96 offset:33280
s_lshl_b32 s70, s55, 15
v_mfma_f32_16x16x32_bf16 v[140:143], v[92:95], a[112:115], v[140:143]
ds_read_b64_tr_b16 v[82:83], v96 offset:33536
s_lshl_b32 s71, s55, 15
v_mfma_f32_16x16x32_bf16 v[156:159], v[88:91], a[100:103], v[156:159]
s_mov_b32 s69, 0
s_mov_b32 s88, 0
v_mfma_f32_16x16x32_bf16 v[156:159], v[92:95], a[116:119], v[156:159]
s_sub_u32 s72, s55, 1
s_mov_b32 s54, -1
v_mfma_f32_16x16x32_bf16 v[172:175], v[88:91], a[104:107], v[172:175]
ds_read_b64_tr_b16 v[84:85], v96 offset:37376
s_mov_b32 s56, s55
v_mfma_f32_16x16x32_bf16 v[172:175], v[92:95], a[120:123], v[172:175]
ds_read_b64_tr_b16 v[86:87], v96 offset:37632
s_add_u32 s73, s55, 2
v_mfma_f32_16x16x32_bf16 v[188:191], v[88:91], a[108:111], v[188:191]
s_add_u32 s94, s94, 0x3400000
s_addc_u32 s95, s95, 0
v_mfma_f32_16x16x32_bf16 v[188:191], v[92:95], a[124:127], v[188:191]
global_load_dwordx4 a[232:235], v113, s[46:47] offset:2048 sc1
s_waitcnt vmcnt(43) lgkmcnt(8)
v_mfma_f32_16x16x32_bf16 v[192:195], v[64:67], a[128:131], v[192:195]
ds_read_b64_tr_b16 v[88:89], v97 offset:33280
v_mfma_f32_16x16x32_bf16 v[192:195], v[68:71], a[144:147], v[192:195]
ds_read_b64_tr_b16 v[90:91], v97 offset:33536
v_mfma_f32_16x16x32_bf16 v[208:211], v[64:67], a[132:135], v[208:211]
s_add_u32 s94, s94, s91
s_addc_u32 s95, s95, 0
v_mfma_f32_16x16x32_bf16 v[208:211], v[68:71], a[148:151], v[208:211]
s_lshl_b32 s91, s29, 2
s_sub_u32 s91, 60, s91
v_mfma_f32_16x16x32_bf16 v[224:227], v[64:67], a[136:139], v[224:227]
ds_read_b64_tr_b16 v[92:93], v97 offset:37376
s_add_u32 s91, s91, %2
v_mfma_f32_16x16x32_bf16 v[224:227], v[68:71], a[152:155], v[224:227]
ds_read_b64_tr_b16 v[94:95], v97 offset:37632
s_mul_i32 s91, s91, 376832
v_mfma_f32_16x16x32_bf16 v[240:243], v[64:67], a[140:143], v[240:243]
s_add_u32 s91, s91, s98
s_mov_b64 s[96:97], s[10:11]
v_mfma_f32_16x16x32_bf16 v[240:243], v[68:71], a[156:159], v[240:243]
global_load_dwordx4 a[236:239], v115, s[46:47] offset:2048 sc1
s_waitcnt lgkmcnt(8)
v_mfma_f32_16x16x32_bf16 v[196:199], v[72:75], a[128:131], v[196:199]
ds_read_b64_tr_b16 v[64:65], v96 offset:40960
v_mfma_f32_16x16x32_bf16 v[196:199], v[76:79], a[144:147], v[196:199]
ds_read_b64_tr_b16 v[66:67], v96 offset:41216
v_mfma_f32_16x16x32_bf16 v[212:215], v[72:75], a[132:135], v[212:215]
s_add_u32 s96, s96, 0x3400000
s_addc_u32 s97, s97, 0
v_mfma_f32_16x16x32_bf16 v[212:215], v[76:79], a[148:151], v[212:215]
s_add_u32 s96, s96, s91
s_addc_u32 s97, s97, 0
v_mfma_f32_16x16x32_bf16 v[228:231], v[72:75], a[136:139], v[228:231]
ds_read_b64_tr_b16 v[68:69], v96 offset:45056
v_xor_b32 v117, 0x0, v22
v_mfma_f32_16x16x32_bf16 v[228:231], v[76:79], a[152:155], v[228:231]
ds_read_b64_tr_b16 v[70:71], v96 offset:45312
v_bfe_u32 v21, v22, 4, 4
v_mfma_f32_16x16x32_bf16 v[244:247], v[72:75], a[140:143], v[244:247]
v_lshlrev_b32 v21, 10, v21
v_lshrrev_b32 v20, 8, v22
v_mfma_f32_16x16x32_bf16 v[244:247], v[76:79], a[156:159], v[244:247]
v_lshl_add_u32 v21, v20, 4, v21
s_waitcnt lgkmcnt(8)
v_mfma_f32_16x16x32_bf16 v[200:203], v[80:83], a[128:131], v[200:203]
ds_read_b64_tr_b16 v[72:73], v97 offset:40960
v_add_u32 v3, s75, v22
)"
R"(v_mfma_f32_16x16x32_bf16 v[200:203], v[84:87], a[144:147], v[200:203]
ds_read_b64_tr_b16 v[74:75], v97 offset:41216
v_add_u32 v4, 0x1000, v3
v_mfma_f32_16x16x32_bf16 v[216:219], v[80:83], a[132:135], v[216:219]
v_and_b32 v1, 0x2000, v3
v_lshl_add_u32 v1, v1, 1, v22
v_mfma_f32_16x16x32_bf16 v[216:219], v[84:87], a[148:151], v[216:219]
v_add_u32 v2, 0x10000, v1
v_bfe_u32 v18, v22, 4, 4
v_mfma_f32_16x16x32_bf16 v[232:235], v[80:83], a[136:139], v[232:235]
ds_read_b64_tr_b16 v[76:77], v97 offset:45056
v_lshrrev_b32 v19, 8, v22
v_mfma_f32_16x16x32_bf16 v[232:235], v[84:87], a[152:155], v[232:235]
ds_read_b64_tr_b16 v[78:79], v97 offset:45312
v_lshlrev_b32 v19, 2, v19
v_mfma_f32_16x16x32_bf16 v[248:251], v[80:83], a[140:143], v[248:251]
v_sub_u32 v18, v18, v19
v_mov_b32 v20, 0xff800000
v_mfma_f32_16x16x32_bf16 v[248:251], v[84:87], a[156:159], v[248:251]
v_xor_b32 v23, 0x22000, v3
s_waitcnt lgkmcnt(8)
v_mfma_f32_16x16x32_bf16 v[204:207], v[88:91], a[128:131], v[204:207]
ds_read_b64_tr_b16 v[80:81], v96 offset:41472
v_mov_b32 v10, 0
v_mfma_f32_16x16x32_bf16 v[204:207], v[92:95], a[144:147], v[204:207]
ds_read_b64_tr_b16 v[82:83], v96 offset:41728
v_mov_b32 v11, 0
v_mfma_f32_16x16x32_bf16 v[220:223], v[88:91], a[132:135], v[220:223]
v_mov_b32 v12, 0
v_mov_b32 v13, 0
v_mfma_f32_16x16x32_bf16 v[220:223], v[92:95], a[148:151], v[220:223]
v_mov_b32 v14, 0
v_mov_b32 v15, 0
v_mfma_f32_16x16x32_bf16 v[236:239], v[88:91], a[136:139], v[236:239]
ds_read_b64_tr_b16 v[84:85], v96 offset:45568
v_mov_b32 v16, 0
v_mfma_f32_16x16x32_bf16 v[236:239], v[92:95], a[152:155], v[236:239]
ds_read_b64_tr_b16 v[86:87], v96 offset:45824
v_mov_b32 v17, 0
v_mfma_f32_16x16x32_bf16 v[252:255], v[88:91], a[140:143], v[252:255]
global_load_dwordx4 a[240:243], v113, s[44:45] offset:3072 sc1
v_lshl_add_u32 v19, v9, 6, v7
v_mfma_f32_16x16x32_bf16 v[252:255], v[92:95], a[156:159], v[252:255]
s_waitcnt vmcnt(34) lgkmcnt(8)
v_mfma_f32_16x16x32_bf16 v[192:195], v[64:67], a[160:163], v[192:195]
ds_read_b64_tr_b16 v[88:89], v97 offset:41472
v_mfma_f32_16x16x32_bf16 v[192:195], v[68:71], a[176:179], v[192:195]
ds_read_b64_tr_b16 v[90:91], v97 offset:41728
v_mfma_f32_16x16x32_bf16 v[208:211], v[64:67], a[164:167], v[208:211]
global_load_dwordx4 a[244:247], v115, s[44:45] offset:3072 sc1
v_mfma_f32_16x16x32_bf16 v[208:211], v[68:71], a[180:183], v[208:211]
s_cmp_le_u32 s73, 12
s_cselect_b32 s79, s73, 2
v_mfma_f32_16x16x32_bf16 v[224:227], v[64:67], a[168:171], v[224:227]
ds_read_b64_tr_b16 v[92:93], v97 offset:45568
s_lshl_b32 s27, 6, s55
v_mfma_f32_16x16x32_bf16 v[224:227], v[68:71], a[184:187], v[224:227]
ds_read_b64_tr_b16 v[94:95], v97 offset:45824
s_and_b32 s27, s27, 0xffff
v_mfma_f32_16x16x32_bf16 v[240:243], v[64:67], a[172:175], v[240:243]
global_load_dwordx4 a[248:251], v113, s[46:47] offset:3072 sc1
s_bitset1_b32 s27, s74
v_mfma_f32_16x16x32_bf16 v[240:243], v[68:71], a[188:191], v[240:243]
s_bitset1_b32 s27, s79
s_waitcnt lgkmcnt(8)
v_mfma_f32_16x16x32_bf16 v[196:199], v[72:75], a[160:163], v[196:199]
ds_read_b64_tr_b16 v[64:65], v96 offset:49152
s_or_b32 s57, s27, 0x8000
v_mfma_f32_16x16x32_bf16 v[196:199], v[76:79], a[176:179], v[196:199]
ds_read_b64_tr_b16 v[66:67], v96 offset:49408
s_add_u32 m0, s75, 98304
v_mfma_f32_16x16x32_bf16 v[212:215], v[72:75], a[164:167], v[212:215]
global_load_dwordx4 a[252:255], v115, s[46:47] offset:3072 sc1
s_xor_b32 s48, s65, 128
v_mfma_f32_16x16x32_bf16 v[212:215], v[76:79], a[180:183], v[212:215]
s_lshl_b32 s76, s55, 5
s_add_u32 s76, s76, s28
v_mfma_f32_16x16x32_bf16 v[228:231], v[72:75], a[168:171], v[228:231]
ds_read_b64_tr_b16 v[68:69], v96 offset:53248
s_lshl2_add_u32 s76, s76, 49152
v_mfma_f32_16x16x32_bf16 v[228:231], v[76:79], a[184:187], v[228:231]
ds_read_b64_tr_b16 v[70:71], v96 offset:53504
s_min_u32 s66, s28, 20
v_mfma_f32_16x16x32_bf16 v[244:247], v[72:75], a[172:175], v[244:247]
buffer_load_dwordx4 v104, s[84:87], s48 offen offset:0 lds
s_mul_i32 s66, s66, 205
v_mfma_f32_16x16x32_bf16 v[244:247], v[76:79], a[188:191], v[244:247]
s_and_b32 s66, s66, 0x1800
s_waitcnt lgkmcnt(8)
v_mfma_f32_16x16x32_bf16 v[200:203], v[80:83], a[160:163], v[200:203]
ds_read_b64_tr_b16 v[72:73], v97 offset:49152
s_add_u32 s76, s76, s66
v_mfma_f32_16x16x32_bf16 v[200:203], v[84:87], a[176:179], v[200:203]
ds_read_b64_tr_b16 v[74:75], v97 offset:49408
s_lshr_b32 s47, s30, 8
v_mfma_f32_16x16x32_bf16 v[216:219], v[80:83], a[164:167], v[216:219]
buffer_load_dwordx4 v105, s[84:87], s48 offen offset:1024 lds
s_and_b32 s47, s47, 14
v_mfma_f32_16x16x32_bf16 v[216:219], v[84:87], a[180:183], v[216:219]
s_add_u32 s66, s92, s76
s_addc_u32 s67, s93, 0
v_mfma_f32_16x16x32_bf16 v[232:235], v[80:83], a[168:171], v[232:235]
ds_read_b64_tr_b16 v[76:77], v97 offset:53248
s_xor_b32 s76, s76, 1920
v_mfma_f32_16x16x32_bf16 v[232:235], v[84:87], a[184:187], v[232:235]
ds_read_b64_tr_b16 v[78:79], v97 offset:53504
s_add_u32 s47, s101, s47
v_mfma_f32_16x16x32_bf16 v[248:251], v[80:83], a[172:175], v[248:251]
buffer_load_dwordx4 v106, s[84:87], s48 offen offset:2048 lds
s_mov_b32 s46, s47
v_mfma_f32_16x16x32_bf16 v[248:251], v[84:87], a[188:191], v[248:251]
s_mov_b32 s29, 1
s_waitcnt lgkmcnt(8)
v_mfma_f32_16x16x32_bf16 v[204:207], v[88:91], a[160:163], v[204:207]
ds_read_b64_tr_b16 v[80:81], v96 offset:49664
s_mov_b32 s98, 128
v_mfma_f32_16x16x32_bf16 v[204:207], v[92:95], a[176:179], v[204:207]
ds_read_b64_tr_b16 v[82:83], v96 offset:49920
s_add_u32 s46, s46, s29
v_mfma_f32_16x16x32_bf16 v[220:223], v[88:91], a[164:167], v[220:223]
buffer_load_dwordx4 v107, s[84:87], s48 offen offset:3072 lds
v_mfma_f32_16x16x32_bf16 v[220:223], v[92:95], a[180:183], v[220:223]
s_add_u32 s16, s92, s76
s_addc_u32 s17, s93, 0
v_mfma_f32_16x16x32_bf16 v[236:239], v[88:91], a[168:171], v[236:239]
ds_read_b64_tr_b16 v[84:85], v96 offset:53760
s_and_b32 s76, s26, 7
v_mfma_f32_16x16x32_bf16 v[236:239], v[92:95], a[184:187], v[236:239]
ds_read_b64_tr_b16 v[86:87], v96 offset:54016
s_lshl_b32 s76, s76, 7
v_mfma_f32_16x16x32_bf16 v[252:255], v[88:91], a[172:175], v[252:255]
s_add_u32 s76, s76, 4096
v_mfma_f32_16x16x32_bf16 v[252:255], v[92:95], a[188:191], v[252:255]
s_waitcnt vmcnt(30) lgkmcnt(8)
v_mfma_f32_16x16x32_bf16 v[192:195], v[64:67], a[192:195], v[192:195]
ds_read_b64_tr_b16 v[88:89], v97 offset:49664
v_mfma_f32_16x16x32_bf16 v[192:195], v[68:71], a[208:211], v[192:195]
ds_read_b64_tr_b16 v[90:91], v97 offset:49920
v_mfma_f32_16x16x32_bf16 v[208:211], v[64:67], a[196:199], v[208:211]
s_add_u32 s18, s92, s76
s_addc_u32 s19, s93, 0
v_mfma_f32_16x16x32_bf16 v[208:211], v[68:71], a[212:215], v[208:211]
s_mov_b32 s78, 0x41a00000
s_mov_b32 s68, 1
v_mfma_f32_16x16x32_bf16 v[224:227], v[64:67], a[200:203], v[224:227]
ds_read_b64_tr_b16 v[92:93], v97 offset:53760
s_and_b32 s76, %3, 4
v_mfma_f32_16x16x32_bf16 v[224:227], v[68:71], a[216:219], v[224:227]
ds_read_b64_tr_b16 v[94:95], v97 offset:54016
s_lshl_b32 s76, s76, 14
v_mfma_f32_16x16x32_bf16 v[240:243], v[64:67], a[204:207], v[240:243]
s_add_u32 s18, s18, s76
s_addc_u32 s19, s19, 0
v_mfma_f32_16x16x32_bf16 v[240:243], v[68:71], a[220:223], v[240:243]
s_and_b32 s76, %3, 16
s_waitcnt lgkmcnt(8)
v_mfma_f32_16x16x32_bf16 v[196:199], v[72:75], a[192:195], v[196:199]
ds_read_b64_tr_b16 v[64:65], v96 offset:57344
s_lshl_b32 s76, s76, 12
v_mfma_f32_16x16x32_bf16 v[196:199], v[76:79], a[208:211], v[196:199]
ds_read_b64_tr_b16 v[66:67], v96 offset:57600
v_mfma_f32_16x16x32_bf16 v[212:215], v[72:75], a[196:199], v[212:215]
s_add_u32 s66, s66, s76
s_addc_u32 s67, s67, 0
v_mfma_f32_16x16x32_bf16 v[212:215], v[76:79], a[212:215], v[212:215]
s_and_b32 s76, %3, 16
s_lshl_b32 s76, s76, 22
v_mfma_f32_16x16x32_bf16 v[228:231], v[72:75], a[200:203], v[228:231]
ds_read_b64_tr_b16 v[68:69], v96 offset:61440
v_mfma_f32_16x16x32_bf16 v[228:231], v[76:79], a[216:219], v[228:231]
ds_read_b64_tr_b16 v[70:71], v96 offset:61696
v_mfma_f32_16x16x32_bf16 v[244:247], v[72:75], a[204:207], v[244:247]
s_add_u32 s50, s50, s76
s_addc_u32 s51, s51, 0
v_mfma_f32_16x16x32_bf16 v[244:247], v[76:79], a[220:223], v[244:247]
v_mov_b32 v28, -1
s_waitcnt lgkmcnt(8)
v_mfma_f32_16x16x32_bf16 v[200:203], v[80:83], a[192:195], v[200:203]
ds_read_b64_tr_b16 v[72:73], v97 offset:57344
v_mov_b32 v29, -1
v_mfma_f32_16x16x32_bf16 v[200:203], v[84:87], a[208:211], v[200:203]
ds_read_b64_tr_b16 v[74:75], v97 offset:57600
v_mov_b32 v30, -1
v_mfma_f32_16x16x32_bf16 v[216:219], v[80:83], a[196:199], v[216:219]
v_mov_b32 v31, -1
s_lshl_b32 s77, s26, 2
v_mfma_f32_16x16x32_bf16 v[216:219], v[84:87], a[212:215], v[216:219]
s_add_u32 s77, s77, s31
s_lshl_b32 s77, s77, 2
v_mfma_f32_16x16x32_bf16 v[232:235], v[80:83], a[200:203], v[232:235]
ds_read_b64_tr_b16 v[76:77], v97 offset:61440
s_add_u32 s77, s77, %2
v_mfma_f32_16x16x32_bf16 v[232:235], v[84:87], a[216:219], v[232:235]
ds_read_b64_tr_b16 v[78:79], v97 offset:61696
s_lshl_b32 s77, s77, 13
v_mfma_f32_16x16x32_bf16 v[248:251], v[80:83], a[204:207], v[248:251]
s_and_b32 s44, %3, 4
s_lshl_b32 s44, s44, 22
v_mfma_f32_16x16x32_bf16 v[248:251], v[84:87], a[220:223], v[248:251]
s_add_u32 s77, s77, s44
s_waitcnt lgkmcnt(8)
v_mfma_f32_16x16x32_bf16 v[204:207], v[88:91], a[192:195], v[204:207]
ds_read_b64_tr_b16 v[80:81], v96 offset:57856
s_mov_b64 s[44:45], s[10:11]
v_mfma_f32_16x16x32_bf16 v[204:207], v[92:95], a[208:211], v[204:207]
ds_read_b64_tr_b16 v[82:83], v96 offset:58112
s_mov_b32 s58, 0x41c00000
v_mfma_f32_16x16x32_bf16 v[220:223], v[88:91], a[196:199], v[220:223]
s_add_u32 s44, s44, 0x5001000
s_addc_u32 s45, s45, 0
v_mfma_f32_16x16x32_bf16 v[220:223], v[92:95], a[212:215], v[220:223]
s_add_u32 s44, s44, s77
s_addc_u32 s45, s45, 0
v_mfma_f32_16x16x32_bf16 v[236:239], v[88:91], a[200:203], v[236:239]
ds_read_b64_tr_b16 v[84:85], v96 offset:61952
v_mfma_f32_16x16x32_bf16 v[236:239], v[92:95], a[216:219], v[236:239]
ds_read_b64_tr_b16 v[86:87], v96 offset:62208
v_mfma_f32_16x16x32_bf16 v[252:255], v[88:91], a[204:207], v[252:255]
s_bitcmp1_b32 %3, 2
s_cselect_b32 s58, 0xff800000, s58
v_mfma_f32_16x16x32_bf16 v[252:255], v[92:95], a[220:223], v[252:255]
s_mov_b32 s76, 0x5d800000
s_waitcnt vmcnt(4) lgkmcnt(8)
v_mfma_f32_16x16x32_bf16 v[192:195], v[64:67], a[224:227], v[192:195]
ds_read_b64_tr_b16 v[88:89], v97 offset:57856
v_mfma_f32_16x16x32_bf16 v[192:195], v[68:71], a[240:243], v[192:195]
ds_read_b64_tr_b16 v[90:91], v97 offset:58112
v_mfma_f32_16x16x32_bf16 v[208:211], v[64:67], a[228:231], v[208:211]
s_bitcmp1_b32 %3, 2
s_cselect_b32 s76, 0x7f800000, s76
v_mfma_f32_16x16x32_bf16 v[208:211], v[68:71], a[244:247], v[208:211]
s_cmp_ge_u32 s46, s100
s_cselect_b32 s46, s101, s46
v_mfma_f32_16x16x32_bf16 v[224:227], v[64:67], a[232:235], v[224:227]
ds_read_b64_tr_b16 v[92:93], v97 offset:61952
s_add_u32 s46, s46, s29
v_mfma_f32_16x16x32_bf16 v[224:227], v[68:71], a[248:251], v[224:227]
ds_read_b64_tr_b16 v[94:95], v97 offset:62208
v_mfma_f32_16x16x32_bf16 v[240:243], v[64:67], a[236:239], v[240:243]
s_cmp_ge_u32 s46, s100
s_cselect_b32 s46, s101, s46
v_mfma_f32_16x16x32_bf16 v[240:243], v[68:71], a[252:255], v[240:243]
s_waitcnt lgkmcnt(8)
v_mfma_f32_16x16x32_bf16 v[196:199], v[72:75], a[224:227], v[196:199]
v_mfma_f32_16x16x32_bf16 v[196:199], v[76:79], a[240:243], v[196:199]
v_mfma_f32_16x16x32_bf16 v[212:215], v[72:75], a[228:231], v[212:215]
v_mfma_f32_16x16x32_bf16 v[212:215], v[76:79], a[244:247], v[212:215]
v_mfma_f32_16x16x32_bf16 v[228:231], v[72:75], a[232:235], v[228:231]
v_mfma_f32_16x16x32_bf16 v[228:231], v[76:79], a[248:251], v[228:231]
s_waitcnt vmcnt(0) lgkmcnt(0)
s_barrier
v_mfma_f32_16x16x32_bf16 v[244:247], v[72:75], a[236:239], v[244:247]
ds_read_b64_tr_b16 v[64:65], v98 offset:0
s_cmp_lg_u32 %3, 0
s_cbranch_scc1 W0S86_%=
v_mov_b32 v24, 1
s_mov_b64 exec, 2
global_atomic_add v101, v24, s[92:93] sc1
s_mov_b64 exec, -1
W0S86_%=:
v_mfma_f32_16x16x32_bf16 v[244:247], v[76:79], a[252:255], v[244:247]
ds_read_b64_tr_b16 v[66:67], v98 offset:256
v_mfma_f32_16x16x32_bf16 v[200:203], v[80:83], a[224:227], v[200:203]
ds_read_b64_tr_b16 v[68:69], v98 offset:8192
v_mfma_f32_16x16x32_bf16 v[200:203], v[84:87], a[240:243], v[200:203]
ds_read_b64_tr_b16 v[70:71], v98 offset:8448
v_mfma_f32_16x16x32_bf16 v[216:219], v[80:83], a[228:231], v[216:219]
ds_read_b64_tr_b16 v[72:73], v99 offset:0
v_mfma_f32_16x16x32_bf16 v[216:219], v[84:87], a[244:247], v[216:219]
ds_read_b64_tr_b16 v[74:75], v99 offset:256
v_mfma_f32_16x16x32_bf16 v[232:235], v[80:83], a[232:235], v[232:235]
ds_read_b64_tr_b16 v[76:77], v99 offset:8192
v_mfma_f32_16x16x32_bf16 v[232:235], v[84:87], a[248:251], v[232:235]
ds_read_b64_tr_b16 v[78:79], v99 offset:8448
v_mfma_f32_16x16x32_bf16 v[248:251], v[80:83], a[236:239], v[248:251]
global_store_dwordx4 v22, v[28:31], s[44:45] offset:-4096 sc1
v_mfma_f32_16x16x32_bf16 v[248:251], v[84:87], a[252:255], v[248:251]
v_mfma_f32_16x16x32_bf16 v[204:207], v[88:91], a[224:227], v[204:207]
ds_read_b64_tr_b16 v[80:81], v98 offset:4096
v_mfma_f32_16x16x32_bf16 v[204:207], v[92:95], a[240:243], v[204:207]
ds_read_b64_tr_b16 v[82:83], v98 offset:4352
v_mfma_f32_16x16x32_bf16 v[220:223], v[88:91], a[228:231], v[220:223]
global_store_dwordx4 v22, v[28:31], s[44:45] offset:-3072 sc1
v_mfma_f32_16x16x32_bf16 v[220:223], v[92:95], a[244:247], v[220:223]
v_mfma_f32_16x16x32_bf16 v[236:239], v[88:91], a[232:235], v[236:239]
ds_read_b64_tr_b16 v[84:85], v98 offset:12288
v_mfma_f32_16x16x32_bf16 v[236:239], v[92:95], a[248:251], v[236:239]
ds_read_b64_tr_b16 v[86:87], v98 offset:12544
v_mfma_f32_16x16x32_bf16 v[252:255], v[88:91], a[236:239], v[252:255]
global_store_dwordx4 v22, v[28:31], s[44:45] offset:-2048 sc1
v_mfma_f32_16x16x32_bf16 v[252:255], v[92:95], a[252:255], v[252:255]
s_waitcnt lgkmcnt(8)
v_mfma_f32_16x16x32_bf16 v[128:131], v[64:67], a[128:131], v[128:131]
ds_read_b64_tr_b16 v[88:89], v99 offset:4096
v_mfma_f32_16x16x32_bf16 v[128:131], v[68:71], a[144:147], v[128:131]
ds_read_b64_tr_b16 v[90:91], v99 offset:4352
v_mfma_f32_16x16x32_bf16 v[144:147], v[64:67], a[132:135], v[144:147]
global_store_dwordx4 v22, v[28:31], s[44:45] offset:-1024 sc1
v_mfma_f32_16x16x32_bf16 v[144:147], v[68:71], a[148:151], v[144:147]
v_mfma_f32_16x16x32_bf16 v[160:163], v[64:67], a[136:139], v[160:163]
ds_read_b64_tr_b16 v[92:93], v99 offset:12288
v_mul_f32 v192, 0x3e0293ee, v192
v_mfma_f32_16x16x32_bf16 v[160:163], v[68:71], a[152:155], v[160:163]
ds_read_b64_tr_b16 v[94:95], v99 offset:12544
v_mul_f32 v193, 0x3e0293ee, v193
v_mfma_f32_16x16x32_bf16 v[176:179], v[64:67], a[140:143], v[176:179]
global_store_dwordx4 v22, v[28:31], s[44:45] offset:0 sc1
v_cvt_pk_bf16_f32 v32, v192, v193
v_mfma_f32_16x16x32_bf16 v[176:179], v[68:71], a[156:159], v[176:179]
v_mul_f32 v194, 0x3e0293ee, v194
s_waitcnt lgkmcnt(8)
v_mfma_f32_16x16x32_bf16 v[132:135], v[72:75], a[128:131], v[132:135]
ds_read_b64_tr_b16 v[64:65], v98 offset:16384
v_mul_f32 v195, 0x3e0293ee, v195
v_mfma_f32_16x16x32_bf16 v[132:135], v[76:79], a[144:147], v[132:135]
ds_read_b64_tr_b16 v[66:67], v98 offset:16640
s_add_u32 m0, s75, 102400
v_mfma_f32_16x16x32_bf16 v[148:151], v[72:75], a[132:135], v[148:151]
global_store_dwordx4 v22, v[28:31], s[44:45] offset:1024 sc1
s_xor_b32 s48, s65, 262272
v_mfma_f32_16x16x32_bf16 v[148:151], v[76:79], a[148:151], v[148:151]
v_cvt_pk_bf16_f32 v33, v194, v195
v_mul_f32 v196, 0x3e0293ee, v196
v_mfma_f32_16x16x32_bf16 v[164:167], v[72:75], a[136:139], v[164:167]
ds_read_b64_tr_b16 v[68:69], v98 offset:24576
v_mul_f32 v197, 0x3e0293ee, v197
v_mfma_f32_16x16x32_bf16 v[164:167], v[76:79], a[152:155], v[164:167]
ds_read_b64_tr_b16 v[70:71], v98 offset:24832
v_cvt_pk_bf16_f32 v34, v196, v197
v_mfma_f32_16x16x32_bf16 v[180:183], v[72:75], a[140:143], v[180:183]
buffer_load_dwordx4 v104, s[84:87], s48 offen offset:0 lds
v_mul_f32 v198, 0x3e0293ee, v198
v_mfma_f32_16x16x32_bf16 v[180:183], v[76:79], a[156:159], v[180:183]
v_mul_f32 v199, 0x3e0293ee, v199
s_waitcnt lgkmcnt(8)
v_mfma_f32_16x16x32_bf16 v[136:139], v[80:83], a[128:131], v[136:139]
ds_read_b64_tr_b16 v[72:73], v99 offset:16384
v_cvt_pk_bf16_f32 v35, v198, v199
v_mfma_f32_16x16x32_bf16 v[136:139], v[84:87], a[144:147], v[136:139]
ds_read_b64_tr_b16 v[74:75], v99 offset:16640
v_mul_f32 v200, 0x3e0293ee, v200
v_mfma_f32_16x16x32_bf16 v[152:155], v[80:83], a[132:135], v[152:155]
buffer_load_dwordx4 v105, s[84:87], s48 offen offset:1024 lds
v_mul_f32 v201, 0x3e0293ee, v201
v_mfma_f32_16x16x32_bf16 v[152:155], v[84:87], a[148:151], v[152:155]
v_cvt_pk_bf16_f32 v36, v200, v201
s_mov_b32 s44, 14
v_mfma_f32_16x16x32_bf16 v[168:171], v[80:83], a[136:139], v[168:171]
ds_read_b64_tr_b16 v[76:77], v99 offset:24576
v_mul_f32 v202, 0x3e0293ee, v202
v_mfma_f32_16x16x32_bf16 v[168:171], v[84:87], a[152:155], v[168:171]
ds_read_b64_tr_b16 v[78:79], v99 offset:24832
v_mul_f32 v203, 0x3e0293ee, v203
v_mfma_f32_16x16x32_bf16 v[184:187], v[80:83], a[140:143], v[184:187]
buffer_load_dwordx4 v106, s[84:87], s48 offen offset:2048 lds
s_mov_b32 s45, 64
v_mfma_f32_16x16x32_bf16 v[184:187], v[84:87], a[156:159], v[184:187]
v_cvt_pk_bf16_f32 v37, v202, v203
s_waitcnt lgkmcnt(8)
v_mfma_f32_16x16x32_bf16 v[140:143], v[88:91], a[128:131], v[140:143]
ds_read_b64_tr_b16 v[80:81], v98 offset:20480
v_mul_f32 v204, 0x3e0293ee, v204
v_mfma_f32_16x16x32_bf16 v[140:143], v[92:95], a[144:147], v[140:143]
ds_read_b64_tr_b16 v[82:83], v98 offset:20736
v_mul_f32 v205, 0x3e0293ee, v205
v_mfma_f32_16x16x32_bf16 v[156:159], v[88:91], a[132:135], v[156:159]
buffer_load_dwordx4 v107, s[84:87], s48 offen offset:3072 lds
s_add_u32 m0, s75, 32768
v_mfma_f32_16x16x32_bf16 v[156:159], v[92:95], a[148:151], v[156:159]
s_xor_b32 s48, s65, 2097280
v_cvt_pk_bf16_f32 v38, v204, v205
v_mfma_f32_16x16x32_bf16 v[172:175], v[88:91], a[136:139], v[172:175]
ds_read_b64_tr_b16 v[84:85], v98 offset:28672
v_mul_f32 v206, 0x3e0293ee, v206
v_mfma_f32_16x16x32_bf16 v[172:175], v[92:95], a[152:155], v[172:175]
ds_read_b64_tr_b16 v[86:87], v98 offset:28928
v_mul_f32 v207, 0x3e0293ee, v207
v_mfma_f32_16x16x32_bf16 v[188:191], v[88:91], a[140:143], v[188:191]
buffer_load_dwordx4 v104, s[84:87], s48 offen offset:0 lds
v_cvt_pk_bf16_f32 v39, v206, v207
v_mfma_f32_16x16x32_bf16 v[188:191], v[92:95], a[156:159], v[188:191]
s_waitcnt lgkmcnt(8)
v_mfma_f32_16x16x32_bf16 v[128:131], v[64:67], a[160:163], v[128:131]
ds_read_b64_tr_b16 v[88:89], v99 offset:20480
v_mul_f32 v208, 0x3e0293ee, v208
v_mfma_f32_16x16x32_bf16 v[128:131], v[68:71], a[176:179], v[128:131]
ds_read_b64_tr_b16 v[90:91], v99 offset:20736
v_mul_f32 v209, 0x3e0293ee, v209
v_mfma_f32_16x16x32_bf16 v[144:147], v[64:67], a[164:167], v[144:147]
buffer_load_dwordx4 v105, s[84:87], s48 offen offset:1024 lds
v_cvt_pk_bf16_f32 v40, v208, v209
v_mfma_f32_16x16x32_bf16 v[144:147], v[68:71], a[180:183], v[144:147]
v_mfma_f32_16x16x32_bf16 v[160:163], v[64:67], a[168:171], v[160:163]
ds_read_b64_tr_b16 v[92:93], v99 offset:28672
v_mul_f32 v210, 0x3e0293ee, v210
v_mfma_f32_16x16x32_bf16 v[160:163], v[68:71], a[184:187], v[160:163]
ds_read_b64_tr_b16 v[94:95], v99 offset:28928
v_mul_f32 v211, 0x3e0293ee, v211
v_mfma_f32_16x16x32_bf16 v[176:179], v[64:67], a[172:175], v[176:179]
buffer_load_dwordx4 v106, s[84:87], s48 offen offset:2048 lds
)"
R"(v_cvt_pk_bf16_f32 v41, v210, v211
v_mfma_f32_16x16x32_bf16 v[176:179], v[68:71], a[188:191], v[176:179]
v_mul_f32 v212, 0x3e0293ee, v212
s_waitcnt lgkmcnt(8)
v_mfma_f32_16x16x32_bf16 v[132:135], v[72:75], a[160:163], v[132:135]
ds_read_b64_tr_b16 v[64:65], v26 offset:0
v_mfma_f32_16x16x32_bf16 v[132:135], v[76:79], a[176:179], v[132:135]
ds_read_b64_tr_b16 v[66:67], v26 offset:256
v_mul_f32 v213, 0x3e0293ee, v213
v_mfma_f32_16x16x32_bf16 v[148:151], v[72:75], a[164:167], v[148:151]
buffer_load_dwordx4 v107, s[84:87], s48 offen offset:3072 lds
s_add_u32 m0, s75, 36864
v_mfma_f32_16x16x32_bf16 v[148:151], v[76:79], a[180:183], v[148:151]
s_xor_b32 s48, s65, 2359424
v_cvt_pk_bf16_f32 v42, v212, v213
v_mfma_f32_16x16x32_bf16 v[164:167], v[72:75], a[168:171], v[164:167]
ds_read_b64_tr_b16 v[68:69], v26 offset:8192
v_mul_f32 v214, 0x3e0293ee, v214
v_mfma_f32_16x16x32_bf16 v[164:167], v[76:79], a[184:187], v[164:167]
ds_read_b64_tr_b16 v[70:71], v26 offset:8448
v_mul_f32 v215, 0x3e0293ee, v215
v_mfma_f32_16x16x32_bf16 v[180:183], v[72:75], a[172:175], v[180:183]
buffer_load_dwordx4 v104, s[84:87], s48 offen offset:0 lds
v_cvt_pk_bf16_f32 v43, v214, v215
PAD34_%=:
s_cbranch_execz PAD16_%=
v_mfma_f32_16x16x32_bf16 v[180:183], v[76:79], a[188:191], v[180:183]
v_mul_f32 v216, 0x3e0293ee, v216
s_waitcnt lgkmcnt(8)
v_mfma_f32_16x16x32_bf16 v[136:139], v[80:83], a[160:163], v[136:139]
ds_read_b64_tr_b16 v[72:73], v27 offset:0
v_mul_f32 v217, 0x3e0293ee, v217
v_mfma_f32_16x16x32_bf16 v[136:139], v[84:87], a[176:179], v[136:139]
ds_read_b64_tr_b16 v[74:75], v27 offset:256
v_mfma_f32_16x16x32_bf16 v[152:155], v[80:83], a[164:167], v[152:155]
buffer_load_dwordx4 v105, s[84:87], s48 offen offset:1024 lds
v_cvt_pk_bf16_f32 v44, v216, v217
v_mfma_f32_16x16x32_bf16 v[152:155], v[84:87], a[180:183], v[152:155]
v_mul_f32 v218, 0x3e0293ee, v218
v_mfma_f32_16x16x32_bf16 v[168:171], v[80:83], a[168:171], v[168:171]
ds_read_b64_tr_b16 v[76:77], v27 offset:8192
v_mul_f32 v219, 0x3e0293ee, v219
v_mfma_f32_16x16x32_bf16 v[168:171], v[84:87], a[184:187], v[168:171]
ds_read_b64_tr_b16 v[78:79], v27 offset:8448
v_cvt_pk_bf16_f32 v45, v218, v219
v_mfma_f32_16x16x32_bf16 v[184:187], v[80:83], a[172:175], v[184:187]
buffer_load_dwordx4 v106, s[84:87], s48 offen offset:2048 lds
v_mfma_f32_16x16x32_bf16 v[184:187], v[84:87], a[188:191], v[184:187]
v_mul_f32 v220, 0x3e0293ee, v220
s_waitcnt lgkmcnt(8)
v_mfma_f32_16x16x32_bf16 v[140:143], v[88:91], a[160:163], v[140:143]
ds_read_b64_tr_b16 v[80:81], v26 offset:4096
v_mul_f32 v221, 0x3e0293ee, v221
v_mfma_f32_16x16x32_bf16 v[140:143], v[92:95], a[176:179], v[140:143]
ds_read_b64_tr_b16 v[82:83], v26 offset:4352
v_cvt_pk_bf16_f32 v46, v220, v221
v_mfma_f32_16x16x32_bf16 v[156:159], v[88:91], a[164:167], v[156:159]
buffer_load_dwordx4 v107, s[84:87], s48 offen offset:3072 lds
v_mfma_f32_16x16x32_bf16 v[156:159], v[92:95], a[180:183], v[156:159]
v_mul_f32 v222, 0x3e0293ee, v222
v_mfma_f32_16x16x32_bf16 v[172:175], v[88:91], a[168:171], v[172:175]
ds_read_b64_tr_b16 v[84:85], v26 offset:12288
v_mul_f32 v223, 0x3e0293ee, v223
v_mfma_f32_16x16x32_bf16 v[172:175], v[92:95], a[184:187], v[172:175]
ds_read_b64_tr_b16 v[86:87], v26 offset:12544
v_cvt_pk_bf16_f32 v47, v222, v223
v_mfma_f32_16x16x32_bf16 v[188:191], v[88:91], a[172:175], v[188:191]
v_mul_f32 v224, 0x3e0293ee, v224
v_mfma_f32_16x16x32_bf16 v[188:191], v[92:95], a[188:191], v[188:191]
s_waitcnt lgkmcnt(8)
v_mfma_f32_16x16x32_bf16 v[128:131], v[64:67], a[192:195], v[128:131]
ds_read_b64_tr_b16 v[88:89], v27 offset:4096
v_mul_f32 v225, 0x3e0293ee, v225
v_mfma_f32_16x16x32_bf16 v[128:131], v[68:71], a[208:211], v[128:131]
ds_read_b64_tr_b16 v[90:91], v27 offset:4352
v_cvt_pk_bf16_f32 v48, v224, v225
v_mfma_f32_16x16x32_bf16 v[144:147], v[64:67], a[196:199], v[144:147]
v_mul_f32 v226, 0x3e0293ee, v226
v_mfma_f32_16x16x32_bf16 v[144:147], v[68:71], a[212:215], v[144:147]
v_mfma_f32_16x16x32_bf16 v[160:163], v[64:67], a[200:203], v[160:163]
ds_read_b64_tr_b16 v[92:93], v27 offset:12288
v_mul_f32 v227, 0x3e0293ee, v227
v_mfma_f32_16x16x32_bf16 v[160:163], v[68:71], a[216:219], v[160:163]
ds_read_b64_tr_b16 v[94:95], v27 offset:12544
v_cvt_pk_bf16_f32 v49, v226, v227
v_mfma_f32_16x16x32_bf16 v[176:179], v[64:67], a[204:207], v[176:179]
v_mul_f32 v228, 0x3e0293ee, v228
v_mfma_f32_16x16x32_bf16 v[176:179], v[68:71], a[220:223], v[176:179]
v_mul_f32 v229, 0x3e0293ee, v229
s_waitcnt lgkmcnt(8)
v_mfma_f32_16x16x32_bf16 v[132:135], v[72:75], a[192:195], v[132:135]
ds_read_b64_tr_b16 v[64:65], v26 offset:16384
v_mfma_f32_16x16x32_bf16 v[132:135], v[76:79], a[208:211], v[132:135]
ds_read_b64_tr_b16 v[66:67], v26 offset:16640
s_cmp_lg_u32 %3, 0
s_cbranch_scc1 W0S87_%=
s_mov_b64 exec, 2
global_load_dword v24, v101, s[92:93] offset:0 sc1
s_mov_b64 exec, -1
W0S87_%=:
v_mfma_f32_16x16x32_bf16 v[148:151], v[72:75], a[196:199], v[148:151]
v_cvt_pk_bf16_f32 v50, v228, v229
v_mul_f32 v230, 0x3e0293ee, v230
v_mfma_f32_16x16x32_bf16 v[148:151], v[76:79], a[212:215], v[148:151]
v_mul_f32 v231, 0x3e0293ee, v231
v_mfma_f32_16x16x32_bf16 v[164:167], v[72:75], a[200:203], v[164:167]
ds_read_b64_tr_b16 v[68:69], v26 offset:24576
v_mfma_f32_16x16x32_bf16 v[164:167], v[76:79], a[216:219], v[164:167]
ds_read_b64_tr_b16 v[70:71], v26 offset:24832
v_cvt_pk_bf16_f32 v51, v230, v231
v_mfma_f32_16x16x32_bf16 v[180:183], v[72:75], a[204:207], v[180:183]
v_mul_f32 v232, 0x3e0293ee, v232
v_mfma_f32_16x16x32_bf16 v[180:183], v[76:79], a[220:223], v[180:183]
v_mul_f32 v233, 0x3e0293ee, v233
s_waitcnt lgkmcnt(8)
v_mfma_f32_16x16x32_bf16 v[136:139], v[80:83], a[192:195], v[136:139]
ds_read_b64_tr_b16 v[72:73], v27 offset:16384
v_cvt_pk_bf16_f32 v52, v232, v233
v_mfma_f32_16x16x32_bf16 v[136:139], v[84:87], a[208:211], v[136:139]
ds_read_b64_tr_b16 v[74:75], v27 offset:16640
v_mfma_f32_16x16x32_bf16 v[152:155], v[80:83], a[196:199], v[152:155]
v_mul_f32 v234, 0x3e0293ee, v234
v_mfma_f32_16x16x32_bf16 v[152:155], v[84:87], a[212:215], v[152:155]
v_mul_f32 v235, 0x3e0293ee, v235
v_mfma_f32_16x16x32_bf16 v[168:171], v[80:83], a[200:203], v[168:171]
ds_read_b64_tr_b16 v[76:77], v27 offset:24576
v_cvt_pk_bf16_f32 v53, v234, v235
v_mfma_f32_16x16x32_bf16 v[168:171], v[84:87], a[216:219], v[168:171]
ds_read_b64_tr_b16 v[78:79], v27 offset:24832
v_mul_f32 v236, 0x3e0293ee, v236
v_mfma_f32_16x16x32_bf16 v[184:187], v[80:83], a[204:207], v[184:187]
v_mfma_f32_16x16x32_bf16 v[184:187], v[84:87], a[220:223], v[184:187]
v_mul_f32 v237, 0x3e0293ee, v237
s_waitcnt lgkmcnt(8)
v_mfma_f32_16x16x32_bf16 v[140:143], v[88:91], a[192:195], v[140:143]
ds_read_b64_tr_b16 v[80:81], v26 offset:20480
v_cvt_pk_bf16_f32 v54, v236, v237
v_mfma_f32_16x16x32_bf16 v[140:143], v[92:95], a[208:211], v[140:143]
ds_read_b64_tr_b16 v[82:83], v26 offset:20736
v_mul_f32 v238, 0x3e0293ee, v238
v_mfma_f32_16x16x32_bf16 v[156:159], v[88:91], a[196:199], v[156:159]
v_mfma_f32_16x16x32_bf16 v[156:159], v[92:95], a[212:215], v[156:159]
v_mul_f32 v239, 0x3e0293ee, v239
v_mfma_f32_16x16x32_bf16 v[172:175], v[88:91], a[200:203], v[172:175]
ds_read_b64_tr_b16 v[84:85], v26 offset:28672
v_cvt_pk_bf16_f32 v55, v238, v239
v_mfma_f32_16x16x32_bf16 v[172:175], v[92:95], a[216:219], v[172:175]
ds_read_b64_tr_b16 v[86:87], v26 offset:28928
v_mul_f32 v240, 0x3e0293ee, v240
v_mfma_f32_16x16x32_bf16 v[188:191], v[88:91], a[204:207], v[188:191]
v_mul_f32 v241, 0x3e0293ee, v241
v_mfma_f32_16x16x32_bf16 v[188:191], v[92:95], a[220:223], v[188:191]
s_waitcnt lgkmcnt(8)
v_mfma_f32_16x16x32_bf16 v[128:131], v[64:67], a[224:227], v[128:131]
ds_read_b64_tr_b16 v[88:89], v27 offset:20480
v_cvt_pk_bf16_f32 v56, v240, v241
v_mfma_f32_16x16x32_bf16 v[128:131], v[68:71], a[240:243], v[128:131]
ds_read_b64_tr_b16 v[90:91], v27 offset:20736
v_mul_f32 v242, 0x3e0293ee, v242
v_mfma_f32_16x16x32_bf16 v[144:147], v[64:67], a[228:231], v[144:147]
v_mul_f32 v243, 0x3e0293ee, v243
v_mfma_f32_16x16x32_bf16 v[144:147], v[68:71], a[244:247], v[144:147]
v_mfma_f32_16x16x32_bf16 v[160:163], v[64:67], a[232:235], v[160:163]
ds_read_b64_tr_b16 v[92:93], v27 offset:28672
v_cvt_pk_bf16_f32 v57, v242, v243
v_mfma_f32_16x16x32_bf16 v[160:163], v[68:71], a[248:251], v[160:163]
ds_read_b64_tr_b16 v[94:95], v27 offset:28928
v_mul_f32 v244, 0x3e0293ee, v244
v_mfma_f32_16x16x32_bf16 v[176:179], v[64:67], a[236:239], v[176:179]
v_mul_f32 v245, 0x3e0293ee, v245
v_mfma_f32_16x16x32_bf16 v[176:179], v[68:71], a[252:255], v[176:179]
v_cvt_pk_bf16_f32 v58, v244, v245
s_waitcnt lgkmcnt(8)
v_mfma_f32_16x16x32_bf16 v[132:135], v[72:75], a[224:227], v[132:135]
v_mfma_f32_16x16x32_bf16 v[132:135], v[76:79], a[240:243], v[132:135]
v_mul_f32 v246, 0x3e0293ee, v246
v_mfma_f32_16x16x32_bf16 v[148:151], v[72:75], a[228:231], v[148:151]
v_mul_f32 v247, 0x3e0293ee, v247
v_mfma_f32_16x16x32_bf16 v[148:151], v[76:79], a[244:247], v[148:151]
v_cvt_pk_bf16_f32 v59, v246, v247
v_mfma_f32_16x16x32_bf16 v[164:167], v[72:75], a[232:235], v[164:167]
v_mfma_f32_16x16x32_bf16 v[164:167], v[76:79], a[248:251], v[164:167]
s_waitcnt vmcnt(0) lgkmcnt(0)
s_barrier
v_mfma_f32_16x16x32_bf16 v[180:183], v[72:75], a[236:239], v[180:183]
ds_read_b64_tr_b16 v[64:65], v98 offset:32768
v_mul_f32 v248, 0x3e0293ee, v248
v_mfma_f32_16x16x32_bf16 v[180:183], v[76:79], a[252:255], v[180:183]
ds_read_b64_tr_b16 v[66:67], v98 offset:33024
v_mfma_f32_16x16x32_bf16 v[136:139], v[80:83], a[224:227], v[136:139]
ds_read_b64_tr_b16 v[68:69], v98 offset:36864
v_mul_f32 v249, 0x3e0293ee, v249
v_mfma_f32_16x16x32_bf16 v[136:139], v[84:87], a[240:243], v[136:139]
ds_read_b64_tr_b16 v[70:71], v98 offset:37120
v_cvt_pk_bf16_f32 v60, v248, v249
v_mfma_f32_16x16x32_bf16 v[152:155], v[80:83], a[228:231], v[152:155]
ds_read_b64_tr_b16 v[72:73], v99 offset:32768
v_mul_f32 v250, 0x3e0293ee, v250
v_mfma_f32_16x16x32_bf16 v[152:155], v[84:87], a[244:247], v[152:155]
ds_read_b64_tr_b16 v[74:75], v99 offset:33024
v_mul_f32 v251, 0x3e0293ee, v251
v_mfma_f32_16x16x32_bf16 v[168:171], v[80:83], a[232:235], v[168:171]
ds_read_b64_tr_b16 v[76:77], v99 offset:36864
v_cvt_pk_bf16_f32 v61, v250, v251
v_mfma_f32_16x16x32_bf16 v[168:171], v[84:87], a[248:251], v[168:171]
ds_read_b64_tr_b16 v[78:79], v99 offset:37120
v_mul_f32 v252, 0x3e0293ee, v252
v_mfma_f32_16x16x32_bf16 v[184:187], v[80:83], a[236:239], v[184:187]
ds_write_b128 v118, v[32:35] offset:4096
v_mul_f32 v253, 0x3e0293ee, v253
v_mfma_f32_16x16x32_bf16 v[184:187], v[84:87], a[252:255], v[184:187]
s_cmp_lg_u32 %3, 0
s_cbranch_scc1 W0S88_%=
v_cmp_ne_u32 vcc, s90, v24
s_and_b32 s91, vcc_lo, 2
s_cbranch_scc1 HSP1S_%=
HOK1S_%=:
buffer_inv sc1
W0S88_%=:
v_mfma_f32_16x16x32_bf16 v[140:143], v[88:91], a[224:227], v[140:143]
ds_read_b64_tr_b16 v[80:81], v98 offset:33280
v_cvt_pk_bf16_f32 v62, v252, v253
v_mfma_f32_16x16x32_bf16 v[140:143], v[92:95], a[240:243], v[140:143]
ds_read_b64_tr_b16 v[82:83], v98 offset:33536
v_mul_f32 v254, 0x3e0293ee, v254
v_mfma_f32_16x16x32_bf16 v[156:159], v[88:91], a[228:231], v[156:159]
ds_write_b128 v118, v[36:39] offset:6144
v_mul_f32 v255, 0x3e0293ee, v255
v_mfma_f32_16x16x32_bf16 v[156:159], v[92:95], a[244:247], v[156:159]
ds_write_b128 v118, v[40:43] offset:5120
v_cvt_pk_bf16_f32 v63, v254, v255
v_mfma_f32_16x16x32_bf16 v[172:175], v[88:91], a[232:235], v[172:175]
ds_read_b64_tr_b16 v[84:85], v98 offset:37376
s_mov_b32 s90, 64
v_mfma_f32_16x16x32_bf16 v[172:175], v[92:95], a[248:251], v[172:175]
ds_read_b64_tr_b16 v[86:87], v98 offset:37632
v_mfma_f32_16x16x32_bf16 v[188:191], v[88:91], a[236:239], v[188:191]
ds_write_b128 v118, v[44:47] offset:7168
v_mfma_f32_16x16x32_bf16 v[188:191], v[92:95], a[252:255], v[188:191]
ds_write_b128 v118, v[48:51] offset:0
s_waitcnt lgkmcnt(13)
v_mfma_f32_16x16x32_bf16 v[192:195], v[64:67], a[0:3], 0
ds_read_b64_tr_b16 v[88:89], v99 offset:33280
v_mfma_f32_16x16x32_bf16 v[192:195], v[68:71], a[16:19], v[192:195]
ds_read_b64_tr_b16 v[90:91], v99 offset:33536
v_mfma_f32_16x16x32_bf16 v[208:211], v[64:67], a[4:7], 0
ds_write_b128 v118, v[52:55] offset:2048
v_mfma_f32_16x16x32_bf16 v[208:211], v[68:71], a[20:23], v[208:211]
ds_write_b128 v118, v[56:59] offset:1024
v_mfma_f32_16x16x32_bf16 v[224:227], v[64:67], a[8:11], 0
ds_read_b64_tr_b16 v[92:93], v99 offset:37376
v_mul_f32 v124, v132, v132
v_mfma_f32_16x16x32_bf16 v[224:227], v[68:71], a[24:27], v[224:227]
ds_read_b64_tr_b16 v[94:95], v99 offset:37632
v_mul_f32 v125, v133, v133
v_mfma_f32_16x16x32_bf16 v[240:243], v[64:67], a[12:15], 0
ds_write_b128 v118, v[60:63] offset:3072
v_mul_f32 v126, v134, v134
v_mfma_f32_16x16x32_bf16 v[240:243], v[68:71], a[28:31], v[240:243]
v_add_u32 v112, 0x20000, v3
s_waitcnt lgkmcnt(14)
v_mfma_f32_16x16x32_bf16 v[196:199], v[72:75], a[0:3], 0
ds_read_b64_tr_b16 v[64:65], v98 offset:40960
v_xor_b32 v113, 0x2000, v112
v_mfma_f32_16x16x32_bf16 v[196:199], v[76:79], a[16:19], v[196:199]
ds_read_b64_tr_b16 v[66:67], v98 offset:41216
v_mul_f32 v127, v135, v135
v_mfma_f32_16x16x32_bf16 v[212:215], v[72:75], a[4:7], 0
v_fma_f32 v124, v124, s49, v119
v_fma_f32 v125, v125, s49, v119
v_mfma_f32_16x16x32_bf16 v[212:215], v[76:79], a[20:23], v[212:215]
v_fma_f32 v126, v126, s49, v119
v_fma_f32 v127, v127, s49, v119
v_mfma_f32_16x16x32_bf16 v[228:231], v[72:75], a[8:11], 0
ds_read_b64_tr_b16 v[68:69], v98 offset:45056
v_mul_f32 v124, v124, v132
v_mfma_f32_16x16x32_bf16 v[228:231], v[76:79], a[24:27], v[228:231]
ds_read_b64_tr_b16 v[70:71], v98 offset:45312
v_mul_f32 v125, v125, v133
v_mfma_f32_16x16x32_bf16 v[244:247], v[72:75], a[12:15], 0
v_exp_f32 v124, v124
v_mfma_f32_16x16x32_bf16 v[244:247], v[76:79], a[28:31], v[244:247]
v_mul_f32 v126, v126, v134
s_waitcnt lgkmcnt(13)
v_mfma_f32_16x16x32_bf16 v[200:203], v[80:83], a[0:3], 0
ds_read_b64_tr_b16 v[72:73], v99 offset:40960
v_mul_f32 v127, v127, v135
v_mfma_f32_16x16x32_bf16 v[200:203], v[84:87], a[16:19], v[200:203]
ds_read_b64_tr_b16 v[74:75], v99 offset:41216
v_mul_f32 v140, v140, v132
v_mfma_f32_16x16x32_bf16 v[216:219], v[80:83], a[4:7], 0
v_exp_f32 v125, v125
v_mfma_f32_16x16x32_bf16 v[216:219], v[84:87], a[20:23], v[216:219]
v_exp_f32 v126, v126
v_mfma_f32_16x16x32_bf16 v[232:235], v[80:83], a[8:11], 0
ds_read_b64_tr_b16 v[76:77], v99 offset:45056
v_mul_f32 v141, v141, v133
v_mfma_f32_16x16x32_bf16 v[232:235], v[84:87], a[24:27], v[232:235]
ds_read_b64_tr_b16 v[78:79], v99 offset:45312
v_mul_f32 v142, v142, v134
v_mfma_f32_16x16x32_bf16 v[248:251], v[80:83], a[12:15], 0
v_exp_f32 v127, v127
v_mfma_f32_16x16x32_bf16 v[248:251], v[84:87], a[28:31], v[248:251]
v_mul_f32 v143, v143, v135
s_waitcnt lgkmcnt(9)
v_mfma_f32_16x16x32_bf16 v[204:207], v[88:91], a[0:3], 0
ds_read_b64_tr_b16 v[80:81], v98 offset:41472
v_add_f32 v124, 1.0, v124
v_mfma_f32_16x16x32_bf16 v[204:207], v[92:95], a[16:19], v[204:207]
ds_read_b64_tr_b16 v[82:83], v98 offset:41728
v_add_f32 v125, 1.0, v125
v_mfma_f32_16x16x32_bf16 v[220:223], v[88:91], a[4:7], 0
v_rcp_f32 v124, v124
v_mfma_f32_16x16x32_bf16 v[220:223], v[92:95], a[20:23], v[220:223]
v_rcp_f32 v125, v125
v_mfma_f32_16x16x32_bf16 v[236:239], v[88:91], a[8:11], 0
ds_read_b64_tr_b16 v[84:85], v98 offset:45568
v_add_f32 v126, 1.0, v126
v_mfma_f32_16x16x32_bf16 v[236:239], v[92:95], a[24:27], v[236:239]
ds_read_b64_tr_b16 v[86:87], v98 offset:45824
v_add_f32 v127, 1.0, v127
v_mfma_f32_16x16x32_bf16 v[252:255], v[88:91], a[12:15], 0
v_rcp_f32 v126, v126
v_mfma_f32_16x16x32_bf16 v[252:255], v[92:95], a[28:31], v[252:255]
v_mul_f32 v140, v140, v124
s_waitcnt lgkmcnt(8)
v_mfma_f32_16x16x32_bf16 v[192:195], v[64:67], a[32:35], v[192:195]
ds_read_b64_tr_b16 v[88:89], v99 offset:41472
v_mul_f32 v141, v141, v125
v_mfma_f32_16x16x32_bf16 v[192:195], v[68:71], a[48:51], v[192:195]
ds_read_b64_tr_b16 v[90:91], v99 offset:41728
v_mul_f32 v142, v142, v126
v_mfma_f32_16x16x32_bf16 v[208:211], v[64:67], a[36:39], v[208:211]
v_rcp_f32 v127, v127
v_mfma_f32_16x16x32_bf16 v[208:211], v[68:71], a[52:55], v[208:211]
v_mul_f32 v143, v143, v127
v_cvt_pk_bf16_f32 v143, v142, v143
v_mfma_f32_16x16x32_bf16 v[224:227], v[64:67], a[40:43], v[224:227]
ds_read_b64_tr_b16 v[92:93], v99 offset:45568
v_cvt_pk_bf16_f32 v142, v140, v141
v_mfma_f32_16x16x32_bf16 v[224:227], v[68:71], a[56:59], v[224:227]
ds_read_b64_tr_b16 v[94:95], v99 offset:45824
v_mul_f32 v120, v128, v128
v_mfma_f32_16x16x32_bf16 v[240:243], v[64:67], a[44:47], v[240:243]
v_mul_f32 v121, v129, v129
v_mul_f32 v122, v130, v130
v_mfma_f32_16x16x32_bf16 v[240:243], v[68:71], a[60:63], v[240:243]
v_mul_f32 v123, v131, v131
s_waitcnt lgkmcnt(8)
v_mfma_f32_16x16x32_bf16 v[196:199], v[72:75], a[32:35], v[196:199]
ds_read_b64_tr_b16 v[64:65], v98 offset:49152
v_fma_f32 v120, v120, s49, v119
v_mfma_f32_16x16x32_bf16 v[196:199], v[76:79], a[48:51], v[196:199]
ds_read_b64_tr_b16 v[66:67], v98 offset:49408
v_fma_f32 v121, v121, s49, v119
v_mfma_f32_16x16x32_bf16 v[212:215], v[72:75], a[36:39], v[212:215]
v_fma_f32 v122, v122, s49, v119
v_fma_f32 v123, v123, s49, v119
v_mfma_f32_16x16x32_bf16 v[212:215], v[76:79], a[52:55], v[212:215]
v_mul_f32 v120, v120, v128
v_mul_f32 v121, v121, v129
v_mfma_f32_16x16x32_bf16 v[228:231], v[72:75], a[40:43], v[228:231]
ds_read_b64_tr_b16 v[68:69], v98 offset:53248
v_mul_f32 v122, v122, v130
v_mfma_f32_16x16x32_bf16 v[228:231], v[76:79], a[56:59], v[228:231]
ds_read_b64_tr_b16 v[70:71], v98 offset:53504
v_mul_f32 v123, v123, v131
v_mfma_f32_16x16x32_bf16 v[244:247], v[72:75], a[44:47], v[244:247]
v_exp_f32 v120, v120
v_mfma_f32_16x16x32_bf16 v[244:247], v[76:79], a[60:63], v[244:247]
v_mul_f32 v136, v136, v128
s_waitcnt lgkmcnt(8)
v_mfma_f32_16x16x32_bf16 v[200:203], v[80:83], a[32:35], v[200:203]
ds_read_b64_tr_b16 v[72:73], v99 offset:49152
v_mul_f32 v137, v137, v129
v_mfma_f32_16x16x32_bf16 v[200:203], v[84:87], a[48:51], v[200:203]
ds_read_b64_tr_b16 v[74:75], v99 offset:49408
v_mul_f32 v138, v138, v130
v_mfma_f32_16x16x32_bf16 v[216:219], v[80:83], a[36:39], v[216:219]
v_exp_f32 v121, v121
v_mfma_f32_16x16x32_bf16 v[216:219], v[84:87], a[52:55], v[216:219]
v_exp_f32 v122, v122
v_mfma_f32_16x16x32_bf16 v[232:235], v[80:83], a[40:43], v[232:235]
ds_read_b64_tr_b16 v[76:77], v99 offset:53248
v_mul_f32 v139, v139, v131
v_mfma_f32_16x16x32_bf16 v[232:235], v[84:87], a[56:59], v[232:235]
ds_read_b64_tr_b16 v[78:79], v99 offset:53504
v_add_f32 v120, 1.0, v120
v_mfma_f32_16x16x32_bf16 v[248:251], v[80:83], a[44:47], v[248:251]
v_exp_f32 v123, v123
v_mfma_f32_16x16x32_bf16 v[248:251], v[84:87], a[60:63], v[248:251]
v_add_f32 v121, 1.0, v121
s_waitcnt lgkmcnt(8)
v_mfma_f32_16x16x32_bf16 v[204:207], v[88:91], a[32:35], v[204:207]
ds_read_b64_tr_b16 v[80:81], v98 offset:49664
v_add_f32 v122, 1.0, v122
v_mfma_f32_16x16x32_bf16 v[204:207], v[92:95], a[48:51], v[204:207]
ds_read_b64_tr_b16 v[82:83], v98 offset:49920
v_add_f32 v123, 1.0, v123
v_mfma_f32_16x16x32_bf16 v[220:223], v[88:91], a[36:39], v[220:223]
)"
R"(v_rcp_f32 v120, v120
v_mfma_f32_16x16x32_bf16 v[220:223], v[92:95], a[52:55], v[220:223]
v_rcp_f32 v121, v121
v_mfma_f32_16x16x32_bf16 v[236:239], v[88:91], a[40:43], v[236:239]
ds_read_b64_tr_b16 v[84:85], v98 offset:53760
v_mul_f32 v136, v136, v120
v_mfma_f32_16x16x32_bf16 v[236:239], v[92:95], a[56:59], v[236:239]
ds_read_b64_tr_b16 v[86:87], v98 offset:54016
v_mul_f32 v137, v137, v121
v_mfma_f32_16x16x32_bf16 v[252:255], v[88:91], a[44:47], v[252:255]
v_rcp_f32 v122, v122
v_mfma_f32_16x16x32_bf16 v[252:255], v[92:95], a[60:63], v[252:255]
s_waitcnt lgkmcnt(8)
v_mfma_f32_16x16x32_bf16 v[192:195], v[64:67], a[64:67], v[192:195]
ds_read_b64_tr_b16 v[88:89], v99 offset:49664
v_mul_f32 v138, v138, v122
v_mfma_f32_16x16x32_bf16 v[192:195], v[68:71], a[80:83], v[192:195]
ds_read_b64_tr_b16 v[90:91], v99 offset:49920
v_cvt_pk_bf16_f32 v140, v136, v137
v_mfma_f32_16x16x32_bf16 v[208:211], v[64:67], a[68:71], v[208:211]
v_rcp_f32 v123, v123
v_mfma_f32_16x16x32_bf16 v[208:211], v[68:71], a[84:87], v[208:211]
v_mfma_f32_16x16x32_bf16 v[224:227], v[64:67], a[72:75], v[224:227]
ds_read_b64_tr_b16 v[92:93], v99 offset:53760
v_mul_f32 v139, v139, v123
v_mfma_f32_16x16x32_bf16 v[224:227], v[68:71], a[88:91], v[224:227]
ds_read_b64_tr_b16 v[94:95], v99 offset:54016
v_cvt_pk_bf16_f32 v141, v138, v139
v_mfma_f32_16x16x32_bf16 v[240:243], v[64:67], a[76:79], v[240:243]
v_mul_f32 v124, v148, v148
v_mfma_f32_16x16x32_bf16 v[240:243], v[68:71], a[92:95], v[240:243]
v_mul_f32 v125, v149, v149
s_waitcnt lgkmcnt(8)
v_mfma_f32_16x16x32_bf16 v[196:199], v[72:75], a[64:67], v[196:199]
ds_read_b64_tr_b16 v[64:65], v98 offset:57344
v_mul_f32 v126, v150, v150
v_mfma_f32_16x16x32_bf16 v[196:199], v[76:79], a[80:83], v[196:199]
ds_read_b64_tr_b16 v[66:67], v98 offset:57600
v_mul_f32 v127, v151, v151
v_mfma_f32_16x16x32_bf16 v[212:215], v[72:75], a[68:71], v[212:215]
v_fma_f32 v124, v124, s49, v119
v_mfma_f32_16x16x32_bf16 v[212:215], v[76:79], a[84:87], v[212:215]
v_fma_f32 v125, v125, s49, v119
v_fma_f32 v126, v126, s49, v119
v_mfma_f32_16x16x32_bf16 v[228:231], v[72:75], a[72:75], v[228:231]
ds_read_b64_tr_b16 v[68:69], v98 offset:61440
v_fma_f32 v127, v127, s49, v119
v_mfma_f32_16x16x32_bf16 v[228:231], v[76:79], a[88:91], v[228:231]
ds_read_b64_tr_b16 v[70:71], v98 offset:61696
v_mul_f32 v124, v124, v148
v_mfma_f32_16x16x32_bf16 v[244:247], v[72:75], a[76:79], v[244:247]
v_exp_f32 v124, v124
v_mfma_f32_16x16x32_bf16 v[244:247], v[76:79], a[92:95], v[244:247]
v_mul_f32 v125, v125, v149
s_waitcnt lgkmcnt(8)
v_mfma_f32_16x16x32_bf16 v[200:203], v[80:83], a[64:67], v[200:203]
ds_read_b64_tr_b16 v[72:73], v99 offset:57344
v_mul_f32 v126, v126, v150
v_mfma_f32_16x16x32_bf16 v[200:203], v[84:87], a[80:83], v[200:203]
ds_read_b64_tr_b16 v[74:75], v99 offset:57600
v_mfma_f32_16x16x32_bf16 v[216:219], v[80:83], a[68:71], v[216:219]
v_exp_f32 v125, v125
v_mfma_f32_16x16x32_bf16 v[216:219], v[84:87], a[84:87], v[216:219]
v_exp_f32 v126, v126
v_mfma_f32_16x16x32_bf16 v[232:235], v[80:83], a[72:75], v[232:235]
ds_read_b64_tr_b16 v[76:77], v99 offset:61440
v_mfma_f32_16x16x32_bf16 v[232:235], v[84:87], a[88:91], v[232:235]
ds_read_b64_tr_b16 v[78:79], v99 offset:61696
v_mul_f32 v127, v127, v151
v_mfma_f32_16x16x32_bf16 v[248:251], v[80:83], a[76:79], v[248:251]
v_exp_f32 v127, v127
v_mfma_f32_16x16x32_bf16 v[248:251], v[84:87], a[92:95], v[248:251]
v_mul_f32 v156, v156, v148
s_waitcnt lgkmcnt(8)
v_mfma_f32_16x16x32_bf16 v[204:207], v[88:91], a[64:67], v[204:207]
ds_read_b64_tr_b16 v[80:81], v98 offset:57856
v_mfma_f32_16x16x32_bf16 v[204:207], v[92:95], a[80:83], v[204:207]
ds_read_b64_tr_b16 v[82:83], v98 offset:58112
v_mul_f32 v157, v157, v149
v_mfma_f32_16x16x32_bf16 v[220:223], v[88:91], a[68:71], v[220:223]
v_mul_f32 v158, v158, v150
v_mfma_f32_16x16x32_bf16 v[220:223], v[92:95], a[84:87], v[220:223]
v_mul_f32 v159, v159, v151
v_add_f32 v124, 1.0, v124
v_mfma_f32_16x16x32_bf16 v[236:239], v[88:91], a[72:75], v[236:239]
ds_read_b64_tr_b16 v[84:85], v98 offset:61952
v_add_f32 v125, 1.0, v125
v_mfma_f32_16x16x32_bf16 v[236:239], v[92:95], a[88:91], v[236:239]
ds_read_b64_tr_b16 v[86:87], v98 offset:62208
v_add_f32 v126, 1.0, v126
v_mfma_f32_16x16x32_bf16 v[252:255], v[88:91], a[76:79], v[252:255]
v_rcp_f32 v124, v124
v_mfma_f32_16x16x32_bf16 v[252:255], v[92:95], a[92:95], v[252:255]
s_waitcnt lgkmcnt(8)
v_mfma_f32_16x16x32_bf16 v[192:195], v[64:67], a[96:99], v[192:195]
ds_read_b64_tr_b16 v[88:89], v99 offset:57856
v_add_f32 v127, 1.0, v127
v_mfma_f32_16x16x32_bf16 v[192:195], v[68:71], a[112:115], v[192:195]
ds_read_b64_tr_b16 v[90:91], v99 offset:58112
v_mul_f32 v156, v156, v124
v_mfma_f32_16x16x32_bf16 v[208:211], v[64:67], a[100:103], v[208:211]
v_rcp_f32 v125, v125
v_mfma_f32_16x16x32_bf16 v[208:211], v[68:71], a[116:119], v[208:211]
v_mfma_f32_16x16x32_bf16 v[224:227], v[64:67], a[104:107], v[224:227]
ds_read_b64_tr_b16 v[92:93], v99 offset:61952
v_mul_f32 v157, v157, v125
v_mfma_f32_16x16x32_bf16 v[224:227], v[68:71], a[120:123], v[224:227]
ds_read_b64_tr_b16 v[94:95], v99 offset:62208
v_mul_f32 v120, v144, v144
v_mfma_f32_16x16x32_bf16 v[240:243], v[64:67], a[108:111], v[240:243]
v_rcp_f32 v126, v126
v_mfma_f32_16x16x32_bf16 v[240:243], v[68:71], a[124:127], v[240:243]
v_mul_f32 v158, v158, v126
s_waitcnt lgkmcnt(8)
v_mfma_f32_16x16x32_bf16 v[196:199], v[72:75], a[96:99], v[196:199]
v_rcp_f32 v127, v127
v_mfma_f32_16x16x32_bf16 v[196:199], v[76:79], a[112:115], v[196:199]
v_mul_f32 v159, v159, v127
v_mfma_f32_16x16x32_bf16 v[212:215], v[72:75], a[100:103], v[212:215]
v_cvt_pk_bf16_f32 v159, v158, v159
v_mfma_f32_16x16x32_bf16 v[212:215], v[76:79], a[116:119], v[212:215]
v_cvt_pk_bf16_f32 v158, v156, v157
v_mul_f32 v121, v145, v145
v_mfma_f32_16x16x32_bf16 v[228:231], v[72:75], a[104:107], v[228:231]
v_mul_f32 v122, v146, v146
v_mfma_f32_16x16x32_bf16 v[228:231], v[76:79], a[120:123], v[228:231]
s_waitcnt lgkmcnt(0)
s_barrier
v_mfma_f32_16x16x32_bf16 v[244:247], v[72:75], a[108:111], v[244:247]
ds_read_b64_tr_b16 v[64:65], v96 offset:32768
v_mul_f32 v123, v147, v147
v_mfma_f32_16x16x32_bf16 v[244:247], v[76:79], a[124:127], v[244:247]
ds_read_b64_tr_b16 v[66:67], v96 offset:33024
v_mfma_f32_16x16x32_bf16 v[200:203], v[80:83], a[96:99], v[200:203]
ds_read_b64_tr_b16 v[68:69], v96 offset:36864
v_fma_f32 v120, v120, s49, v119
v_mfma_f32_16x16x32_bf16 v[200:203], v[84:87], a[112:115], v[200:203]
ds_read_b64_tr_b16 v[70:71], v96 offset:37120
v_fma_f32 v121, v121, s49, v119
v_mfma_f32_16x16x32_bf16 v[216:219], v[80:83], a[100:103], v[216:219]
ds_read_b64_tr_b16 v[72:73], v97 offset:32768
v_fma_f32 v122, v122, s49, v119
v_mfma_f32_16x16x32_bf16 v[216:219], v[84:87], a[116:119], v[216:219]
ds_read_b64_tr_b16 v[74:75], v97 offset:33024
v_fma_f32 v123, v123, s49, v119
v_mfma_f32_16x16x32_bf16 v[232:235], v[80:83], a[104:107], v[232:235]
ds_read_b64_tr_b16 v[76:77], v97 offset:36864
v_mul_f32 v120, v120, v144
v_mfma_f32_16x16x32_bf16 v[232:235], v[84:87], a[120:123], v[232:235]
ds_read_b64_tr_b16 v[78:79], v97 offset:37120
v_mov_b32 v40, v119
v_mfma_f32_16x16x32_bf16 v[248:251], v[80:83], a[108:111], v[248:251]
v_mov_b32 v41, v117
v_mov_b32 v42, v118
v_mfma_f32_16x16x32_bf16 v[248:251], v[84:87], a[124:127], v[248:251]
v_subrev_u32 v43, 0x18000, v118
v_mfma_f32_16x16x32_bf16 v[204:207], v[88:91], a[96:99], v[204:207]
ds_read_b64_tr_b16 v[80:81], v96 offset:33280
v_xor_b32 v44, 0x2000, v3
v_mfma_f32_16x16x32_bf16 v[204:207], v[92:95], a[112:115], v[204:207]
ds_read_b64_tr_b16 v[82:83], v96 offset:33536
v_mul_f32 v121, v121, v145
v_mfma_f32_16x16x32_bf16 v[220:223], v[88:91], a[100:103], v[220:223]
v_exp_f32 v120, v120
v_mfma_f32_16x16x32_bf16 v[220:223], v[92:95], a[116:119], v[220:223]
v_mfma_f32_16x16x32_bf16 v[236:239], v[88:91], a[104:107], v[236:239]
ds_read_b64_tr_b16 v[84:85], v96 offset:37376
v_mul_f32 v122, v122, v146
v_mfma_f32_16x16x32_bf16 v[236:239], v[92:95], a[120:123], v[236:239]
ds_read_b64_tr_b16 v[86:87], v96 offset:37632
v_mul_f32 v123, v123, v147
v_mfma_f32_16x16x32_bf16 v[252:255], v[88:91], a[108:111], v[252:255]
v_exp_f32 v121, v121
v_mfma_f32_16x16x32_bf16 v[252:255], v[92:95], a[124:127], v[252:255]
v_mul_f32 v152, v152, v144
s_waitcnt lgkmcnt(8)
v_mfma_f32_16x16x32_bf16 v[192:195], v[64:67], a[128:131], v[192:195]
ds_read_b64_tr_b16 v[88:89], v97 offset:33280
v_mul_f32 v153, v153, v145
v_mfma_f32_16x16x32_bf16 v[192:195], v[68:71], a[144:147], v[192:195]
ds_read_b64_tr_b16 v[90:91], v97 offset:33536
v_mul_f32 v154, v154, v146
v_mfma_f32_16x16x32_bf16 v[208:211], v[64:67], a[132:135], v[208:211]
v_mfma_f32_16x16x32_bf16 v[208:211], v[68:71], a[148:151], v[208:211]
v_exp_f32 v122, v122
v_mfma_f32_16x16x32_bf16 v[224:227], v[64:67], a[136:139], v[224:227]
ds_read_b64_tr_b16 v[92:93], v97 offset:37376
v_mul_f32 v155, v155, v147
v_mfma_f32_16x16x32_bf16 v[224:227], v[68:71], a[152:155], v[224:227]
ds_read_b64_tr_b16 v[94:95], v97 offset:37632
v_add_f32 v120, 1.0, v120
v_mfma_f32_16x16x32_bf16 v[240:243], v[64:67], a[140:143], v[240:243]
v_exp_f32 v123, v123
v_mfma_f32_16x16x32_bf16 v[240:243], v[68:71], a[156:159], v[240:243]
v_add_f32 v121, 1.0, v121
s_waitcnt lgkmcnt(8)
v_mfma_f32_16x16x32_bf16 v[196:199], v[72:75], a[128:131], v[196:199]
ds_read_b64_tr_b16 v[64:65], v96 offset:40960
v_add_f32 v122, 1.0, v122
v_mfma_f32_16x16x32_bf16 v[196:199], v[76:79], a[144:147], v[196:199]
ds_read_b64_tr_b16 v[66:67], v96 offset:41216
v_mfma_f32_16x16x32_bf16 v[212:215], v[72:75], a[132:135], v[212:215]
s_waitcnt vmcnt(0)
s_barrier
v_mfma_f32_16x16x32_bf16 v[212:215], v[76:79], a[148:151], v[212:215]
s_cmp_lg_u32 %3, 0
s_cbranch_scc1 W0S78_%=
s_lshl_b32 s91, s26, 5
s_add_u32 s91, s91, s31
s_lshl_b32 s91, s91, 2
s_add_u32 s91, s91, 12320
v_lshl_add_u32 v116, v22, 9, s91
v_mov_b32 v24, 1
s_mov_b64 exec, 15
global_store_dword v116, v24, s[92:93] sc1
s_mov_b64 exec, -1
W0S78_%=:
v_mfma_f32_16x16x32_bf16 v[228:231], v[72:75], a[136:139], v[228:231]
ds_read_b64_tr_b16 v[68:69], v96 offset:45056
s_add_u32 m0, s75, 0
PAD35_%=:
s_cbranch_execz PAD17_%=
v_mfma_f32_16x16x32_bf16 v[228:231], v[76:79], a[152:155], v[228:231]
ds_read_b64_tr_b16 v[70:71], v96 offset:45312
v_add_f32 v123, 1.0, v123
v_mfma_f32_16x16x32_bf16 v[244:247], v[72:75], a[140:143], v[244:247]
buffer_load_dwordx4 v3, s[36:39], s70 offen offset:0 lds
v_mul_f32 v124, v164, v164
v_mfma_f32_16x16x32_bf16 v[244:247], v[76:79], a[156:159], v[244:247]
global_store_dwordx4 v117, v[140:143], s[94:95] offset:0 sc1
v_mul_f32 v125, v165, v165
s_waitcnt lgkmcnt(8)
v_mfma_f32_16x16x32_bf16 v[200:203], v[80:83], a[128:131], v[200:203]
ds_read_b64_tr_b16 v[72:73], v97 offset:40960
v_mul_f32 v126, v166, v166
v_mfma_f32_16x16x32_bf16 v[200:203], v[84:87], a[144:147], v[200:203]
ds_read_b64_tr_b16 v[74:75], v97 offset:41216
s_lshl_b32 s86, %2, 3
v_mfma_f32_16x16x32_bf16 v[216:219], v[80:83], a[132:135], v[216:219]
buffer_load_dwordx4 v3, s[36:39], s70 offen offset:1024 lds
v_mul_f32 v127, v167, v167
v_mfma_f32_16x16x32_bf16 v[216:219], v[84:87], a[148:151], v[216:219]
v_rcp_f32 v120, v120
v_mfma_f32_16x16x32_bf16 v[232:235], v[80:83], a[136:139], v[232:235]
ds_read_b64_tr_b16 v[76:77], v97 offset:45056
v_mul_f32 v152, v152, v120
v_mfma_f32_16x16x32_bf16 v[232:235], v[84:87], a[152:155], v[232:235]
ds_read_b64_tr_b16 v[78:79], v97 offset:45312
s_lshr_b32 s86, 0x1020f000, s86
v_mfma_f32_16x16x32_bf16 v[248:251], v[80:83], a[140:143], v[248:251]
buffer_load_dwordx4 v3, s[36:39], s70 offen offset:2048 lds
v_fma_f32 v124, v124, s49, v119
v_mfma_f32_16x16x32_bf16 v[248:251], v[84:87], a[156:159], v[248:251]
v_fma_f32 v125, v125, s49, v119
s_waitcnt lgkmcnt(8)
v_mfma_f32_16x16x32_bf16 v[204:207], v[88:91], a[128:131], v[204:207]
ds_read_b64_tr_b16 v[80:81], v96 offset:41472
v_fma_f32 v126, v126, s49, v119
v_mfma_f32_16x16x32_bf16 v[204:207], v[92:95], a[144:147], v[204:207]
ds_read_b64_tr_b16 v[82:83], v96 offset:41728
v_fma_f32 v127, v127, s49, v119
v_mfma_f32_16x16x32_bf16 v[220:223], v[88:91], a[132:135], v[220:223]
buffer_load_dwordx4 v3, s[36:39], s70 offen offset:3072 lds
s_sext_i32_i8 s86, s86
v_mfma_f32_16x16x32_bf16 v[220:223], v[92:95], a[148:151], v[220:223]
s_add_u32 m0, s75, 4096
v_mul_f32 v124, v124, v164
v_mfma_f32_16x16x32_bf16 v[236:239], v[88:91], a[136:139], v[236:239]
ds_read_b64_tr_b16 v[84:85], v96 offset:45568
v_mul_f32 v125, v125, v165
v_mfma_f32_16x16x32_bf16 v[236:239], v[92:95], a[152:155], v[236:239]
ds_read_b64_tr_b16 v[86:87], v96 offset:45824
v_mul_f32 v126, v126, v166
v_mfma_f32_16x16x32_bf16 v[252:255], v[88:91], a[140:143], v[252:255]
buffer_load_dwordx4 v4, s[36:39], s70 offen offset:0 lds
v_mul_f32 v127, v127, v167
v_mfma_f32_16x16x32_bf16 v[252:255], v[92:95], a[156:159], v[252:255]
ds_read_b128 a[128:131], v112 offset:4096
s_waitcnt lgkmcnt(9)
v_mfma_f32_16x16x32_bf16 v[192:195], v[64:67], a[160:163], v[192:195]
ds_read_b64_tr_b16 v[88:89], v97 offset:41472
v_add_u32 v5, s86, v18
v_mfma_f32_16x16x32_bf16 v[192:195], v[68:71], a[176:179], v[192:195]
ds_read_b64_tr_b16 v[90:91], v97 offset:41728
v_mul_f32 v172, v172, v164
v_mfma_f32_16x16x32_bf16 v[208:211], v[64:67], a[164:167], v[208:211]
ds_read_b128 a[132:135], v112 offset:5120
v_mul_f32 v173, v173, v165
v_mfma_f32_16x16x32_bf16 v[208:211], v[68:71], a[180:183], v[208:211]
ds_read_b128 a[136:139], v112 offset:6144
v_mul_f32 v174, v174, v166
v_mfma_f32_16x16x32_bf16 v[224:227], v[64:67], a[168:171], v[224:227]
ds_read_b64_tr_b16 v[92:93], v97 offset:45568
v_mul_f32 v175, v175, v167
v_mfma_f32_16x16x32_bf16 v[224:227], v[68:71], a[184:187], v[224:227]
ds_read_b64_tr_b16 v[94:95], v97 offset:45824
s_lshl_b32 s86, %2, 3
v_mfma_f32_16x16x32_bf16 v[240:243], v[64:67], a[172:175], v[240:243]
ds_read_b128 a[140:143], v112 offset:7168
v_mul_f32 v120, v160, v160
v_mfma_f32_16x16x32_bf16 v[240:243], v[68:71], a[188:191], v[240:243]
buffer_load_dwordx4 v4, s[36:39], s70 offen offset:1024 lds
s_waitcnt lgkmcnt(12)
v_mfma_f32_16x16x32_bf16 v[196:199], v[72:75], a[160:163], v[196:199]
ds_read_b64_tr_b16 v[64:65], v96 offset:49152
s_lshr_b32 s86, 0x30e010, s86
v_mfma_f32_16x16x32_bf16 v[196:199], v[76:79], a[176:179], v[196:199]
ds_read_b64_tr_b16 v[66:67], v96 offset:49408
s_sext_i32_i8 s86, s86
v_mfma_f32_16x16x32_bf16 v[212:215], v[72:75], a[164:167], v[212:215]
buffer_load_dwordx4 v4, s[36:39], s70 offen offset:2048 lds
v_add_u32 v6, s86, v18
v_mfma_f32_16x16x32_bf16 v[212:215], v[76:79], a[180:183], v[212:215]
v_rcp_f32 v121, v121
v_mfma_f32_16x16x32_bf16 v[228:231], v[72:75], a[168:171], v[228:231]
ds_read_b64_tr_b16 v[68:69], v96 offset:53248
v_mul_f32 v153, v153, v121
v_mfma_f32_16x16x32_bf16 v[228:231], v[76:79], a[184:187], v[228:231]
ds_read_b64_tr_b16 v[70:71], v96 offset:53504
v_cvt_pk_bf16_f32 v156, v152, v153
v_mfma_f32_16x16x32_bf16 v[244:247], v[72:75], a[172:175], v[244:247]
buffer_load_dwordx4 v4, s[36:39], s70 offen offset:3072 lds
s_mov_b32 s70, 0
v_mfma_f32_16x16x32_bf16 v[244:247], v[76:79], a[188:191], v[244:247]
s_add_u32 m0, s75, 65536
s_waitcnt lgkmcnt(12)
v_mfma_f32_16x16x32_bf16 v[200:203], v[80:83], a[160:163], v[200:203]
ds_read_b64_tr_b16 v[72:73], v97 offset:49152
v_mul_f32 v121, v161, v161
v_mfma_f32_16x16x32_bf16 v[200:203], v[84:87], a[176:179], v[200:203]
ds_read_b64_tr_b16 v[74:75], v97 offset:49408
v_fma_f32 v120, v120, s49, v119
v_mfma_f32_16x16x32_bf16 v[216:219], v[80:83], a[164:167], v[216:219]
buffer_load_dwordx4 v3, s[36:39], s70 offen offset:0 lds
v_fma_f32 v121, v121, s49, v119
v_mfma_f32_16x16x32_bf16 v[216:219], v[84:87], a[180:183], v[216:219]
v_rcp_f32 v122, v122
v_mfma_f32_16x16x32_bf16 v[232:235], v[80:83], a[168:171], v[232:235]
ds_read_b64_tr_b16 v[76:77], v97 offset:53248
v_mul_f32 v154, v154, v122
v_mfma_f32_16x16x32_bf16 v[232:235], v[84:87], a[184:187], v[232:235]
ds_read_b64_tr_b16 v[78:79], v97 offset:53504
v_mul_f32 v122, v162, v162
v_mfma_f32_16x16x32_bf16 v[248:251], v[80:83], a[172:175], v[248:251]
buffer_load_dwordx4 v3, s[36:39], s70 offen offset:1024 lds
v_fma_f32 v122, v122, s49, v119
v_mfma_f32_16x16x32_bf16 v[248:251], v[84:87], a[188:191], v[248:251]
v_mul_f32 v120, v120, v160
s_waitcnt lgkmcnt(9)
v_mfma_f32_16x16x32_bf16 v[204:207], v[88:91], a[160:163], v[204:207]
ds_read_b64_tr_b16 v[80:81], v96 offset:49664
v_mul_f32 v121, v121, v161
v_mfma_f32_16x16x32_bf16 v[204:207], v[92:95], a[176:179], v[204:207]
ds_read_b64_tr_b16 v[82:83], v96 offset:49920
v_mul_f32 v122, v122, v162
v_mfma_f32_16x16x32_bf16 v[220:223], v[88:91], a[164:167], v[220:223]
buffer_load_dwordx4 v3, s[36:39], s70 offen offset:2048 lds
v_mul_f32 v168, v168, v160
v_mfma_f32_16x16x32_bf16 v[220:223], v[92:95], a[180:183], v[220:223]
v_rcp_f32 v123, v123
v_mfma_f32_16x16x32_bf16 v[236:239], v[88:91], a[168:171], v[236:239]
ds_read_b64_tr_b16 v[84:85], v96 offset:53760
v_mul_f32 v155, v155, v123
v_mfma_f32_16x16x32_bf16 v[236:239], v[92:95], a[184:187], v[236:239]
ds_read_b64_tr_b16 v[86:87], v96 offset:54016
v_cvt_pk_bf16_f32 v157, v154, v155
v_mfma_f32_16x16x32_bf16 v[252:255], v[88:91], a[172:175], v[252:255]
buffer_load_dwordx4 v3, s[36:39], s70 offen offset:3072 lds
v_mul_f32 v123, v163, v163
v_mfma_f32_16x16x32_bf16 v[252:255], v[92:95], a[188:191], v[252:255]
ds_read_b128 a[160:163], v113 offset:4096
s_waitcnt lgkmcnt(9)
v_mfma_f32_16x16x32_bf16 v[192:195], v[64:67], a[192:195], v[192:195]
ds_read_b64_tr_b16 v[88:89], v97 offset:49664
s_add_u32 m0, s75, 69632
v_mfma_f32_16x16x32_bf16 v[192:195], v[68:71], a[208:211], v[192:195]
ds_read_b64_tr_b16 v[90:91], v97 offset:49920
v_fma_f32 v123, v123, s49, v119
v_mfma_f32_16x16x32_bf16 v[208:211], v[64:67], a[196:199], v[208:211]
ds_read_b128 a[164:167], v113 offset:5120
v_mul_f32 v123, v123, v163
v_mfma_f32_16x16x32_bf16 v[208:211], v[68:71], a[212:215], v[208:211]
ds_read_b128 a[168:171], v113 offset:6144
v_mul_f32 v169, v169, v161
v_mfma_f32_16x16x32_bf16 v[224:227], v[64:67], a[200:203], v[224:227]
ds_read_b64_tr_b16 v[92:93], v97 offset:53760
v_mul_f32 v170, v170, v162
v_mfma_f32_16x16x32_bf16 v[224:227], v[68:71], a[216:219], v[224:227]
ds_read_b64_tr_b16 v[94:95], v97 offset:54016
v_mul_f32 v171, v171, v163
v_mfma_f32_16x16x32_bf16 v[240:243], v[64:67], a[204:207], v[240:243]
ds_read_b128 a[172:175], v113 offset:7168
v_cmp_gt_i32 s[80:81], 16, v5
v_mfma_f32_16x16x32_bf16 v[240:243], v[68:71], a[220:223], v[240:243]
buffer_load_dwordx4 v4, s[36:39], s70 offen offset:0 lds
s_waitcnt lgkmcnt(12)
v_mfma_f32_16x16x32_bf16 v[196:199], v[72:75], a[192:195], v[196:199]
ds_read_b64_tr_b16 v[64:65], v96 offset:57344
v_cmp_gt_i32 s[82:83], 17, v5
v_mfma_f32_16x16x32_bf16 v[196:199], v[76:79], a[208:211], v[196:199]
)"
R"(ds_read_b64_tr_b16 v[66:67], v96 offset:57600
v_cmp_gt_i32 s[84:85], 18, v5
v_mfma_f32_16x16x32_bf16 v[212:215], v[72:75], a[196:199], v[212:215]
buffer_load_dwordx4 v4, s[36:39], s70 offen offset:1024 lds
v_cmp_gt_i32 s[86:87], 19, v5
v_mfma_f32_16x16x32_bf16 v[212:215], v[76:79], a[212:215], v[212:215]
global_store_dwordx4 v117, v[156:159], s[94:95] offset:1024 sc1
v_exp_f32 v124, v124
v_mfma_f32_16x16x32_bf16 v[228:231], v[72:75], a[200:203], v[228:231]
ds_read_b64_tr_b16 v[68:69], v96 offset:61440
v_add_f32 v124, 1.0, v124
v_mfma_f32_16x16x32_bf16 v[228:231], v[76:79], a[216:219], v[228:231]
ds_read_b64_tr_b16 v[70:71], v96 offset:61696
v_cndmask_b32 v100, 0, v20, s[80:81]
v_mfma_f32_16x16x32_bf16 v[244:247], v[72:75], a[204:207], v[244:247]
buffer_load_dwordx4 v4, s[36:39], s70 offen offset:2048 lds
v_cndmask_b32 v101, 0, v20, s[82:83]
v_mfma_f32_16x16x32_bf16 v[244:247], v[76:79], a[220:223], v[244:247]
v_cndmask_b32 v102, 0, v20, s[84:85]
s_waitcnt lgkmcnt(12)
v_mfma_f32_16x16x32_bf16 v[200:203], v[80:83], a[192:195], v[200:203]
ds_read_b64_tr_b16 v[72:73], v97 offset:57344
v_cndmask_b32 v103, 0, v20, s[86:87]
v_mfma_f32_16x16x32_bf16 v[200:203], v[84:87], a[208:211], v[200:203]
ds_read_b64_tr_b16 v[74:75], v97 offset:57600
v_cmp_gt_i32 s[80:81], 0, v6
v_mfma_f32_16x16x32_bf16 v[216:219], v[80:83], a[196:199], v[216:219]
buffer_load_dwordx4 v4, s[36:39], s70 offen offset:3072 lds
v_cmp_gt_i32 s[82:83], 1, v6
v_mfma_f32_16x16x32_bf16 v[216:219], v[84:87], a[212:215], v[216:219]
v_exp_f32 v125, v125
v_mfma_f32_16x16x32_bf16 v[232:235], v[80:83], a[200:203], v[232:235]
ds_read_b64_tr_b16 v[76:77], v97 offset:61440
v_add_f32 v125, 1.0, v125
v_mfma_f32_16x16x32_bf16 v[232:235], v[84:87], a[216:219], v[232:235]
ds_read_b64_tr_b16 v[78:79], v97 offset:61696
v_cmp_gt_i32 s[84:85], 2, v6
v_mfma_f32_16x16x32_bf16 v[248:251], v[80:83], a[204:207], v[248:251]
v_exp_f32 v126, v126
v_mfma_f32_16x16x32_bf16 v[248:251], v[84:87], a[220:223], v[248:251]
v_add_f32 v126, 1.0, v126
s_waitcnt lgkmcnt(9)
v_mfma_f32_16x16x32_bf16 v[204:207], v[88:91], a[192:195], v[204:207]
ds_read_b64_tr_b16 v[80:81], v96 offset:57856
v_cmp_gt_i32 s[86:87], 3, v6
v_mfma_f32_16x16x32_bf16 v[204:207], v[92:95], a[208:211], v[204:207]
ds_read_b64_tr_b16 v[82:83], v96 offset:58112
v_cndmask_b32 v112, 0, v20, s[80:81]
v_mfma_f32_16x16x32_bf16 v[220:223], v[88:91], a[196:199], v[220:223]
v_exp_f32 v127, v127
v_mfma_f32_16x16x32_bf16 v[220:223], v[92:95], a[212:215], v[220:223]
v_rcp_f32 v124, v124
v_mfma_f32_16x16x32_bf16 v[236:239], v[88:91], a[200:203], v[236:239]
ds_read_b64_tr_b16 v[84:85], v96 offset:61952
v_add_f32 v127, 1.0, v127
v_mfma_f32_16x16x32_bf16 v[236:239], v[92:95], a[216:219], v[236:239]
ds_read_b64_tr_b16 v[86:87], v96 offset:62208
v_mul_f32 v172, v172, v124
v_mfma_f32_16x16x32_bf16 v[252:255], v[88:91], a[204:207], v[252:255]
v_rcp_f32 v125, v125
v_mfma_f32_16x16x32_bf16 v[252:255], v[92:95], a[220:223], v[252:255]
v_mul_f32 v173, v173, v125
s_waitcnt lgkmcnt(8)
v_mfma_f32_16x16x32_bf16 v[192:195], v[64:67], a[224:227], v[192:195]
ds_read_b64_tr_b16 v[88:89], v97 offset:57856
v_cndmask_b32 v113, 0, v20, s[82:83]
v_mfma_f32_16x16x32_bf16 v[192:195], v[68:71], a[240:243], v[192:195]
ds_read_b64_tr_b16 v[90:91], v97 offset:58112
v_cndmask_b32 v114, 0, v20, s[84:85]
v_mfma_f32_16x16x32_bf16 v[208:211], v[64:67], a[228:231], v[208:211]
v_rcp_f32 v126, v126
v_mfma_f32_16x16x32_bf16 v[208:211], v[68:71], a[244:247], v[208:211]
v_rcp_f32 v127, v127
v_mfma_f32_16x16x32_bf16 v[224:227], v[64:67], a[232:235], v[224:227]
ds_read_b64_tr_b16 v[92:93], v97 offset:61952
v_mul_f32 v174, v174, v126
v_mfma_f32_16x16x32_bf16 v[224:227], v[68:71], a[248:251], v[224:227]
ds_read_b64_tr_b16 v[94:95], v97 offset:62208
v_mul_f32 v175, v175, v127
v_mfma_f32_16x16x32_bf16 v[240:243], v[64:67], a[236:239], v[240:243]
v_exp_f32 v120, v120
v_mfma_f32_16x16x32_bf16 v[240:243], v[68:71], a[252:255], v[240:243]
v_cvt_pk_bf16_f32 v175, v174, v175
s_waitcnt lgkmcnt(8)
v_mfma_f32_16x16x32_bf16 v[196:199], v[72:75], a[224:227], v[196:199]
v_exp_f32 v121, v121
v_mfma_f32_16x16x32_bf16 v[196:199], v[76:79], a[240:243], v[196:199]
v_exp_f32 v122, v122
v_mfma_f32_16x16x32_bf16 v[212:215], v[72:75], a[228:231], v[212:215]
v_exp_f32 v123, v123
v_mfma_f32_16x16x32_bf16 v[212:215], v[76:79], a[244:247], v[212:215]
v_cvt_pk_bf16_f32 v174, v172, v173
v_add_f32 v120, 1.0, v120
v_mfma_f32_16x16x32_bf16 v[228:231], v[72:75], a[232:235], v[228:231]
v_rcp_f32 v120, v120
v_mfma_f32_16x16x32_bf16 v[228:231], v[76:79], a[248:251], v[228:231]
s_waitcnt vmcnt(9) lgkmcnt(0)
s_barrier
v_mfma_f32_16x16x32_bf16 v[244:247], v[72:75], a[236:239], v[244:247]
ds_read_b128 a[192:195], v1 offset:0
v_add_f32 v121, 1.0, v121
v_mfma_f32_16x16x32_bf16 v[244:247], v[76:79], a[252:255], v[244:247]
ds_read_b128 a[196:199], v1 offset:1024
v_mfma_f32_16x16x32_bf16 v[200:203], v[80:83], a[224:227], v[200:203]
ds_read_b128 a[200:203], v1 offset:4096
v_add_f32 v122, 1.0, v122
v_mfma_f32_16x16x32_bf16 v[200:203], v[84:87], a[240:243], v[200:203]
ds_read_b128 a[204:207], v1 offset:5120
v_add_f32 v123, 1.0, v123
v_mfma_f32_16x16x32_bf16 v[216:219], v[80:83], a[228:231], v[216:219]
v_rcp_f32 v121, v121
v_mfma_f32_16x16x32_bf16 v[216:219], v[84:87], a[244:247], v[216:219]
v_rcp_f32 v122, v122
v_mfma_f32_16x16x32_bf16 v[232:235], v[80:83], a[232:235], v[232:235]
v_rcp_f32 v123, v123
v_mfma_f32_16x16x32_bf16 v[232:235], v[84:87], a[248:251], v[232:235]
v_mul_f32 v168, v168, v120
v_mul_f32 v169, v169, v121
v_mfma_f32_16x16x32_bf16 v[248:251], v[80:83], a[236:239], v[248:251]
v_mul_f32 v170, v170, v122
v_mul_f32 v171, v171, v123
v_mfma_f32_16x16x32_bf16 v[248:251], v[84:87], a[252:255], v[248:251]
v_cvt_pk_bf16_f32 v173, v170, v171
v_mfma_f32_16x16x32_bf16 v[204:207], v[88:91], a[224:227], v[204:207]
v_cvt_pk_bf16_f32 v172, v168, v169
v_cndmask_b32 v115, 0, v20, s[86:87]
v_mfma_f32_16x16x32_bf16 v[204:207], v[92:95], a[240:243], v[204:207]
global_store_dwordx4 v117, v[172:175], s[96:97] offset:0 sc1
v_cmp_gt_i32 s[80:81], 0, v5
v_mfma_f32_16x16x32_bf16 v[220:223], v[88:91], a[228:231], v[220:223]
v_cmp_gt_i32 s[82:83], 1, v5
v_cmp_gt_i32 s[84:85], 2, v5
v_mfma_f32_16x16x32_bf16 v[220:223], v[92:95], a[244:247], v[220:223]
v_cmp_gt_i32 s[86:87], 3, v5
v_cndmask_b32 v96, 0, v20, s[80:81]
v_mfma_f32_16x16x32_bf16 v[236:239], v[88:91], a[232:235], v[236:239]
v_cndmask_b32 v97, 0, v20, s[82:83]
v_cndmask_b32 v98, 0, v20, s[84:85]
v_mfma_f32_16x16x32_bf16 v[236:239], v[92:95], a[248:251], v[236:239]
v_cndmask_b32 v99, 0, v20, s[86:87]
v_cmp_gt_i32 s[80:81], 16, v6
v_mfma_f32_16x16x32_bf16 v[252:255], v[88:91], a[236:239], v[252:255]
v_cmp_gt_i32 s[82:83], 17, v6
v_cmp_gt_i32 s[84:85], 18, v6
v_cmp_gt_i32 s[86:87], 19, v6
v_cndmask_b32 v116, 0, v20, s[80:81]
v_cndmask_b32 v117, 0, v20, s[82:83]
v_cndmask_b32 v118, 0, v20, s[84:85]
v_cndmask_b32 v119, 0, v20, s[86:87]
v_mfma_f32_16x16x32_bf16 v[252:255], v[92:95], a[252:255], v[252:255]
s_branch P2_%=
DONE_%=:
RARE0_%=:
v_mov_b32 v14, 0
v_mov_b32 v192, 0
v_mov_b32 v193, 0
v_mov_b32 v194, 0
v_mov_b32 v195, 0
v_mov_b32 v196, 0
v_mov_b32 v197, 0
v_mov_b32 v198, 0
v_mov_b32 v199, 0
v_mov_b32 v200, 0
v_mov_b32 v201, 0
v_mov_b32 v202, 0
v_mov_b32 v203, 0
v_mov_b32 v204, 0
v_mov_b32 v205, 0
v_mov_b32 v206, 0
v_mov_b32 v207, 0
s_bitset1_b32 s57, s55
s_bitset1_b32 s27, s55
s_mov_b32 s58, 0xff800000
v_mfma_f32_32x32x16_bf16 a[0:15], v[10:13], v[10:13], 0
v_mfma_f32_32x32x16_bf16 a[16:31], v[10:13], v[10:13], 0
v_mfma_f32_32x32x16_bf16 a[32:47], v[10:13], v[10:13], 0
v_mfma_f32_32x32x16_bf16 a[48:63], v[10:13], v[10:13], 0
v_mfma_f32_32x32x16_bf16 a[64:79], v[10:13], v[10:13], 0
v_mfma_f32_32x32x16_bf16 a[80:95], v[10:13], v[10:13], 0
v_mfma_f32_32x32x16_bf16 a[96:111], v[10:13], v[10:13], 0
v_mfma_f32_32x32x16_bf16 a[112:127], v[10:13], v[10:13], 0
s_cmp_lg_u32 %2, 7
s_cbranch_scc1 MASK0Z_%=
MASKR0Z_%=:
v_max3_f32 v5, v64, v65, v66
v_max3_f32 v6, v72, v73, v74
v_max3_f32 v7, v80, v81, v82
v_max3_f32 v8, v88, v89, v90
v_max3_f32 v5, v5, v67, v68
v_max3_f32 v6, v6, v75, v76
v_max3_f32 v7, v7, v83, v84
v_max3_f32 v8, v8, v91, v92
v_max3_f32 v5, v5, v69, v70
v_max3_f32 v6, v6, v77, v78
v_max3_f32 v7, v7, v85, v86
v_max3_f32 v8, v8, v93, v94
v_max3_f32 v5, v5, v71, v96
v_max3_f32 v6, v6, v79, v104
v_max3_f32 v7, v7, v87, v112
v_max3_f32 v8, v8, v95, v120
v_max3_f32 v5, v5, v97, v98
v_max3_f32 v6, v6, v105, v106
v_max3_f32 v7, v7, v113, v114
v_max3_f32 v8, v8, v121, v122
v_max3_f32 v5, v5, v99, v100
v_max3_f32 v6, v6, v107, v108
v_max3_f32 v7, v7, v115, v116
v_max3_f32 v8, v8, v123, v124
v_max3_f32 v5, v5, v101, v102
v_max3_f32 v6, v6, v109, v110
v_max3_f32 v7, v7, v117, v118
v_max3_f32 v8, v8, v125, v126
v_max_f32 v5, v5, v103
v_max_f32 v6, v6, v111
v_max_f32 v7, v7, v119
v_max_f32 v8, v8, v127
v_max3_f32 v9, v5, v6, v7
v_max_f32 v9, v9, v8
v_cmp_le_f32 vcc, s58, v9
s_cbranch_vccnz RARE0Z_%=
RARER0Z_%=:
v_exp_f32 v64, v64
v_exp_f32 v65, v65
v_exp_f32 v66, v66
v_exp_f32 v67, v67
v_exp_f32 v68, v68
v_exp_f32 v69, v69
v_exp_f32 v70, v70
v_exp_f32 v71, v71
v_exp_f32 v72, v72
v_exp_f32 v73, v73
v_exp_f32 v74, v74
v_cvt_pk_bf16_f32 v64, v64, v65
s_mov_b32 s71, s70
v_exp_f32 v75, v75
s_add_u32 s70, s70, 32768
v_exp_f32 v76, v76
s_cmp_eq_u32 s54, s72
v_exp_f32 v77, v77
s_cselect_b32 s70, 0, s70
v_exp_f32 v78, v78
v_exp_f32 v79, v79
v_exp_f32 v80, v80
v_exp_f32 v81, v81
v_exp_f32 v82, v82
v_exp_f32 v83, v83
v_exp_f32 v84, v84
v_exp_f32 v85, v85
s_waitcnt vmcnt(3) lgkmcnt(0)
s_barrier
ds_read_b128 a[192:195], v2 offset:0
v_cvt_pk_bf16_f32 v65, v66, v67
v_exp_f32 v86, v86
ds_read_b128 a[196:199], v2 offset:1024
v_cvt_pk_bf16_f32 v66, v68, v69
v_exp_f32 v87, v87
ds_read_b128 a[200:203], v2 offset:2048
v_cvt_pk_bf16_f32 v67, v70, v71
v_exp_f32 v88, v88
ds_read_b128 a[204:207], v2 offset:3072
v_cvt_pk_bf16_f32 v72, v72, v73
v_exp_f32 v89, v89
v_exp_f32 v90, v90
v_exp_f32 v91, v91
v_exp_f32 v92, v92
v_exp_f32 v93, v93
v_exp_f32 v94, v94
v_exp_f32 v95, v95
v_exp_f32 v96, v96
v_exp_f32 v97, v97
v_exp_f32 v98, v98
v_exp_f32 v99, v99
v_cvt_pk_bf16_f32 v73, v74, v75
s_bitcmp1_b32 s57, s54
s_cbranch_scc1 END0Z_%=
ENDR0Z_%=:
s_branch LOOPZ_%=
LOOPEZ_%=:
s_waitcnt lgkmcnt(0)
v_mfma_f32_16x16x32_bf16 v[64:67], a[192:195], a[128:131], v[192:195]
ds_read_b128 a[208:211], v1 offset:4096
s_add_u32 s54, s54, 1
v_mfma_f32_16x16x32_bf16 v[64:67], a[196:199], a[136:139], v[64:67]
v_exp_f32 v164, v164
v_mfma_f32_16x16x32_bf16 v[64:67], a[200:203], a[144:147], v[64:67]
ds_read_b128 a[212:215], v1 offset:5120
s_add_u32 m0, s75, 98304
v_mfma_f32_16x16x32_bf16 v[64:67], a[204:207], a[152:155], v[64:67]
buffer_load_dwordx4 v3, s[40:43], s71 offen offset:0 lds
v_cvt_pk_bf16_f32 v138, v140, v141
v_mfma_f32_16x16x32_bf16 v[72:75], a[192:195], a[132:135], v[196:199]
ds_read_b128 a[216:219], v1 offset:6144
v_cvt_pk_bf16_f32 v139, v142, v143
v_mfma_f32_16x16x32_bf16 v[72:75], a[196:199], a[140:143], v[72:75]
v_exp_f32 v165, v165
v_mfma_f32_16x16x32_bf16 v[72:75], a[200:203], a[148:151], v[72:75]
ds_read_b128 a[220:223], v1 offset:7168
v_cvt_pk_bf16_f32 v144, v144, v145
v_mfma_f32_16x16x32_bf16 v[72:75], a[204:207], a[156:159], v[72:75]
buffer_load_dwordx4 v3, s[40:43], s71 offen offset:1024 lds
v_cvt_pk_bf16_f32 v145, v146, v147
v_mfma_f32_16x16x32_bf16 v[80:83], a[192:195], a[160:163], v[200:203]
v_exp_f32 v166, v166
v_mfma_f32_16x16x32_bf16 v[80:83], a[196:199], a[168:171], v[80:83]
v_exp_f32 v167, v167
v_mfma_f32_16x16x32_bf16 v[80:83], a[200:203], a[176:179], v[80:83]
v_exp_f32 v168, v168
v_mfma_f32_16x16x32_bf16 v[80:83], a[204:207], a[184:187], v[80:83]
buffer_load_dwordx4 v3, s[40:43], s71 offen offset:2048 lds
v_cvt_pk_bf16_f32 v146, v148, v149
v_mfma_f32_16x16x32_bf16 v[88:91], a[192:195], a[164:167], v[204:207]
v_exp_f32 v169, v169
v_mfma_f32_16x16x32_bf16 v[88:91], a[196:199], a[172:175], v[88:91]
v_exp_f32 v170, v170
v_mfma_f32_16x16x32_bf16 v[88:91], a[200:203], a[180:183], v[88:91]
buffer_load_dwordx4 v3, s[40:43], s71 offen offset:3072 lds
v_cvt_pk_bf16_f32 v147, v150, v151
v_mfma_f32_16x16x32_bf16 v[88:91], a[204:207], a[188:191], v[88:91]
v_cvt_pk_bf16_f32 v152, v152, v153
s_waitcnt lgkmcnt(0)
v_mfma_f32_16x16x32_bf16 v[68:71], a[208:211], a[128:131], v[192:195]
ds_read_b128 a[192:195], v1 offset:8192
v_cvt_pk_bf16_f32 v153, v154, v155
v_mfma_f32_16x16x32_bf16 v[68:71], a[212:215], a[136:139], v[68:71]
v_exp_f32 v171, v171
v_mfma_f32_16x16x32_bf16 v[68:71], a[216:219], a[144:147], v[68:71]
ds_read_b128 a[196:199], v1 offset:9216
s_add_u32 m0, s75, 102400
v_mfma_f32_16x16x32_bf16 v[68:71], a[220:223], a[152:155], v[68:71]
buffer_load_dwordx4 v4, s[40:43], s71 offen offset:0 lds
v_cvt_pk_bf16_f32 v154, v156, v157
v_mfma_f32_16x16x32_bf16 v[76:79], a[208:211], a[132:135], v[196:199]
ds_read_b128 a[200:203], v1 offset:10240
v_cvt_pk_bf16_f32 v155, v158, v159
v_mfma_f32_16x16x32_bf16 v[76:79], a[212:215], a[140:143], v[76:79]
v_exp_f32 v172, v172
v_mfma_f32_16x16x32_bf16 v[76:79], a[216:219], a[148:151], v[76:79]
ds_read_b128 a[204:207], v1 offset:11264
v_cvt_pk_bf16_f32 v160, v160, v161
v_mfma_f32_16x16x32_bf16 v[76:79], a[220:223], a[156:159], v[76:79]
buffer_load_dwordx4 v4, s[40:43], s71 offen offset:1024 lds
v_cvt_pk_bf16_f32 v161, v162, v163
v_mfma_f32_16x16x32_bf16 v[84:87], a[208:211], a[160:163], v[200:203]
v_exp_f32 v173, v173
v_mfma_f32_16x16x32_bf16 v[84:87], a[212:215], a[168:171], v[84:87]
v_exp_f32 v174, v174
v_mfma_f32_16x16x32_bf16 v[84:87], a[216:219], a[176:179], v[84:87]
v_exp_f32 v175, v175
v_mfma_f32_16x16x32_bf16 v[84:87], a[220:223], a[184:187], v[84:87]
buffer_load_dwordx4 v4, s[40:43], s71 offen offset:2048 lds
v_cvt_pk_bf16_f32 v162, v164, v165
v_mfma_f32_16x16x32_bf16 v[92:95], a[208:211], a[164:167], v[204:207]
v_exp_f32 v176, v176
v_mfma_f32_16x16x32_bf16 v[92:95], a[212:215], a[172:175], v[92:95]
v_exp_f32 v177, v177
v_mfma_f32_16x16x32_bf16 v[92:95], a[216:219], a[180:183], v[92:95]
buffer_load_dwordx4 v4, s[40:43], s71 offen offset:3072 lds
v_cvt_pk_bf16_f32 v163, v166, v167
v_mfma_f32_16x16x32_bf16 v[92:95], a[220:223], a[188:191], v[92:95]
v_cvt_pk_bf16_f32 v168, v168, v169
s_waitcnt lgkmcnt(0)
v_mfma_f32_16x16x32_bf16 v[96:99], a[192:195], a[128:131], v[192:195]
ds_read_b128 a[208:211], v1 offset:12288
v_cvt_pk_bf16_f32 v169, v170, v171
v_mfma_f32_16x16x32_bf16 v[96:99], a[196:199], a[136:139], v[96:99]
v_exp_f32 v178, v178
v_mfma_f32_16x16x32_bf16 v[96:99], a[200:203], a[144:147], v[96:99]
ds_read_b128 a[212:215], v1 offset:13312
s_add_u32 m0, s75, 65536
v_mfma_f32_16x16x32_bf16 v[96:99], a[204:207], a[152:155], v[96:99]
buffer_load_dwordx4 v3, s[36:39], s70 offen offset:0 lds
v_cvt_pk_bf16_f32 v170, v172, v173
v_mfma_f32_16x16x32_bf16 v[104:107], a[192:195], a[132:135], v[196:199]
ds_read_b128 a[216:219], v1 offset:14336
v_cvt_pk_bf16_f32 v171, v174, v175
v_mfma_f32_16x16x32_bf16 v[104:107], a[196:199], a[140:143], v[104:107]
v_exp_f32 v179, v179
v_mfma_f32_16x16x32_bf16 v[104:107], a[200:203], a[148:151], v[104:107]
ds_read_b128 a[220:223], v1 offset:15360
v_cvt_pk_bf16_f32 v176, v176, v177
v_mfma_f32_16x16x32_bf16 v[104:107], a[204:207], a[156:159], v[104:107]
buffer_load_dwordx4 v3, s[36:39], s70 offen offset:1024 lds
v_cvt_pk_bf16_f32 v177, v178, v179
v_mfma_f32_16x16x32_bf16 v[112:115], a[192:195], a[160:163], v[200:203]
v_exp_f32 v180, v180
v_mfma_f32_16x16x32_bf16 v[112:115], a[196:199], a[168:171], v[112:115]
v_exp_f32 v181, v181
v_mfma_f32_16x16x32_bf16 v[112:115], a[200:203], a[176:179], v[112:115]
v_exp_f32 v182, v182
v_mfma_f32_16x16x32_bf16 v[112:115], a[204:207], a[184:187], v[112:115]
buffer_load_dwordx4 v3, s[36:39], s70 offen offset:2048 lds
v_cvt_pk_bf16_f32 v178, v180, v181
v_mfma_f32_16x16x32_bf16 v[120:123], a[192:195], a[164:167], v[204:207]
v_exp_f32 v183, v183
v_mfma_f32_16x16x32_bf16 v[120:123], a[196:199], a[172:175], v[120:123]
v_exp_f32 v184, v184
v_mfma_f32_16x16x32_bf16 v[120:123], a[200:203], a[180:183], v[120:123]
buffer_load_dwordx4 v3, s[36:39], s70 offen offset:3072 lds
v_cvt_pk_bf16_f32 v179, v182, v183
v_mfma_f32_16x16x32_bf16 v[120:123], a[204:207], a[188:191], v[120:123]
)"
R"(s_waitcnt lgkmcnt(0)
v_mfma_f32_16x16x32_bf16 v[100:103], a[208:211], a[128:131], v[192:195]
ds_read_b128 a[192:195], v1 offset:32768
v_mfma_f32_16x16x32_bf16 v[100:103], a[212:215], a[136:139], v[100:103]
v_exp_f32 v185, v185
v_mfma_f32_16x16x32_bf16 v[100:103], a[216:219], a[144:147], v[100:103]
ds_read_b128 a[196:199], v1 offset:40960
s_add_u32 m0, s75, 69632
v_mfma_f32_16x16x32_bf16 v[100:103], a[220:223], a[152:155], v[100:103]
buffer_load_dwordx4 v4, s[36:39], s70 offen offset:0 lds
v_cvt_pk_bf16_f32 v184, v184, v185
v_mfma_f32_16x16x32_bf16 v[108:111], a[208:211], a[132:135], v[196:199]
ds_read_b128 a[200:203], v1 offset:33792
v_mfma_f32_16x16x32_bf16 v[108:111], a[212:215], a[140:143], v[108:111]
v_exp_f32 v186, v186
v_mfma_f32_16x16x32_bf16 v[108:111], a[216:219], a[148:151], v[108:111]
ds_read_b128 a[204:207], v1 offset:41984
v_mfma_f32_16x16x32_bf16 v[108:111], a[220:223], a[156:159], v[108:111]
buffer_load_dwordx4 v4, s[36:39], s70 offen offset:1024 lds
v_mfma_f32_16x16x32_bf16 v[116:119], a[208:211], a[160:163], v[200:203]
v_exp_f32 v187, v187
v_mfma_f32_16x16x32_bf16 v[116:119], a[212:215], a[168:171], v[116:119]
v_exp_f32 v188, v188
v_mfma_f32_16x16x32_bf16 v[116:119], a[216:219], a[176:179], v[116:119]
v_exp_f32 v189, v189
v_mfma_f32_16x16x32_bf16 v[116:119], a[220:223], a[184:187], v[116:119]
buffer_load_dwordx4 v4, s[36:39], s70 offen offset:2048 lds
v_cvt_pk_bf16_f32 v185, v186, v187
v_mfma_f32_16x16x32_bf16 v[124:127], a[208:211], a[164:167], v[204:207]
v_exp_f32 v190, v190
v_mfma_f32_16x16x32_bf16 v[124:127], a[212:215], a[172:175], v[124:127]
v_exp_f32 v191, v191
v_mfma_f32_16x16x32_bf16 v[124:127], a[216:219], a[180:183], v[124:127]
buffer_load_dwordx4 v4, s[36:39], s70 offen offset:3072 lds
v_cvt_pk_bf16_f32 v186, v188, v189
v_mfma_f32_16x16x32_bf16 v[124:127], a[220:223], a[188:191], v[124:127]
v_cvt_pk_bf16_f32 v187, v190, v191
s_waitcnt lgkmcnt(0)
v_mfma_f32_16x16x32_bf16 a[0:3], a[192:195], v[128:131], a[0:3]
ds_read_b128 a[208:211], v1 offset:34816
v_mfma_f32_16x16x32_bf16 a[0:3], a[196:199], v[160:163], a[0:3]
s_cmp_eq_u32 s54, s56
s_cbranch_scc1 MASK2Z_%=
MASKR2Z_%=:
v_mfma_f32_16x16x32_bf16 a[4:7], a[200:203], v[128:131], a[4:7]
ds_read_b128 a[212:215], v1 offset:43008
v_max3_f32 v5, v64, v65, v66
v_mfma_f32_16x16x32_bf16 a[4:7], a[204:207], v[160:163], a[4:7]
v_max3_f32 v6, v72, v73, v74
v_max3_f32 v7, v80, v81, v82
v_mfma_f32_16x16x32_bf16 a[32:35], a[192:195], v[136:139], a[32:35]
ds_read_b128 a[216:219], v1 offset:35840
v_max3_f32 v8, v88, v89, v90
v_mfma_f32_16x16x32_bf16 a[32:35], a[196:199], v[168:171], a[32:35]
v_max3_f32 v5, v5, v67, v68
v_max3_f32 v6, v6, v75, v76
v_mfma_f32_16x16x32_bf16 a[36:39], a[200:203], v[136:139], a[36:39]
ds_read_b128 a[220:223], v1 offset:44032
v_max3_f32 v7, v7, v83, v84
v_mfma_f32_16x16x32_bf16 a[36:39], a[204:207], v[168:171], a[36:39]
v_max3_f32 v8, v8, v91, v92
v_max3_f32 v5, v5, v69, v70
v_mfma_f32_16x16x32_bf16 a[64:67], a[192:195], v[144:147], a[64:67]
v_max3_f32 v6, v6, v77, v78
v_max3_f32 v7, v7, v85, v86
v_mfma_f32_16x16x32_bf16 a[64:67], a[196:199], v[176:179], a[64:67]
v_max3_f32 v8, v8, v93, v94
v_max3_f32 v5, v5, v71, v96
v_mfma_f32_16x16x32_bf16 a[68:71], a[200:203], v[144:147], a[68:71]
v_max3_f32 v6, v6, v79, v104
v_max3_f32 v7, v7, v87, v112
v_mfma_f32_16x16x32_bf16 a[68:71], a[204:207], v[176:179], a[68:71]
v_max3_f32 v8, v8, v95, v120
v_max3_f32 v5, v5, v97, v98
v_mfma_f32_16x16x32_bf16 a[96:99], a[192:195], v[152:155], a[96:99]
v_max3_f32 v6, v6, v105, v106
v_max3_f32 v7, v7, v113, v114
v_mfma_f32_16x16x32_bf16 a[96:99], a[196:199], v[184:187], a[96:99]
v_max3_f32 v8, v8, v121, v122
v_max3_f32 v5, v5, v99, v100
v_mfma_f32_16x16x32_bf16 a[100:103], a[200:203], v[152:155], a[100:103]
v_max3_f32 v6, v6, v107, v108
v_max3_f32 v7, v7, v115, v116
v_mfma_f32_16x16x32_bf16 a[100:103], a[204:207], v[184:187], a[100:103]
v_max3_f32 v8, v8, v123, v124
s_waitcnt lgkmcnt(0)
v_mfma_f32_16x16x32_bf16 a[8:11], a[208:211], v[128:131], a[8:11]
ds_read_b128 a[192:195], v1 offset:36864
v_max3_f32 v5, v5, v101, v102
v_mfma_f32_16x16x32_bf16 a[8:11], a[212:215], v[160:163], a[8:11]
v_max3_f32 v6, v6, v109, v110
v_max3_f32 v7, v7, v117, v118
v_mfma_f32_16x16x32_bf16 a[12:15], a[216:219], v[128:131], a[12:15]
ds_read_b128 a[196:199], v1 offset:45056
v_max3_f32 v8, v8, v125, v126
v_mfma_f32_16x16x32_bf16 a[12:15], a[220:223], v[160:163], a[12:15]
v_max_f32 v5, v5, v103
v_max_f32 v6, v6, v111
v_mfma_f32_16x16x32_bf16 a[40:43], a[208:211], v[136:139], a[40:43]
ds_read_b128 a[200:203], v1 offset:37888
v_max_f32 v7, v7, v119
v_mfma_f32_16x16x32_bf16 a[40:43], a[212:215], v[168:171], a[40:43]
v_max_f32 v8, v8, v127
v_max3_f32 v9, v5, v6, v7
v_mfma_f32_16x16x32_bf16 a[44:47], a[216:219], v[136:139], a[44:47]
ds_read_b128 a[204:207], v1 offset:46080
v_max_f32 v9, v9, v8
v_mfma_f32_16x16x32_bf16 a[44:47], a[220:223], v[168:171], a[44:47]
v_cmp_le_f32 vcc, s58, v9
s_cbranch_vccnz RARE2Z_%=
RARER2Z_%=:
v_mfma_f32_16x16x32_bf16 a[72:75], a[208:211], v[144:147], a[72:75]
v_exp_f32 v64, v64
v_mfma_f32_16x16x32_bf16 a[72:75], a[212:215], v[176:179], a[72:75]
v_exp_f32 v65, v65
v_mfma_f32_16x16x32_bf16 a[76:79], a[216:219], v[144:147], a[76:79]
v_exp_f32 v66, v66
v_mfma_f32_16x16x32_bf16 a[76:79], a[220:223], v[176:179], a[76:79]
v_exp_f32 v67, v67
v_mfma_f32_16x16x32_bf16 a[104:107], a[208:211], v[152:155], a[104:107]
v_exp_f32 v68, v68
v_mfma_f32_16x16x32_bf16 a[104:107], a[212:215], v[184:187], a[104:107]
v_exp_f32 v69, v69
v_mfma_f32_16x16x32_bf16 a[108:111], a[216:219], v[152:155], a[108:111]
v_exp_f32 v70, v70
v_mfma_f32_16x16x32_bf16 a[108:111], a[220:223], v[184:187], a[108:111]
v_exp_f32 v71, v71
v_mfma_f32_16x16x32_bf16 a[224:227], a[240:243], v[128:131], a[224:227]
v_exp_f32 v72, v72
v_mfma_f32_16x16x32_bf16 a[224:227], a[240:243], v[160:163], a[224:227]
v_exp_f32 v73, v73
v_mfma_f32_16x16x32_bf16 a[228:231], a[240:243], v[136:139], a[228:231]
v_exp_f32 v74, v74
v_mfma_f32_16x16x32_bf16 a[228:231], a[240:243], v[168:171], a[228:231]
v_cvt_pk_bf16_f32 v64, v64, v65
s_waitcnt lgkmcnt(0)
v_mfma_f32_16x16x32_bf16 a[16:19], a[192:195], v[128:131], a[16:19]
ds_read_b128 a[208:211], v1 offset:38912
s_mov_b32 s71, s70
v_mfma_f32_16x16x32_bf16 a[16:19], a[196:199], v[160:163], a[16:19]
v_exp_f32 v75, v75
v_mfma_f32_16x16x32_bf16 a[20:23], a[200:203], v[128:131], a[20:23]
ds_read_b128 a[212:215], v1 offset:47104
s_add_u32 s70, s70, 32768
v_mfma_f32_16x16x32_bf16 a[20:23], a[204:207], v[160:163], a[20:23]
v_exp_f32 v76, v76
v_mfma_f32_16x16x32_bf16 a[48:51], a[192:195], v[136:139], a[48:51]
ds_read_b128 a[216:219], v1 offset:39936
s_cmp_eq_u32 s54, s72
v_mfma_f32_16x16x32_bf16 a[48:51], a[196:199], v[168:171], a[48:51]
v_exp_f32 v77, v77
v_mfma_f32_16x16x32_bf16 a[52:55], a[200:203], v[136:139], a[52:55]
ds_read_b128 a[220:223], v1 offset:48128
s_cselect_b32 s70, 0, s70
v_mfma_f32_16x16x32_bf16 a[52:55], a[204:207], v[168:171], a[52:55]
v_exp_f32 v78, v78
v_mfma_f32_16x16x32_bf16 a[80:83], a[192:195], v[144:147], a[80:83]
v_exp_f32 v79, v79
v_mfma_f32_16x16x32_bf16 a[80:83], a[196:199], v[176:179], a[80:83]
v_exp_f32 v80, v80
v_mfma_f32_16x16x32_bf16 a[84:87], a[200:203], v[144:147], a[84:87]
v_exp_f32 v81, v81
v_mfma_f32_16x16x32_bf16 a[84:87], a[204:207], v[176:179], a[84:87]
v_exp_f32 v82, v82
v_mfma_f32_16x16x32_bf16 a[112:115], a[192:195], v[152:155], a[112:115]
v_exp_f32 v83, v83
v_mfma_f32_16x16x32_bf16 a[112:115], a[196:199], v[184:187], a[112:115]
v_exp_f32 v84, v84
v_mfma_f32_16x16x32_bf16 a[116:119], a[200:203], v[152:155], a[116:119]
v_exp_f32 v85, v85
v_mfma_f32_16x16x32_bf16 a[116:119], a[204:207], v[184:187], a[116:119]
s_waitcnt vmcnt(0) lgkmcnt(0)
s_barrier
v_mfma_f32_16x16x32_bf16 a[24:27], a[208:211], v[128:131], a[24:27]
ds_read_b128 a[192:195], v2 offset:0
v_cvt_pk_bf16_f32 v65, v66, v67
v_mfma_f32_16x16x32_bf16 a[24:27], a[212:215], v[160:163], a[24:27]
v_exp_f32 v86, v86
v_mfma_f32_16x16x32_bf16 a[28:31], a[216:219], v[128:131], a[28:31]
ds_read_b128 a[196:199], v2 offset:1024
v_cvt_pk_bf16_f32 v66, v68, v69
v_mfma_f32_16x16x32_bf16 a[28:31], a[220:223], v[160:163], a[28:31]
v_exp_f32 v87, v87
v_mfma_f32_16x16x32_bf16 a[56:59], a[208:211], v[136:139], a[56:59]
ds_read_b128 a[200:203], v2 offset:2048
v_cvt_pk_bf16_f32 v67, v70, v71
v_mfma_f32_16x16x32_bf16 a[56:59], a[212:215], v[168:171], a[56:59]
v_exp_f32 v88, v88
v_mfma_f32_16x16x32_bf16 a[60:63], a[216:219], v[136:139], a[60:63]
ds_read_b128 a[204:207], v2 offset:3072
v_cvt_pk_bf16_f32 v72, v72, v73
v_mfma_f32_16x16x32_bf16 a[60:63], a[220:223], v[168:171], a[60:63]
v_exp_f32 v89, v89
v_mfma_f32_16x16x32_bf16 a[88:91], a[208:211], v[144:147], a[88:91]
v_exp_f32 v90, v90
v_mfma_f32_16x16x32_bf16 a[88:91], a[212:215], v[176:179], a[88:91]
v_exp_f32 v91, v91
v_mfma_f32_16x16x32_bf16 a[92:95], a[216:219], v[144:147], a[92:95]
v_exp_f32 v92, v92
v_mfma_f32_16x16x32_bf16 a[92:95], a[220:223], v[176:179], a[92:95]
v_exp_f32 v93, v93
v_mfma_f32_16x16x32_bf16 a[120:123], a[208:211], v[152:155], a[120:123]
v_exp_f32 v94, v94
v_mfma_f32_16x16x32_bf16 a[120:123], a[212:215], v[184:187], a[120:123]
v_exp_f32 v95, v95
v_mfma_f32_16x16x32_bf16 a[124:127], a[216:219], v[152:155], a[124:127]
v_exp_f32 v96, v96
v_mfma_f32_16x16x32_bf16 a[124:127], a[220:223], v[184:187], a[124:127]
v_exp_f32 v97, v97
v_mfma_f32_16x16x32_bf16 a[232:235], a[240:243], v[144:147], a[232:235]
v_exp_f32 v98, v98
v_mfma_f32_16x16x32_bf16 a[232:235], a[240:243], v[176:179], a[232:235]
v_exp_f32 v99, v99
v_mfma_f32_16x16x32_bf16 a[236:239], a[240:243], v[152:155], a[236:239]
v_cvt_pk_bf16_f32 v73, v74, v75
s_bitcmp1_b32 s57, s54
v_mfma_f32_16x16x32_bf16 a[236:239], a[240:243], v[184:187], a[236:239]
s_cbranch_scc1 END2Z_%=
ENDR2Z_%=:
LOOPZ_%=:
s_waitcnt lgkmcnt(0)
v_mfma_f32_16x16x32_bf16 v[128:131], a[192:195], a[128:131], v[192:195]
ds_read_b128 a[208:211], v2 offset:4096
s_add_u32 s54, s54, 1
v_mfma_f32_16x16x32_bf16 v[128:131], a[196:199], a[136:139], v[128:131]
v_exp_f32 v100, v100
v_mfma_f32_16x16x32_bf16 v[128:131], a[200:203], a[144:147], v[128:131]
ds_read_b128 a[212:215], v2 offset:5120
s_add_u32 m0, s75, 32768
v_mfma_f32_16x16x32_bf16 v[128:131], a[204:207], a[152:155], v[128:131]
buffer_load_dwordx4 v3, s[40:43], s71 offen offset:0 lds
v_cvt_pk_bf16_f32 v74, v76, v77
v_mfma_f32_16x16x32_bf16 v[136:139], a[192:195], a[132:135], v[196:199]
ds_read_b128 a[216:219], v2 offset:6144
v_cvt_pk_bf16_f32 v75, v78, v79
v_mfma_f32_16x16x32_bf16 v[136:139], a[196:199], a[140:143], v[136:139]
v_exp_f32 v101, v101
v_mfma_f32_16x16x32_bf16 v[136:139], a[200:203], a[148:151], v[136:139]
ds_read_b128 a[220:223], v2 offset:7168
v_cvt_pk_bf16_f32 v80, v80, v81
v_mfma_f32_16x16x32_bf16 v[136:139], a[204:207], a[156:159], v[136:139]
buffer_load_dwordx4 v3, s[40:43], s71 offen offset:1024 lds
v_cvt_pk_bf16_f32 v81, v82, v83
v_mfma_f32_16x16x32_bf16 v[144:147], a[192:195], a[160:163], v[200:203]
v_exp_f32 v102, v102
v_mfma_f32_16x16x32_bf16 v[144:147], a[196:199], a[168:171], v[144:147]
v_exp_f32 v103, v103
v_mfma_f32_16x16x32_bf16 v[144:147], a[200:203], a[176:179], v[144:147]
v_exp_f32 v104, v104
v_mfma_f32_16x16x32_bf16 v[144:147], a[204:207], a[184:187], v[144:147]
buffer_load_dwordx4 v3, s[40:43], s71 offen offset:2048 lds
v_cvt_pk_bf16_f32 v82, v84, v85
v_mfma_f32_16x16x32_bf16 v[152:155], a[192:195], a[164:167], v[204:207]
v_exp_f32 v105, v105
v_mfma_f32_16x16x32_bf16 v[152:155], a[196:199], a[172:175], v[152:155]
v_exp_f32 v106, v106
v_mfma_f32_16x16x32_bf16 v[152:155], a[200:203], a[180:183], v[152:155]
buffer_load_dwordx4 v3, s[40:43], s71 offen offset:3072 lds
v_cvt_pk_bf16_f32 v83, v86, v87
v_mfma_f32_16x16x32_bf16 v[152:155], a[204:207], a[188:191], v[152:155]
v_cvt_pk_bf16_f32 v88, v88, v89
s_waitcnt lgkmcnt(0)
v_mfma_f32_16x16x32_bf16 v[132:135], a[208:211], a[128:131], v[192:195]
ds_read_b128 a[192:195], v2 offset:8192
v_cvt_pk_bf16_f32 v89, v90, v91
v_mfma_f32_16x16x32_bf16 v[132:135], a[212:215], a[136:139], v[132:135]
v_exp_f32 v107, v107
v_mfma_f32_16x16x32_bf16 v[132:135], a[216:219], a[144:147], v[132:135]
ds_read_b128 a[196:199], v2 offset:9216
s_add_u32 m0, s75, 36864
v_mfma_f32_16x16x32_bf16 v[132:135], a[220:223], a[152:155], v[132:135]
buffer_load_dwordx4 v4, s[40:43], s71 offen offset:0 lds
v_cvt_pk_bf16_f32 v90, v92, v93
v_mfma_f32_16x16x32_bf16 v[140:143], a[208:211], a[132:135], v[196:199]
ds_read_b128 a[200:203], v2 offset:10240
v_cvt_pk_bf16_f32 v91, v94, v95
v_mfma_f32_16x16x32_bf16 v[140:143], a[212:215], a[140:143], v[140:143]
v_exp_f32 v108, v108
v_mfma_f32_16x16x32_bf16 v[140:143], a[216:219], a[148:151], v[140:143]
ds_read_b128 a[204:207], v2 offset:11264
v_cvt_pk_bf16_f32 v96, v96, v97
v_mfma_f32_16x16x32_bf16 v[140:143], a[220:223], a[156:159], v[140:143]
buffer_load_dwordx4 v4, s[40:43], s71 offen offset:1024 lds
v_cvt_pk_bf16_f32 v97, v98, v99
v_mfma_f32_16x16x32_bf16 v[148:151], a[208:211], a[160:163], v[200:203]
v_exp_f32 v109, v109
v_mfma_f32_16x16x32_bf16 v[148:151], a[212:215], a[168:171], v[148:151]
v_exp_f32 v110, v110
v_mfma_f32_16x16x32_bf16 v[148:151], a[216:219], a[176:179], v[148:151]
v_exp_f32 v111, v111
v_mfma_f32_16x16x32_bf16 v[148:151], a[220:223], a[184:187], v[148:151]
buffer_load_dwordx4 v4, s[40:43], s71 offen offset:2048 lds
v_cvt_pk_bf16_f32 v98, v100, v101
v_mfma_f32_16x16x32_bf16 v[156:159], a[208:211], a[164:167], v[204:207]
v_exp_f32 v112, v112
v_mfma_f32_16x16x32_bf16 v[156:159], a[212:215], a[172:175], v[156:159]
v_exp_f32 v113, v113
v_mfma_f32_16x16x32_bf16 v[156:159], a[216:219], a[180:183], v[156:159]
buffer_load_dwordx4 v4, s[40:43], s71 offen offset:3072 lds
v_cvt_pk_bf16_f32 v99, v102, v103
v_mfma_f32_16x16x32_bf16 v[156:159], a[220:223], a[188:191], v[156:159]
v_cvt_pk_bf16_f32 v104, v104, v105
s_waitcnt lgkmcnt(0)
v_mfma_f32_16x16x32_bf16 v[160:163], a[192:195], a[128:131], v[192:195]
ds_read_b128 a[208:211], v2 offset:12288
v_cvt_pk_bf16_f32 v105, v106, v107
v_mfma_f32_16x16x32_bf16 v[160:163], a[196:199], a[136:139], v[160:163]
v_exp_f32 v114, v114
v_mfma_f32_16x16x32_bf16 v[160:163], a[200:203], a[144:147], v[160:163]
ds_read_b128 a[212:215], v2 offset:13312
s_add_u32 m0, s75, 0
v_mfma_f32_16x16x32_bf16 v[160:163], a[204:207], a[152:155], v[160:163]
buffer_load_dwordx4 v3, s[36:39], s70 offen offset:0 lds
v_cvt_pk_bf16_f32 v106, v108, v109
v_mfma_f32_16x16x32_bf16 v[168:171], a[192:195], a[132:135], v[196:199]
ds_read_b128 a[216:219], v2 offset:14336
v_cvt_pk_bf16_f32 v107, v110, v111
v_mfma_f32_16x16x32_bf16 v[168:171], a[196:199], a[140:143], v[168:171]
v_exp_f32 v115, v115
v_mfma_f32_16x16x32_bf16 v[168:171], a[200:203], a[148:151], v[168:171]
ds_read_b128 a[220:223], v2 offset:15360
v_cvt_pk_bf16_f32 v112, v112, v113
v_mfma_f32_16x16x32_bf16 v[168:171], a[204:207], a[156:159], v[168:171]
buffer_load_dwordx4 v3, s[36:39], s70 offen offset:1024 lds
v_cvt_pk_bf16_f32 v113, v114, v115
v_mfma_f32_16x16x32_bf16 v[176:179], a[192:195], a[160:163], v[200:203]
v_exp_f32 v116, v116
v_mfma_f32_16x16x32_bf16 v[176:179], a[196:199], a[168:171], v[176:179]
v_exp_f32 v117, v117
v_mfma_f32_16x16x32_bf16 v[176:179], a[200:203], a[176:179], v[176:179]
v_exp_f32 v118, v118
v_mfma_f32_16x16x32_bf16 v[176:179], a[204:207], a[184:187], v[176:179]
buffer_load_dwordx4 v3, s[36:39], s70 offen offset:2048 lds
v_cvt_pk_bf16_f32 v114, v116, v117
v_mfma_f32_16x16x32_bf16 v[184:187], a[192:195], a[164:167], v[204:207]
v_exp_f32 v119, v119
v_mfma_f32_16x16x32_bf16 v[184:187], a[196:199], a[172:175], v[184:187]
v_exp_f32 v120, v120
v_mfma_f32_16x16x32_bf16 v[184:187], a[200:203], a[180:183], v[184:187]
buffer_load_dwordx4 v3, s[36:39], s70 offen offset:3072 lds
v_cvt_pk_bf16_f32 v115, v118, v119
v_mfma_f32_16x16x32_bf16 v[184:187], a[204:207], a[188:191], v[184:187]
s_waitcnt lgkmcnt(0)
v_mfma_f32_16x16x32_bf16 v[164:167], a[208:211], a[128:131], v[192:195]
ds_read_b128 a[192:195], v2 offset:32768
v_mfma_f32_16x16x32_bf16 v[164:167], a[212:215], a[136:139], v[164:167]
v_exp_f32 v121, v121
v_mfma_f32_16x16x32_bf16 v[164:167], a[216:219], a[144:147], v[164:167]
ds_read_b128 a[196:199], v2 offset:40960
s_add_u32 m0, s75, 4096
v_mfma_f32_16x16x32_bf16 v[164:167], a[220:223], a[152:155], v[164:167]
buffer_load_dwordx4 v4, s[36:39], s70 offen offset:0 lds
v_cvt_pk_bf16_f32 v120, v120, v121
v_mfma_f32_16x16x32_bf16 v[172:175], a[208:211], a[132:135], v[196:199]
ds_read_b128 a[200:203], v2 offset:33792
v_mfma_f32_16x16x32_bf16 v[172:175], a[212:215], a[140:143], v[172:175]
v_exp_f32 v122, v122
v_mfma_f32_16x16x32_bf16 v[172:175], a[216:219], a[148:151], v[172:175]
ds_read_b128 a[204:207], v2 offset:41984
v_mfma_f32_16x16x32_bf16 v[172:175], a[220:223], a[156:159], v[172:175]
buffer_load_dwordx4 v4, s[36:39], s70 offen offset:1024 lds
v_mfma_f32_16x16x32_bf16 v[180:183], a[208:211], a[160:163], v[200:203]
v_exp_f32 v123, v123
v_mfma_f32_16x16x32_bf16 v[180:183], a[212:215], a[168:171], v[180:183]
v_exp_f32 v124, v124
v_mfma_f32_16x16x32_bf16 v[180:183], a[216:219], a[176:179], v[180:183]
v_exp_f32 v125, v125
v_mfma_f32_16x16x32_bf16 v[180:183], a[220:223], a[184:187], v[180:183]
buffer_load_dwordx4 v4, s[36:39], s70 offen offset:2048 lds
v_cvt_pk_bf16_f32 v121, v122, v123
v_mfma_f32_16x16x32_bf16 v[188:191], a[208:211], a[164:167], v[204:207]
v_exp_f32 v126, v126
v_mfma_f32_16x16x32_bf16 v[188:191], a[212:215], a[172:175], v[188:191]
v_exp_f32 v127, v127
v_mfma_f32_16x16x32_bf16 v[188:191], a[216:219], a[180:183], v[188:191]
buffer_load_dwordx4 v4, s[36:39], s70 offen offset:3072 lds
v_cvt_pk_bf16_f32 v122, v124, v125
v_mfma_f32_16x16x32_bf16 v[188:191], a[220:223], a[188:191], v[188:191]
v_cvt_pk_bf16_f32 v123, v126, v127
s_waitcnt lgkmcnt(0)
v_mfma_f32_16x16x32_bf16 a[0:3], a[192:195], v[64:67], a[0:3]
ds_read_b128 a[208:211], v2 offset:34816
v_mfma_f32_16x16x32_bf16 a[0:3], a[196:199], v[96:99], a[0:3]
s_cmp_eq_u32 s54, s56
s_cbranch_scc1 MASK1Z_%=
MASKR1Z_%=:
v_mfma_f32_16x16x32_bf16 a[4:7], a[200:203], v[64:67], a[4:7]
ds_read_b128 a[212:215], v2 offset:43008
v_max3_f32 v5, v128, v129, v130
v_mfma_f32_16x16x32_bf16 a[4:7], a[204:207], v[96:99], a[4:7]
v_max3_f32 v6, v136, v137, v138
v_max3_f32 v7, v144, v145, v146
v_mfma_f32_16x16x32_bf16 a[32:35], a[192:195], v[72:75], a[32:35]
ds_read_b128 a[216:219], v2 offset:35840
v_max3_f32 v8, v152, v153, v154
v_mfma_f32_16x16x32_bf16 a[32:35], a[196:199], v[104:107], a[32:35]
v_max3_f32 v5, v5, v131, v132
v_max3_f32 v6, v6, v139, v140
v_mfma_f32_16x16x32_bf16 a[36:39], a[200:203], v[72:75], a[36:39]
ds_read_b128 a[220:223], v2 offset:44032
)"
R"(v_max3_f32 v7, v7, v147, v148
v_mfma_f32_16x16x32_bf16 a[36:39], a[204:207], v[104:107], a[36:39]
v_max3_f32 v8, v8, v155, v156
v_max3_f32 v5, v5, v133, v134
v_mfma_f32_16x16x32_bf16 a[64:67], a[192:195], v[80:83], a[64:67]
v_max3_f32 v6, v6, v141, v142
v_max3_f32 v7, v7, v149, v150
v_mfma_f32_16x16x32_bf16 a[64:67], a[196:199], v[112:115], a[64:67]
v_max3_f32 v8, v8, v157, v158
v_max3_f32 v5, v5, v135, v160
v_mfma_f32_16x16x32_bf16 a[68:71], a[200:203], v[80:83], a[68:71]
v_max3_f32 v6, v6, v143, v168
v_max3_f32 v7, v7, v151, v176
v_mfma_f32_16x16x32_bf16 a[68:71], a[204:207], v[112:115], a[68:71]
v_max3_f32 v8, v8, v159, v184
v_max3_f32 v5, v5, v161, v162
v_mfma_f32_16x16x32_bf16 a[96:99], a[192:195], v[88:91], a[96:99]
v_max3_f32 v6, v6, v169, v170
v_max3_f32 v7, v7, v177, v178
v_mfma_f32_16x16x32_bf16 a[96:99], a[196:199], v[120:123], a[96:99]
v_max3_f32 v8, v8, v185, v186
v_max3_f32 v5, v5, v163, v164
v_mfma_f32_16x16x32_bf16 a[100:103], a[200:203], v[88:91], a[100:103]
v_max3_f32 v6, v6, v171, v172
v_max3_f32 v7, v7, v179, v180
v_mfma_f32_16x16x32_bf16 a[100:103], a[204:207], v[120:123], a[100:103]
v_max3_f32 v8, v8, v187, v188
s_waitcnt lgkmcnt(0)
v_mfma_f32_16x16x32_bf16 a[8:11], a[208:211], v[64:67], a[8:11]
ds_read_b128 a[192:195], v2 offset:36864
v_max3_f32 v5, v5, v165, v166
v_mfma_f32_16x16x32_bf16 a[8:11], a[212:215], v[96:99], a[8:11]
v_max3_f32 v6, v6, v173, v174
v_max3_f32 v7, v7, v181, v182
v_mfma_f32_16x16x32_bf16 a[12:15], a[216:219], v[64:67], a[12:15]
ds_read_b128 a[196:199], v2 offset:45056
v_max3_f32 v8, v8, v189, v190
v_mfma_f32_16x16x32_bf16 a[12:15], a[220:223], v[96:99], a[12:15]
v_max_f32 v5, v5, v167
v_max_f32 v6, v6, v175
v_mfma_f32_16x16x32_bf16 a[40:43], a[208:211], v[72:75], a[40:43]
ds_read_b128 a[200:203], v2 offset:37888
v_max_f32 v7, v7, v183
v_mfma_f32_16x16x32_bf16 a[40:43], a[212:215], v[104:107], a[40:43]
v_max_f32 v8, v8, v191
v_max3_f32 v9, v5, v6, v7
v_mfma_f32_16x16x32_bf16 a[44:47], a[216:219], v[72:75], a[44:47]
ds_read_b128 a[204:207], v2 offset:46080
v_max_f32 v9, v9, v8
v_mfma_f32_16x16x32_bf16 a[44:47], a[220:223], v[104:107], a[44:47]
v_cmp_le_f32 vcc, s58, v9
s_cbranch_vccnz RARE1Z_%=
RARER1Z_%=:
v_mfma_f32_16x16x32_bf16 a[72:75], a[208:211], v[80:83], a[72:75]
v_exp_f32 v128, v128
v_mfma_f32_16x16x32_bf16 a[72:75], a[212:215], v[112:115], a[72:75]
v_exp_f32 v129, v129
v_mfma_f32_16x16x32_bf16 a[76:79], a[216:219], v[80:83], a[76:79]
v_exp_f32 v130, v130
v_mfma_f32_16x16x32_bf16 a[76:79], a[220:223], v[112:115], a[76:79]
v_exp_f32 v131, v131
v_mfma_f32_16x16x32_bf16 a[104:107], a[208:211], v[88:91], a[104:107]
v_exp_f32 v132, v132
v_mfma_f32_16x16x32_bf16 a[104:107], a[212:215], v[120:123], a[104:107]
v_exp_f32 v133, v133
v_mfma_f32_16x16x32_bf16 a[108:111], a[216:219], v[88:91], a[108:111]
v_exp_f32 v134, v134
v_mfma_f32_16x16x32_bf16 a[108:111], a[220:223], v[120:123], a[108:111]
v_exp_f32 v135, v135
v_mfma_f32_16x16x32_bf16 a[224:227], a[240:243], v[64:67], a[224:227]
v_exp_f32 v136, v136
v_mfma_f32_16x16x32_bf16 a[224:227], a[240:243], v[96:99], a[224:227]
v_exp_f32 v137, v137
v_mfma_f32_16x16x32_bf16 a[228:231], a[240:243], v[72:75], a[228:231]
v_exp_f32 v138, v138
v_mfma_f32_16x16x32_bf16 a[228:231], a[240:243], v[104:107], a[228:231]
v_cvt_pk_bf16_f32 v128, v128, v129
s_waitcnt lgkmcnt(0)
v_mfma_f32_16x16x32_bf16 a[16:19], a[192:195], v[64:67], a[16:19]
ds_read_b128 a[208:211], v2 offset:38912
s_mov_b32 s71, s70
v_mfma_f32_16x16x32_bf16 a[16:19], a[196:199], v[96:99], a[16:19]
v_exp_f32 v139, v139
v_mfma_f32_16x16x32_bf16 a[20:23], a[200:203], v[64:67], a[20:23]
ds_read_b128 a[212:215], v2 offset:47104
s_add_u32 s70, s70, 32768
v_mfma_f32_16x16x32_bf16 a[20:23], a[204:207], v[96:99], a[20:23]
v_exp_f32 v140, v140
v_mfma_f32_16x16x32_bf16 a[48:51], a[192:195], v[72:75], a[48:51]
ds_read_b128 a[216:219], v2 offset:39936
s_cmp_eq_u32 s54, s72
v_mfma_f32_16x16x32_bf16 a[48:51], a[196:199], v[104:107], a[48:51]
v_exp_f32 v141, v141
v_mfma_f32_16x16x32_bf16 a[52:55], a[200:203], v[72:75], a[52:55]
ds_read_b128 a[220:223], v2 offset:48128
s_cselect_b32 s70, 0, s70
v_mfma_f32_16x16x32_bf16 a[52:55], a[204:207], v[104:107], a[52:55]
v_exp_f32 v142, v142
v_mfma_f32_16x16x32_bf16 a[80:83], a[192:195], v[80:83], a[80:83]
v_exp_f32 v143, v143
v_mfma_f32_16x16x32_bf16 a[80:83], a[196:199], v[112:115], a[80:83]
v_exp_f32 v144, v144
v_mfma_f32_16x16x32_bf16 a[84:87], a[200:203], v[80:83], a[84:87]
v_exp_f32 v145, v145
v_mfma_f32_16x16x32_bf16 a[84:87], a[204:207], v[112:115], a[84:87]
v_exp_f32 v146, v146
v_mfma_f32_16x16x32_bf16 a[112:115], a[192:195], v[88:91], a[112:115]
v_exp_f32 v147, v147
v_mfma_f32_16x16x32_bf16 a[112:115], a[196:199], v[120:123], a[112:115]
v_exp_f32 v148, v148
v_mfma_f32_16x16x32_bf16 a[116:119], a[200:203], v[88:91], a[116:119]
v_exp_f32 v149, v149
v_mfma_f32_16x16x32_bf16 a[116:119], a[204:207], v[120:123], a[116:119]
s_waitcnt vmcnt(0) lgkmcnt(0)
s_barrier
v_mfma_f32_16x16x32_bf16 a[24:27], a[208:211], v[64:67], a[24:27]
ds_read_b128 a[192:195], v1 offset:0
v_cvt_pk_bf16_f32 v129, v130, v131
v_mfma_f32_16x16x32_bf16 a[24:27], a[212:215], v[96:99], a[24:27]
v_exp_f32 v150, v150
v_mfma_f32_16x16x32_bf16 a[28:31], a[216:219], v[64:67], a[28:31]
ds_read_b128 a[196:199], v1 offset:1024
v_cvt_pk_bf16_f32 v130, v132, v133
v_mfma_f32_16x16x32_bf16 a[28:31], a[220:223], v[96:99], a[28:31]
v_exp_f32 v151, v151
v_mfma_f32_16x16x32_bf16 a[56:59], a[208:211], v[72:75], a[56:59]
ds_read_b128 a[200:203], v1 offset:2048
v_cvt_pk_bf16_f32 v131, v134, v135
v_mfma_f32_16x16x32_bf16 a[56:59], a[212:215], v[104:107], a[56:59]
v_exp_f32 v152, v152
v_mfma_f32_16x16x32_bf16 a[60:63], a[216:219], v[72:75], a[60:63]
ds_read_b128 a[204:207], v1 offset:3072
v_cvt_pk_bf16_f32 v136, v136, v137
v_mfma_f32_16x16x32_bf16 a[60:63], a[220:223], v[104:107], a[60:63]
v_exp_f32 v153, v153
v_mfma_f32_16x16x32_bf16 a[88:91], a[208:211], v[80:83], a[88:91]
v_exp_f32 v154, v154
v_mfma_f32_16x16x32_bf16 a[88:91], a[212:215], v[112:115], a[88:91]
v_exp_f32 v155, v155
v_mfma_f32_16x16x32_bf16 a[92:95], a[216:219], v[80:83], a[92:95]
v_exp_f32 v156, v156
v_mfma_f32_16x16x32_bf16 a[92:95], a[220:223], v[112:115], a[92:95]
v_exp_f32 v157, v157
v_mfma_f32_16x16x32_bf16 a[120:123], a[208:211], v[88:91], a[120:123]
v_exp_f32 v158, v158
v_mfma_f32_16x16x32_bf16 a[120:123], a[212:215], v[120:123], a[120:123]
v_exp_f32 v159, v159
v_mfma_f32_16x16x32_bf16 a[124:127], a[216:219], v[88:91], a[124:127]
v_exp_f32 v160, v160
v_mfma_f32_16x16x32_bf16 a[124:127], a[220:223], v[120:123], a[124:127]
v_exp_f32 v161, v161
v_mfma_f32_16x16x32_bf16 a[232:235], a[240:243], v[80:83], a[232:235]
v_exp_f32 v162, v162
v_mfma_f32_16x16x32_bf16 a[232:235], a[240:243], v[112:115], a[232:235]
v_exp_f32 v163, v163
v_mfma_f32_16x16x32_bf16 a[236:239], a[240:243], v[88:91], a[236:239]
v_cvt_pk_bf16_f32 v137, v138, v139
s_bitcmp1_b32 s57, s54
v_mfma_f32_16x16x32_bf16 a[236:239], a[240:243], v[120:123], a[236:239]
s_cbranch_scc0 LOOPEZ_%=
s_bitcmp1_b32 s27, s54
s_cbranch_scc1 END1Z_%=
ENDR1Z_%=:
s_cmp_lt_u32 s54, 15
s_cbranch_scc1 LOOPEZ_%=
s_waitcnt lgkmcnt(0)
v_mfma_f32_16x16x32_bf16 v[64:67], a[192:195], a[128:131], v[192:195]
ds_read_b128 a[208:211], v1 offset:4096
s_add_u32 s54, s54, 1
v_mfma_f32_16x16x32_bf16 v[64:67], a[196:199], a[136:139], v[64:67]
v_exp_f32 v164, v164
v_mfma_f32_16x16x32_bf16 v[64:67], a[200:203], a[144:147], v[64:67]
ds_read_b128 a[212:215], v1 offset:5120
s_add_u32 m0, s75, 98304
v_mfma_f32_16x16x32_bf16 v[64:67], a[204:207], a[152:155], v[64:67]
buffer_load_dwordx4 v3, s[40:43], s71 offen offset:0 lds
v_cvt_pk_bf16_f32 v138, v140, v141
v_mfma_f32_16x16x32_bf16 v[72:75], a[192:195], a[132:135], v[196:199]
ds_read_b128 a[216:219], v1 offset:6144
v_cvt_pk_bf16_f32 v139, v142, v143
v_mfma_f32_16x16x32_bf16 v[72:75], a[196:199], a[140:143], v[72:75]
v_exp_f32 v165, v165
v_and_b32 v220, 0x1ffff, v19
v_mfma_f32_16x16x32_bf16 v[72:75], a[200:203], a[148:151], v[72:75]
ds_read_b128 a[220:223], v1 offset:7168
v_cvt_pk_bf16_f32 v144, v144, v145
v_mfma_f32_16x16x32_bf16 v[72:75], a[204:207], a[156:159], v[72:75]
buffer_load_dwordx4 v3, s[40:43], s71 offen offset:1024 lds
v_cvt_pk_bf16_f32 v145, v146, v147
v_mfma_f32_16x16x32_bf16 v[80:83], a[192:195], a[160:163], v[200:203]
v_exp_f32 v166, v166
v_mfma_f32_16x16x32_bf16 v[80:83], a[196:199], a[168:171], v[80:83]
v_exp_f32 v167, v167
v_xor_b32 v223, 16, v220
v_mfma_f32_16x16x32_bf16 v[80:83], a[200:203], a[176:179], v[80:83]
v_exp_f32 v168, v168
v_mfma_f32_16x16x32_bf16 v[80:83], a[204:207], a[184:187], v[80:83]
buffer_load_dwordx4 v3, s[40:43], s71 offen offset:2048 lds
v_cvt_pk_bf16_f32 v146, v148, v149
v_mfma_f32_16x16x32_bf16 v[88:91], a[192:195], a[164:167], v[204:207]
v_exp_f32 v169, v169
v_mfma_f32_16x16x32_bf16 v[88:91], a[196:199], a[172:175], v[88:91]
v_exp_f32 v170, v170
v_add_u32 v221, 3072, v223
v_mfma_f32_16x16x32_bf16 v[88:91], a[200:203], a[180:183], v[88:91]
buffer_load_dwordx4 v3, s[40:43], s71 offen offset:3072 lds
v_cvt_pk_bf16_f32 v147, v150, v151
v_mfma_f32_16x16x32_bf16 v[88:91], a[204:207], a[188:191], v[88:91]
v_cvt_pk_bf16_f32 v152, v152, v153
s_waitcnt lgkmcnt(0)
v_mfma_f32_16x16x32_bf16 v[68:71], a[208:211], a[128:131], v[192:195]
ds_read_b128 a[192:195], v1 offset:8192
v_cvt_pk_bf16_f32 v153, v154, v155
v_mfma_f32_16x16x32_bf16 v[68:71], a[212:215], a[136:139], v[68:71]
v_exp_f32 v171, v171
v_add_u32 v222, 6144, v220
v_mfma_f32_16x16x32_bf16 v[68:71], a[216:219], a[144:147], v[68:71]
ds_read_b128 a[196:199], v1 offset:9216
s_add_u32 m0, s75, 102400
v_mfma_f32_16x16x32_bf16 v[68:71], a[220:223], a[152:155], v[68:71]
buffer_load_dwordx4 v4, s[40:43], s71 offen offset:0 lds
v_cvt_pk_bf16_f32 v154, v156, v157
v_mfma_f32_16x16x32_bf16 v[76:79], a[208:211], a[132:135], v[196:199]
ds_read_b128 a[200:203], v1 offset:10240
v_cvt_pk_bf16_f32 v155, v158, v159
v_mfma_f32_16x16x32_bf16 v[76:79], a[212:215], a[140:143], v[76:79]
v_exp_f32 v172, v172
v_add_u32 v223, 9216, v223
v_mfma_f32_16x16x32_bf16 v[76:79], a[216:219], a[148:151], v[76:79]
ds_read_b128 a[204:207], v1 offset:11264
v_cvt_pk_bf16_f32 v160, v160, v161
v_mfma_f32_16x16x32_bf16 v[76:79], a[220:223], a[156:159], v[76:79]
buffer_load_dwordx4 v4, s[40:43], s71 offen offset:1024 lds
v_cvt_pk_bf16_f32 v161, v162, v163
v_mfma_f32_16x16x32_bf16 v[84:87], a[208:211], a[160:163], v[200:203]
v_exp_f32 v173, v173
v_mfma_f32_16x16x32_bf16 v[84:87], a[212:215], a[168:171], v[84:87]
v_exp_f32 v174, v174
v_mfma_f32_16x16x32_bf16 v[84:87], a[216:219], a[176:179], v[84:87]
v_exp_f32 v175, v175
v_mfma_f32_16x16x32_bf16 v[84:87], a[220:223], a[184:187], v[84:87]
buffer_load_dwordx4 v4, s[40:43], s71 offen offset:2048 lds
v_cvt_pk_bf16_f32 v162, v164, v165
v_mfma_f32_16x16x32_bf16 v[92:95], a[208:211], a[164:167], v[204:207]
v_exp_f32 v176, v176
v_mfma_f32_16x16x32_bf16 v[92:95], a[212:215], a[172:175], v[92:95]
v_exp_f32 v177, v177
v_mfma_f32_16x16x32_bf16 v[92:95], a[216:219], a[180:183], v[92:95]
buffer_load_dwordx4 v4, s[40:43], s71 offen offset:3072 lds
v_cvt_pk_bf16_f32 v163, v166, v167
v_mfma_f32_16x16x32_bf16 v[92:95], a[220:223], a[188:191], v[92:95]
v_cvt_pk_bf16_f32 v168, v168, v169
s_waitcnt lgkmcnt(0)
v_mfma_f32_16x16x32_bf16 v[96:99], a[192:195], a[128:131], v[192:195]
ds_read_b128 a[208:211], v1 offset:12288
v_cvt_pk_bf16_f32 v169, v170, v171
v_mfma_f32_16x16x32_bf16 v[96:99], a[196:199], a[136:139], v[96:99]
v_exp_f32 v178, v178
v_mfma_f32_16x16x32_bf16 v[96:99], a[200:203], a[144:147], v[96:99]
ds_read_b128 a[212:215], v1 offset:13312
s_add_u32 m0, s75, 65536
s_lshl_b32 s70, s46, 17
v_mfma_f32_16x16x32_bf16 v[96:99], a[204:207], a[152:155], v[96:99]
buffer_load_dwordx4 v220, s[20:23], s70 offen offset:0 lds
v_cvt_pk_bf16_f32 v170, v172, v173
v_mfma_f32_16x16x32_bf16 v[104:107], a[192:195], a[132:135], v[196:199]
ds_read_b128 a[216:219], v1 offset:14336
v_cvt_pk_bf16_f32 v171, v174, v175
v_mfma_f32_16x16x32_bf16 v[104:107], a[196:199], a[140:143], v[104:107]
v_exp_f32 v179, v179
v_mfma_f32_16x16x32_bf16 v[104:107], a[200:203], a[148:151], v[104:107]
ds_read_b128 a[220:223], v1 offset:15360
v_cvt_pk_bf16_f32 v176, v176, v177
v_mfma_f32_16x16x32_bf16 v[104:107], a[204:207], a[156:159], v[104:107]
buffer_load_dwordx4 v221, s[20:23], s70 offen offset:1024 lds
v_cvt_pk_bf16_f32 v177, v178, v179
v_mfma_f32_16x16x32_bf16 v[112:115], a[192:195], a[160:163], v[200:203]
v_exp_f32 v180, v180
v_mfma_f32_16x16x32_bf16 v[112:115], a[196:199], a[168:171], v[112:115]
v_exp_f32 v181, v181
v_mfma_f32_16x16x32_bf16 v[112:115], a[200:203], a[176:179], v[112:115]
v_exp_f32 v182, v182
v_mfma_f32_16x16x32_bf16 v[112:115], a[204:207], a[184:187], v[112:115]
buffer_load_dwordx4 v222, s[20:23], s70 offen offset:2048 lds
v_cvt_pk_bf16_f32 v178, v180, v181
v_mfma_f32_16x16x32_bf16 v[120:123], a[192:195], a[164:167], v[204:207]
v_exp_f32 v183, v183
v_mfma_f32_16x16x32_bf16 v[120:123], a[196:199], a[172:175], v[120:123]
v_exp_f32 v184, v184
v_mfma_f32_16x16x32_bf16 v[120:123], a[200:203], a[180:183], v[120:123]
buffer_load_dwordx4 v223, s[20:23], s70 offen offset:3072 lds
v_cvt_pk_bf16_f32 v179, v182, v183
v_mfma_f32_16x16x32_bf16 v[120:123], a[204:207], a[188:191], v[120:123]
s_waitcnt lgkmcnt(0)
v_mfma_f32_16x16x32_bf16 v[100:103], a[208:211], a[128:131], v[192:195]
ds_read_b128 a[192:195], v1 offset:32768
v_mfma_f32_16x16x32_bf16 v[100:103], a[212:215], a[136:139], v[100:103]
v_exp_f32 v185, v185
v_mfma_f32_16x16x32_bf16 v[100:103], a[216:219], a[144:147], v[100:103]
ds_read_b128 a[196:199], v1 offset:40960
s_add_u32 m0, s75, 69632
s_add_u32 s70, s70, 0x4000
v_mfma_f32_16x16x32_bf16 v[100:103], a[220:223], a[152:155], v[100:103]
buffer_load_dwordx4 v220, s[20:23], s70 offen offset:0 lds
v_cvt_pk_bf16_f32 v184, v184, v185
v_mfma_f32_16x16x32_bf16 v[108:111], a[208:211], a[132:135], v[196:199]
ds_read_b128 a[200:203], v1 offset:33792
v_bfe_u32 v4, v22, 2, 7
v_mfma_f32_16x16x32_bf16 v[108:111], a[212:215], a[140:143], v[108:111]
v_exp_f32 v186, v186
v_mfma_f32_16x16x32_bf16 v[108:111], a[216:219], a[148:151], v[108:111]
ds_read_b128 a[204:207], v1 offset:41984
v_add_u32 v4, s59, v4
v_mfma_f32_16x16x32_bf16 v[108:111], a[220:223], a[156:159], v[108:111]
buffer_load_dwordx4 v221, s[20:23], s70 offen offset:1024 lds
v_mfma_f32_16x16x32_bf16 v[116:119], a[208:211], a[160:163], v[200:203]
v_exp_f32 v187, v187
v_mfma_f32_16x16x32_bf16 v[116:119], a[212:215], a[168:171], v[116:119]
v_exp_f32 v188, v188
v_mfma_f32_16x16x32_bf16 v[116:119], a[216:219], a[176:179], v[116:119]
v_exp_f32 v189, v189
v_mfma_f32_16x16x32_bf16 v[116:119], a[220:223], a[184:187], v[116:119]
buffer_load_dwordx4 v222, s[20:23], s70 offen offset:2048 lds
v_cvt_pk_bf16_f32 v185, v186, v187
v_mfma_f32_16x16x32_bf16 v[124:127], a[208:211], a[164:167], v[204:207]
v_exp_f32 v190, v190
v_mfma_f32_16x16x32_bf16 v[124:127], a[212:215], a[172:175], v[124:127]
v_exp_f32 v191, v191
v_mfma_f32_16x16x32_bf16 v[124:127], a[216:219], a[180:183], v[124:127]
v_cvt_pk_bf16_f32 v186, v188, v189
v_mfma_f32_16x16x32_bf16 v[124:127], a[220:223], a[188:191], v[124:127]
v_cvt_pk_bf16_f32 v187, v190, v191
s_waitcnt lgkmcnt(0)
v_mfma_f32_16x16x32_bf16 a[0:3], a[192:195], v[128:131], a[0:3]
ds_read_b128 a[208:211], v1 offset:34816
v_mfma_f32_16x16x32_bf16 a[0:3], a[196:199], v[160:163], a[0:3]
s_cmp_lg_u32 %2, 7
s_cbranch_scc1 MASK4Z_%=
MASKR4Z_%=:
v_mfma_f32_16x16x32_bf16 a[4:7], a[200:203], v[128:131], a[4:7]
ds_read_b128 a[212:215], v1 offset:43008
v_max3_f32 v5, v64, v65, v66
v_mfma_f32_16x16x32_bf16 a[4:7], a[204:207], v[160:163], a[4:7]
v_max3_f32 v6, v72, v73, v74
v_max3_f32 v7, v80, v81, v82
v_mfma_f32_16x16x32_bf16 a[32:35], a[192:195], v[136:139], a[32:35]
ds_read_b128 a[216:219], v1 offset:35840
v_max3_f32 v8, v88, v89, v90
v_mfma_f32_16x16x32_bf16 a[32:35], a[196:199], v[168:171], a[32:35]
v_max3_f32 v5, v5, v67, v68
v_max3_f32 v6, v6, v75, v76
v_mfma_f32_16x16x32_bf16 a[36:39], a[200:203], v[136:139], a[36:39]
ds_read_b128 a[220:223], v1 offset:44032
v_max3_f32 v7, v7, v83, v84
v_mfma_f32_16x16x32_bf16 a[36:39], a[204:207], v[168:171], a[36:39]
v_max3_f32 v8, v8, v91, v92
v_max3_f32 v5, v5, v69, v70
v_mfma_f32_16x16x32_bf16 a[64:67], a[192:195], v[144:147], a[64:67]
v_max3_f32 v6, v6, v77, v78
v_max3_f32 v7, v7, v85, v86
v_mfma_f32_16x16x32_bf16 a[64:67], a[196:199], v[176:179], a[64:67]
v_max3_f32 v8, v8, v93, v94
v_max3_f32 v5, v5, v71, v96
v_mfma_f32_16x16x32_bf16 a[68:71], a[200:203], v[144:147], a[68:71]
v_max3_f32 v6, v6, v79, v104
v_max3_f32 v7, v7, v87, v112
v_mfma_f32_16x16x32_bf16 a[68:71], a[204:207], v[176:179], a[68:71]
v_max3_f32 v8, v8, v95, v120
v_max3_f32 v5, v5, v97, v98
v_mfma_f32_16x16x32_bf16 a[96:99], a[192:195], v[152:155], a[96:99]
v_max3_f32 v6, v6, v105, v106
v_max3_f32 v7, v7, v113, v114
v_mfma_f32_16x16x32_bf16 a[96:99], a[196:199], v[184:187], a[96:99]
v_max3_f32 v8, v8, v121, v122
v_max3_f32 v5, v5, v99, v100
v_mfma_f32_16x16x32_bf16 a[100:103], a[200:203], v[152:155], a[100:103]
v_max3_f32 v6, v6, v107, v108
v_max3_f32 v7, v7, v115, v116
v_mfma_f32_16x16x32_bf16 a[100:103], a[204:207], v[184:187], a[100:103]
v_max3_f32 v8, v8, v123, v124
s_waitcnt lgkmcnt(0)
v_mfma_f32_16x16x32_bf16 a[8:11], a[208:211], v[128:131], a[8:11]
ds_read_b128 a[192:195], v1 offset:36864
v_max3_f32 v5, v5, v101, v102
v_mfma_f32_16x16x32_bf16 a[8:11], a[212:215], v[160:163], a[8:11]
v_max3_f32 v6, v6, v109, v110
v_max3_f32 v7, v7, v117, v118
v_mfma_f32_16x16x32_bf16 a[12:15], a[216:219], v[128:131], a[12:15]
ds_read_b128 a[196:199], v1 offset:45056
v_max3_f32 v8, v8, v125, v126
v_mfma_f32_16x16x32_bf16 a[12:15], a[220:223], v[160:163], a[12:15]
v_max_f32 v5, v5, v103
v_max_f32 v6, v6, v111
v_mfma_f32_16x16x32_bf16 a[40:43], a[208:211], v[136:139], a[40:43]
ds_read_b128 a[200:203], v1 offset:37888
v_max_f32 v7, v7, v119
v_mfma_f32_16x16x32_bf16 a[40:43], a[212:215], v[168:171], a[40:43]
v_max_f32 v8, v8, v127
v_max3_f32 v9, v5, v6, v7
v_mfma_f32_16x16x32_bf16 a[44:47], a[216:219], v[136:139], a[44:47]
)"
R"(ds_read_b128 a[204:207], v1 offset:46080
v_max_f32 v9, v9, v8
v_mfma_f32_16x16x32_bf16 a[44:47], a[220:223], v[168:171], a[44:47]
v_cmp_le_f32 vcc, s58, v9
s_cbranch_vccnz RARE4Z_%=
RARER4Z_%=:
v_mfma_f32_16x16x32_bf16 a[72:75], a[208:211], v[144:147], a[72:75]
v_exp_f32 v64, v64
v_mfma_f32_16x16x32_bf16 a[72:75], a[212:215], v[176:179], a[72:75]
v_exp_f32 v65, v65
v_mfma_f32_16x16x32_bf16 a[76:79], a[216:219], v[144:147], a[76:79]
v_exp_f32 v66, v66
v_mfma_f32_16x16x32_bf16 a[76:79], a[220:223], v[176:179], a[76:79]
v_exp_f32 v67, v67
v_mfma_f32_16x16x32_bf16 a[104:107], a[208:211], v[152:155], a[104:107]
v_exp_f32 v68, v68
v_mfma_f32_16x16x32_bf16 a[104:107], a[212:215], v[184:187], a[104:107]
v_exp_f32 v69, v69
v_mfma_f32_16x16x32_bf16 a[108:111], a[216:219], v[152:155], a[108:111]
v_exp_f32 v70, v70
v_mfma_f32_16x16x32_bf16 a[108:111], a[220:223], v[184:187], a[108:111]
v_exp_f32 v71, v71
v_mfma_f32_16x16x32_bf16 a[224:227], a[240:243], v[128:131], a[224:227]
v_exp_f32 v72, v72
v_mfma_f32_16x16x32_bf16 a[224:227], a[240:243], v[160:163], a[224:227]
v_exp_f32 v73, v73
v_mfma_f32_16x16x32_bf16 a[228:231], a[240:243], v[136:139], a[228:231]
v_exp_f32 v74, v74
v_mfma_f32_16x16x32_bf16 a[228:231], a[240:243], v[168:171], a[228:231]
v_cvt_pk_bf16_f32 v64, v64, v65
s_waitcnt lgkmcnt(0)
v_mfma_f32_16x16x32_bf16 a[16:19], a[192:195], v[128:131], a[16:19]
ds_read_b128 a[208:211], v1 offset:38912
v_lshrrev_b32 v196, 17, v19
v_mfma_f32_16x16x32_bf16 a[16:19], a[196:199], v[160:163], a[16:19]
v_exp_f32 v75, v75
v_mfma_f32_16x16x32_bf16 a[20:23], a[200:203], v[128:131], a[20:23]
ds_read_b128 a[212:215], v1 offset:47104
v_xor_b32 v197, 16, v196
v_mfma_f32_16x16x32_bf16 a[20:23], a[204:207], v[160:163], a[20:23]
v_exp_f32 v76, v76
v_mfma_f32_16x16x32_bf16 a[48:51], a[192:195], v[136:139], a[48:51]
ds_read_b128 a[216:219], v1 offset:39936
v_add_u32 v198, 0x10000, v196
v_mfma_f32_16x16x32_bf16 a[48:51], a[196:199], v[168:171], a[48:51]
v_exp_f32 v77, v77
v_mfma_f32_16x16x32_bf16 a[52:55], a[200:203], v[136:139], a[52:55]
ds_read_b128 a[220:223], v1 offset:48128
v_add_u32 v199, 0x10000, v197
v_mfma_f32_16x16x32_bf16 a[52:55], a[204:207], v[168:171], a[52:55]
v_exp_f32 v78, v78
v_mfma_f32_16x16x32_bf16 a[80:83], a[192:195], v[144:147], a[80:83]
v_exp_f32 v79, v79
v_mfma_f32_16x16x32_bf16 a[80:83], a[196:199], v[176:179], a[80:83]
v_exp_f32 v80, v80
v_mfma_f32_16x16x32_bf16 a[84:87], a[200:203], v[144:147], a[84:87]
v_exp_f32 v81, v81
v_mfma_f32_16x16x32_bf16 a[84:87], a[204:207], v[176:179], a[84:87]
v_exp_f32 v82, v82
v_mfma_f32_16x16x32_bf16 a[112:115], a[192:195], v[152:155], a[112:115]
v_exp_f32 v83, v83
v_mfma_f32_16x16x32_bf16 a[112:115], a[196:199], v[184:187], a[112:115]
v_exp_f32 v84, v84
s_lshl_b32 s70, s47, 17
v_mfma_f32_16x16x32_bf16 a[116:119], a[200:203], v[152:155], a[116:119]
v_exp_f32 v85, v85
s_add_u32 s70, s70, 0x4000
v_mfma_f32_16x16x32_bf16 a[116:119], a[204:207], v[184:187], a[116:119]
s_add_u32 m0, s75, 4096
s_waitcnt vmcnt(7) lgkmcnt(0)
s_barrier
s_cmp_eq_u32 s55, 14
s_cbranch_scc1 SIG4Z_%=
SIGR4Z_%=:
v_mfma_f32_16x16x32_bf16 a[24:27], a[208:211], v[128:131], a[24:27]
ds_read_b128 a[192:195], v2 offset:32768
buffer_load_dwordx4 v220, s[20:23], s70 offen offset:0 lds
v_cvt_pk_bf16_f32 v65, v66, v67
v_mfma_f32_16x16x32_bf16 a[24:27], a[212:215], v[160:163], a[24:27]
v_exp_f32 v86, v86
v_mfma_f32_16x16x32_bf16 a[28:31], a[216:219], v[128:131], a[28:31]
ds_read_b128 a[196:199], v2 offset:40960
v_cvt_pk_bf16_f32 v66, v68, v69
v_mfma_f32_16x16x32_bf16 a[28:31], a[220:223], v[160:163], a[28:31]
buffer_load_dwordx4 v221, s[20:23], s70 offen offset:1024 lds
v_exp_f32 v87, v87
v_mfma_f32_16x16x32_bf16 a[56:59], a[208:211], v[136:139], a[56:59]
ds_read_b128 a[200:203], v2 offset:33792
v_cvt_pk_bf16_f32 v67, v70, v71
v_mfma_f32_16x16x32_bf16 a[56:59], a[212:215], v[168:171], a[56:59]
v_exp_f32 v88, v88
v_mfma_f32_16x16x32_bf16 a[60:63], a[216:219], v[136:139], a[60:63]
ds_read_b128 a[204:207], v2 offset:41984
buffer_load_dwordx4 v222, s[20:23], s70 offen offset:2048 lds
v_cvt_pk_bf16_f32 v72, v72, v73
v_mfma_f32_16x16x32_bf16 a[60:63], a[220:223], v[168:171], a[60:63]
v_exp_f32 v89, v89
v_mfma_f32_16x16x32_bf16 a[88:91], a[208:211], v[144:147], a[88:91]
v_exp_f32 v90, v90
v_mfma_f32_16x16x32_bf16 a[88:91], a[212:215], v[176:179], a[88:91]
buffer_load_dwordx4 v223, s[20:23], s70 offen offset:3072 lds
v_exp_f32 v91, v91
v_mfma_f32_16x16x32_bf16 a[92:95], a[216:219], v[144:147], a[92:95]
v_exp_f32 v92, v92
s_lshl_b32 s70, s47, 17
v_mfma_f32_16x16x32_bf16 a[92:95], a[220:223], v[176:179], a[92:95]
v_exp_f32 v93, v93
s_add_u32 m0, s75, 0
v_mfma_f32_16x16x32_bf16 a[120:123], a[208:211], v[152:155], a[120:123]
buffer_load_dwordx4 v221, s[20:23], s70 offen offset:1024 lds
v_exp_f32 v94, v94
v_mfma_f32_16x16x32_bf16 a[120:123], a[212:215], v[184:187], a[120:123]
v_exp_f32 v95, v95
v_mfma_f32_16x16x32_bf16 a[124:127], a[216:219], v[152:155], a[124:127]
v_exp_f32 v96, v96
v_mfma_f32_16x16x32_bf16 a[124:127], a[220:223], v[184:187], a[124:127]
buffer_load_dwordx4 v222, s[20:23], s70 offen offset:2048 lds
v_exp_f32 v97, v97
v_mfma_f32_16x16x32_bf16 a[232:235], a[240:243], v[144:147], a[232:235]
v_exp_f32 v98, v98
v_mfma_f32_16x16x32_bf16 a[232:235], a[240:243], v[176:179], a[232:235]
buffer_load_dwordx4 v223, s[20:23], s70 offen offset:3072 lds
v_exp_f32 v99, v99
v_mfma_f32_16x16x32_bf16 a[236:239], a[240:243], v[152:155], a[236:239]
v_cvt_pk_bf16_f32 v73, v74, v75
s_bitcmp1_b32 s57, s54
v_mfma_f32_16x16x32_bf16 a[236:239], a[240:243], v[184:187], a[236:239]
s_cbranch_scc1 END4Z_%=
ENDR4Z_%=:
DRNSZ_%=:
s_add_u32 m0, s75, 0
s_lshl_b32 s82, s47, 13
buffer_load_dwordx4 v220, s[20:23], s70 offen offset:0 lds
v_exp_f32 v100, v100
v_cvt_pk_bf16_f32 v74, v76, v77
v_cvt_pk_bf16_f32 v75, v78, v79
v_exp_f32 v101, v101
v_cvt_pk_bf16_f32 v80, v80, v81
v_cvt_pk_bf16_f32 v81, v82, v83
v_exp_f32 v102, v102
v_exp_f32 v103, v103
v_exp_f32 v104, v104
v_cvt_pk_bf16_f32 v82, v84, v85
v_exp_f32 v105, v105
v_exp_f32 v106, v106
v_cvt_pk_bf16_f32 v83, v86, v87
v_cvt_pk_bf16_f32 v88, v88, v89
s_add_u32 s47, s47, s29
s_cmp_ge_u32 s47, s100
s_cselect_b32 s47, s101, s47
s_add_u32 m0, s75, 32768
s_lshl_b32 s70, s47, 17
buffer_load_dwordx4 v220, s[20:23], s70 offen offset:0 lds
v_cvt_pk_bf16_f32 v89, v90, v91
v_exp_f32 v107, v107
v_cvt_pk_bf16_f32 v90, v92, v93
v_cvt_pk_bf16_f32 v91, v94, v95
v_exp_f32 v108, v108
v_cvt_pk_bf16_f32 v96, v96, v97
v_cvt_pk_bf16_f32 v97, v98, v99
v_exp_f32 v109, v109
v_exp_f32 v110, v110
v_exp_f32 v111, v111
v_cvt_pk_bf16_f32 v98, v100, v101
v_exp_f32 v112, v112
v_exp_f32 v113, v113
buffer_load_dwordx4 v221, s[20:23], s70 offen offset:1024 lds
v_cvt_pk_bf16_f32 v99, v102, v103
v_cvt_pk_bf16_f32 v104, v104, v105
v_cvt_pk_bf16_f32 v105, v106, v107
v_exp_f32 v114, v114
v_cvt_pk_bf16_f32 v106, v108, v109
v_cvt_pk_bf16_f32 v107, v110, v111
v_exp_f32 v115, v115
v_cvt_pk_bf16_f32 v112, v112, v113
v_cvt_pk_bf16_f32 v113, v114, v115
v_exp_f32 v116, v116
v_exp_f32 v117, v117
v_exp_f32 v118, v118
v_cvt_pk_bf16_f32 v114, v116, v117
v_exp_f32 v119, v119
buffer_load_dwordx4 v222, s[20:23], s70 offen offset:2048 lds
v_exp_f32 v120, v120
v_cvt_pk_bf16_f32 v115, v118, v119
v_exp_f32 v121, v121
s_nop 0
v_cvt_pk_bf16_f32 v120, v120, v121
v_exp_f32 v122, v122
v_exp_f32 v123, v123
v_exp_f32 v124, v124
v_exp_f32 v125, v125
v_cvt_pk_bf16_f32 v121, v122, v123
v_exp_f32 v126, v126
v_exp_f32 v127, v127
v_cvt_pk_bf16_f32 v122, v124, v125
v_cvt_pk_bf16_f32 v123, v126, v127
s_waitcnt lgkmcnt(3)
v_mfma_f32_16x16x32_bf16 a[0:3], a[192:195], v[64:67], a[0:3]
ds_read_b128 a[208:211], v2 offset:34816
s_waitcnt lgkmcnt(3)
v_mfma_f32_16x16x32_bf16 a[0:3], a[196:199], v[96:99], a[0:3]
buffer_load_dwordx4 v223, s[20:23], s70 offen offset:3072 lds
v_add_u32 v30, 0x20000, v3
s_waitcnt lgkmcnt(2)
v_mfma_f32_16x16x32_bf16 a[4:7], a[200:203], v[64:67], a[4:7]
ds_read_b128 a[212:215], v2 offset:43008
v_add_u32 v31, 0x18000, v3
s_waitcnt lgkmcnt(2)
v_mfma_f32_16x16x32_bf16 a[4:7], a[204:207], v[96:99], a[4:7]
v_xor_b32 v28, 0x2000, v30
v_xor_b32 v29, 0x2000, v31
v_mfma_f32_16x16x32_bf16 a[32:35], a[192:195], v[72:75], a[32:35]
ds_read_b128 a[216:219], v2 offset:35840
v_add_u32 v26, 0x10000, v3
v_mfma_f32_16x16x32_bf16 a[32:35], a[196:199], v[104:107], a[32:35]
s_add_u32 m0, s75, 36864
s_add_u32 s70, s70, 0x4000
buffer_load_dwordx4 v220, s[20:23], s70 offen offset:0 lds
v_mfma_f32_16x16x32_bf16 a[36:39], a[200:203], v[72:75], a[36:39]
ds_read_b128 a[220:223], v2 offset:44032
v_and_b32 v26, 0x1fcff, v26
v_mfma_f32_16x16x32_bf16 a[36:39], a[204:207], v[104:107], a[36:39]
buffer_load_dwordx4 v221, s[20:23], s70 offen offset:1024 lds
ds_write_b32 v26, v10 offset:7424
v_mfma_f32_16x16x32_bf16 a[64:67], a[192:195], v[80:83], a[64:67]
v_xor_b32 v27, 0x2000, v26
v_mov_b32 v212, v14
v_mfma_f32_16x16x32_bf16 a[64:67], a[196:199], v[112:115], a[64:67]
v_mov_b32 v214, v15
v_mov_b32 v216, v16
v_mfma_f32_16x16x32_bf16 a[68:71], a[200:203], v[80:83], a[68:71]
buffer_load_dwordx4 v222, s[20:23], s70 offen offset:2048 lds
v_mov_b32 v218, v17
v_mfma_f32_16x16x32_bf16 a[68:71], a[204:207], v[112:115], a[68:71]
v_accvgpr_read_b32 v32, a0
v_accvgpr_read_b32 v33, a1
v_mfma_f32_16x16x32_bf16 a[96:99], a[192:195], v[88:91], a[96:99]
v_accvgpr_read_b32 v34, a2
v_accvgpr_read_b32 v35, a3
v_mfma_f32_16x16x32_bf16 a[96:99], a[196:199], v[120:123], a[96:99]
buffer_load_dwordx4 v223, s[20:23], s70 offen offset:3072 lds
v_accvgpr_read_b32 v36, a4
v_mfma_f32_16x16x32_bf16 a[100:103], a[200:203], v[88:91], a[100:103]
global_load_dword v193, v4, s[92:93] sc1
v_accvgpr_read_b32 v37, a5
v_mfma_f32_16x16x32_bf16 a[100:103], a[204:207], v[120:123], a[100:103]
v_accvgpr_read_b32 v38, a6
s_waitcnt lgkmcnt(1)
v_mfma_f32_16x16x32_bf16 a[8:11], a[208:211], v[64:67], a[8:11]
ds_read_b128 a[192:195], v2 offset:36864
v_accvgpr_read_b32 v39, a7
v_mfma_f32_16x16x32_bf16 a[8:11], a[212:215], v[96:99], a[8:11]
v_accvgpr_read_b32 v224, a32
v_accvgpr_read_b32 v225, a33
v_mfma_f32_16x16x32_bf16 a[12:15], a[216:219], v[64:67], a[12:15]
ds_read_b128 a[196:199], v2 offset:45056
v_accvgpr_read_b32 v226, a34
v_mfma_f32_16x16x32_bf16 a[12:15], a[220:223], v[96:99], a[12:15]
v_accvgpr_read_b32 v227, a35
v_accvgpr_read_b32 v228, a36
v_mfma_f32_16x16x32_bf16 a[40:43], a[208:211], v[72:75], a[40:43]
ds_read_b128 a[200:203], v2 offset:37888
v_accvgpr_read_b32 v229, a37
v_mfma_f32_16x16x32_bf16 a[40:43], a[212:215], v[104:107], a[40:43]
v_accvgpr_read_b32 v230, a38
v_accvgpr_read_b32 v231, a39
v_mfma_f32_16x16x32_bf16 a[44:47], a[216:219], v[72:75], a[44:47]
ds_read_b128 a[204:207], v2 offset:46080
v_accvgpr_read_b32 v40, a8
v_mfma_f32_16x16x32_bf16 a[44:47], a[220:223], v[104:107], a[44:47]
ds_write_b128 v30, a[64:67] offset:0
v_accvgpr_read_b32 v41, a9
v_mfma_f32_16x16x32_bf16 a[72:75], a[208:211], v[80:83], a[72:75]
v_accvgpr_read_b32 v42, a10
v_accvgpr_read_b32 v43, a11
v_mfma_f32_16x16x32_bf16 a[72:75], a[212:215], v[112:115], a[72:75]
ds_write_b128 v30, a[68:71] offset:1024
v_accvgpr_read_b32 v44, a12
v_mfma_f32_16x16x32_bf16 a[76:79], a[216:219], v[80:83], a[76:79]
v_accvgpr_read_b32 v45, a13
v_accvgpr_read_b32 v46, a14
v_mfma_f32_16x16x32_bf16 a[76:79], a[220:223], v[112:115], a[76:79]
v_accvgpr_read_b32 v47, a15
v_accvgpr_read_b32 v232, a40
v_mfma_f32_16x16x32_bf16 a[104:107], a[208:211], v[88:91], a[104:107]
ds_write_b128 v30, a[96:99] offset:4096
v_accvgpr_read_b32 v233, a41
v_mfma_f32_16x16x32_bf16 a[104:107], a[212:215], v[120:123], a[104:107]
v_accvgpr_read_b32 v234, a42
v_accvgpr_read_b32 v235, a43
v_mfma_f32_16x16x32_bf16 a[108:111], a[216:219], v[88:91], a[108:111]
ds_write_b128 v30, a[100:103] offset:5120
v_accvgpr_read_b32 v236, a44
v_mfma_f32_16x16x32_bf16 a[108:111], a[220:223], v[120:123], a[108:111]
v_accvgpr_read_b32 v237, a45
v_accvgpr_read_b32 v238, a46
v_mfma_f32_16x16x32_bf16 a[224:227], a[240:243], v[64:67], a[224:227]
v_accvgpr_read_b32 v239, a47
v_mfma_f32_16x16x32_bf16 a[224:227], a[240:243], v[96:99], a[224:227]
v_mfma_f32_16x16x32_bf16 a[228:231], a[240:243], v[72:75], a[228:231]
ds_write_b128 v30, a[72:75] offset:2048
v_mfma_f32_16x16x32_bf16 a[228:231], a[240:243], v[104:107], a[228:231]
v_mfma_f32_16x16x32_bf16 a[232:235], a[240:243], v[80:83], a[232:235]
ds_write_b128 v30, a[76:79] offset:3072
v_mfma_f32_16x16x32_bf16 a[232:235], a[240:243], v[112:115], a[232:235]
v_mfma_f32_16x16x32_bf16 a[236:239], a[240:243], v[88:91], a[236:239]
ds_write_b128 v30, a[104:107] offset:6144
v_mfma_f32_16x16x32_bf16 a[236:239], a[240:243], v[120:123], a[236:239]
s_waitcnt lgkmcnt(7)
v_mfma_f32_16x16x32_bf16 a[16:19], a[192:195], v[64:67], a[16:19]
ds_read_b128 a[208:211], v2 offset:38912
v_mfma_f32_16x16x32_bf16 a[16:19], a[196:199], v[96:99], a[16:19]
v_mfma_f32_16x16x32_bf16 a[20:23], a[200:203], v[64:67], a[20:23]
ds_read_b128 a[212:215], v2 offset:47104
v_mfma_f32_16x16x32_bf16 a[20:23], a[204:207], v[96:99], a[20:23]
v_accvgpr_read_b32 v217, a232
v_accvgpr_read_b32 v219, a236
v_mfma_f32_16x16x32_bf16 a[48:51], a[192:195], v[72:75], a[48:51]
ds_read_b128 a[216:219], v2 offset:39936
v_accvgpr_read_b32 v213, a224
v_mfma_f32_16x16x32_bf16 a[48:51], a[196:199], v[104:107], a[48:51]
v_accvgpr_read_b32 v215, a228
v_accvgpr_read_b32 v48, a16
v_mfma_f32_16x16x32_bf16 a[52:55], a[200:203], v[72:75], a[52:55]
ds_read_b128 a[220:223], v2 offset:48128
ds_write_b128 v27, v[216:219] offset:7168
v_mfma_f32_16x16x32_bf16 a[52:55], a[204:207], v[104:107], a[52:55]
ds_write_b128 v30, a[108:111] offset:7168
v_accvgpr_read_b32 v49, a17
v_mfma_f32_16x16x32_bf16 a[80:83], a[192:195], v[80:83], a[80:83]
v_accvgpr_read_b32 v50, a18
v_accvgpr_read_b32 v51, a19
v_mfma_f32_16x16x32_bf16 a[80:83], a[196:199], v[112:115], a[80:83]
v_accvgpr_read_b32 v52, a20
v_accvgpr_read_b32 v53, a21
v_mfma_f32_16x16x32_bf16 a[84:87], a[200:203], v[80:83], a[84:87]
v_accvgpr_read_b32 v54, a22
v_accvgpr_read_b32 v55, a23
v_mfma_f32_16x16x32_bf16 a[84:87], a[204:207], v[112:115], a[84:87]
v_accvgpr_read_b32 v240, a48
v_accvgpr_read_b32 v241, a49
v_mfma_f32_16x16x32_bf16 a[112:115], a[192:195], v[88:91], a[112:115]
v_accvgpr_read_b32 v242, a50
v_accvgpr_read_b32 v243, a51
v_mfma_f32_16x16x32_bf16 a[112:115], a[196:199], v[120:123], a[112:115]
s_cmp_eq_u32 s55, 15
s_cbranch_scc1 EPX4Z_%=
EPXR4Z_%=:
v_mfma_f32_16x16x32_bf16 a[116:119], a[200:203], v[88:91], a[116:119]
s_waitcnt vmcnt(0)
v_cmp_eq_u32 vcc, s98, v193
s_andn2_b32 s84, s89, vcc_lo
s_cselect_b32 s14, 0, s14
v_mfma_f32_16x16x32_bf16 a[116:119], a[204:207], v[120:123], a[116:119]
s_waitcnt lgkmcnt(0)
s_barrier
v_mfma_f32_16x16x32_bf16 a[88:91], a[208:211], v[80:83], a[88:91]
buffer_load_dwordx4 a[132:135], v22, s[12:15], s82 offen offset:1024 sc1
ds_read_b128 v[6:9], v26 offset:7168
v_mfma_f32_16x16x32_bf16 a[88:91], a[212:215], v[112:115], a[88:91]
ds_write_b128 v31, a[80:83] offset:0
ds_write_b128 v31, a[84:87] offset:1024
v_mfma_f32_16x16x32_bf16 a[92:95], a[216:219], v[80:83], a[92:95]
buffer_load_dwordx4 a[136:139], v22, s[12:15], s82 offen offset:2048 sc1
ds_read_b128 v[128:131], v28 offset:0
v_mfma_f32_16x16x32_bf16 a[92:95], a[220:223], v[112:115], a[92:95]
ds_write_b128 v31, a[112:115] offset:4096
ds_read_b128 v[132:135], v28 offset:1024
v_mfma_f32_16x16x32_bf16 a[120:123], a[208:211], v[88:91], a[120:123]
buffer_load_dwordx4 a[140:143], v22, s[12:15], s82 offen offset:3072 sc1
ds_write_b128 v31, a[116:119] offset:5120
v_mfma_f32_16x16x32_bf16 a[120:123], a[212:215], v[120:123], a[120:123]
s_waitcnt lgkmcnt(6)
v_max_f32 v24, v6, v212
v_max_f32 v25, v8, v214
v_mfma_f32_16x16x32_bf16 a[124:127], a[216:219], v[88:91], a[124:127]
buffer_load_dwordx4 a[128:131], v22, s[12:15], s82 offen offset:0 sc1
ds_write_b128 v31, a[88:91] offset:2048
v_mfma_f32_16x16x32_bf16 a[124:127], a[220:223], v[120:123], a[124:127]
v_sub_f32 v208, v212, v24
v_sub_f32 v209, v6, v24
v_mfma_f32_16x16x32_bf16 a[24:27], a[208:211], v[64:67], a[24:27]
s_add_u32 s71, s82, 0x1000
buffer_load_dwordx4 a[148:151], v22, s[12:15], s71 offen offset:1024 sc1
v_mfma_f32_16x16x32_bf16 a[24:27], a[212:215], v[96:99], a[24:27]
ds_write_b128 v31, a[92:95] offset:3072
v_sub_f32 v210, v214, v25
v_mfma_f32_16x16x32_bf16 a[28:31], a[216:219], v[64:67], a[28:31]
buffer_load_dwordx4 a[152:155], v22, s[12:15], s71 offen offset:2048 sc1
ds_write_b128 v31, a[120:123] offset:6144
v_mfma_f32_16x16x32_bf16 a[28:31], a[220:223], v[96:99], a[28:31]
v_sub_f32 v211, v8, v25
ds_read_b128 v[136:139], v28 offset:2048
v_mfma_f32_16x16x32_bf16 a[56:59], a[208:211], v[72:75], a[56:59]
buffer_load_dwordx4 a[156:159], v22, s[12:15], s71 offen offset:3072 sc1
ds_write_b128 v31, a[124:127] offset:7168
v_mfma_f32_16x16x32_bf16 a[56:59], a[212:215], v[104:107], a[56:59]
v_exp_f32 v208, v208
v_mfma_f32_16x16x32_bf16 a[60:63], a[216:219], v[72:75], a[60:63]
buffer_load_dwordx4 a[144:147], v22, s[12:15], s71 offen offset:0 sc1
)"
R"(ds_read_b128 v[140:143], v28 offset:3072
v_mfma_f32_16x16x32_bf16 a[60:63], a[220:223], v[104:107], a[60:63]
v_exp_f32 v209, v209
v_exp_f32 v210, v210
v_exp_f32 v211, v211
v_mul_f32 v24, v208, v213
v_mul_f32 v25, v210, v215
v_fmac_f32 v24, v209, v7
v_fmac_f32 v25, v211, v9
v_rcp_f32 v24, v24
v_rcp_f32 v25, v25
ds_read_b128 v[160:163], v28 offset:4096
ds_read_b128 v[164:167], v28 offset:5120
ds_read_b128 v[168:171], v28 offset:6144
ds_read_b128 v[172:175], v28 offset:7168
v_accvgpr_read_b32 v244, a52
v_accvgpr_read_b32 v245, a53
v_accvgpr_read_b32 v246, a54
v_accvgpr_read_b32 v247, a55
v_mul_f32 v208, v208, v24
v_mul_f32 v209, v209, v24
v_mul_f32 v210, v210, v25
v_mul_f32 v211, v211, v25
v_max3_f32 v5, v213, v215, v7
v_max3_f32 v5, v5, v9, v24
v_max_f32 v5, v5, v25
v_cmp_lt_f32 vcc, s76, v5
s_cbranch_vccnz LFDZ_%=
LFDRZ_%=:
v_pk_mul_f32 v[32:33], v[32:33], v[208:209] op_sel_hi:[1,0]
v_pk_mul_f32 v[34:35], v[34:35], v[208:209] op_sel_hi:[1,0]
v_pk_mul_f32 v[36:37], v[36:37], v[208:209] op_sel_hi:[1,0]
v_pk_mul_f32 v[38:39], v[38:39], v[208:209] op_sel_hi:[1,0]
s_lshl_b32 s83, s47, 13
buffer_load_dwordx4 a[164:167], v22, s[12:15], s83 offen offset:1024 sc1
s_waitcnt lgkmcnt(13)
v_pk_fma_f32 v[32:33], v[128:129], v[208:209], v[32:33] op_sel:[0,1,0] op_sel_hi:[1,1,1]
v_pk_fma_f32 v[34:35], v[130:131], v[208:209], v[34:35] op_sel:[0,1,0] op_sel_hi:[1,1,1]
s_waitcnt lgkmcnt(11)
v_pk_fma_f32 v[36:37], v[132:133], v[208:209], v[36:37] op_sel:[0,1,0] op_sel_hi:[1,1,1]
v_pk_fma_f32 v[38:39], v[134:135], v[208:209], v[38:39] op_sel:[0,1,0] op_sel_hi:[1,1,1]
v_cvt_pk_bf16_f32 v32, v32, v33
v_cvt_pk_bf16_f32 v33, v34, v35
v_cvt_pk_bf16_f32 v34, v36, v37
v_cvt_pk_bf16_f32 v35, v38, v39
global_store_dwordx4 v22, v[32:35], s[52:53] offset:-4096 sc1
v_pk_mul_f32 v[40:41], v[40:41], v[208:209] op_sel_hi:[1,0]
v_pk_mul_f32 v[42:43], v[42:43], v[208:209] op_sel_hi:[1,0]
v_pk_mul_f32 v[44:45], v[44:45], v[208:209] op_sel_hi:[1,0]
v_pk_mul_f32 v[46:47], v[46:47], v[208:209] op_sel_hi:[1,0]
buffer_load_dwordx4 a[168:171], v22, s[12:15], s83 offen offset:2048 sc1
s_waitcnt lgkmcnt(6)
v_pk_fma_f32 v[40:41], v[136:137], v[208:209], v[40:41] op_sel:[0,1,0] op_sel_hi:[1,1,1]
v_pk_fma_f32 v[42:43], v[138:139], v[208:209], v[42:43] op_sel:[0,1,0] op_sel_hi:[1,1,1]
s_waitcnt lgkmcnt(4)
v_pk_fma_f32 v[44:45], v[140:141], v[208:209], v[44:45] op_sel:[0,1,0] op_sel_hi:[1,1,1]
v_pk_fma_f32 v[46:47], v[142:143], v[208:209], v[46:47] op_sel:[0,1,0] op_sel_hi:[1,1,1]
v_cvt_pk_bf16_f32 v40, v40, v41
v_cvt_pk_bf16_f32 v41, v42, v43
v_cvt_pk_bf16_f32 v42, v44, v45
v_cvt_pk_bf16_f32 v43, v46, v47
global_store_dwordx4 v22, v[40:43], s[52:53] offset:-2048 sc1
s_waitcnt lgkmcnt(0)
s_barrier
ds_read_b32 v5, v26 offset:7424
ds_read_b128 v[144:147], v29 offset:0
ds_read_b128 v[148:151], v29 offset:1024
ds_read_b128 v[176:179], v29 offset:4096
ds_read_b128 v[180:183], v29 offset:5120
ds_read_b128 v[152:155], v29 offset:2048
ds_read_b128 v[156:159], v29 offset:3072
ds_read_b128 v[184:187], v29 offset:6144
ds_read_b128 v[188:191], v29 offset:7168
v_pk_mul_f32 v[224:225], v[224:225], v[210:211] op_sel_hi:[1,0]
v_pk_mul_f32 v[226:227], v[226:227], v[210:211] op_sel_hi:[1,0]
v_pk_mul_f32 v[228:229], v[228:229], v[210:211] op_sel_hi:[1,0]
v_pk_mul_f32 v[230:231], v[230:231], v[210:211] op_sel_hi:[1,0]
buffer_load_dwordx4 a[172:175], v22, s[12:15], s83 offen offset:3072 sc1
v_pk_fma_f32 v[224:225], v[160:161], v[210:211], v[224:225] op_sel:[0,1,0] op_sel_hi:[1,1,1]
v_pk_fma_f32 v[226:227], v[162:163], v[210:211], v[226:227] op_sel:[0,1,0] op_sel_hi:[1,1,1]
v_pk_fma_f32 v[228:229], v[164:165], v[210:211], v[228:229] op_sel:[0,1,0] op_sel_hi:[1,1,1]
v_pk_fma_f32 v[230:231], v[166:167], v[210:211], v[230:231] op_sel:[0,1,0] op_sel_hi:[1,1,1]
v_cvt_pk_bf16_f32 v224, v224, v225
v_cvt_pk_bf16_f32 v225, v226, v227
v_cvt_pk_bf16_f32 v226, v228, v229
v_cvt_pk_bf16_f32 v227, v230, v231
global_load_dword v193, v4, s[92:93] sc1
global_store_dwordx4 v22, v[224:227], s[52:53] offset:-3072 sc1
v_pk_mul_f32 v[232:233], v[232:233], v[210:211] op_sel_hi:[1,0]
v_pk_mul_f32 v[234:235], v[234:235], v[210:211] op_sel_hi:[1,0]
v_pk_mul_f32 v[236:237], v[236:237], v[210:211] op_sel_hi:[1,0]
v_pk_mul_f32 v[238:239], v[238:239], v[210:211] op_sel_hi:[1,0]
buffer_load_dwordx4 a[160:163], v22, s[12:15], s83 offen offset:0 sc1
v_pk_fma_f32 v[232:233], v[168:169], v[210:211], v[232:233] op_sel:[0,1,0] op_sel_hi:[1,1,1]
v_pk_fma_f32 v[234:235], v[170:171], v[210:211], v[234:235] op_sel:[0,1,0] op_sel_hi:[1,1,1]
v_pk_fma_f32 v[236:237], v[172:173], v[210:211], v[236:237] op_sel:[0,1,0] op_sel_hi:[1,1,1]
v_pk_fma_f32 v[238:239], v[174:175], v[210:211], v[238:239] op_sel:[0,1,0] op_sel_hi:[1,1,1]
v_cvt_pk_bf16_f32 v232, v232, v233
v_cvt_pk_bf16_f32 v233, v234, v235
v_cvt_pk_bf16_f32 v234, v236, v237
v_cvt_pk_bf16_f32 v235, v238, v239
global_store_dwordx4 v22, v[232:235], s[52:53] offset:-1024 sc1
s_waitcnt lgkmcnt(8)
v_cmp_ne_u32 vcc, 0, v5
s_cbranch_vccnz REDO_%=
s_add_u32 m0, s75, 69632
s_lshl_b32 s70, s46, 17
s_add_u32 s70, s70, 0x4000
buffer_load_dwordx4 v223, s[20:23], s70 offen offset:3072 lds
v_pk_mul_f32 v[48:49], v[48:49], v[208:209] op_sel_hi:[1,0]
v_pk_mul_f32 v[50:51], v[50:51], v[208:209] op_sel_hi:[1,0]
v_pk_mul_f32 v[52:53], v[52:53], v[208:209] op_sel_hi:[1,0]
v_pk_mul_f32 v[54:55], v[54:55], v[208:209] op_sel_hi:[1,0]
s_add_u32 s71, s83, 0x1000
buffer_load_dwordx4 a[180:183], v22, s[12:15], s71 offen offset:1024 sc1
s_waitcnt lgkmcnt(7)
v_pk_fma_f32 v[48:49], v[144:145], v[208:209], v[48:49] op_sel:[0,1,0] op_sel_hi:[1,1,1]
v_pk_fma_f32 v[50:51], v[146:147], v[208:209], v[50:51] op_sel:[0,1,0] op_sel_hi:[1,1,1]
s_waitcnt lgkmcnt(6)
v_pk_fma_f32 v[52:53], v[148:149], v[208:209], v[52:53] op_sel:[0,1,0] op_sel_hi:[1,1,1]
v_pk_fma_f32 v[54:55], v[150:151], v[208:209], v[54:55] op_sel:[0,1,0] op_sel_hi:[1,1,1]
v_cvt_pk_bf16_f32 v48, v48, v49
v_cvt_pk_bf16_f32 v49, v50, v51
v_cvt_pk_bf16_f32 v50, v52, v53
v_cvt_pk_bf16_f32 v51, v54, v55
global_store_dwordx4 v22, v[48:51], s[52:53] offset:0 sc1
v_pk_mul_f32 v[240:241], v[240:241], v[210:211] op_sel_hi:[1,0]
v_pk_mul_f32 v[242:243], v[242:243], v[210:211] op_sel_hi:[1,0]
v_pk_mul_f32 v[244:245], v[244:245], v[210:211] op_sel_hi:[1,0]
v_pk_mul_f32 v[246:247], v[246:247], v[210:211] op_sel_hi:[1,0]
buffer_load_dwordx4 a[184:187], v22, s[12:15], s71 offen offset:2048 sc1
s_waitcnt lgkmcnt(5)
v_pk_fma_f32 v[240:241], v[176:177], v[210:211], v[240:241] op_sel:[0,1,0] op_sel_hi:[1,1,1]
v_pk_fma_f32 v[242:243], v[178:179], v[210:211], v[242:243] op_sel:[0,1,0] op_sel_hi:[1,1,1]
s_waitcnt lgkmcnt(4)
v_pk_fma_f32 v[244:245], v[180:181], v[210:211], v[244:245] op_sel:[0,1,0] op_sel_hi:[1,1,1]
v_pk_fma_f32 v[246:247], v[182:183], v[210:211], v[246:247] op_sel:[0,1,0] op_sel_hi:[1,1,1]
v_cvt_pk_bf16_f32 v240, v240, v241
v_cvt_pk_bf16_f32 v241, v242, v243
v_cvt_pk_bf16_f32 v242, v244, v245
v_cvt_pk_bf16_f32 v243, v246, v247
global_store_dwordx4 v22, v[240:243], s[52:53] offset:1024 sc1
v_accvgpr_read_b32 v56, a24
v_accvgpr_read_b32 v57, a25
v_accvgpr_read_b32 v58, a26
v_accvgpr_read_b32 v59, a27
v_accvgpr_read_b32 v60, a28
v_accvgpr_read_b32 v61, a29
v_accvgpr_read_b32 v62, a30
v_accvgpr_read_b32 v63, a31
v_pk_mul_f32 v[56:57], v[56:57], v[208:209] op_sel_hi:[1,0]
v_pk_mul_f32 v[58:59], v[58:59], v[208:209] op_sel_hi:[1,0]
v_pk_mul_f32 v[60:61], v[60:61], v[208:209] op_sel_hi:[1,0]
v_pk_mul_f32 v[62:63], v[62:63], v[208:209] op_sel_hi:[1,0]
buffer_load_dwordx4 a[188:191], v22, s[12:15], s71 offen offset:3072 sc1
s_waitcnt lgkmcnt(3)
v_pk_fma_f32 v[56:57], v[152:153], v[208:209], v[56:57] op_sel:[0,1,0] op_sel_hi:[1,1,1]
v_pk_fma_f32 v[58:59], v[154:155], v[208:209], v[58:59] op_sel:[0,1,0] op_sel_hi:[1,1,1]
s_waitcnt lgkmcnt(2)
v_pk_fma_f32 v[60:61], v[156:157], v[208:209], v[60:61] op_sel:[0,1,0] op_sel_hi:[1,1,1]
v_pk_fma_f32 v[62:63], v[158:159], v[208:209], v[62:63] op_sel:[0,1,0] op_sel_hi:[1,1,1]
v_cvt_pk_bf16_f32 v56, v56, v57
v_cvt_pk_bf16_f32 v57, v58, v59
v_cvt_pk_bf16_f32 v58, v60, v61
v_cvt_pk_bf16_f32 v59, v62, v63
global_store_dwordx4 v22, v[56:59], s[52:53] offset:2048 sc1
v_accvgpr_read_b32 v248, a56
v_accvgpr_read_b32 v249, a57
v_accvgpr_read_b32 v250, a58
v_accvgpr_read_b32 v251, a59
v_accvgpr_read_b32 v252, a60
v_accvgpr_read_b32 v253, a61
v_accvgpr_read_b32 v254, a62
v_accvgpr_read_b32 v255, a63
v_pk_mul_f32 v[248:249], v[248:249], v[210:211] op_sel_hi:[1,0]
v_pk_mul_f32 v[250:251], v[250:251], v[210:211] op_sel_hi:[1,0]
v_pk_mul_f32 v[252:253], v[252:253], v[210:211] op_sel_hi:[1,0]
v_pk_mul_f32 v[254:255], v[254:255], v[210:211] op_sel_hi:[1,0]
buffer_load_dwordx4 a[176:179], v22, s[12:15], s71 offen offset:0 sc1
s_mov_b32 s47, s46
s_add_u32 s46, s46, s29
s_cmp_ge_u32 s46, s100
s_cselect_b32 s46, s101, s46
s_waitcnt lgkmcnt(1)
v_pk_fma_f32 v[248:249], v[184:185], v[210:211], v[248:249] op_sel:[0,1,0] op_sel_hi:[1,1,1]
v_pk_fma_f32 v[250:251], v[186:187], v[210:211], v[250:251] op_sel:[0,1,0] op_sel_hi:[1,1,1]
s_waitcnt lgkmcnt(0)
v_pk_fma_f32 v[252:253], v[188:189], v[210:211], v[252:253] op_sel:[0,1,0] op_sel_hi:[1,1,1]
v_pk_fma_f32 v[254:255], v[190:191], v[210:211], v[254:255] op_sel:[0,1,0] op_sel_hi:[1,1,1]
v_cvt_pk_bf16_f32 v248, v248, v249
v_cvt_pk_bf16_f32 v249, v250, v251
v_cvt_pk_bf16_f32 v250, v252, v253
v_cvt_pk_bf16_f32 v251, v254, v255
global_store_dwordx4 v22, v[248:251], s[52:53] offset:3072 sc1
s_waitcnt vmcnt(0) lgkmcnt(0)
s_branch JOIN_%=
RARE0Z_%=:
s_cmp_eq_u32 s68, 0
s_cbranch_scc1 RSL0Z_%=
v_min3_f32 v24, v5, v6, v7
v_cmp_le_f32 vcc, s78, v9
v_min_f32 v24, v24, v8
v_cmp_nle_f32 s[80:81], -s78, v24
s_mov_b32 s58, s78
s_or_b64 vcc, vcc, s[80:81]
s_cbranch_scc1 RSL0Z_%=
s_mov_b32 s68, 0
s_branch RARER0Z_%=
END0Z_%=:
s_cmp_lg_u32 s54, s55
s_cbranch_scc1 ENDR0Z_%=
v_mov_b32 v212, v14
v_mov_b32 v214, v15
v_mov_b32 v216, v16
v_mov_b32 v218, v17
s_mov_b32 s58, 0xff800000
s_mov_b32 s68, 1
s_cmp_eq_u32 s88, 0
s_cbranch_scc1 ENDR0Z_%=
v_mov_b32 v14, 0
v_mov_b32 v15, 0
v_mov_b32 v16, 0
v_mov_b32 v17, 0
v_mov_b32 v192, 0
v_mov_b32 v193, 0
v_mov_b32 v194, 0
v_mov_b32 v195, 0
v_mov_b32 v196, 0
v_mov_b32 v197, 0
v_mov_b32 v198, 0
v_mov_b32 v199, 0
v_mov_b32 v200, 0
v_mov_b32 v201, 0
v_mov_b32 v202, 0
v_mov_b32 v203, 0
v_mov_b32 v204, 0
v_mov_b32 v205, 0
v_mov_b32 v206, 0
v_mov_b32 v207, 0
s_mov_b32 s88, 0
s_nop 0
s_branch ENDR0Z_%=
RARE1Z_%=:
s_cmp_eq_u32 s68, 0
s_cbranch_scc1 RSL1Z_%=
v_min3_f32 v24, v5, v6, v7
v_cmp_le_f32 vcc, s78, v9
v_min_f32 v24, v24, v8
v_cmp_nle_f32 s[80:81], -s78, v24
s_mov_b32 s58, s78
s_or_b64 vcc, vcc, s[80:81]
s_cbranch_scc1 RSL1Z_%=
s_mov_b32 s68, 0
s_branch RARER1Z_%=
END1Z_%=:
s_cmp_eq_u32 s54, s74
s_cbranch_scc1 EPI1Z_%=
ENE1Z_%=:
s_cmp_lg_u32 s69, 0
s_cbranch_scc1 RSC1Z_%=
ENP1Z_%=:
v_mov_b32 v24, 1
s_waitcnt vmcnt(0)
s_cmp_eq_u32 s54, s73
s_cselect_b64 exec, 1, 0
global_atomic_add v10, v24, s[66:67] sc1
s_cmp_eq_u32 s54, s79
s_cselect_b64 exec, 1, 0
global_atomic_add v10, v24, s[18:19] sc1
s_mov_b64 exec, -1
s_cmp_lg_u32 s54, s55
s_cbranch_scc1 ENDR1Z_%=
v_mov_b32 v212, v14
v_mov_b32 v214, v15
v_mov_b32 v216, v16
v_mov_b32 v218, v17
s_mov_b32 s58, 0xff800000
s_mov_b32 s68, 1
s_cmp_eq_u32 s88, 0
s_cbranch_scc1 ENDR1Z_%=
v_mov_b32 v14, 0
v_mov_b32 v15, 0
v_mov_b32 v16, 0
v_mov_b32 v17, 0
v_mov_b32 v192, 0
v_mov_b32 v193, 0
v_mov_b32 v194, 0
v_mov_b32 v195, 0
v_mov_b32 v196, 0
v_mov_b32 v197, 0
v_mov_b32 v198, 0
v_mov_b32 v199, 0
v_mov_b32 v200, 0
v_mov_b32 v201, 0
v_mov_b32 v202, 0
v_mov_b32 v203, 0
v_mov_b32 v204, 0
v_mov_b32 v205, 0
v_mov_b32 v206, 0
v_mov_b32 v207, 0
s_mov_b32 s88, 0
s_nop 0
s_branch ENDR1Z_%=
RARE2Z_%=:
s_cmp_eq_u32 s68, 0
s_cbranch_scc1 RSL2Z_%=
v_min3_f32 v24, v5, v6, v7
v_cmp_le_f32 vcc, s78, v9
v_min_f32 v24, v24, v8
v_cmp_nle_f32 s[80:81], -s78, v24
s_mov_b32 s58, s78
s_or_b64 vcc, vcc, s[80:81]
s_cbranch_scc1 RSL2Z_%=
s_mov_b32 s68, 0
s_branch RARER2Z_%=
END2Z_%=:
s_cmp_eq_u32 s54, s74
s_cbranch_scc1 EPI2Z_%=
ENE2Z_%=:
s_cmp_lg_u32 s69, 0
s_cbranch_scc1 RSC2Z_%=
ENP2Z_%=:
v_mov_b32 v24, 1
s_waitcnt vmcnt(0)
s_cmp_eq_u32 s54, s73
s_cselect_b64 exec, 1, 0
global_atomic_add v10, v24, s[66:67] sc1
s_cmp_eq_u32 s54, s79
s_cselect_b64 exec, 1, 0
global_atomic_add v10, v24, s[18:19] sc1
s_mov_b64 exec, -1
s_cmp_lg_u32 s54, s55
s_cbranch_scc1 ENDR2Z_%=
v_mov_b32 v212, v14
v_mov_b32 v214, v15
v_mov_b32 v216, v16
v_mov_b32 v218, v17
s_mov_b32 s58, 0xff800000
s_mov_b32 s68, 1
s_cmp_eq_u32 s88, 0
s_cbranch_scc1 ENDR2Z_%=
v_mov_b32 v14, 0
v_mov_b32 v15, 0
v_mov_b32 v16, 0
v_mov_b32 v17, 0
v_mov_b32 v192, 0
v_mov_b32 v193, 0
v_mov_b32 v194, 0
v_mov_b32 v195, 0
v_mov_b32 v196, 0
v_mov_b32 v197, 0
v_mov_b32 v198, 0
v_mov_b32 v199, 0
v_mov_b32 v200, 0
v_mov_b32 v201, 0
v_mov_b32 v202, 0
v_mov_b32 v203, 0
v_mov_b32 v204, 0
v_mov_b32 v205, 0
v_mov_b32 v206, 0
v_mov_b32 v207, 0
s_mov_b32 s88, 0
s_nop 0
s_branch ENDR2Z_%=
RARE4Z_%=:
s_cmp_eq_u32 s68, 0
s_cbranch_scc1 RSL4Z_%=
v_min3_f32 v24, v5, v6, v7
v_cmp_le_f32 vcc, s78, v9
v_min_f32 v24, v24, v8
v_cmp_nle_f32 s[80:81], -s78, v24
s_mov_b32 s58, s78
s_or_b64 vcc, vcc, s[80:81]
s_cbranch_scc1 RSL4Z_%=
s_mov_b32 s68, 0
s_branch RARER4Z_%=
END4Z_%=:
s_cmp_eq_u32 s54, s74
s_cbranch_scc1 EPI2Z_%=
s_branch RSC2Z_%=
SIG4Z_%=:
v_mov_b32 v24, 1
s_waitcnt vmcnt(0)
s_mov_b64 exec, 1
global_atomic_add v10, v24, s[66:67] sc1
s_mov_b64 exec, -1
s_branch SIGR4Z_%=
EPX4Z_%=:
v_mov_b32 v5, 1
s_waitcnt vmcnt(0)
s_mov_b64 exec, 1
global_atomic_add v10, v5, s[66:67] sc1
s_mov_b64 exec, -1
s_branch EPXR4Z_%=
MASK4Z_%=:
v_mov_b32 v28, 0xff80ff80
v_mov_b32 v29, 0xff80ff80
v_mov_b32 v30, 0xff80ff80
v_mov_b32 v31, 0xff80ff80
s_cmp_eq_u32 %2, 2
)"
R"(s_cbranch_scc1 MKCN4Z_%=
s_cmp_eq_u32 %2, 1
s_cbranch_scc1 MKAN4Z_%=
v_cmp_gt_i32 s[80:81], 0, v18
v_cmp_gt_i32 s[82:83], 1, v18
v_cmp_gt_i32 s[84:85], 2, v18
v_cmp_gt_i32 s[86:87], 3, v18
s_cmp_eq_u32 %2, 3
s_cbranch_scc1 MKBN4Z_%=
v_mfma_f32_16x16x32_bf16 v[68:71], a[240:243], v[28:31], 0
v_cndmask_b32 v64, v64, v20, s[80:81]
v_mfma_f32_16x16x32_bf16 v[96:99], a[240:243], v[28:31], 0
v_cndmask_b32 v65, v65, v20, s[82:83]
v_mfma_f32_16x16x32_bf16 v[112:115], a[240:243], v[28:31], 0
v_cndmask_b32 v66, v66, v20, s[84:85]
v_mfma_f32_16x16x32_bf16 v[100:103], a[240:243], v[28:31], 0
v_cndmask_b32 v67, v67, v20, s[86:87]
v_mfma_f32_16x16x32_bf16 v[108:111], a[240:243], v[28:31], 0
v_cndmask_b32 v84, v84, v20, s[80:81]
v_mfma_f32_16x16x32_bf16 v[116:119], a[240:243], v[28:31], 0
v_cndmask_b32 v85, v85, v20, s[82:83]
v_cndmask_b32 v86, v86, v20, s[84:85]
v_cndmask_b32 v87, v87, v20, s[86:87]
v_cndmask_b32 v104, v104, v20, s[80:81]
v_cndmask_b32 v105, v105, v20, s[82:83]
v_cndmask_b32 v106, v106, v20, s[84:85]
v_cndmask_b32 v107, v107, v20, s[86:87]
v_cndmask_b32 v124, v124, v20, s[80:81]
v_cndmask_b32 v125, v125, v20, s[82:83]
v_cndmask_b32 v126, v126, v20, s[84:85]
v_cndmask_b32 v127, v127, v20, s[86:87]
s_branch MASKR4Z_%=
MKBN4Z_%=:
v_mfma_f32_16x16x32_bf16 v[84:87], a[240:243], v[28:31], 0
v_cndmask_b32 v80, v80, v20, s[80:81]
v_mfma_f32_16x16x32_bf16 v[96:99], a[240:243], v[28:31], 0
v_cndmask_b32 v81, v81, v20, s[82:83]
v_mfma_f32_16x16x32_bf16 v[112:115], a[240:243], v[28:31], 0
v_cndmask_b32 v82, v82, v20, s[84:85]
v_mfma_f32_16x16x32_bf16 v[100:103], a[240:243], v[28:31], 0
v_cndmask_b32 v83, v83, v20, s[86:87]
v_mfma_f32_16x16x32_bf16 v[116:119], a[240:243], v[28:31], 0
v_cndmask_b32 v68, v68, v20, s[80:81]
v_mfma_f32_16x16x32_bf16 v[124:127], a[240:243], v[28:31], 0
v_cndmask_b32 v69, v69, v20, s[82:83]
v_cndmask_b32 v70, v70, v20, s[84:85]
v_cndmask_b32 v71, v71, v20, s[86:87]
v_cndmask_b32 v120, v120, v20, s[80:81]
v_cndmask_b32 v121, v121, v20, s[82:83]
v_cndmask_b32 v122, v122, v20, s[84:85]
v_cndmask_b32 v123, v123, v20, s[86:87]
v_cndmask_b32 v108, v108, v20, s[80:81]
v_cndmask_b32 v109, v109, v20, s[82:83]
v_cndmask_b32 v110, v110, v20, s[84:85]
v_cndmask_b32 v111, v111, v20, s[86:87]
s_branch MASKR4Z_%=
MKAN4Z_%=:
v_mfma_f32_16x16x32_bf16 v[64:67], a[240:243], v[28:31], 0
v_mfma_f32_16x16x32_bf16 v[80:83], a[240:243], v[28:31], 0
v_mfma_f32_16x16x32_bf16 v[68:71], a[240:243], v[28:31], 0
v_mfma_f32_16x16x32_bf16 v[84:87], a[240:243], v[28:31], 0
v_mfma_f32_32x32x16_bf16 v[96:111], a[240:243], v[28:31], 0
v_mfma_f32_32x32x16_bf16 v[112:127], a[240:243], v[28:31], 0
s_nop 10
s_branch MASKR4Z_%=
MKCN4Z_%=:
v_mfma_f32_16x16x32_bf16 v[96:99], a[240:243], v[28:31], 0
v_mfma_f32_16x16x32_bf16 v[112:115], a[240:243], v[28:31], 0
v_mfma_f32_16x16x32_bf16 v[100:103], a[240:243], v[28:31], 0
v_mfma_f32_16x16x32_bf16 v[116:119], a[240:243], v[28:31], 0
s_nop 6
s_branch MASKR4Z_%=
MASK2Z_%=:
v_add_u32 v24, 0x20000, v3
v_xor_b32 v25, 0x2000, v24
ds_read_b128 a[128:131], v24 offset:0
ds_read_b128 a[160:163], v25 offset:0
ds_read_b128 a[132:135], v24 offset:1024
ds_read_b128 a[164:167], v25 offset:1024
ds_read_b128 a[136:139], v24 offset:2048
ds_read_b128 a[168:171], v25 offset:2048
ds_read_b128 a[140:143], v24 offset:3072
ds_read_b128 a[172:175], v25 offset:3072
ds_read_b128 a[144:147], v24 offset:4096
ds_read_b128 a[176:179], v25 offset:4096
ds_read_b128 a[148:151], v24 offset:5120
ds_read_b128 a[180:183], v25 offset:5120
ds_read_b128 a[152:155], v24 offset:6144
ds_read_b128 a[184:187], v25 offset:6144
ds_read_b128 a[156:159], v24 offset:7168
ds_read_b128 a[188:191], v25 offset:7168
s_branch MASKR2Z_%=
MASK1Z_%=:
v_add_u32 v24, 0x20000, v3
v_xor_b32 v25, 0x2000, v24
ds_read_b128 a[128:131], v24 offset:0
ds_read_b128 a[160:163], v25 offset:0
ds_read_b128 a[132:135], v24 offset:1024
ds_read_b128 a[164:167], v25 offset:1024
ds_read_b128 a[136:139], v24 offset:2048
ds_read_b128 a[168:171], v25 offset:2048
ds_read_b128 a[140:143], v24 offset:3072
ds_read_b128 a[172:175], v25 offset:3072
ds_read_b128 a[144:147], v24 offset:4096
ds_read_b128 a[176:179], v25 offset:4096
ds_read_b128 a[148:151], v24 offset:5120
ds_read_b128 a[180:183], v25 offset:5120
ds_read_b128 a[152:155], v24 offset:6144
ds_read_b128 a[184:187], v25 offset:6144
ds_read_b128 a[156:159], v24 offset:7168
ds_read_b128 a[188:191], v25 offset:7168
s_branch MASKR1Z_%=
MASK0Z_%=:
s_cmp_lg_u32 s55, 0
s_cbranch_scc1 MKQ0Z_%=
v_add_u32 v24, 0x20000, v3
v_xor_b32 v25, 0x2000, v24
ds_read_b128 a[128:131], v24 offset:0
ds_read_b128 a[160:163], v25 offset:0
ds_read_b128 a[132:135], v24 offset:1024
ds_read_b128 a[164:167], v25 offset:1024
ds_read_b128 a[136:139], v24 offset:2048
ds_read_b128 a[168:171], v25 offset:2048
ds_read_b128 a[140:143], v24 offset:3072
ds_read_b128 a[172:175], v25 offset:3072
ds_read_b128 a[144:147], v24 offset:4096
ds_read_b128 a[176:179], v25 offset:4096
ds_read_b128 a[148:151], v24 offset:5120
ds_read_b128 a[180:183], v25 offset:5120
ds_read_b128 a[152:155], v24 offset:6144
ds_read_b128 a[184:187], v25 offset:6144
ds_read_b128 a[156:159], v24 offset:7168
ds_read_b128 a[188:191], v25 offset:7168
MKQ0Z_%=:
v_mov_b32 v28, 0xff80ff80
v_mov_b32 v29, 0xff80ff80
v_mov_b32 v30, 0xff80ff80
v_mov_b32 v31, 0xff80ff80
s_cmp_eq_u32 %2, 2
s_cbranch_scc1 MKCN0Z_%=
s_cmp_eq_u32 %2, 1
s_cbranch_scc1 MKAN0Z_%=
v_cmp_gt_i32 s[80:81], 0, v18
v_cmp_gt_i32 s[82:83], 1, v18
v_cmp_gt_i32 s[84:85], 2, v18
v_cmp_gt_i32 s[86:87], 3, v18
s_cmp_eq_u32 %2, 3
s_cbranch_scc1 MKBN0Z_%=
v_mfma_f32_16x16x32_bf16 v[68:71], a[240:243], v[28:31], 0
v_cndmask_b32 v64, v64, v20, s[80:81]
v_mfma_f32_16x16x32_bf16 v[96:99], a[240:243], v[28:31], 0
v_cndmask_b32 v65, v65, v20, s[82:83]
v_mfma_f32_16x16x32_bf16 v[112:115], a[240:243], v[28:31], 0
v_cndmask_b32 v66, v66, v20, s[84:85]
v_mfma_f32_16x16x32_bf16 v[100:103], a[240:243], v[28:31], 0
v_cndmask_b32 v67, v67, v20, s[86:87]
v_mfma_f32_16x16x32_bf16 v[108:111], a[240:243], v[28:31], 0
v_cndmask_b32 v84, v84, v20, s[80:81]
v_mfma_f32_16x16x32_bf16 v[116:119], a[240:243], v[28:31], 0
v_cndmask_b32 v85, v85, v20, s[82:83]
v_cndmask_b32 v86, v86, v20, s[84:85]
v_cndmask_b32 v87, v87, v20, s[86:87]
v_cndmask_b32 v104, v104, v20, s[80:81]
v_cndmask_b32 v105, v105, v20, s[82:83]
v_cndmask_b32 v106, v106, v20, s[84:85]
v_cndmask_b32 v107, v107, v20, s[86:87]
v_cndmask_b32 v124, v124, v20, s[80:81]
v_cndmask_b32 v125, v125, v20, s[82:83]
v_cndmask_b32 v126, v126, v20, s[84:85]
v_cndmask_b32 v127, v127, v20, s[86:87]
s_branch MASKR0Z_%=
MKBN0Z_%=:
v_mfma_f32_16x16x32_bf16 v[84:87], a[240:243], v[28:31], 0
v_cndmask_b32 v80, v80, v20, s[80:81]
v_mfma_f32_16x16x32_bf16 v[96:99], a[240:243], v[28:31], 0
v_cndmask_b32 v81, v81, v20, s[82:83]
v_mfma_f32_16x16x32_bf16 v[112:115], a[240:243], v[28:31], 0
v_cndmask_b32 v82, v82, v20, s[84:85]
v_mfma_f32_16x16x32_bf16 v[100:103], a[240:243], v[28:31], 0
v_cndmask_b32 v83, v83, v20, s[86:87]
v_mfma_f32_16x16x32_bf16 v[116:119], a[240:243], v[28:31], 0
v_cndmask_b32 v68, v68, v20, s[80:81]
v_mfma_f32_16x16x32_bf16 v[124:127], a[240:243], v[28:31], 0
v_cndmask_b32 v69, v69, v20, s[82:83]
v_cndmask_b32 v70, v70, v20, s[84:85]
v_cndmask_b32 v71, v71, v20, s[86:87]
v_cndmask_b32 v120, v120, v20, s[80:81]
v_cndmask_b32 v121, v121, v20, s[82:83]
v_cndmask_b32 v122, v122, v20, s[84:85]
v_cndmask_b32 v123, v123, v20, s[86:87]
v_cndmask_b32 v108, v108, v20, s[80:81]
v_cndmask_b32 v109, v109, v20, s[82:83]
v_cndmask_b32 v110, v110, v20, s[84:85]
v_cndmask_b32 v111, v111, v20, s[86:87]
s_branch MASKR0Z_%=
MKAN0Z_%=:
v_mfma_f32_16x16x32_bf16 v[64:67], a[240:243], v[28:31], 0
v_mfma_f32_16x16x32_bf16 v[80:83], a[240:243], v[28:31], 0
v_mfma_f32_16x16x32_bf16 v[68:71], a[240:243], v[28:31], 0
v_mfma_f32_16x16x32_bf16 v[84:87], a[240:243], v[28:31], 0
v_mfma_f32_32x32x16_bf16 v[96:111], a[240:243], v[28:31], 0
v_mfma_f32_32x32x16_bf16 v[112:127], a[240:243], v[28:31], 0
s_nop 10
s_branch MASKR0Z_%=
MKCN0Z_%=:
v_mfma_f32_16x16x32_bf16 v[96:99], a[240:243], v[28:31], 0
v_mfma_f32_16x16x32_bf16 v[112:115], a[240:243], v[28:31], 0
v_mfma_f32_16x16x32_bf16 v[100:103], a[240:243], v[28:31], 0
v_mfma_f32_16x16x32_bf16 v[116:119], a[240:243], v[28:31], 0
s_nop 6
s_branch MASKR0Z_%=
EPI1Z_%=:
s_cmp_gt_u32 s55, 11
s_cbranch_scc1 ENF1Z_%=
v_lshlrev_b32 v223, 3, v22
global_load_dword v223, v223, s[34:35]
ENF1Z_%=:
v_add_u32 v28, 0x10000, v3
v_xor_b32 v30, 0x2000, v28
v_add_u32 v29, 0x20000, v3
s_nop 3
ds_write_b128 v30, a[64:67] offset:32768
v_accvgpr_read_b32 v32, a0
v_accvgpr_read_b32 v33, a1
v_accvgpr_read_b32 v34, a2
v_accvgpr_read_b32 v35, a3
ds_write_b128 v30, a[68:71] offset:33792
v_accvgpr_read_b32 v217, a232
v_accvgpr_read_b32 v219, a236
v_accvgpr_read_b32 v213, a224
v_accvgpr_read_b32 v215, a228
ds_write_b128 v30, v[216:219]
v_accvgpr_read_b32 v36, a4
v_accvgpr_read_b32 v37, a5
v_accvgpr_read_b32 v38, a6
v_accvgpr_read_b32 v39, a7
ds_write_b128 v30, a[72:75] offset:34816
v_accvgpr_read_b32 v40, a8
v_accvgpr_read_b32 v41, a9
v_accvgpr_read_b32 v42, a10
v_accvgpr_read_b32 v43, a11
ds_write_b128 v30, a[76:79] offset:35840
v_accvgpr_read_b32 v44, a12
v_accvgpr_read_b32 v45, a13
v_accvgpr_read_b32 v46, a14
v_accvgpr_read_b32 v47, a15
v_mfma_f32_32x32x16_bf16 a[0:15], v[10:13], v[10:13], 0
ds_write_b128 v30, a[80:83] offset:36864
v_accvgpr_read_b32 v48, a16
v_accvgpr_read_b32 v49, a17
v_accvgpr_read_b32 v50, a18
v_accvgpr_read_b32 v51, a19
ds_write_b128 v30, a[84:87] offset:37888
v_accvgpr_read_b32 v52, a20
v_accvgpr_read_b32 v53, a21
v_accvgpr_read_b32 v54, a22
v_accvgpr_read_b32 v55, a23
ds_write_b128 v30, a[88:91] offset:38912
v_accvgpr_read_b32 v56, a24
v_accvgpr_read_b32 v57, a25
v_accvgpr_read_b32 v58, a26
v_accvgpr_read_b32 v59, a27
ds_write_b128 v30, a[92:95] offset:39936
v_accvgpr_read_b32 v60, a28
v_accvgpr_read_b32 v61, a29
v_accvgpr_read_b32 v62, a30
v_accvgpr_read_b32 v63, a31
v_mfma_f32_32x32x16_bf16 a[16:31], v[10:13], v[10:13], 0
ds_write_b128 v23, a[96:99] offset:0
v_accvgpr_read_b32 v224, a32
v_accvgpr_read_b32 v225, a33
v_accvgpr_read_b32 v226, a34
v_accvgpr_read_b32 v227, a35
ds_write_b128 v23, a[100:103] offset:1024
v_accvgpr_read_b32 v228, a36
v_accvgpr_read_b32 v229, a37
v_accvgpr_read_b32 v230, a38
v_accvgpr_read_b32 v231, a39
ds_write_b128 v23, a[104:107] offset:2048
v_accvgpr_read_b32 v232, a40
v_accvgpr_read_b32 v233, a41
v_accvgpr_read_b32 v234, a42
v_accvgpr_read_b32 v235, a43
ds_write_b128 v23, a[108:111] offset:3072
v_accvgpr_read_b32 v236, a44
v_accvgpr_read_b32 v237, a45
v_accvgpr_read_b32 v238, a46
v_accvgpr_read_b32 v239, a47
v_mfma_f32_32x32x16_bf16 a[32:47], v[10:13], v[10:13], 0
ds_write_b128 v23, a[112:115] offset:4096
v_accvgpr_read_b32 v240, a48
v_accvgpr_read_b32 v241, a49
v_accvgpr_read_b32 v242, a50
v_accvgpr_read_b32 v243, a51
ds_write_b128 v23, a[116:119] offset:5120
v_accvgpr_read_b32 v244, a52
v_accvgpr_read_b32 v245, a53
v_accvgpr_read_b32 v246, a54
v_accvgpr_read_b32 v247, a55
ds_write_b128 v23, a[120:123] offset:6144
v_accvgpr_read_b32 v248, a56
v_accvgpr_read_b32 v249, a57
v_accvgpr_read_b32 v250, a58
v_accvgpr_read_b32 v251, a59
ds_write_b128 v23, a[124:127] offset:7168
v_accvgpr_read_b32 v252, a60
v_accvgpr_read_b32 v253, a61
v_accvgpr_read_b32 v254, a62
v_accvgpr_read_b32 v255, a63
s_waitcnt lgkmcnt(0)
s_barrier
ds_read_b128 v[6:9], v28
ds_read_b128 v[64:67], v28 offset:32768
ds_read_b128 v[68:71], v28 offset:33792
v_mfma_f32_32x32x16_bf16 a[48:63], v[10:13], v[10:13], 0
ds_read_b128 v[72:75], v28 offset:34816
ds_read_b128 v[76:79], v28 offset:35840
ds_read_b128 v[80:83], v28 offset:36864
ds_read_b128 v[84:87], v28 offset:37888
v_mfma_f32_32x32x16_bf16 a[224:239], v[10:13], v[10:13], 0
ds_read_b128 v[88:91], v28 offset:38912
ds_read_b128 v[92:95], v28 offset:39936
ds_read_b128 v[96:99], v29 offset:0
ds_read_b128 v[100:103], v29 offset:1024
v_mfma_f32_32x32x16_bf16 a[64:79], v[10:13], v[10:13], 0
ds_read_b128 v[104:107], v29 offset:2048
ds_read_b128 v[108:111], v29 offset:3072
ds_read_b128 v[112:115], v29 offset:4096
ds_read_b128 v[116:119], v29 offset:5120
v_mfma_f32_32x32x16_bf16 a[80:95], v[10:13], v[10:13], 0
ds_read_b128 v[120:123], v29 offset:6144
ds_read_b128 v[124:127], v29 offset:7168
s_waitcnt lgkmcnt(14)
v_max_f32 v24, v6, v212
v_max_f32 v25, v8, v214
v_sub_f32 v208, v212, v24
v_sub_f32 v209, v6, v24
v_sub_f32 v210, v214, v25
v_sub_f32 v211, v8, v25
v_mfma_f32_32x32x16_bf16 a[96:111], v[10:13], v[10:13], 0
v_exp_f32 v208, v208
v_exp_f32 v209, v209
v_exp_f32 v210, v210
v_exp_f32 v211, v211
v_mul_f32 v24, v208, v213
v_mul_f32 v25, v210, v215
v_fmac_f32 v24, v209, v7
v_fmac_f32 v25, v211, v9
v_mfma_f32_32x32x16_bf16 a[112:127], v[10:13], v[10:13], 0
v_rcp_f32 v24, v24
v_rcp_f32 v25, v25
v_mul_f32 v208, v208, v24
v_mul_f32 v209, v209, v24
v_mul_f32 v210, v210, v25
v_mul_f32 v211, v211, v25
v_max3_f32 v5, v213, v215, v7
v_max3_f32 v5, v5, v9, v24
v_max_f32 v5, v5, v25
v_cmp_lt_f32 vcc, s76, v5
s_cbranch_vccnz LF1Z_%=
LF1RZ_%=:
v_pk_mul_f32 v[32:33], v[32:33], v[208:209] op_sel_hi:[1,0]
v_pk_mul_f32 v[34:35], v[34:35], v[208:209] op_sel_hi:[1,0]
v_pk_mul_f32 v[36:37], v[36:37], v[208:209] op_sel_hi:[1,0]
v_pk_mul_f32 v[38:39], v[38:39], v[208:209] op_sel_hi:[1,0]
v_pk_fma_f32 v[32:33], v[64:65], v[208:209], v[32:33] op_sel:[0,1,0] op_sel_hi:[1,1,1]
v_pk_fma_f32 v[34:35], v[66:67], v[208:209], v[34:35] op_sel:[0,1,0] op_sel_hi:[1,1,1]
v_pk_fma_f32 v[36:37], v[68:69], v[208:209], v[36:37] op_sel:[0,1,0] op_sel_hi:[1,1,1]
v_pk_fma_f32 v[38:39], v[70:71], v[208:209], v[38:39] op_sel:[0,1,0] op_sel_hi:[1,1,1]
v_cvt_pk_bf16_f32 v32, v32, v33
v_cvt_pk_bf16_f32 v33, v34, v35
v_cvt_pk_bf16_f32 v34, v36, v37
v_cvt_pk_bf16_f32 v35, v38, v39
global_store_dwordx4 v22, v[32:35], s[50:51] offset:-4096 sc1
v_pk_mul_f32 v[40:41], v[40:41], v[208:209] op_sel_hi:[1,0]
v_pk_mul_f32 v[42:43], v[42:43], v[208:209] op_sel_hi:[1,0]
v_pk_mul_f32 v[44:45], v[44:45], v[208:209] op_sel_hi:[1,0]
v_pk_mul_f32 v[46:47], v[46:47], v[208:209] op_sel_hi:[1,0]
s_waitcnt lgkmcnt(13)
v_pk_fma_f32 v[40:41], v[72:73], v[208:209], v[40:41] op_sel:[0,1,0] op_sel_hi:[1,1,1]
v_pk_fma_f32 v[42:43], v[74:75], v[208:209], v[42:43] op_sel:[0,1,0] op_sel_hi:[1,1,1]
s_waitcnt lgkmcnt(12)
v_pk_fma_f32 v[44:45], v[76:77], v[208:209], v[44:45] op_sel:[0,1,0] op_sel_hi:[1,1,1]
v_pk_fma_f32 v[46:47], v[78:79], v[208:209], v[46:47] op_sel:[0,1,0] op_sel_hi:[1,1,1]
v_cvt_pk_bf16_f32 v40, v40, v41
v_cvt_pk_bf16_f32 v41, v42, v43
v_cvt_pk_bf16_f32 v42, v44, v45
v_cvt_pk_bf16_f32 v43, v46, v47
global_store_dwordx4 v22, v[40:43], s[50:51] offset:-2048 sc1
v_pk_mul_f32 v[48:49], v[48:49], v[208:209] op_sel_hi:[1,0]
v_pk_mul_f32 v[50:51], v[50:51], v[208:209] op_sel_hi:[1,0]
v_pk_mul_f32 v[52:53], v[52:53], v[208:209] op_sel_hi:[1,0]
v_pk_mul_f32 v[54:55], v[54:55], v[208:209] op_sel_hi:[1,0]
s_waitcnt lgkmcnt(11)
v_pk_fma_f32 v[48:49], v[80:81], v[208:209], v[48:49] op_sel:[0,1,0] op_sel_hi:[1,1,1]
v_pk_fma_f32 v[50:51], v[82:83], v[208:209], v[50:51] op_sel:[0,1,0] op_sel_hi:[1,1,1]
s_waitcnt lgkmcnt(10)
v_pk_fma_f32 v[52:53], v[84:85], v[208:209], v[52:53] op_sel:[0,1,0] op_sel_hi:[1,1,1]
v_pk_fma_f32 v[54:55], v[86:87], v[208:209], v[54:55] op_sel:[0,1,0] op_sel_hi:[1,1,1]
v_cvt_pk_bf16_f32 v48, v48, v49
)"
R"(v_cvt_pk_bf16_f32 v49, v50, v51
v_cvt_pk_bf16_f32 v50, v52, v53
v_cvt_pk_bf16_f32 v51, v54, v55
global_store_dwordx4 v22, v[48:51], s[50:51] offset:0 sc1
v_pk_mul_f32 v[56:57], v[56:57], v[208:209] op_sel_hi:[1,0]
v_pk_mul_f32 v[58:59], v[58:59], v[208:209] op_sel_hi:[1,0]
v_pk_mul_f32 v[60:61], v[60:61], v[208:209] op_sel_hi:[1,0]
v_pk_mul_f32 v[62:63], v[62:63], v[208:209] op_sel_hi:[1,0]
s_waitcnt lgkmcnt(9)
v_pk_fma_f32 v[56:57], v[88:89], v[208:209], v[56:57] op_sel:[0,1,0] op_sel_hi:[1,1,1]
v_pk_fma_f32 v[58:59], v[90:91], v[208:209], v[58:59] op_sel:[0,1,0] op_sel_hi:[1,1,1]
s_waitcnt lgkmcnt(8)
v_pk_fma_f32 v[60:61], v[92:93], v[208:209], v[60:61] op_sel:[0,1,0] op_sel_hi:[1,1,1]
v_pk_fma_f32 v[62:63], v[94:95], v[208:209], v[62:63] op_sel:[0,1,0] op_sel_hi:[1,1,1]
v_cvt_pk_bf16_f32 v56, v56, v57
v_cvt_pk_bf16_f32 v57, v58, v59
v_cvt_pk_bf16_f32 v58, v60, v61
v_cvt_pk_bf16_f32 v59, v62, v63
global_store_dwordx4 v22, v[56:59], s[50:51] offset:2048 sc1
v_pk_mul_f32 v[224:225], v[224:225], v[210:211] op_sel_hi:[1,0]
v_pk_mul_f32 v[226:227], v[226:227], v[210:211] op_sel_hi:[1,0]
v_pk_mul_f32 v[228:229], v[228:229], v[210:211] op_sel_hi:[1,0]
v_pk_mul_f32 v[230:231], v[230:231], v[210:211] op_sel_hi:[1,0]
s_waitcnt lgkmcnt(7)
v_pk_fma_f32 v[224:225], v[96:97], v[210:211], v[224:225] op_sel:[0,1,0] op_sel_hi:[1,1,1]
v_pk_fma_f32 v[226:227], v[98:99], v[210:211], v[226:227] op_sel:[0,1,0] op_sel_hi:[1,1,1]
s_waitcnt lgkmcnt(6)
v_pk_fma_f32 v[228:229], v[100:101], v[210:211], v[228:229] op_sel:[0,1,0] op_sel_hi:[1,1,1]
v_pk_fma_f32 v[230:231], v[102:103], v[210:211], v[230:231] op_sel:[0,1,0] op_sel_hi:[1,1,1]
v_cvt_pk_bf16_f32 v224, v224, v225
v_cvt_pk_bf16_f32 v225, v226, v227
v_cvt_pk_bf16_f32 v226, v228, v229
v_cvt_pk_bf16_f32 v227, v230, v231
global_store_dwordx4 v22, v[224:227], s[50:51] offset:-3072 sc1
v_pk_mul_f32 v[232:233], v[232:233], v[210:211] op_sel_hi:[1,0]
v_pk_mul_f32 v[234:235], v[234:235], v[210:211] op_sel_hi:[1,0]
v_pk_mul_f32 v[236:237], v[236:237], v[210:211] op_sel_hi:[1,0]
v_pk_mul_f32 v[238:239], v[238:239], v[210:211] op_sel_hi:[1,0]
s_waitcnt lgkmcnt(5)
v_pk_fma_f32 v[232:233], v[104:105], v[210:211], v[232:233] op_sel:[0,1,0] op_sel_hi:[1,1,1]
v_pk_fma_f32 v[234:235], v[106:107], v[210:211], v[234:235] op_sel:[0,1,0] op_sel_hi:[1,1,1]
s_waitcnt lgkmcnt(4)
v_pk_fma_f32 v[236:237], v[108:109], v[210:211], v[236:237] op_sel:[0,1,0] op_sel_hi:[1,1,1]
v_pk_fma_f32 v[238:239], v[110:111], v[210:211], v[238:239] op_sel:[0,1,0] op_sel_hi:[1,1,1]
v_cvt_pk_bf16_f32 v232, v232, v233
v_cvt_pk_bf16_f32 v233, v234, v235
v_cvt_pk_bf16_f32 v234, v236, v237
v_cvt_pk_bf16_f32 v235, v238, v239
global_store_dwordx4 v22, v[232:235], s[50:51] offset:-1024 sc1
v_pk_mul_f32 v[240:241], v[240:241], v[210:211] op_sel_hi:[1,0]
v_pk_mul_f32 v[242:243], v[242:243], v[210:211] op_sel_hi:[1,0]
v_pk_mul_f32 v[244:245], v[244:245], v[210:211] op_sel_hi:[1,0]
v_pk_mul_f32 v[246:247], v[246:247], v[210:211] op_sel_hi:[1,0]
s_waitcnt lgkmcnt(3)
v_pk_fma_f32 v[240:241], v[112:113], v[210:211], v[240:241] op_sel:[0,1,0] op_sel_hi:[1,1,1]
v_pk_fma_f32 v[242:243], v[114:115], v[210:211], v[242:243] op_sel:[0,1,0] op_sel_hi:[1,1,1]
s_waitcnt lgkmcnt(2)
v_pk_fma_f32 v[244:245], v[116:117], v[210:211], v[244:245] op_sel:[0,1,0] op_sel_hi:[1,1,1]
v_pk_fma_f32 v[246:247], v[118:119], v[210:211], v[246:247] op_sel:[0,1,0] op_sel_hi:[1,1,1]
v_cvt_pk_bf16_f32 v240, v240, v241
v_cvt_pk_bf16_f32 v241, v242, v243
v_cvt_pk_bf16_f32 v242, v244, v245
v_cvt_pk_bf16_f32 v243, v246, v247
global_store_dwordx4 v22, v[240:243], s[50:51] offset:1024 sc1
v_pk_mul_f32 v[248:249], v[248:249], v[210:211] op_sel_hi:[1,0]
v_pk_mul_f32 v[250:251], v[250:251], v[210:211] op_sel_hi:[1,0]
v_pk_mul_f32 v[252:253], v[252:253], v[210:211] op_sel_hi:[1,0]
v_pk_mul_f32 v[254:255], v[254:255], v[210:211] op_sel_hi:[1,0]
s_waitcnt lgkmcnt(1)
v_pk_fma_f32 v[248:249], v[120:121], v[210:211], v[248:249] op_sel:[0,1,0] op_sel_hi:[1,1,1]
v_pk_fma_f32 v[250:251], v[122:123], v[210:211], v[250:251] op_sel:[0,1,0] op_sel_hi:[1,1,1]
s_waitcnt lgkmcnt(0)
v_pk_fma_f32 v[252:253], v[124:125], v[210:211], v[252:253] op_sel:[0,1,0] op_sel_hi:[1,1,1]
v_pk_fma_f32 v[254:255], v[126:127], v[210:211], v[254:255] op_sel:[0,1,0] op_sel_hi:[1,1,1]
v_cvt_pk_bf16_f32 v248, v248, v249
v_cvt_pk_bf16_f32 v249, v250, v251
v_cvt_pk_bf16_f32 v250, v252, v253
v_cvt_pk_bf16_f32 v251, v254, v255
global_store_dwordx4 v22, v[248:251], s[50:51] offset:3072 sc1
s_branch ENDR1Z_%=
EPI2Z_%=:
s_cmp_gt_u32 s55, 11
s_cbranch_scc1 ENF2Z_%=
v_lshlrev_b32 v223, 3, v22
global_load_dword v223, v223, s[34:35]
ENF2Z_%=:
v_add_u32 v28, 0x0, v3
v_xor_b32 v30, 0x2000, v28
v_add_u32 v29, 0x20000, v3
s_nop 3
ds_write_b128 v30, a[64:67] offset:32768
v_accvgpr_read_b32 v32, a0
v_accvgpr_read_b32 v33, a1
v_accvgpr_read_b32 v34, a2
v_accvgpr_read_b32 v35, a3
ds_write_b128 v30, a[68:71] offset:33792
v_accvgpr_read_b32 v217, a232
v_accvgpr_read_b32 v219, a236
v_accvgpr_read_b32 v213, a224
v_accvgpr_read_b32 v215, a228
ds_write_b128 v30, v[216:219]
v_accvgpr_read_b32 v36, a4
v_accvgpr_read_b32 v37, a5
v_accvgpr_read_b32 v38, a6
v_accvgpr_read_b32 v39, a7
ds_write_b128 v30, a[72:75] offset:34816
v_accvgpr_read_b32 v40, a8
v_accvgpr_read_b32 v41, a9
v_accvgpr_read_b32 v42, a10
v_accvgpr_read_b32 v43, a11
ds_write_b128 v30, a[76:79] offset:35840
v_accvgpr_read_b32 v44, a12
v_accvgpr_read_b32 v45, a13
v_accvgpr_read_b32 v46, a14
v_accvgpr_read_b32 v47, a15
v_mfma_f32_32x32x16_bf16 a[0:15], v[10:13], v[10:13], 0
ds_write_b128 v30, a[80:83] offset:36864
v_accvgpr_read_b32 v48, a16
v_accvgpr_read_b32 v49, a17
v_accvgpr_read_b32 v50, a18
v_accvgpr_read_b32 v51, a19
ds_write_b128 v30, a[84:87] offset:37888
v_accvgpr_read_b32 v52, a20
v_accvgpr_read_b32 v53, a21
v_accvgpr_read_b32 v54, a22
v_accvgpr_read_b32 v55, a23
ds_write_b128 v30, a[88:91] offset:38912
v_accvgpr_read_b32 v56, a24
v_accvgpr_read_b32 v57, a25
v_accvgpr_read_b32 v58, a26
v_accvgpr_read_b32 v59, a27
ds_write_b128 v30, a[92:95] offset:39936
v_accvgpr_read_b32 v60, a28
v_accvgpr_read_b32 v61, a29
v_accvgpr_read_b32 v62, a30
v_accvgpr_read_b32 v63, a31
v_mfma_f32_32x32x16_bf16 a[16:31], v[10:13], v[10:13], 0
ds_write_b128 v23, a[96:99] offset:0
v_accvgpr_read_b32 v224, a32
v_accvgpr_read_b32 v225, a33
v_accvgpr_read_b32 v226, a34
v_accvgpr_read_b32 v227, a35
ds_write_b128 v23, a[100:103] offset:1024
v_accvgpr_read_b32 v228, a36
v_accvgpr_read_b32 v229, a37
v_accvgpr_read_b32 v230, a38
v_accvgpr_read_b32 v231, a39
ds_write_b128 v23, a[104:107] offset:2048
v_accvgpr_read_b32 v232, a40
v_accvgpr_read_b32 v233, a41
v_accvgpr_read_b32 v234, a42
v_accvgpr_read_b32 v235, a43
ds_write_b128 v23, a[108:111] offset:3072
v_accvgpr_read_b32 v236, a44
v_accvgpr_read_b32 v237, a45
v_accvgpr_read_b32 v238, a46
v_accvgpr_read_b32 v239, a47
v_mfma_f32_32x32x16_bf16 a[32:47], v[10:13], v[10:13], 0
ds_write_b128 v23, a[112:115] offset:4096
v_accvgpr_read_b32 v240, a48
v_accvgpr_read_b32 v241, a49
v_accvgpr_read_b32 v242, a50
v_accvgpr_read_b32 v243, a51
ds_write_b128 v23, a[116:119] offset:5120
v_accvgpr_read_b32 v244, a52
v_accvgpr_read_b32 v245, a53
v_accvgpr_read_b32 v246, a54
v_accvgpr_read_b32 v247, a55
ds_write_b128 v23, a[120:123] offset:6144
v_accvgpr_read_b32 v248, a56
v_accvgpr_read_b32 v249, a57
v_accvgpr_read_b32 v250, a58
v_accvgpr_read_b32 v251, a59
ds_write_b128 v23, a[124:127] offset:7168
v_accvgpr_read_b32 v252, a60
v_accvgpr_read_b32 v253, a61
v_accvgpr_read_b32 v254, a62
v_accvgpr_read_b32 v255, a63
s_waitcnt lgkmcnt(0)
s_barrier
ds_read_b128 v[6:9], v28
ds_read_b128 v[128:131], v28 offset:32768
ds_read_b128 v[132:135], v28 offset:33792
v_mfma_f32_32x32x16_bf16 a[48:63], v[10:13], v[10:13], 0
ds_read_b128 v[136:139], v28 offset:34816
ds_read_b128 v[140:143], v28 offset:35840
ds_read_b128 v[144:147], v28 offset:36864
ds_read_b128 v[148:151], v28 offset:37888
v_mfma_f32_32x32x16_bf16 a[224:239], v[10:13], v[10:13], 0
ds_read_b128 v[152:155], v28 offset:38912
ds_read_b128 v[156:159], v28 offset:39936
ds_read_b128 v[160:163], v29 offset:0
ds_read_b128 v[164:167], v29 offset:1024
v_mfma_f32_32x32x16_bf16 a[64:79], v[10:13], v[10:13], 0
ds_read_b128 v[168:171], v29 offset:2048
ds_read_b128 v[172:175], v29 offset:3072
ds_read_b128 v[176:179], v29 offset:4096
ds_read_b128 v[180:183], v29 offset:5120
v_mfma_f32_32x32x16_bf16 a[80:95], v[10:13], v[10:13], 0
ds_read_b128 v[184:187], v29 offset:6144
ds_read_b128 v[188:191], v29 offset:7168
s_waitcnt lgkmcnt(14)
v_max_f32 v24, v6, v212
v_max_f32 v25, v8, v214
v_sub_f32 v208, v212, v24
v_sub_f32 v209, v6, v24
v_sub_f32 v210, v214, v25
v_sub_f32 v211, v8, v25
v_mfma_f32_32x32x16_bf16 a[96:111], v[10:13], v[10:13], 0
v_exp_f32 v208, v208
v_exp_f32 v209, v209
v_exp_f32 v210, v210
v_exp_f32 v211, v211
v_mul_f32 v24, v208, v213
v_mul_f32 v25, v210, v215
v_fmac_f32 v24, v209, v7
v_fmac_f32 v25, v211, v9
v_mfma_f32_32x32x16_bf16 a[112:127], v[10:13], v[10:13], 0
v_rcp_f32 v24, v24
v_rcp_f32 v25, v25
v_mul_f32 v208, v208, v24
v_mul_f32 v209, v209, v24
v_mul_f32 v210, v210, v25
v_mul_f32 v211, v211, v25
v_max3_f32 v5, v213, v215, v7
v_max3_f32 v5, v5, v9, v24
v_max_f32 v5, v5, v25
v_cmp_lt_f32 vcc, s76, v5
s_cbranch_vccnz LF0Z_%=
LF0RZ_%=:
v_pk_mul_f32 v[32:33], v[32:33], v[208:209] op_sel_hi:[1,0]
v_pk_mul_f32 v[34:35], v[34:35], v[208:209] op_sel_hi:[1,0]
v_pk_mul_f32 v[36:37], v[36:37], v[208:209] op_sel_hi:[1,0]
v_pk_mul_f32 v[38:39], v[38:39], v[208:209] op_sel_hi:[1,0]
v_pk_fma_f32 v[32:33], v[128:129], v[208:209], v[32:33] op_sel:[0,1,0] op_sel_hi:[1,1,1]
v_pk_fma_f32 v[34:35], v[130:131], v[208:209], v[34:35] op_sel:[0,1,0] op_sel_hi:[1,1,1]
v_pk_fma_f32 v[36:37], v[132:133], v[208:209], v[36:37] op_sel:[0,1,0] op_sel_hi:[1,1,1]
v_pk_fma_f32 v[38:39], v[134:135], v[208:209], v[38:39] op_sel:[0,1,0] op_sel_hi:[1,1,1]
v_cvt_pk_bf16_f32 v32, v32, v33
v_cvt_pk_bf16_f32 v33, v34, v35
v_cvt_pk_bf16_f32 v34, v36, v37
v_cvt_pk_bf16_f32 v35, v38, v39
global_store_dwordx4 v22, v[32:35], s[50:51] offset:-4096 sc1
v_pk_mul_f32 v[40:41], v[40:41], v[208:209] op_sel_hi:[1,0]
v_pk_mul_f32 v[42:43], v[42:43], v[208:209] op_sel_hi:[1,0]
v_pk_mul_f32 v[44:45], v[44:45], v[208:209] op_sel_hi:[1,0]
v_pk_mul_f32 v[46:47], v[46:47], v[208:209] op_sel_hi:[1,0]
s_waitcnt lgkmcnt(13)
v_pk_fma_f32 v[40:41], v[136:137], v[208:209], v[40:41] op_sel:[0,1,0] op_sel_hi:[1,1,1]
v_pk_fma_f32 v[42:43], v[138:139], v[208:209], v[42:43] op_sel:[0,1,0] op_sel_hi:[1,1,1]
s_waitcnt lgkmcnt(12)
v_pk_fma_f32 v[44:45], v[140:141], v[208:209], v[44:45] op_sel:[0,1,0] op_sel_hi:[1,1,1]
v_pk_fma_f32 v[46:47], v[142:143], v[208:209], v[46:47] op_sel:[0,1,0] op_sel_hi:[1,1,1]
v_cvt_pk_bf16_f32 v40, v40, v41
v_cvt_pk_bf16_f32 v41, v42, v43
v_cvt_pk_bf16_f32 v42, v44, v45
v_cvt_pk_bf16_f32 v43, v46, v47
global_store_dwordx4 v22, v[40:43], s[50:51] offset:-2048 sc1
v_pk_mul_f32 v[48:49], v[48:49], v[208:209] op_sel_hi:[1,0]
v_pk_mul_f32 v[50:51], v[50:51], v[208:209] op_sel_hi:[1,0]
v_pk_mul_f32 v[52:53], v[52:53], v[208:209] op_sel_hi:[1,0]
v_pk_mul_f32 v[54:55], v[54:55], v[208:209] op_sel_hi:[1,0]
s_waitcnt lgkmcnt(11)
v_pk_fma_f32 v[48:49], v[144:145], v[208:209], v[48:49] op_sel:[0,1,0] op_sel_hi:[1,1,1]
v_pk_fma_f32 v[50:51], v[146:147], v[208:209], v[50:51] op_sel:[0,1,0] op_sel_hi:[1,1,1]
s_waitcnt lgkmcnt(10)
v_pk_fma_f32 v[52:53], v[148:149], v[208:209], v[52:53] op_sel:[0,1,0] op_sel_hi:[1,1,1]
v_pk_fma_f32 v[54:55], v[150:151], v[208:209], v[54:55] op_sel:[0,1,0] op_sel_hi:[1,1,1]
v_cvt_pk_bf16_f32 v48, v48, v49
v_cvt_pk_bf16_f32 v49, v50, v51
v_cvt_pk_bf16_f32 v50, v52, v53
v_cvt_pk_bf16_f32 v51, v54, v55
global_store_dwordx4 v22, v[48:51], s[50:51] offset:0 sc1
v_pk_mul_f32 v[56:57], v[56:57], v[208:209] op_sel_hi:[1,0]
v_pk_mul_f32 v[58:59], v[58:59], v[208:209] op_sel_hi:[1,0]
v_pk_mul_f32 v[60:61], v[60:61], v[208:209] op_sel_hi:[1,0]
v_pk_mul_f32 v[62:63], v[62:63], v[208:209] op_sel_hi:[1,0]
s_waitcnt lgkmcnt(9)
v_pk_fma_f32 v[56:57], v[152:153], v[208:209], v[56:57] op_sel:[0,1,0] op_sel_hi:[1,1,1]
v_pk_fma_f32 v[58:59], v[154:155], v[208:209], v[58:59] op_sel:[0,1,0] op_sel_hi:[1,1,1]
s_waitcnt lgkmcnt(8)
v_pk_fma_f32 v[60:61], v[156:157], v[208:209], v[60:61] op_sel:[0,1,0] op_sel_hi:[1,1,1]
v_pk_fma_f32 v[62:63], v[158:159], v[208:209], v[62:63] op_sel:[0,1,0] op_sel_hi:[1,1,1]
v_cvt_pk_bf16_f32 v56, v56, v57
v_cvt_pk_bf16_f32 v57, v58, v59
v_cvt_pk_bf16_f32 v58, v60, v61
v_cvt_pk_bf16_f32 v59, v62, v63
global_store_dwordx4 v22, v[56:59], s[50:51] offset:2048 sc1
v_pk_mul_f32 v[224:225], v[224:225], v[210:211] op_sel_hi:[1,0]
v_pk_mul_f32 v[226:227], v[226:227], v[210:211] op_sel_hi:[1,0]
v_pk_mul_f32 v[228:229], v[228:229], v[210:211] op_sel_hi:[1,0]
v_pk_mul_f32 v[230:231], v[230:231], v[210:211] op_sel_hi:[1,0]
s_waitcnt lgkmcnt(7)
v_pk_fma_f32 v[224:225], v[160:161], v[210:211], v[224:225] op_sel:[0,1,0] op_sel_hi:[1,1,1]
v_pk_fma_f32 v[226:227], v[162:163], v[210:211], v[226:227] op_sel:[0,1,0] op_sel_hi:[1,1,1]
s_waitcnt lgkmcnt(6)
v_pk_fma_f32 v[228:229], v[164:165], v[210:211], v[228:229] op_sel:[0,1,0] op_sel_hi:[1,1,1]
v_pk_fma_f32 v[230:231], v[166:167], v[210:211], v[230:231] op_sel:[0,1,0] op_sel_hi:[1,1,1]
v_cvt_pk_bf16_f32 v224, v224, v225
v_cvt_pk_bf16_f32 v225, v226, v227
v_cvt_pk_bf16_f32 v226, v228, v229
v_cvt_pk_bf16_f32 v227, v230, v231
global_store_dwordx4 v22, v[224:227], s[50:51] offset:-3072 sc1
v_pk_mul_f32 v[232:233], v[232:233], v[210:211] op_sel_hi:[1,0]
v_pk_mul_f32 v[234:235], v[234:235], v[210:211] op_sel_hi:[1,0]
v_pk_mul_f32 v[236:237], v[236:237], v[210:211] op_sel_hi:[1,0]
v_pk_mul_f32 v[238:239], v[238:239], v[210:211] op_sel_hi:[1,0]
s_waitcnt lgkmcnt(5)
v_pk_fma_f32 v[232:233], v[168:169], v[210:211], v[232:233] op_sel:[0,1,0] op_sel_hi:[1,1,1]
v_pk_fma_f32 v[234:235], v[170:171], v[210:211], v[234:235] op_sel:[0,1,0] op_sel_hi:[1,1,1]
s_waitcnt lgkmcnt(4)
v_pk_fma_f32 v[236:237], v[172:173], v[210:211], v[236:237] op_sel:[0,1,0] op_sel_hi:[1,1,1]
v_pk_fma_f32 v[238:239], v[174:175], v[210:211], v[238:239] op_sel:[0,1,0] op_sel_hi:[1,1,1]
v_cvt_pk_bf16_f32 v232, v232, v233
v_cvt_pk_bf16_f32 v233, v234, v235
v_cvt_pk_bf16_f32 v234, v236, v237
v_cvt_pk_bf16_f32 v235, v238, v239
global_store_dwordx4 v22, v[232:235], s[50:51] offset:-1024 sc1
v_pk_mul_f32 v[240:241], v[240:241], v[210:211] op_sel_hi:[1,0]
v_pk_mul_f32 v[242:243], v[242:243], v[210:211] op_sel_hi:[1,0]
v_pk_mul_f32 v[244:245], v[244:245], v[210:211] op_sel_hi:[1,0]
v_pk_mul_f32 v[246:247], v[246:247], v[210:211] op_sel_hi:[1,0]
s_waitcnt lgkmcnt(3)
v_pk_fma_f32 v[240:241], v[176:177], v[210:211], v[240:241] op_sel:[0,1,0] op_sel_hi:[1,1,1]
v_pk_fma_f32 v[242:243], v[178:179], v[210:211], v[242:243] op_sel:[0,1,0] op_sel_hi:[1,1,1]
s_waitcnt lgkmcnt(2)
v_pk_fma_f32 v[244:245], v[180:181], v[210:211], v[244:245] op_sel:[0,1,0] op_sel_hi:[1,1,1]
v_pk_fma_f32 v[246:247], v[182:183], v[210:211], v[246:247] op_sel:[0,1,0] op_sel_hi:[1,1,1]
v_cvt_pk_bf16_f32 v240, v240, v241
v_cvt_pk_bf16_f32 v241, v242, v243
v_cvt_pk_bf16_f32 v242, v244, v245
v_cvt_pk_bf16_f32 v243, v246, v247
global_store_dwordx4 v22, v[240:243], s[50:51] offset:1024 sc1
v_pk_mul_f32 v[248:249], v[248:249], v[210:211] op_sel_hi:[1,0]
v_pk_mul_f32 v[250:251], v[250:251], v[210:211] op_sel_hi:[1,0]
v_pk_mul_f32 v[252:253], v[252:253], v[210:211] op_sel_hi:[1,0]
v_pk_mul_f32 v[254:255], v[254:255], v[210:211] op_sel_hi:[1,0]
s_waitcnt lgkmcnt(1)
v_pk_fma_f32 v[248:249], v[184:185], v[210:211], v[248:249] op_sel:[0,1,0] op_sel_hi:[1,1,1]
v_pk_fma_f32 v[250:251], v[186:187], v[210:211], v[250:251] op_sel:[0,1,0] op_sel_hi:[1,1,1]
s_waitcnt lgkmcnt(0)
v_pk_fma_f32 v[252:253], v[188:189], v[210:211], v[252:253] op_sel:[0,1,0] op_sel_hi:[1,1,1]
v_pk_fma_f32 v[254:255], v[190:191], v[210:211], v[254:255] op_sel:[0,1,0] op_sel_hi:[1,1,1]
v_cvt_pk_bf16_f32 v248, v248, v249
v_cvt_pk_bf16_f32 v249, v250, v251
v_cvt_pk_bf16_f32 v250, v252, v253
v_cvt_pk_bf16_f32 v251, v254, v255
global_store_dwordx4 v22, v[248:251], s[50:51] offset:3072 sc1
s_cmp_eq_u32 s54, 16
s_cbranch_scc1 ENDR4Z_%=
s_branch ENDR2Z_%=
RSL0Z_%=:
s_cmp_lg_u32 s68, 0
s_cselect_b32 s77, 0xf149f2ca, 0
s_mov_b32 s88, 1
v_mov_b32 v24, v5
v_mov_b32 v25, v6
v_mov_b32 v26, v7
v_mov_b32 v27, v8
v_permlane32_swap_b32 v5, v24
v_permlane32_swap_b32 v6, v25
v_permlane32_swap_b32 v7, v26
v_permlane32_swap_b32 v8, v27
v_max_f32 v5, v5, v24
v_max_f32 v6, v6, v25
v_max_f32 v7, v7, v26
v_max_f32 v8, v8, v27
v_mov_b32 v24, v5
v_mov_b32 v25, v6
v_mov_b32 v26, v7
v_mov_b32 v27, v8
v_permlane16_swap_b32 v5, v24
v_permlane16_swap_b32 v6, v25
v_permlane16_swap_b32 v7, v26
v_permlane16_swap_b32 v8, v27
v_mov_b32 v29, s77
v_mov_b32 v30, 0xefa18f08
v_cmp_lt_f32 vcc, v14, v30
s_nop 1
v_cndmask_b32 v28, v29, v14, vcc
v_cndmask_b32 v14, v14, v10, vcc
v_max3_f32 v5, v5, v24, v28
v_cmp_lt_f32 vcc, v15, v30
s_nop 1
v_cndmask_b32 v28, v29, v15, vcc
v_cndmask_b32 v15, v15, v10, vcc
v_max3_f32 v6, v6, v25, v28
v_cmp_lt_f32 vcc, v16, v30
s_nop 1
v_cndmask_b32 v28, v29, v16, vcc
v_cndmask_b32 v16, v16, v10, vcc
v_max3_f32 v7, v7, v26, v28
v_cmp_lt_f32 vcc, v17, v30
s_nop 1
v_cndmask_b32 v28, v29, v17, vcc
v_cndmask_b32 v17, v17, v10, vcc
v_max3_f32 v8, v8, v27, v28
v_add_f32 v14, v14, v5
v_exp_f32 v208, -v5
v_add_f32 v15, v15, v6
)"
R"(v_exp_f32 v209, -v6
v_add_f32 v16, v16, v7
v_exp_f32 v210, -v7
v_add_f32 v17, v17, v8
v_exp_f32 v211, -v8
v_sub_f32 v64, v64, v5
v_sub_f32 v65, v65, v5
v_sub_f32 v66, v66, v5
v_sub_f32 v67, v67, v5
v_sub_f32 v72, v72, v6
v_sub_f32 v73, v73, v6
v_sub_f32 v74, v74, v6
v_sub_f32 v75, v75, v6
v_sub_f32 v80, v80, v7
v_sub_f32 v81, v81, v7
v_sub_f32 v82, v82, v7
v_sub_f32 v83, v83, v7
v_sub_f32 v88, v88, v8
v_sub_f32 v89, v89, v8
v_sub_f32 v90, v90, v8
v_sub_f32 v91, v91, v8
v_sub_f32 v68, v68, v5
v_sub_f32 v69, v69, v5
v_sub_f32 v70, v70, v5
v_sub_f32 v71, v71, v5
v_sub_f32 v76, v76, v6
v_sub_f32 v77, v77, v6
v_sub_f32 v78, v78, v6
v_sub_f32 v79, v79, v6
v_sub_f32 v84, v84, v7
v_sub_f32 v85, v85, v7
v_sub_f32 v86, v86, v7
v_sub_f32 v87, v87, v7
v_sub_f32 v92, v92, v8
v_sub_f32 v93, v93, v8
v_sub_f32 v94, v94, v8
v_sub_f32 v95, v95, v8
v_sub_f32 v96, v96, v5
v_sub_f32 v97, v97, v5
v_sub_f32 v98, v98, v5
v_sub_f32 v99, v99, v5
v_sub_f32 v104, v104, v6
v_sub_f32 v105, v105, v6
v_sub_f32 v106, v106, v6
v_sub_f32 v107, v107, v6
v_sub_f32 v112, v112, v7
v_sub_f32 v113, v113, v7
v_sub_f32 v114, v114, v7
v_sub_f32 v115, v115, v7
v_sub_f32 v120, v120, v8
v_sub_f32 v121, v121, v8
v_sub_f32 v122, v122, v8
v_sub_f32 v123, v123, v8
v_sub_f32 v100, v100, v5
v_sub_f32 v101, v101, v5
v_sub_f32 v102, v102, v5
v_sub_f32 v103, v103, v5
v_sub_f32 v108, v108, v6
v_sub_f32 v109, v109, v6
v_sub_f32 v110, v110, v6
v_sub_f32 v111, v111, v6
v_sub_f32 v116, v116, v7
v_sub_f32 v117, v117, v7
v_sub_f32 v118, v118, v7
v_sub_f32 v119, v119, v7
v_sub_f32 v124, v124, v8
v_sub_f32 v125, v125, v8
v_sub_f32 v126, v126, v8
v_sub_f32 v127, v127, v8
s_mov_b32 s58, s78
v_min_f32 v208, 1.0, v208
v_cmp_lt_f32 vcc, v14, v30
s_nop 1
v_cndmask_b32 v24, v14, v10, vcc
s_cmp_lg_u64 vcc, 0
s_cselect_b32 s58, 0xff800000, s58
v_min_f32 v209, 1.0, v209
v_cmp_lt_f32 vcc, v15, v30
s_nop 1
v_cndmask_b32 v25, v15, v10, vcc
s_cmp_lg_u64 vcc, 0
s_cselect_b32 s58, 0xff800000, s58
v_min_f32 v210, 1.0, v210
v_cmp_lt_f32 vcc, v16, v30
s_nop 1
v_cndmask_b32 v26, v16, v10, vcc
s_cmp_lg_u64 vcc, 0
s_cselect_b32 s58, 0xff800000, s58
v_min_f32 v211, 1.0, v211
v_cmp_lt_f32 vcc, v17, v30
s_nop 1
v_cndmask_b32 v27, v17, v10, vcc
s_cmp_lg_u64 vcc, 0
s_cselect_b32 s58, 0xff800000, s58
v_sub_f32 v192, 0, v24
v_sub_f32 v193, 0, v24
v_sub_f32 v194, 0, v24
v_sub_f32 v195, 0, v24
v_sub_f32 v196, 0, v25
v_sub_f32 v197, 0, v25
v_sub_f32 v198, 0, v25
v_sub_f32 v199, 0, v25
v_sub_f32 v200, 0, v26
v_sub_f32 v201, 0, v26
v_sub_f32 v202, 0, v26
v_sub_f32 v203, 0, v26
v_sub_f32 v204, 0, v27
v_sub_f32 v205, 0, v27
v_sub_f32 v206, 0, v27
v_sub_f32 v207, 0, v27
s_cmp_lg_u32 s68, 0
s_mov_b32 s68, 0
s_cbranch_scc1 RARER0Z_%=
s_mov_b32 s69, 1
s_bitset1_b32 s57, s54
s_bitset1_b32 s27, s54
s_branch RARER0Z_%=
RSL1Z_%=:
s_cmp_lg_u32 s68, 0
s_cselect_b32 s77, 0xf149f2ca, 0
s_mov_b32 s88, 1
v_mov_b32 v24, v5
v_mov_b32 v25, v6
v_mov_b32 v26, v7
v_mov_b32 v27, v8
v_permlane32_swap_b32 v5, v24
v_permlane32_swap_b32 v6, v25
v_permlane32_swap_b32 v7, v26
v_permlane32_swap_b32 v8, v27
v_max_f32 v5, v5, v24
v_max_f32 v6, v6, v25
v_max_f32 v7, v7, v26
v_max_f32 v8, v8, v27
v_mov_b32 v24, v5
v_mov_b32 v25, v6
v_mov_b32 v26, v7
v_mov_b32 v27, v8
v_permlane16_swap_b32 v5, v24
v_permlane16_swap_b32 v6, v25
v_permlane16_swap_b32 v7, v26
v_permlane16_swap_b32 v8, v27
v_mov_b32 v29, s77
v_mov_b32 v30, 0xefa18f08
v_cmp_lt_f32 vcc, v14, v30
s_nop 1
v_cndmask_b32 v28, v29, v14, vcc
v_cndmask_b32 v14, v14, v10, vcc
v_max3_f32 v5, v5, v24, v28
v_cmp_lt_f32 vcc, v15, v30
s_nop 1
v_cndmask_b32 v28, v29, v15, vcc
v_cndmask_b32 v15, v15, v10, vcc
v_max3_f32 v6, v6, v25, v28
v_cmp_lt_f32 vcc, v16, v30
s_nop 1
v_cndmask_b32 v28, v29, v16, vcc
v_cndmask_b32 v16, v16, v10, vcc
v_max3_f32 v7, v7, v26, v28
v_cmp_lt_f32 vcc, v17, v30
s_nop 1
v_cndmask_b32 v28, v29, v17, vcc
v_cndmask_b32 v17, v17, v10, vcc
v_max3_f32 v8, v8, v27, v28
v_add_f32 v14, v14, v5
v_exp_f32 v208, -v5
v_add_f32 v15, v15, v6
v_exp_f32 v209, -v6
v_add_f32 v16, v16, v7
v_exp_f32 v210, -v7
v_add_f32 v17, v17, v8
v_exp_f32 v211, -v8
v_sub_f32 v128, v128, v5
v_sub_f32 v129, v129, v5
v_sub_f32 v130, v130, v5
v_sub_f32 v131, v131, v5
v_sub_f32 v136, v136, v6
v_sub_f32 v137, v137, v6
v_sub_f32 v138, v138, v6
v_sub_f32 v139, v139, v6
v_sub_f32 v144, v144, v7
v_sub_f32 v145, v145, v7
v_sub_f32 v146, v146, v7
v_sub_f32 v147, v147, v7
v_sub_f32 v152, v152, v8
v_sub_f32 v153, v153, v8
v_sub_f32 v154, v154, v8
v_sub_f32 v155, v155, v8
v_sub_f32 v132, v132, v5
v_sub_f32 v133, v133, v5
v_sub_f32 v134, v134, v5
v_sub_f32 v135, v135, v5
v_sub_f32 v140, v140, v6
v_sub_f32 v141, v141, v6
v_sub_f32 v142, v142, v6
v_sub_f32 v143, v143, v6
v_sub_f32 v148, v148, v7
v_sub_f32 v149, v149, v7
v_sub_f32 v150, v150, v7
v_sub_f32 v151, v151, v7
v_sub_f32 v156, v156, v8
v_sub_f32 v157, v157, v8
v_sub_f32 v158, v158, v8
v_sub_f32 v159, v159, v8
v_sub_f32 v160, v160, v5
v_sub_f32 v161, v161, v5
v_sub_f32 v162, v162, v5
v_sub_f32 v163, v163, v5
v_sub_f32 v168, v168, v6
v_sub_f32 v169, v169, v6
v_sub_f32 v170, v170, v6
v_sub_f32 v171, v171, v6
v_sub_f32 v176, v176, v7
v_sub_f32 v177, v177, v7
v_sub_f32 v178, v178, v7
v_sub_f32 v179, v179, v7
v_sub_f32 v184, v184, v8
v_sub_f32 v185, v185, v8
v_sub_f32 v186, v186, v8
v_sub_f32 v187, v187, v8
v_sub_f32 v164, v164, v5
v_sub_f32 v165, v165, v5
v_sub_f32 v166, v166, v5
v_sub_f32 v167, v167, v5
v_sub_f32 v172, v172, v6
v_sub_f32 v173, v173, v6
v_sub_f32 v174, v174, v6
v_sub_f32 v175, v175, v6
v_sub_f32 v180, v180, v7
v_sub_f32 v181, v181, v7
v_sub_f32 v182, v182, v7
v_sub_f32 v183, v183, v7
v_sub_f32 v188, v188, v8
v_sub_f32 v189, v189, v8
v_sub_f32 v190, v190, v8
v_sub_f32 v191, v191, v8
s_mov_b32 s58, s78
v_min_f32 v208, 1.0, v208
v_cmp_lt_f32 vcc, v14, v30
s_nop 1
v_cndmask_b32 v24, v14, v10, vcc
s_cmp_lg_u64 vcc, 0
s_cselect_b32 s58, 0xff800000, s58
v_min_f32 v209, 1.0, v209
v_cmp_lt_f32 vcc, v15, v30
s_nop 1
v_cndmask_b32 v25, v15, v10, vcc
s_cmp_lg_u64 vcc, 0
s_cselect_b32 s58, 0xff800000, s58
v_min_f32 v210, 1.0, v210
v_cmp_lt_f32 vcc, v16, v30
s_nop 1
v_cndmask_b32 v26, v16, v10, vcc
s_cmp_lg_u64 vcc, 0
s_cselect_b32 s58, 0xff800000, s58
v_min_f32 v211, 1.0, v211
v_cmp_lt_f32 vcc, v17, v30
s_nop 1
v_cndmask_b32 v27, v17, v10, vcc
s_cmp_lg_u64 vcc, 0
s_cselect_b32 s58, 0xff800000, s58
v_sub_f32 v192, 0, v24
v_sub_f32 v193, 0, v24
v_sub_f32 v194, 0, v24
v_sub_f32 v195, 0, v24
v_sub_f32 v196, 0, v25
v_sub_f32 v197, 0, v25
v_sub_f32 v198, 0, v25
v_sub_f32 v199, 0, v25
v_sub_f32 v200, 0, v26
v_sub_f32 v201, 0, v26
v_sub_f32 v202, 0, v26
v_sub_f32 v203, 0, v26
v_sub_f32 v204, 0, v27
v_sub_f32 v205, 0, v27
v_sub_f32 v206, 0, v27
v_sub_f32 v207, 0, v27
s_cmp_lg_u32 s68, 0
s_mov_b32 s68, 0
s_cbranch_scc1 RARER1Z_%=
s_mov_b32 s69, 1
s_bitset1_b32 s57, s54
s_bitset1_b32 s27, s54
s_branch RARER1Z_%=
LF1Z_%=:
s_mov_b32 s76, -1.0
s_add_u32 s66, s66, 0x10000
s_addc_u32 s67, s67, 0
s_branch LF1RZ_%=
RSC1Z_%=:
s_nop 7
v_accvgpr_read_b32 v32, a0
v_accvgpr_read_b32 v33, a1
v_accvgpr_read_b32 v34, a2
v_accvgpr_read_b32 v35, a3
v_accvgpr_read_b32 v36, a4
v_accvgpr_read_b32 v37, a5
v_accvgpr_read_b32 v38, a6
v_accvgpr_read_b32 v39, a7
v_mul_f32 v32, v32, v208
v_mul_f32 v33, v33, v208
v_mul_f32 v34, v34, v208
v_mul_f32 v35, v35, v208
v_mul_f32 v36, v36, v208
v_mul_f32 v37, v37, v208
v_mul_f32 v38, v38, v208
v_mul_f32 v39, v39, v208
v_accvgpr_write_b32 a0, v32
v_accvgpr_write_b32 a1, v33
v_accvgpr_write_b32 a2, v34
v_accvgpr_write_b32 a3, v35
v_accvgpr_write_b32 a4, v36
v_accvgpr_write_b32 a5, v37
v_accvgpr_write_b32 a6, v38
v_accvgpr_write_b32 a7, v39
v_accvgpr_read_b32 v32, a8
v_accvgpr_read_b32 v33, a9
v_accvgpr_read_b32 v34, a10
v_accvgpr_read_b32 v35, a11
v_accvgpr_read_b32 v36, a12
v_accvgpr_read_b32 v37, a13
v_accvgpr_read_b32 v38, a14
v_accvgpr_read_b32 v39, a15
v_mul_f32 v32, v32, v208
v_mul_f32 v33, v33, v208
v_mul_f32 v34, v34, v208
v_mul_f32 v35, v35, v208
v_mul_f32 v36, v36, v208
v_mul_f32 v37, v37, v208
v_mul_f32 v38, v38, v208
v_mul_f32 v39, v39, v208
v_accvgpr_write_b32 a8, v32
v_accvgpr_write_b32 a9, v33
v_accvgpr_write_b32 a10, v34
v_accvgpr_write_b32 a11, v35
v_accvgpr_write_b32 a12, v36
v_accvgpr_write_b32 a13, v37
v_accvgpr_write_b32 a14, v38
v_accvgpr_write_b32 a15, v39
v_accvgpr_read_b32 v32, a16
v_accvgpr_read_b32 v33, a17
v_accvgpr_read_b32 v34, a18
v_accvgpr_read_b32 v35, a19
v_accvgpr_read_b32 v36, a20
v_accvgpr_read_b32 v37, a21
v_accvgpr_read_b32 v38, a22
v_accvgpr_read_b32 v39, a23
v_mul_f32 v32, v32, v208
v_mul_f32 v33, v33, v208
v_mul_f32 v34, v34, v208
v_mul_f32 v35, v35, v208
v_mul_f32 v36, v36, v208
v_mul_f32 v37, v37, v208
v_mul_f32 v38, v38, v208
v_mul_f32 v39, v39, v208
v_accvgpr_write_b32 a16, v32
v_accvgpr_write_b32 a17, v33
v_accvgpr_write_b32 a18, v34
v_accvgpr_write_b32 a19, v35
v_accvgpr_write_b32 a20, v36
v_accvgpr_write_b32 a21, v37
v_accvgpr_write_b32 a22, v38
v_accvgpr_write_b32 a23, v39
v_accvgpr_read_b32 v32, a24
v_accvgpr_read_b32 v33, a25
v_accvgpr_read_b32 v34, a26
v_accvgpr_read_b32 v35, a27
v_accvgpr_read_b32 v36, a28
v_accvgpr_read_b32 v37, a29
v_accvgpr_read_b32 v38, a30
v_accvgpr_read_b32 v39, a31
v_mul_f32 v32, v32, v208
v_mul_f32 v33, v33, v208
v_mul_f32 v34, v34, v208
v_mul_f32 v35, v35, v208
v_mul_f32 v36, v36, v208
v_mul_f32 v37, v37, v208
v_mul_f32 v38, v38, v208
v_mul_f32 v39, v39, v208
v_accvgpr_write_b32 a24, v32
v_accvgpr_write_b32 a25, v33
v_accvgpr_write_b32 a26, v34
v_accvgpr_write_b32 a27, v35
v_accvgpr_write_b32 a28, v36
v_accvgpr_write_b32 a29, v37
v_accvgpr_write_b32 a30, v38
v_accvgpr_write_b32 a31, v39
v_accvgpr_read_b32 v32, a224
v_accvgpr_read_b32 v33, a225
v_accvgpr_read_b32 v34, a226
v_accvgpr_read_b32 v35, a227
v_mul_f32 v32, v32, v208
v_mul_f32 v33, v33, v208
v_mul_f32 v34, v34, v208
v_mul_f32 v35, v35, v208
v_accvgpr_write_b32 a224, v32
v_accvgpr_write_b32 a225, v33
v_accvgpr_write_b32 a226, v34
v_accvgpr_write_b32 a227, v35
v_accvgpr_read_b32 v32, a32
v_accvgpr_read_b32 v33, a33
)"
R"(v_accvgpr_read_b32 v34, a34
v_accvgpr_read_b32 v35, a35
v_accvgpr_read_b32 v36, a36
v_accvgpr_read_b32 v37, a37
v_accvgpr_read_b32 v38, a38
v_accvgpr_read_b32 v39, a39
v_mul_f32 v32, v32, v209
v_mul_f32 v33, v33, v209
v_mul_f32 v34, v34, v209
v_mul_f32 v35, v35, v209
v_mul_f32 v36, v36, v209
v_mul_f32 v37, v37, v209
v_mul_f32 v38, v38, v209
v_mul_f32 v39, v39, v209
v_accvgpr_write_b32 a32, v32
v_accvgpr_write_b32 a33, v33
v_accvgpr_write_b32 a34, v34
v_accvgpr_write_b32 a35, v35
v_accvgpr_write_b32 a36, v36
v_accvgpr_write_b32 a37, v37
v_accvgpr_write_b32 a38, v38
v_accvgpr_write_b32 a39, v39
v_accvgpr_read_b32 v32, a40
v_accvgpr_read_b32 v33, a41
v_accvgpr_read_b32 v34, a42
v_accvgpr_read_b32 v35, a43
v_accvgpr_read_b32 v36, a44
v_accvgpr_read_b32 v37, a45
v_accvgpr_read_b32 v38, a46
v_accvgpr_read_b32 v39, a47
v_mul_f32 v32, v32, v209
v_mul_f32 v33, v33, v209
v_mul_f32 v34, v34, v209
v_mul_f32 v35, v35, v209
v_mul_f32 v36, v36, v209
v_mul_f32 v37, v37, v209
v_mul_f32 v38, v38, v209
v_mul_f32 v39, v39, v209
v_accvgpr_write_b32 a40, v32
v_accvgpr_write_b32 a41, v33
v_accvgpr_write_b32 a42, v34
v_accvgpr_write_b32 a43, v35
v_accvgpr_write_b32 a44, v36
v_accvgpr_write_b32 a45, v37
v_accvgpr_write_b32 a46, v38
v_accvgpr_write_b32 a47, v39
v_accvgpr_read_b32 v32, a48
v_accvgpr_read_b32 v33, a49
v_accvgpr_read_b32 v34, a50
v_accvgpr_read_b32 v35, a51
v_accvgpr_read_b32 v36, a52
v_accvgpr_read_b32 v37, a53
v_accvgpr_read_b32 v38, a54
v_accvgpr_read_b32 v39, a55
v_mul_f32 v32, v32, v209
v_mul_f32 v33, v33, v209
v_mul_f32 v34, v34, v209
v_mul_f32 v35, v35, v209
v_mul_f32 v36, v36, v209
v_mul_f32 v37, v37, v209
v_mul_f32 v38, v38, v209
v_mul_f32 v39, v39, v209
v_accvgpr_write_b32 a48, v32
v_accvgpr_write_b32 a49, v33
v_accvgpr_write_b32 a50, v34
v_accvgpr_write_b32 a51, v35
v_accvgpr_write_b32 a52, v36
v_accvgpr_write_b32 a53, v37
v_accvgpr_write_b32 a54, v38
v_accvgpr_write_b32 a55, v39
v_accvgpr_read_b32 v32, a56
v_accvgpr_read_b32 v33, a57
v_accvgpr_read_b32 v34, a58
v_accvgpr_read_b32 v35, a59
v_accvgpr_read_b32 v36, a60
v_accvgpr_read_b32 v37, a61
v_accvgpr_read_b32 v38, a62
v_accvgpr_read_b32 v39, a63
v_mul_f32 v32, v32, v209
v_mul_f32 v33, v33, v209
v_mul_f32 v34, v34, v209
v_mul_f32 v35, v35, v209
v_mul_f32 v36, v36, v209
v_mul_f32 v37, v37, v209
v_mul_f32 v38, v38, v209
v_mul_f32 v39, v39, v209
v_accvgpr_write_b32 a56, v32
v_accvgpr_write_b32 a57, v33
v_accvgpr_write_b32 a58, v34
v_accvgpr_write_b32 a59, v35
v_accvgpr_write_b32 a60, v36
v_accvgpr_write_b32 a61, v37
v_accvgpr_write_b32 a62, v38
v_accvgpr_write_b32 a63, v39
v_accvgpr_read_b32 v32, a228
v_accvgpr_read_b32 v33, a229
v_accvgpr_read_b32 v34, a230
v_accvgpr_read_b32 v35, a231
v_mul_f32 v32, v32, v209
v_mul_f32 v33, v33, v209
v_mul_f32 v34, v34, v209
v_mul_f32 v35, v35, v209
v_accvgpr_write_b32 a228, v32
v_accvgpr_write_b32 a229, v33
v_accvgpr_write_b32 a230, v34
v_accvgpr_write_b32 a231, v35
v_accvgpr_read_b32 v32, a64
v_accvgpr_read_b32 v33, a65
v_accvgpr_read_b32 v34, a66
v_accvgpr_read_b32 v35, a67
v_accvgpr_read_b32 v36, a68
v_accvgpr_read_b32 v37, a69
v_accvgpr_read_b32 v38, a70
v_accvgpr_read_b32 v39, a71
v_mul_f32 v32, v32, v210
v_mul_f32 v33, v33, v210
v_mul_f32 v34, v34, v210
v_mul_f32 v35, v35, v210
v_mul_f32 v36, v36, v210
v_mul_f32 v37, v37, v210
v_mul_f32 v38, v38, v210
v_mul_f32 v39, v39, v210
v_accvgpr_write_b32 a64, v32
v_accvgpr_write_b32 a65, v33
v_accvgpr_write_b32 a66, v34
v_accvgpr_write_b32 a67, v35
v_accvgpr_write_b32 a68, v36
v_accvgpr_write_b32 a69, v37
v_accvgpr_write_b32 a70, v38
v_accvgpr_write_b32 a71, v39
v_accvgpr_read_b32 v32, a72
v_accvgpr_read_b32 v33, a73
v_accvgpr_read_b32 v34, a74
v_accvgpr_read_b32 v35, a75
v_accvgpr_read_b32 v36, a76
v_accvgpr_read_b32 v37, a77
v_accvgpr_read_b32 v38, a78
v_accvgpr_read_b32 v39, a79
v_mul_f32 v32, v32, v210
v_mul_f32 v33, v33, v210
v_mul_f32 v34, v34, v210
v_mul_f32 v35, v35, v210
v_mul_f32 v36, v36, v210
v_mul_f32 v37, v37, v210
v_mul_f32 v38, v38, v210
v_mul_f32 v39, v39, v210
v_accvgpr_write_b32 a72, v32
v_accvgpr_write_b32 a73, v33
v_accvgpr_write_b32 a74, v34
v_accvgpr_write_b32 a75, v35
v_accvgpr_write_b32 a76, v36
v_accvgpr_write_b32 a77, v37
v_accvgpr_write_b32 a78, v38
v_accvgpr_write_b32 a79, v39
v_accvgpr_read_b32 v32, a80
v_accvgpr_read_b32 v33, a81
v_accvgpr_read_b32 v34, a82
v_accvgpr_read_b32 v35, a83
v_accvgpr_read_b32 v36, a84
v_accvgpr_read_b32 v37, a85
v_accvgpr_read_b32 v38, a86
v_accvgpr_read_b32 v39, a87
v_mul_f32 v32, v32, v210
v_mul_f32 v33, v33, v210
v_mul_f32 v34, v34, v210
v_mul_f32 v35, v35, v210
v_mul_f32 v36, v36, v210
v_mul_f32 v37, v37, v210
v_mul_f32 v38, v38, v210
v_mul_f32 v39, v39, v210
v_accvgpr_write_b32 a80, v32
v_accvgpr_write_b32 a81, v33
v_accvgpr_write_b32 a82, v34
v_accvgpr_write_b32 a83, v35
v_accvgpr_write_b32 a84, v36
v_accvgpr_write_b32 a85, v37
v_accvgpr_write_b32 a86, v38
v_accvgpr_write_b32 a87, v39
v_accvgpr_read_b32 v32, a88
v_accvgpr_read_b32 v33, a89
v_accvgpr_read_b32 v34, a90
v_accvgpr_read_b32 v35, a91
v_accvgpr_read_b32 v36, a92
v_accvgpr_read_b32 v37, a93
v_accvgpr_read_b32 v38, a94
v_accvgpr_read_b32 v39, a95
v_mul_f32 v32, v32, v210
v_mul_f32 v33, v33, v210
v_mul_f32 v34, v34, v210
v_mul_f32 v35, v35, v210
v_mul_f32 v36, v36, v210
v_mul_f32 v37, v37, v210
v_mul_f32 v38, v38, v210
v_mul_f32 v39, v39, v210
v_accvgpr_write_b32 a88, v32
v_accvgpr_write_b32 a89, v33
v_accvgpr_write_b32 a90, v34
v_accvgpr_write_b32 a91, v35
v_accvgpr_write_b32 a92, v36
v_accvgpr_write_b32 a93, v37
v_accvgpr_write_b32 a94, v38
v_accvgpr_write_b32 a95, v39
v_accvgpr_read_b32 v32, a232
v_accvgpr_read_b32 v33, a233
v_accvgpr_read_b32 v34, a234
v_accvgpr_read_b32 v35, a235
v_mul_f32 v32, v32, v210
v_mul_f32 v33, v33, v210
v_mul_f32 v34, v34, v210
v_mul_f32 v35, v35, v210
v_accvgpr_write_b32 a232, v32
v_accvgpr_write_b32 a233, v33
v_accvgpr_write_b32 a234, v34
v_accvgpr_write_b32 a235, v35
v_accvgpr_read_b32 v32, a96
v_accvgpr_read_b32 v33, a97
v_accvgpr_read_b32 v34, a98
v_accvgpr_read_b32 v35, a99
v_accvgpr_read_b32 v36, a100
v_accvgpr_read_b32 v37, a101
v_accvgpr_read_b32 v38, a102
v_accvgpr_read_b32 v39, a103
v_mul_f32 v32, v32, v211
v_mul_f32 v33, v33, v211
v_mul_f32 v34, v34, v211
v_mul_f32 v35, v35, v211
v_mul_f32 v36, v36, v211
v_mul_f32 v37, v37, v211
v_mul_f32 v38, v38, v211
v_mul_f32 v39, v39, v211
v_accvgpr_write_b32 a96, v32
v_accvgpr_write_b32 a97, v33
v_accvgpr_write_b32 a98, v34
v_accvgpr_write_b32 a99, v35
v_accvgpr_write_b32 a100, v36
v_accvgpr_write_b32 a101, v37
v_accvgpr_write_b32 a102, v38
v_accvgpr_write_b32 a103, v39
v_accvgpr_read_b32 v32, a104
v_accvgpr_read_b32 v33, a105
v_accvgpr_read_b32 v34, a106
v_accvgpr_read_b32 v35, a107
v_accvgpr_read_b32 v36, a108
v_accvgpr_read_b32 v37, a109
v_accvgpr_read_b32 v38, a110
v_accvgpr_read_b32 v39, a111
v_mul_f32 v32, v32, v211
v_mul_f32 v33, v33, v211
v_mul_f32 v34, v34, v211
v_mul_f32 v35, v35, v211
v_mul_f32 v36, v36, v211
v_mul_f32 v37, v37, v211
v_mul_f32 v38, v38, v211
v_mul_f32 v39, v39, v211
v_accvgpr_write_b32 a104, v32
v_accvgpr_write_b32 a105, v33
v_accvgpr_write_b32 a106, v34
v_accvgpr_write_b32 a107, v35
v_accvgpr_write_b32 a108, v36
v_accvgpr_write_b32 a109, v37
v_accvgpr_write_b32 a110, v38
v_accvgpr_write_b32 a111, v39
v_accvgpr_read_b32 v32, a112
v_accvgpr_read_b32 v33, a113
v_accvgpr_read_b32 v34, a114
v_accvgpr_read_b32 v35, a115
v_accvgpr_read_b32 v36, a116
v_accvgpr_read_b32 v37, a117
v_accvgpr_read_b32 v38, a118
v_accvgpr_read_b32 v39, a119
v_mul_f32 v32, v32, v211
v_mul_f32 v33, v33, v211
v_mul_f32 v34, v34, v211
v_mul_f32 v35, v35, v211
v_mul_f32 v36, v36, v211
v_mul_f32 v37, v37, v211
v_mul_f32 v38, v38, v211
v_mul_f32 v39, v39, v211
v_accvgpr_write_b32 a112, v32
v_accvgpr_write_b32 a113, v33
v_accvgpr_write_b32 a114, v34
v_accvgpr_write_b32 a115, v35
v_accvgpr_write_b32 a116, v36
v_accvgpr_write_b32 a117, v37
v_accvgpr_write_b32 a118, v38
v_accvgpr_write_b32 a119, v39
v_accvgpr_read_b32 v32, a120
v_accvgpr_read_b32 v33, a121
v_accvgpr_read_b32 v34, a122
v_accvgpr_read_b32 v35, a123
v_accvgpr_read_b32 v36, a124
v_accvgpr_read_b32 v37, a125
v_accvgpr_read_b32 v38, a126
v_accvgpr_read_b32 v39, a127
v_mul_f32 v32, v32, v211
v_mul_f32 v33, v33, v211
v_mul_f32 v34, v34, v211
v_mul_f32 v35, v35, v211
v_mul_f32 v36, v36, v211
v_mul_f32 v37, v37, v211
v_mul_f32 v38, v38, v211
v_mul_f32 v39, v39, v211
v_accvgpr_write_b32 a120, v32
v_accvgpr_write_b32 a121, v33
v_accvgpr_write_b32 a122, v34
v_accvgpr_write_b32 a123, v35
v_accvgpr_write_b32 a124, v36
v_accvgpr_write_b32 a125, v37
v_accvgpr_write_b32 a126, v38
v_accvgpr_write_b32 a127, v39
v_accvgpr_read_b32 v32, a236
v_accvgpr_read_b32 v33, a237
v_accvgpr_read_b32 v34, a238
v_accvgpr_read_b32 v35, a239
v_mul_f32 v32, v32, v211
v_mul_f32 v33, v33, v211
v_mul_f32 v34, v34, v211
v_mul_f32 v35, v35, v211
v_accvgpr_write_b32 a236, v32
v_accvgpr_write_b32 a237, v33
v_accvgpr_write_b32 a238, v34
v_accvgpr_write_b32 a239, v35
s_mov_b32 s69, 0
s_branch ENP1Z_%=
RSL2Z_%=:
s_cmp_lg_u32 s68, 0
s_cselect_b32 s77, 0xf149f2ca, 0
s_mov_b32 s88, 1
v_mov_b32 v24, v5
v_mov_b32 v25, v6
v_mov_b32 v26, v7
v_mov_b32 v27, v8
v_permlane32_swap_b32 v5, v24
v_permlane32_swap_b32 v6, v25
v_permlane32_swap_b32 v7, v26
v_permlane32_swap_b32 v8, v27
v_max_f32 v5, v5, v24
v_max_f32 v6, v6, v25
v_max_f32 v7, v7, v26
v_max_f32 v8, v8, v27
v_mov_b32 v24, v5
v_mov_b32 v25, v6
v_mov_b32 v26, v7
v_mov_b32 v27, v8
v_permlane16_swap_b32 v5, v24
v_permlane16_swap_b32 v6, v25
v_permlane16_swap_b32 v7, v26
v_permlane16_swap_b32 v8, v27
v_mov_b32 v29, s77
v_mov_b32 v30, 0xefa18f08
v_cmp_lt_f32 vcc, v14, v30
s_nop 1
v_cndmask_b32 v28, v29, v14, vcc
v_cndmask_b32 v14, v14, v10, vcc
v_max3_f32 v5, v5, v24, v28
v_cmp_lt_f32 vcc, v15, v30
s_nop 1
v_cndmask_b32 v28, v29, v15, vcc
v_cndmask_b32 v15, v15, v10, vcc
v_max3_f32 v6, v6, v25, v28
v_cmp_lt_f32 vcc, v16, v30
s_nop 1
v_cndmask_b32 v28, v29, v16, vcc
v_cndmask_b32 v16, v16, v10, vcc
v_max3_f32 v7, v7, v26, v28
v_cmp_lt_f32 vcc, v17, v30
s_nop 1
v_cndmask_b32 v28, v29, v17, vcc
v_cndmask_b32 v17, v17, v10, vcc
v_max3_f32 v8, v8, v27, v28
v_add_f32 v14, v14, v5
v_exp_f32 v208, -v5
v_add_f32 v15, v15, v6
v_exp_f32 v209, -v6
v_add_f32 v16, v16, v7
v_exp_f32 v210, -v7
v_add_f32 v17, v17, v8
v_exp_f32 v211, -v8
v_sub_f32 v64, v64, v5
v_sub_f32 v65, v65, v5
v_sub_f32 v66, v66, v5
v_sub_f32 v67, v67, v5
v_sub_f32 v72, v72, v6
v_sub_f32 v73, v73, v6
v_sub_f32 v74, v74, v6
v_sub_f32 v75, v75, v6
v_sub_f32 v80, v80, v7
v_sub_f32 v81, v81, v7
v_sub_f32 v82, v82, v7
v_sub_f32 v83, v83, v7
v_sub_f32 v88, v88, v8
v_sub_f32 v89, v89, v8
v_sub_f32 v90, v90, v8
v_sub_f32 v91, v91, v8
v_sub_f32 v68, v68, v5
v_sub_f32 v69, v69, v5
v_sub_f32 v70, v70, v5
v_sub_f32 v71, v71, v5
v_sub_f32 v76, v76, v6
v_sub_f32 v77, v77, v6
)"
R"(v_sub_f32 v78, v78, v6
v_sub_f32 v79, v79, v6
v_sub_f32 v84, v84, v7
v_sub_f32 v85, v85, v7
v_sub_f32 v86, v86, v7
v_sub_f32 v87, v87, v7
v_sub_f32 v92, v92, v8
v_sub_f32 v93, v93, v8
v_sub_f32 v94, v94, v8
v_sub_f32 v95, v95, v8
v_sub_f32 v96, v96, v5
v_sub_f32 v97, v97, v5
v_sub_f32 v98, v98, v5
v_sub_f32 v99, v99, v5
v_sub_f32 v104, v104, v6
v_sub_f32 v105, v105, v6
v_sub_f32 v106, v106, v6
v_sub_f32 v107, v107, v6
v_sub_f32 v112, v112, v7
v_sub_f32 v113, v113, v7
v_sub_f32 v114, v114, v7
v_sub_f32 v115, v115, v7
v_sub_f32 v120, v120, v8
v_sub_f32 v121, v121, v8
v_sub_f32 v122, v122, v8
v_sub_f32 v123, v123, v8
v_sub_f32 v100, v100, v5
v_sub_f32 v101, v101, v5
v_sub_f32 v102, v102, v5
v_sub_f32 v103, v103, v5
v_sub_f32 v108, v108, v6
v_sub_f32 v109, v109, v6
v_sub_f32 v110, v110, v6
v_sub_f32 v111, v111, v6
v_sub_f32 v116, v116, v7
v_sub_f32 v117, v117, v7
v_sub_f32 v118, v118, v7
v_sub_f32 v119, v119, v7
v_sub_f32 v124, v124, v8
v_sub_f32 v125, v125, v8
v_sub_f32 v126, v126, v8
v_sub_f32 v127, v127, v8
s_mov_b32 s58, s78
v_min_f32 v208, 1.0, v208
v_cmp_lt_f32 vcc, v14, v30
s_nop 1
v_cndmask_b32 v24, v14, v10, vcc
s_cmp_lg_u64 vcc, 0
s_cselect_b32 s58, 0xff800000, s58
v_min_f32 v209, 1.0, v209
v_cmp_lt_f32 vcc, v15, v30
s_nop 1
v_cndmask_b32 v25, v15, v10, vcc
s_cmp_lg_u64 vcc, 0
s_cselect_b32 s58, 0xff800000, s58
v_min_f32 v210, 1.0, v210
v_cmp_lt_f32 vcc, v16, v30
s_nop 1
v_cndmask_b32 v26, v16, v10, vcc
s_cmp_lg_u64 vcc, 0
s_cselect_b32 s58, 0xff800000, s58
v_min_f32 v211, 1.0, v211
v_cmp_lt_f32 vcc, v17, v30
s_nop 1
v_cndmask_b32 v27, v17, v10, vcc
s_cmp_lg_u64 vcc, 0
s_cselect_b32 s58, 0xff800000, s58
v_sub_f32 v192, 0, v24
v_sub_f32 v193, 0, v24
v_sub_f32 v194, 0, v24
v_sub_f32 v195, 0, v24
v_sub_f32 v196, 0, v25
v_sub_f32 v197, 0, v25
v_sub_f32 v198, 0, v25
v_sub_f32 v199, 0, v25
v_sub_f32 v200, 0, v26
v_sub_f32 v201, 0, v26
v_sub_f32 v202, 0, v26
v_sub_f32 v203, 0, v26
v_sub_f32 v204, 0, v27
v_sub_f32 v205, 0, v27
v_sub_f32 v206, 0, v27
v_sub_f32 v207, 0, v27
s_cmp_lg_u32 s68, 0
s_mov_b32 s68, 0
s_cbranch_scc1 RARER2Z_%=
s_mov_b32 s69, 1
s_bitset1_b32 s57, s54
s_bitset1_b32 s27, s54
s_branch RARER2Z_%=
LF0Z_%=:
s_mov_b32 s76, -1.0
s_add_u32 s66, s66, 0x10000
s_addc_u32 s67, s67, 0
s_branch LF0RZ_%=
RSC2Z_%=:
s_nop 7
v_accvgpr_read_b32 v32, a0
v_accvgpr_read_b32 v33, a1
v_accvgpr_read_b32 v34, a2
v_accvgpr_read_b32 v35, a3
v_accvgpr_read_b32 v36, a4
v_accvgpr_read_b32 v37, a5
v_accvgpr_read_b32 v38, a6
v_accvgpr_read_b32 v39, a7
v_mul_f32 v32, v32, v208
v_mul_f32 v33, v33, v208
v_mul_f32 v34, v34, v208
v_mul_f32 v35, v35, v208
v_mul_f32 v36, v36, v208
v_mul_f32 v37, v37, v208
v_mul_f32 v38, v38, v208
v_mul_f32 v39, v39, v208
v_accvgpr_write_b32 a0, v32
v_accvgpr_write_b32 a1, v33
v_accvgpr_write_b32 a2, v34
v_accvgpr_write_b32 a3, v35
v_accvgpr_write_b32 a4, v36
v_accvgpr_write_b32 a5, v37
v_accvgpr_write_b32 a6, v38
v_accvgpr_write_b32 a7, v39
v_accvgpr_read_b32 v32, a8
v_accvgpr_read_b32 v33, a9
v_accvgpr_read_b32 v34, a10
v_accvgpr_read_b32 v35, a11
v_accvgpr_read_b32 v36, a12
v_accvgpr_read_b32 v37, a13
v_accvgpr_read_b32 v38, a14
v_accvgpr_read_b32 v39, a15
v_mul_f32 v32, v32, v208
v_mul_f32 v33, v33, v208
v_mul_f32 v34, v34, v208
v_mul_f32 v35, v35, v208
v_mul_f32 v36, v36, v208
v_mul_f32 v37, v37, v208
v_mul_f32 v38, v38, v208
v_mul_f32 v39, v39, v208
v_accvgpr_write_b32 a8, v32
v_accvgpr_write_b32 a9, v33
v_accvgpr_write_b32 a10, v34
v_accvgpr_write_b32 a11, v35
v_accvgpr_write_b32 a12, v36
v_accvgpr_write_b32 a13, v37
v_accvgpr_write_b32 a14, v38
v_accvgpr_write_b32 a15, v39
v_accvgpr_read_b32 v32, a16
v_accvgpr_read_b32 v33, a17
v_accvgpr_read_b32 v34, a18
v_accvgpr_read_b32 v35, a19
v_accvgpr_read_b32 v36, a20
v_accvgpr_read_b32 v37, a21
v_accvgpr_read_b32 v38, a22
v_accvgpr_read_b32 v39, a23
v_mul_f32 v32, v32, v208
v_mul_f32 v33, v33, v208
v_mul_f32 v34, v34, v208
v_mul_f32 v35, v35, v208
v_mul_f32 v36, v36, v208
v_mul_f32 v37, v37, v208
v_mul_f32 v38, v38, v208
v_mul_f32 v39, v39, v208
v_accvgpr_write_b32 a16, v32
v_accvgpr_write_b32 a17, v33
v_accvgpr_write_b32 a18, v34
v_accvgpr_write_b32 a19, v35
v_accvgpr_write_b32 a20, v36
v_accvgpr_write_b32 a21, v37
v_accvgpr_write_b32 a22, v38
v_accvgpr_write_b32 a23, v39
v_accvgpr_read_b32 v32, a24
v_accvgpr_read_b32 v33, a25
v_accvgpr_read_b32 v34, a26
v_accvgpr_read_b32 v35, a27
v_accvgpr_read_b32 v36, a28
v_accvgpr_read_b32 v37, a29
v_accvgpr_read_b32 v38, a30
v_accvgpr_read_b32 v39, a31
v_mul_f32 v32, v32, v208
v_mul_f32 v33, v33, v208
v_mul_f32 v34, v34, v208
v_mul_f32 v35, v35, v208
v_mul_f32 v36, v36, v208
v_mul_f32 v37, v37, v208
v_mul_f32 v38, v38, v208
v_mul_f32 v39, v39, v208
v_accvgpr_write_b32 a24, v32
v_accvgpr_write_b32 a25, v33
v_accvgpr_write_b32 a26, v34
v_accvgpr_write_b32 a27, v35
v_accvgpr_write_b32 a28, v36
v_accvgpr_write_b32 a29, v37
v_accvgpr_write_b32 a30, v38
v_accvgpr_write_b32 a31, v39
v_accvgpr_read_b32 v32, a224
v_accvgpr_read_b32 v33, a225
v_accvgpr_read_b32 v34, a226
v_accvgpr_read_b32 v35, a227
v_mul_f32 v32, v32, v208
v_mul_f32 v33, v33, v208
v_mul_f32 v34, v34, v208
v_mul_f32 v35, v35, v208
v_accvgpr_write_b32 a224, v32
v_accvgpr_write_b32 a225, v33
v_accvgpr_write_b32 a226, v34
v_accvgpr_write_b32 a227, v35
v_accvgpr_read_b32 v32, a32
v_accvgpr_read_b32 v33, a33
v_accvgpr_read_b32 v34, a34
v_accvgpr_read_b32 v35, a35
v_accvgpr_read_b32 v36, a36
v_accvgpr_read_b32 v37, a37
v_accvgpr_read_b32 v38, a38
v_accvgpr_read_b32 v39, a39
v_mul_f32 v32, v32, v209
v_mul_f32 v33, v33, v209
v_mul_f32 v34, v34, v209
v_mul_f32 v35, v35, v209
v_mul_f32 v36, v36, v209
v_mul_f32 v37, v37, v209
v_mul_f32 v38, v38, v209
v_mul_f32 v39, v39, v209
v_accvgpr_write_b32 a32, v32
v_accvgpr_write_b32 a33, v33
v_accvgpr_write_b32 a34, v34
v_accvgpr_write_b32 a35, v35
v_accvgpr_write_b32 a36, v36
v_accvgpr_write_b32 a37, v37
v_accvgpr_write_b32 a38, v38
v_accvgpr_write_b32 a39, v39
v_accvgpr_read_b32 v32, a40
v_accvgpr_read_b32 v33, a41
v_accvgpr_read_b32 v34, a42
v_accvgpr_read_b32 v35, a43
v_accvgpr_read_b32 v36, a44
v_accvgpr_read_b32 v37, a45
v_accvgpr_read_b32 v38, a46
v_accvgpr_read_b32 v39, a47
v_mul_f32 v32, v32, v209
v_mul_f32 v33, v33, v209
v_mul_f32 v34, v34, v209
v_mul_f32 v35, v35, v209
v_mul_f32 v36, v36, v209
v_mul_f32 v37, v37, v209
v_mul_f32 v38, v38, v209
v_mul_f32 v39, v39, v209
v_accvgpr_write_b32 a40, v32
v_accvgpr_write_b32 a41, v33
v_accvgpr_write_b32 a42, v34
v_accvgpr_write_b32 a43, v35
v_accvgpr_write_b32 a44, v36
v_accvgpr_write_b32 a45, v37
v_accvgpr_write_b32 a46, v38
v_accvgpr_write_b32 a47, v39
v_accvgpr_read_b32 v32, a48
v_accvgpr_read_b32 v33, a49
v_accvgpr_read_b32 v34, a50
v_accvgpr_read_b32 v35, a51
v_accvgpr_read_b32 v36, a52
v_accvgpr_read_b32 v37, a53
v_accvgpr_read_b32 v38, a54
v_accvgpr_read_b32 v39, a55
v_mul_f32 v32, v32, v209
v_mul_f32 v33, v33, v209
v_mul_f32 v34, v34, v209
v_mul_f32 v35, v35, v209
v_mul_f32 v36, v36, v209
v_mul_f32 v37, v37, v209
v_mul_f32 v38, v38, v209
v_mul_f32 v39, v39, v209
v_accvgpr_write_b32 a48, v32
v_accvgpr_write_b32 a49, v33
v_accvgpr_write_b32 a50, v34
v_accvgpr_write_b32 a51, v35
v_accvgpr_write_b32 a52, v36
v_accvgpr_write_b32 a53, v37
v_accvgpr_write_b32 a54, v38
v_accvgpr_write_b32 a55, v39
v_accvgpr_read_b32 v32, a56
v_accvgpr_read_b32 v33, a57
v_accvgpr_read_b32 v34, a58
v_accvgpr_read_b32 v35, a59
v_accvgpr_read_b32 v36, a60
v_accvgpr_read_b32 v37, a61
v_accvgpr_read_b32 v38, a62
v_accvgpr_read_b32 v39, a63
v_mul_f32 v32, v32, v209
v_mul_f32 v33, v33, v209
v_mul_f32 v34, v34, v209
v_mul_f32 v35, v35, v209
v_mul_f32 v36, v36, v209
v_mul_f32 v37, v37, v209
v_mul_f32 v38, v38, v209
v_mul_f32 v39, v39, v209
v_accvgpr_write_b32 a56, v32
v_accvgpr_write_b32 a57, v33
v_accvgpr_write_b32 a58, v34
v_accvgpr_write_b32 a59, v35
v_accvgpr_write_b32 a60, v36
v_accvgpr_write_b32 a61, v37
v_accvgpr_write_b32 a62, v38
v_accvgpr_write_b32 a63, v39
v_accvgpr_read_b32 v32, a228
v_accvgpr_read_b32 v33, a229
v_accvgpr_read_b32 v34, a230
v_accvgpr_read_b32 v35, a231
v_mul_f32 v32, v32, v209
v_mul_f32 v33, v33, v209
v_mul_f32 v34, v34, v209
v_mul_f32 v35, v35, v209
v_accvgpr_write_b32 a228, v32
v_accvgpr_write_b32 a229, v33
v_accvgpr_write_b32 a230, v34
v_accvgpr_write_b32 a231, v35
v_accvgpr_read_b32 v32, a64
v_accvgpr_read_b32 v33, a65
v_accvgpr_read_b32 v34, a66
v_accvgpr_read_b32 v35, a67
v_accvgpr_read_b32 v36, a68
v_accvgpr_read_b32 v37, a69
v_accvgpr_read_b32 v38, a70
v_accvgpr_read_b32 v39, a71
v_mul_f32 v32, v32, v210
v_mul_f32 v33, v33, v210
v_mul_f32 v34, v34, v210
v_mul_f32 v35, v35, v210
v_mul_f32 v36, v36, v210
v_mul_f32 v37, v37, v210
v_mul_f32 v38, v38, v210
v_mul_f32 v39, v39, v210
v_accvgpr_write_b32 a64, v32
v_accvgpr_write_b32 a65, v33
v_accvgpr_write_b32 a66, v34
v_accvgpr_write_b32 a67, v35
v_accvgpr_write_b32 a68, v36
v_accvgpr_write_b32 a69, v37
v_accvgpr_write_b32 a70, v38
v_accvgpr_write_b32 a71, v39
v_accvgpr_read_b32 v32, a72
v_accvgpr_read_b32 v33, a73
v_accvgpr_read_b32 v34, a74
v_accvgpr_read_b32 v35, a75
v_accvgpr_read_b32 v36, a76
v_accvgpr_read_b32 v37, a77
v_accvgpr_read_b32 v38, a78
v_accvgpr_read_b32 v39, a79
v_mul_f32 v32, v32, v210
v_mul_f32 v33, v33, v210
v_mul_f32 v34, v34, v210
v_mul_f32 v35, v35, v210
v_mul_f32 v36, v36, v210
v_mul_f32 v37, v37, v210
v_mul_f32 v38, v38, v210
v_mul_f32 v39, v39, v210
v_accvgpr_write_b32 a72, v32
v_accvgpr_write_b32 a73, v33
v_accvgpr_write_b32 a74, v34
v_accvgpr_write_b32 a75, v35
v_accvgpr_write_b32 a76, v36
v_accvgpr_write_b32 a77, v37
v_accvgpr_write_b32 a78, v38
v_accvgpr_write_b32 a79, v39
v_accvgpr_read_b32 v32, a80
v_accvgpr_read_b32 v33, a81
v_accvgpr_read_b32 v34, a82
v_accvgpr_read_b32 v35, a83
v_accvgpr_read_b32 v36, a84
v_accvgpr_read_b32 v37, a85
v_accvgpr_read_b32 v38, a86
v_accvgpr_read_b32 v39, a87
v_mul_f32 v32, v32, v210
v_mul_f32 v33, v33, v210
v_mul_f32 v34, v34, v210
v_mul_f32 v35, v35, v210
v_mul_f32 v36, v36, v210
v_mul_f32 v37, v37, v210
v_mul_f32 v38, v38, v210
v_mul_f32 v39, v39, v210
v_accvgpr_write_b32 a80, v32
v_accvgpr_write_b32 a81, v33
v_accvgpr_write_b32 a82, v34
v_accvgpr_write_b32 a83, v35
v_accvgpr_write_b32 a84, v36
v_accvgpr_write_b32 a85, v37
v_accvgpr_write_b32 a86, v38
v_accvgpr_write_b32 a87, v39
v_accvgpr_read_b32 v32, a88
v_accvgpr_read_b32 v33, a89
v_accvgpr_read_b32 v34, a90
v_accvgpr_read_b32 v35, a91
v_accvgpr_read_b32 v36, a92
v_accvgpr_read_b32 v37, a93
v_accvgpr_read_b32 v38, a94
v_accvgpr_read_b32 v39, a95
v_mul_f32 v32, v32, v210
v_mul_f32 v33, v33, v210
v_mul_f32 v34, v34, v210
v_mul_f32 v35, v35, v210
v_mul_f32 v36, v36, v210
v_mul_f32 v37, v37, v210
v_mul_f32 v38, v38, v210
)"
R"(v_mul_f32 v39, v39, v210
v_accvgpr_write_b32 a88, v32
v_accvgpr_write_b32 a89, v33
v_accvgpr_write_b32 a90, v34
v_accvgpr_write_b32 a91, v35
v_accvgpr_write_b32 a92, v36
v_accvgpr_write_b32 a93, v37
v_accvgpr_write_b32 a94, v38
v_accvgpr_write_b32 a95, v39
v_accvgpr_read_b32 v32, a232
v_accvgpr_read_b32 v33, a233
v_accvgpr_read_b32 v34, a234
v_accvgpr_read_b32 v35, a235
v_mul_f32 v32, v32, v210
v_mul_f32 v33, v33, v210
v_mul_f32 v34, v34, v210
v_mul_f32 v35, v35, v210
v_accvgpr_write_b32 a232, v32
v_accvgpr_write_b32 a233, v33
v_accvgpr_write_b32 a234, v34
v_accvgpr_write_b32 a235, v35
v_accvgpr_read_b32 v32, a96
v_accvgpr_read_b32 v33, a97
v_accvgpr_read_b32 v34, a98
v_accvgpr_read_b32 v35, a99
v_accvgpr_read_b32 v36, a100
v_accvgpr_read_b32 v37, a101
v_accvgpr_read_b32 v38, a102
v_accvgpr_read_b32 v39, a103
v_mul_f32 v32, v32, v211
v_mul_f32 v33, v33, v211
v_mul_f32 v34, v34, v211
v_mul_f32 v35, v35, v211
v_mul_f32 v36, v36, v211
v_mul_f32 v37, v37, v211
v_mul_f32 v38, v38, v211
v_mul_f32 v39, v39, v211
v_accvgpr_write_b32 a96, v32
v_accvgpr_write_b32 a97, v33
v_accvgpr_write_b32 a98, v34
v_accvgpr_write_b32 a99, v35
v_accvgpr_write_b32 a100, v36
v_accvgpr_write_b32 a101, v37
v_accvgpr_write_b32 a102, v38
v_accvgpr_write_b32 a103, v39
v_accvgpr_read_b32 v32, a104
v_accvgpr_read_b32 v33, a105
v_accvgpr_read_b32 v34, a106
v_accvgpr_read_b32 v35, a107
v_accvgpr_read_b32 v36, a108
v_accvgpr_read_b32 v37, a109
v_accvgpr_read_b32 v38, a110
v_accvgpr_read_b32 v39, a111
v_mul_f32 v32, v32, v211
v_mul_f32 v33, v33, v211
v_mul_f32 v34, v34, v211
v_mul_f32 v35, v35, v211
v_mul_f32 v36, v36, v211
v_mul_f32 v37, v37, v211
v_mul_f32 v38, v38, v211
v_mul_f32 v39, v39, v211
v_accvgpr_write_b32 a104, v32
v_accvgpr_write_b32 a105, v33
v_accvgpr_write_b32 a106, v34
v_accvgpr_write_b32 a107, v35
v_accvgpr_write_b32 a108, v36
v_accvgpr_write_b32 a109, v37
v_accvgpr_write_b32 a110, v38
v_accvgpr_write_b32 a111, v39
v_accvgpr_read_b32 v32, a112
v_accvgpr_read_b32 v33, a113
v_accvgpr_read_b32 v34, a114
v_accvgpr_read_b32 v35, a115
v_accvgpr_read_b32 v36, a116
v_accvgpr_read_b32 v37, a117
v_accvgpr_read_b32 v38, a118
v_accvgpr_read_b32 v39, a119
v_mul_f32 v32, v32, v211
v_mul_f32 v33, v33, v211
v_mul_f32 v34, v34, v211
v_mul_f32 v35, v35, v211
v_mul_f32 v36, v36, v211
v_mul_f32 v37, v37, v211
v_mul_f32 v38, v38, v211
v_mul_f32 v39, v39, v211
v_accvgpr_write_b32 a112, v32
v_accvgpr_write_b32 a113, v33
v_accvgpr_write_b32 a114, v34
v_accvgpr_write_b32 a115, v35
v_accvgpr_write_b32 a116, v36
v_accvgpr_write_b32 a117, v37
v_accvgpr_write_b32 a118, v38
v_accvgpr_write_b32 a119, v39
v_accvgpr_read_b32 v32, a120
v_accvgpr_read_b32 v33, a121
v_accvgpr_read_b32 v34, a122
v_accvgpr_read_b32 v35, a123
v_accvgpr_read_b32 v36, a124
v_accvgpr_read_b32 v37, a125
v_accvgpr_read_b32 v38, a126
v_accvgpr_read_b32 v39, a127
v_mul_f32 v32, v32, v211
v_mul_f32 v33, v33, v211
v_mul_f32 v34, v34, v211
v_mul_f32 v35, v35, v211
v_mul_f32 v36, v36, v211
v_mul_f32 v37, v37, v211
v_mul_f32 v38, v38, v211
v_mul_f32 v39, v39, v211
v_accvgpr_write_b32 a120, v32
v_accvgpr_write_b32 a121, v33
v_accvgpr_write_b32 a122, v34
v_accvgpr_write_b32 a123, v35
v_accvgpr_write_b32 a124, v36
v_accvgpr_write_b32 a125, v37
v_accvgpr_write_b32 a126, v38
v_accvgpr_write_b32 a127, v39
v_accvgpr_read_b32 v32, a236
v_accvgpr_read_b32 v33, a237
v_accvgpr_read_b32 v34, a238
v_accvgpr_read_b32 v35, a239
v_mul_f32 v32, v32, v211
v_mul_f32 v33, v33, v211
v_mul_f32 v34, v34, v211
v_mul_f32 v35, v35, v211
v_accvgpr_write_b32 a236, v32
v_accvgpr_write_b32 a237, v33
v_accvgpr_write_b32 a238, v34
v_accvgpr_write_b32 a239, v35
s_mov_b32 s69, 0
s_cmp_eq_u32 s54, 16
s_cbranch_scc1 ENDR4Z_%=
s_branch ENP2Z_%=
RSL4Z_%=:
s_cmp_lg_u32 s68, 0
s_cselect_b32 s77, 0xf149f2ca, 0
s_mov_b32 s88, 1
v_mov_b32 v24, v5
v_mov_b32 v25, v6
v_mov_b32 v26, v7
v_mov_b32 v27, v8
v_permlane32_swap_b32 v5, v24
v_permlane32_swap_b32 v6, v25
v_permlane32_swap_b32 v7, v26
v_permlane32_swap_b32 v8, v27
v_max_f32 v5, v5, v24
v_max_f32 v6, v6, v25
v_max_f32 v7, v7, v26
v_max_f32 v8, v8, v27
v_mov_b32 v24, v5
v_mov_b32 v25, v6
v_mov_b32 v26, v7
v_mov_b32 v27, v8
v_permlane16_swap_b32 v5, v24
v_permlane16_swap_b32 v6, v25
v_permlane16_swap_b32 v7, v26
v_permlane16_swap_b32 v8, v27
v_mov_b32 v29, s77
v_mov_b32 v30, 0xefa18f08
v_cmp_lt_f32 vcc, v14, v30
s_nop 1
v_cndmask_b32 v28, v29, v14, vcc
v_cndmask_b32 v14, v14, v10, vcc
v_max3_f32 v5, v5, v24, v28
v_cmp_lt_f32 vcc, v15, v30
s_nop 1
v_cndmask_b32 v28, v29, v15, vcc
v_cndmask_b32 v15, v15, v10, vcc
v_max3_f32 v6, v6, v25, v28
v_cmp_lt_f32 vcc, v16, v30
s_nop 1
v_cndmask_b32 v28, v29, v16, vcc
v_cndmask_b32 v16, v16, v10, vcc
v_max3_f32 v7, v7, v26, v28
v_cmp_lt_f32 vcc, v17, v30
s_nop 1
v_cndmask_b32 v28, v29, v17, vcc
v_cndmask_b32 v17, v17, v10, vcc
v_max3_f32 v8, v8, v27, v28
v_add_f32 v14, v14, v5
v_exp_f32 v208, -v5
v_add_f32 v15, v15, v6
v_exp_f32 v209, -v6
v_add_f32 v16, v16, v7
v_exp_f32 v210, -v7
v_add_f32 v17, v17, v8
v_exp_f32 v211, -v8
v_sub_f32 v64, v64, v5
v_sub_f32 v65, v65, v5
v_sub_f32 v66, v66, v5
v_sub_f32 v67, v67, v5
v_sub_f32 v72, v72, v6
v_sub_f32 v73, v73, v6
v_sub_f32 v74, v74, v6
v_sub_f32 v75, v75, v6
v_sub_f32 v80, v80, v7
v_sub_f32 v81, v81, v7
v_sub_f32 v82, v82, v7
v_sub_f32 v83, v83, v7
v_sub_f32 v88, v88, v8
v_sub_f32 v89, v89, v8
v_sub_f32 v90, v90, v8
v_sub_f32 v91, v91, v8
v_sub_f32 v68, v68, v5
v_sub_f32 v69, v69, v5
v_sub_f32 v70, v70, v5
v_sub_f32 v71, v71, v5
v_sub_f32 v76, v76, v6
v_sub_f32 v77, v77, v6
v_sub_f32 v78, v78, v6
v_sub_f32 v79, v79, v6
v_sub_f32 v84, v84, v7
v_sub_f32 v85, v85, v7
v_sub_f32 v86, v86, v7
v_sub_f32 v87, v87, v7
v_sub_f32 v92, v92, v8
v_sub_f32 v93, v93, v8
v_sub_f32 v94, v94, v8
v_sub_f32 v95, v95, v8
v_sub_f32 v96, v96, v5
v_sub_f32 v97, v97, v5
v_sub_f32 v98, v98, v5
v_sub_f32 v99, v99, v5
v_sub_f32 v104, v104, v6
v_sub_f32 v105, v105, v6
v_sub_f32 v106, v106, v6
v_sub_f32 v107, v107, v6
v_sub_f32 v112, v112, v7
v_sub_f32 v113, v113, v7
v_sub_f32 v114, v114, v7
v_sub_f32 v115, v115, v7
v_sub_f32 v120, v120, v8
v_sub_f32 v121, v121, v8
v_sub_f32 v122, v122, v8
v_sub_f32 v123, v123, v8
v_sub_f32 v100, v100, v5
v_sub_f32 v101, v101, v5
v_sub_f32 v102, v102, v5
v_sub_f32 v103, v103, v5
v_sub_f32 v108, v108, v6
v_sub_f32 v109, v109, v6
v_sub_f32 v110, v110, v6
v_sub_f32 v111, v111, v6
v_sub_f32 v116, v116, v7
v_sub_f32 v117, v117, v7
v_sub_f32 v118, v118, v7
v_sub_f32 v119, v119, v7
v_sub_f32 v124, v124, v8
v_sub_f32 v125, v125, v8
v_sub_f32 v126, v126, v8
v_sub_f32 v127, v127, v8
s_mov_b32 s58, s78
v_min_f32 v208, 1.0, v208
v_cmp_lt_f32 vcc, v14, v30
s_nop 1
v_cndmask_b32 v24, v14, v10, vcc
s_cmp_lg_u64 vcc, 0
s_cselect_b32 s58, 0xff800000, s58
v_min_f32 v209, 1.0, v209
v_cmp_lt_f32 vcc, v15, v30
s_nop 1
v_cndmask_b32 v25, v15, v10, vcc
s_cmp_lg_u64 vcc, 0
s_cselect_b32 s58, 0xff800000, s58
v_min_f32 v210, 1.0, v210
v_cmp_lt_f32 vcc, v16, v30
s_nop 1
v_cndmask_b32 v26, v16, v10, vcc
s_cmp_lg_u64 vcc, 0
s_cselect_b32 s58, 0xff800000, s58
v_min_f32 v211, 1.0, v211
v_cmp_lt_f32 vcc, v17, v30
s_nop 1
v_cndmask_b32 v27, v17, v10, vcc
s_cmp_lg_u64 vcc, 0
s_cselect_b32 s58, 0xff800000, s58
v_sub_f32 v192, 0, v24
v_sub_f32 v193, 0, v24
v_sub_f32 v194, 0, v24
v_sub_f32 v195, 0, v24
v_sub_f32 v196, 0, v25
v_sub_f32 v197, 0, v25
v_sub_f32 v198, 0, v25
v_sub_f32 v199, 0, v25
v_sub_f32 v200, 0, v26
v_sub_f32 v201, 0, v26
v_sub_f32 v202, 0, v26
v_sub_f32 v203, 0, v26
v_sub_f32 v204, 0, v27
v_sub_f32 v205, 0, v27
v_sub_f32 v206, 0, v27
v_sub_f32 v207, 0, v27
s_cmp_lg_u32 s68, 0
s_mov_b32 s68, 0
s_cbranch_scc1 RARER4Z_%=
s_mov_b32 s69, 1
s_bitset1_b32 s57, s54
s_bitset1_b32 s27, s54
s_branch RARER4Z_%=
LFDZ_%=:
v_lshlrev_b32 v5, 9, v22
v_add_u32 v5, 0x10000, v5
s_mov_b64 exec, 15
ds_write_b32 v5, v5 offset:7424
s_mov_b64 exec, -1
s_branch LFDRZ_%=
)"
  : : "s"(__builtin_amdgcn_kernarg_segment_ptr()), "s"(blockIdx.x), "s"(wid), "s"(hw) : "v0", "v1", "v2", "v3", "v4", "v5", "v6", "v7", "v8", "v9", "v10", "v11", "v12", "v13", "v14", "v15", "v16", "v17", "v18", "v19", "v20", "v21", "v22", "v23", "v24", "v25", "v26", "v27", "v28", "v29", "v30", "v31", "v32", "v33", "v34", "v35", "v36", "v37", "v38", "v39", "v40", "v41", "v42", "v43", "v44", "v45", "v46", "v47", "v48", "v49", "v50", "v51", "v52", "v53", "v54", "v55", "v56", "v57", "v58", "v59", "v60", "v61", "v62", "v63", "v64", "v65", "v66", "v67", "v68", "v69", "v70", "v71", "v72", "v73", "v74", "v75", "v76", "v77", "v78", "v79", "v80", "v81", "v82", "v83", "v84", "v85", "v86", "v87", "v88", "v89", "v90", "v91", "v92", "v93", "v94", "v95", "v96", "v97", "v98", "v99", "v100", "v101", "v102", "v103", "v104", "v105", "v106", "v107", "v108", "v109", "v110", "v111", "v112", "v113", "v114", "v115", "v116", "v117", "v118", "v119", "v120", "v121", "v122", "v123", "v124", "v125", "v126", "v127", "v128", "v129", "v130", "v131", "v132", "v133", "v134", "v135", "v136", "v137", "v138", "v139", "v140", "v141", "v142", "v143", "v144", "v145", "v146", "v147", "v148", "v149", "v150", "v151", "v152", "v153", "v154", "v155", "v156", "v157", "v158", "v159", "v160", "v161", "v162", "v163", "v164", "v165", "v166", "v167", "v168", "v169", "v170", "v171", "v172", "v173", "v174", "v175", "v176", "v177", "v178", "v179", "v180", "v181", "v182", "v183", "v184", "v185", "v186", "v187", "v188", "v189", "v190", "v191", "v192", "v193", "v194", "v195", "v196", "v197", "v198", "v199", "v200", "v201", "v202", "v203", "v204", "v205", "v206", "v207", "v208", "v209", "v210", "v211", "v212", "v213", "v214", "v215", "v216", "v217", "v218", "v219", "v220", "v221", "v222", "v223", "v224", "v225", "v226", "v227", "v228", "v229", "v230", "v231", "v232", "v233", "v234", "v235", "v236", "v237", "v238", "v239", "v240", "v241", "v242", "v243", "v244", "v245", "v246", "v247", "v248", "v249", "v250", "v251", "v252", "v253", "v254", "v255", "a0", "a1", "a2", "a3", "a4", "a5", "a6", "a7", "a8", "a9", "a10", "a11", "a12", "a13", "a14", "a15", "a16", "a17", "a18", "a19", "a20", "a21", "a22", "a23", "a24", "a25", "a26", "a27", "a28", "a29", "a30", "a31", "a32", "a33", "a34", "a35", "a36", "a37", "a38", "a39", "a40", "a41", "a42", "a43", "a44", "a45", "a46", "a47", "a48", "a49", "a50", "a51", "a52", "a53", "a54", "a55", "a56", "a57", "a58", "a59", "a60", "a61", "a62", "a63", "a64", "a65", "a66", "a67", "a68", "a69", "a70", "a71", "a72", "a73", "a74", "a75", "a76", "a77", "a78", "a79", "a80", "a81", "a82", "a83", "a84", "a85", "a86", "a87", "a88", "a89", "a90", "a91", "a92", "a93", "a94", "a95", "a96", "a97", "a98", "a99", "a100", "a101", "a102", "a103", "a104", "a105", "a106", "a107", "a108", "a109", "a110", "a111", "a112", "a113", "a114", "a115", "a116", "a117", "a118", "a119", "a120", "a121", "a122", "a123", "a124", "a125", "a126", "a127", "a128", "a129", "a130", "a131", "a132", "a133", "a134", "a135", "a136", "a137", "a138", "a139", "a140", "a141", "a142", "a143", "a144", "a145", "a146", "a147", "a148", "a149", "a150", "a151", "a152", "a153", "a154", "a155", "a156", "a157", "a158", "a159", "a160", "a161", "a162", "a163", "a164", "a165", "a166", "a167", "a168", "a169", "a170", "a171", "a172", "a173", "a174", "a175", "a176", "a177", "a178", "a179", "a180", "a181", "a182", "a183", "a184", "a185", "a186", "a187", "a188", "a189", "a190", "a191", "a192", "a193", "a194", "a195", "a196", "a197", "a198", "a199", "a200", "a201", "a202", "a203", "a204", "a205", "a206", "a207", "a208", "a209", "a210", "a211", "a212", "a213", "a214", "a215", "a216", "a217", "a218", "a219", "a220", "a221", "a222", "a223", "a224", "a225", "a226", "a227", "a228", "a229", "a230", "a231", "a232", "a233", "a234", "a235", "a236", "a237", "a238", "a239", "a240", "a241", "a242", "a243", "a244", "a245", "a246", "a247", "a248", "a249", "a250", "a251", "a252", "a253", "a254", "a255", "s4", "s5", "s6", "s7", "s8", "s9", "s10", "s11", "s12", "s13", "s14", "s15", "s16", "s17", "s18", "s19", "s20", "s21", "s22", "s23", "s24", "s25", "s26", "s27", "s28", "s29", "s30", "s31", "s34", "s35", "s36", "s37", "s38", "s39", "s40", "s41", "s42", "s43", "s44", "s45", "s46", "s47", "s48", "s49", "s50", "s51", "s52", "s53", "s54", "s55", "s56", "s57", "s58", "s59", "s60", "s61", "s62", "s63", "s64", "s65", "s66", "s67", "s68", "s69", "s70", "s71", "s72", "s73", "s74", "s75", "s76", "s77", "s78", "s79", "s80", "s81", "s82", "s83", "s84", "s85", "s86", "s87", "s88", "s89", "s90", "s91", "s92", "s93", "s94", "s95", "s96", "s97", "s98", "s99", "s100", "s101", "vcc", "scc", "memory");
}

extern "C" void kernel_launch(void* const* d_in, const int* in_sizes, int n_in, void* d_out, int out_size, void* d_ws, size_t ws_size, hipStream_t stream) {
  k1_mega<<<256, 256, 0, stream>>>((const u16*)d_in[0], (const u16*)d_in[1], (const u16*)d_in[3], (char*)d_ws, (const u16*)d_in[4], (const u16*)d_in[2], (const u16*)d_in[7], (const u16*)d_in[8],
                                   (const u16*)d_in[5], (const u16*)d_in[9], (u16*)d_out);
}
```
